```python
import torch

def _softmax(x, axis=-1):
    return torch.softmax(x, dim=axis)
def _gelu(x):
    return torch.nn.functional.gelu(x, approximate="tanh")
def _silu(x):
    return torch.nn.functional.silu(x)
def _sigmoid(x):
    return torch.sigmoid(x)
def _layernorm(x, axis=-1, eps=1e-5):
    m = x.mean(dim=axis, keepdim=True)
    v = x.var(dim=axis, keepdim=True, unbiased=False)
    return (x - m) / torch.sqrt(v + eps)
def _scatter_add(tgt, idx, src, axis=0):
    return tgt.clone().index_add_(axis, idx, src)
def _segment_sum(src, seg, n):
    out = torch.zeros((n,) + src.shape[1:], dtype=src.dtype)
    return out.index_add_(0, seg, src)
def _pad(x, pw):
    flat = tuple(v for pair in reversed(pw) for v in pair)
    return torch.nn.functional.pad(x, flat)

kernel_name = 'concat_heads__gqa_block_cached__mha_block_cached__near_ridge'

def setup_inputs():

    _w = (lambda a: (lambda t: t.to(torch.bfloat16) if t.is_floating_point() else t)(torch.as_tensor(a)))
    def _A_setup():
        B, Sq, D = 8, 288, 640
        Hq, Hkv, Sk, Dh = 32, 32, 1024, 128
        assert Hq % Hkv == 0, (Hq, Hkv)
        return {
            "x": _w((lambda *s: torch.randn(*s, dtype=torch.bfloat16) * 0.8)(B, Sq, D)),
            "k_cache": _w((lambda *s: torch.randn(*s, dtype=torch.bfloat16) * 0.8)(B, Hkv, Sk, Dh)),
            "v_cache": _w((lambda *s: torch.randn(*s, dtype=torch.bfloat16) * 0.05)(B, Hkv, Sk, Dh)),
            "wq": _w((lambda *s: torch.randn(*s, dtype=torch.bfloat16) * 0.8)(D, Hq * Dh) * (D ** -0.5)),
            "wo": _w((lambda *s: torch.randn(*s, dtype=torch.bfloat16) * 0.05)(Hq * Dh, D) * ((Hq * Dh) ** -0.5)),
        }
    _A_in = _A_setup()

    def _B_setup():
        B, Sq, D = 8, 288, 640
        H, Sk, Dh = 32, 1024, 128
        return {
            "x": _w((lambda *s: torch.randn(*s, dtype=torch.bfloat16) * 0.8)(B, Sq, D)),
            "k_cache": _w((lambda *s: torch.randn(*s, dtype=torch.bfloat16) * 0.8)(B, H, Sk, Dh)),
            "v_cache": _w((lambda *s: torch.randn(*s, dtype=torch.bfloat16) * 0.05)(B, H, Sk, Dh)),
            "wq": _w((lambda *s: torch.randn(*s, dtype=torch.bfloat16) * 0.8)(D, H * Dh) * (D ** -0.5)),
            "wo": _w((lambda *s: torch.randn(*s, dtype=torch.bfloat16) * 0.05)(H * Dh, D) * ((H * Dh) ** -0.5)),
        }
    _B_in = _B_setup()

    return {
        "A_x": _A_in["x"],
        "A_k_cache": _A_in["k_cache"],
        "A_v_cache": _A_in["v_cache"],
        "A_wq": _A_in["wq"],
        "A_wo": _A_in["wo"],
        "B_x": _B_in["x"],
        "B_k_cache": _B_in["k_cache"],
        "B_v_cache": _B_in["v_cache"],
        "B_wq": _B_in["wq"],
        "B_wo": _B_in["wo"],
    }

def reference(A_x, A_k_cache, A_v_cache, A_wq, A_wo, B_x, B_k_cache, B_v_cache, B_wq, B_wo):
    def _A_ref(x, k_cache, v_cache, wq, wo):
        B, Sq, D = x.shape
        Hq, Dh = 32, 128
        Hkv = k_cache.shape[1]
        rep = Hq // Hkv
        q = (lambda x, *d: x.permute(*d))((x @ wq).reshape(B, Sq, Hq, Dh), 0, 2, 1, 3)
        k = (lambda x, n, axis: torch.repeat_interleave(x, n, dim=axis))(k_cache, rep, axis=1)
        v = (lambda x, n, axis: torch.repeat_interleave(x, n, dim=axis))(v_cache, rep, axis=1)
        scores = torch.einsum("bhqd,bhkd->bhqk", q, k) * (Dh ** -0.5)
        p = _softmax(scores, axis=-1)
        o = torch.einsum("bhqk,bhkd->bhqd", p, v)
        return (lambda x, *d: x.permute(*d))(o, 0, 2, 1, 3).reshape(B, Sq, Hq * Dh) @ wo

    def _B_ref(x, k_cache, v_cache, wq, wo):
        B, Sq, D = x.shape
        H, Dh = 32, 128
        q = (lambda x, *d: x.permute(*d))((x @ wq).reshape(B, Sq, H, Dh), 0, 2, 1, 3)
        scores = torch.einsum("bhqd,bhkd->bhqk", q, k_cache) * (Dh ** -0.5)
        p = _softmax(scores, axis=-1)
        o = torch.einsum("bhqk,bhkd->bhqd", p, v_cache)
        return (lambda x, *d: x.permute(*d))(o, 0, 2, 1, 3).reshape(B, Sq, H * Dh) @ wo

    a = _A_ref(A_x, A_k_cache, A_v_cache, A_wq, A_wo)
    b = _B_ref(B_x, B_k_cache, B_v_cache, B_wq, B_wo)
    return torch.concatenate([a, b], axis=-1)
```

```cpp
#include <hip/hip_runtime.h>
#include <stdint.h>

typedef __attribute__((ext_vector_type(8))) __bf16 bf16x8;
typedef __attribute__((ext_vector_type(4))) float f32x4;
typedef __attribute__((ext_vector_type(4))) uint32_t u32x4;
typedef __attribute__((ext_vector_type(2))) uint32_t u32x2;
typedef unsigned short u16;

#define NB 8
#define SQ 288
#define DM 640
#define NH 32
#define SK 1024
#define DH 128

__device__ __forceinline__ float bf2f(u16 v) { return __uint_as_float(((uint32_t)v) << 16); }
__device__ __forceinline__ uint32_t pk2(float a, float b) {
    typedef __attribute__((ext_vector_type(2))) __bf16 bf16x2;
    typedef __attribute__((ext_vector_type(2))) float f32x2;
    f32x2 f = {a, b};
    bf16x2 r = __builtin_convertvector(f, bf16x2);
    return __builtin_bit_cast(uint32_t, r);
}
__device__ __forceinline__ f32x4 mfma16(u32x4 a, u32x4 b, f32x4 c) {
    return __builtin_amdgcn_mfma_f32_16x16x32_bf16(__builtin_bit_cast(bf16x8, a), __builtin_bit_cast(bf16x8, b), c, 0, 0, 0);
}

__device__ __forceinline__ void slow_head(const u16* __restrict__ x, const u16* __restrict__ kc, const u16* __restrict__ vc,
                          const u16* __restrict__ wq, uint32_t* __restrict__ ows, int head, int wave, int lane) {
    const int c = lane & 15, g = lane >> 4;
    const float qs = 1.4426950408889634f * 0.08838834764831845f;
    for (int qt = wave; qt < 18; qt += 4) {
        u32x4 qf[4];
        for (int u = 0; u < 4; ++u) {
            f32x4 acc0 = {0, 0, 0, 0}, acc1 = {0, 0, 0, 0};
            for (int ks = 0; ks < 20; ++ks) {
                u32x4 xb = *(const u32x4*)(x + (size_t)(qt * 16 + c) * DM + ks * 32 + g * 8);
                int d0 = head * DH + 32 * u + 8 * (c >> 2) + (c & 3);
                u32x4 a0, a1;
                for (int j = 0; j < 4; ++j) {
                    const u16* p = wq + (size_t)(ks * 32 + g * 8 + 2 * j) * (NH * DH) + d0;
                    a0[j] = (uint32_t)p[0] | ((uint32_t)p[NH * DH] << 16);
                    a1[j] = (uint32_t)p[4] | ((uint32_t)p[NH * DH + 4] << 16);
                }
                acc0 = mfma16(a0, xb, acc0);
                acc1 = mfma16(a1, xb, acc1);
            }
            qf[u][0] = pk2(acc0[0] * qs, acc0[1] * qs);
            qf[u][1] = pk2(acc0[2] * qs, acc0[3] * qs);
            qf[u][2] = pk2(acc1[0] * qs, acc1[1] * qs);
            qf[u][3] = pk2(acc1[2] * qs, acc1[3] * qs);
        }
        f32x4 o[8];
        for (int i = 0; i < 8; ++i) o[i] = (f32x4){0, 0, 0, 0};
        float m = -INFINITY, l = 0.f;
        for (int kb = 0; kb < SK / 32; ++kb) {
            f32x4 s0 = {0, 0, 0, 0}, s1 = {0, 0, 0, 0};
            for (int u = 0; u < 4; ++u) {
                u32x4 k0 = *(const u32x4*)(kc + (size_t)(kb * 32 + c) * DH + u * 32 + g * 8);
                u32x4 k1 = *(const u32x4*)(kc + (size_t)(kb * 32 + 16 + c) * DH + u * 32 + g * 8);
                s0 = mfma16(k0, qf[u], s0);
                s1 = mfma16(k1, qf[u], s1);
            }
            float mx = fmaxf(fmaxf(fmaxf(s0[0], s0[1]), fmaxf(s0[2], s0[3])), fmaxf(fmaxf(s1[0], s1[1]), fmaxf(s1[2], s1[3])));
            mx = fmaxf(mx, __shfl_xor(mx, 16));
            mx = fmaxf(mx, __shfl_xor(mx, 32));
            float mn = fmaxf(m, mx);
            float al = __builtin_amdgcn_exp2f(m - mn);
            m = mn;
            float p[8];
            for (int j = 0; j < 4; ++j) { p[j] = __builtin_amdgcn_exp2f(s0[j] - mn); p[4 + j] = __builtin_amdgcn_exp2f(s1[j] - mn); }
            u32x4 pf = {pk2(p[0], p[1]), pk2(p[2], p[3]), pk2(p[4], p[5]), pk2(p[6], p[7])};
            float ps = 0.f;
            for (int j = 0; j < 4; ++j) { ps += __uint_as_float(pf[j] << 16) + __uint_as_float(pf[j] & 0xffff0000u); }
            ps += __shfl_xor(ps, 16);
            ps += __shfl_xor(ps, 32);
            l = l * al + ps;
            for (int dt = 0; dt < 8; ++dt) {
                u32x4 a;
                const u16* pv = vc + (size_t)(kb * 32 + 4 * g) * DH + dt * 16 + c;
                a[0] = (uint32_t)pv[0] | ((uint32_t)pv[DH] << 16);
                a[1] = (uint32_t)pv[2 * DH] | ((uint32_t)pv[3 * DH] << 16);
                a[2] = (uint32_t)pv[16 * DH] | ((uint32_t)pv[17 * DH] << 16);
                a[3] = (uint32_t)pv[18 * DH] | ((uint32_t)pv[19 * DH] << 16);
                o[dt] = o[dt] * al;
                o[dt] = mfma16(a, pf, o[dt]);
            }
        }
        float r = 1.f / l;
        for (int u = 0; u < 4; ++u) {
            u32x4 w = {pk2(o[2 * u][0] * r, o[2 * u][1] * r), pk2(o[2 * u][2] * r, o[2 * u][3] * r),
                       pk2(o[2 * u + 1][0] * r, o[2 * u + 1][1] * r), pk2(o[2 * u + 1][2] * r, o[2 * u + 1][3] * r)};
            *(u32x4*)(ows + ((size_t)(qt * 128 + head * 4 + u) * 256 + lane * 4)) = w;
        }
    }
}

__global__ __launch_bounds__(256, 1) void attn_kernel(const u16* xA, const u16* kA, const u16* vA, const u16* wqA,
                                                      const u16* xB, const u16* kB, const u16* vB, const u16* wqB, uint32_t* ws) {
    int wg = blockIdx.x;
    int blk = wg >> 8, b = (wg >> 5) & 7, head = wg & 31;
    const u16* x = (blk ? xB : xA) + (size_t)b * SQ * DM;
    const u16* kc = (blk ? kB : kA) + (size_t)(b * NH + head) * SK * DH;
    const u16* vc = (blk ? vB : vA) + (size_t)(b * NH + head) * SK * DH;
    const u16* wq = blk ? wqB : wqA;
    slow_head(x, kc, vc, wq, ws + (size_t)(blk * 8 + b) * 18 * 128 * 256, head, threadIdx.x >> 6, threadIdx.x & 63);
}

__global__ __launch_bounds__(256, 1) void oproj_kernel(const uint32_t* ws, const u16* woA, const u16* woB, u16* out) {
    int t = blockIdx.x * 4 + (threadIdx.x >> 6);
    int lane = threadIdx.x & 63, c = lane & 15, g = lane >> 4;
    int nt = t % 40, qt = (t / 40) % 18, bb = t / 720;
    const u16* wo = (bb >> 3) ? woB : woA;
    f32x4 acc = {0, 0, 0, 0};
    for (int ks = 0; ks < 128; ++ks) {
        u32x4 ob = *(const u32x4*)(ws + ((size_t)(bb * 18 + qt) * 128 + ks) * 256 + lane * 4);
        const u16* p = wo + (size_t)(ks * 32 + 4 * g) * DM + nt * 16 + c;
        u32x4 a;
        a[0] = (uint32_t)p[0] | ((uint32_t)p[DM] << 16);
        a[1] = (uint32_t)p[2 * DM] | ((uint32_t)p[3 * DM] << 16);
        a[2] = (uint32_t)p[16 * DM] | ((uint32_t)p[17 * DM] << 16);
        a[3] = (uint32_t)p[18 * DM] | ((uint32_t)p[19 * DM] << 16);
        acc = mfma16(a, ob, acc);
    }
    int q = (bb & 7) * SQ + qt * 16 + c;
    u32x2 w = {pk2(acc[0], acc[1]), pk2(acc[2], acc[3])};
    *(u32x2*)(out + (size_t)q * 1280 + (bb >> 3) * 640 + nt * 16 + 4 * g) = w;
}

extern "C" void kernel_launch(void* const* d_in, const int* in_sizes, int n_in, void* d_out, int out_size, void* d_ws,
                              size_t ws_size, hipStream_t stream) {
    const u16* const* in = (const u16* const*)d_in;
    attn_kernel<<<512, 256, 0, stream>>>(in[0], in[1], in[2], in[3], in[5], in[6], in[7], in[8], (uint32_t*)d_ws);
    oproj_kernel<<<16 * 18 * 40 / 4, 256, 0, stream>>>((const uint32_t*)d_ws, in[4], in[9], (u16*)d_out);
}
```

```cpp
#include <hip/hip_runtime.h>
#include <stdint.h>

typedef __attribute__((ext_vector_type(8))) __bf16 bf16x8;
typedef __attribute__((ext_vector_type(4))) float f32x4;
typedef __attribute__((ext_vector_type(4))) uint32_t u32x4;
typedef __attribute__((ext_vector_type(2))) uint32_t u32x2;
typedef unsigned short u16;

#define SQ 288
#define DM 640
#define NH 32
#define SK 1024
#define DH 128
#define TS 135168
#define FOFF ((size_t)40 << 20)
#define POFF ((size_t)72 << 20)

__device__ __forceinline__ uint32_t pk2(float a, float b) {
    typedef __attribute__((ext_vector_type(2))) __bf16 bf16x2;
    typedef __attribute__((ext_vector_type(2))) float f32x2;
    f32x2 f = {a, b};
    bf16x2 r = __builtin_convertvector(f, bf16x2);
    return __builtin_bit_cast(uint32_t, r);
}
__device__ __forceinline__ f32x4 mfma16(u32x4 a, u32x4 b, f32x4 c) {
    return __builtin_amdgcn_mfma_f32_16x16x32_bf16(__builtin_bit_cast(bf16x8, a), __builtin_bit_cast(bf16x8, b), c, 0, 0, 0);
}

__device__ __forceinline__ void slow_tile(const u16* __restrict__ x, const u16* __restrict__ kc, const u16* __restrict__ vc,
                                          const u16* __restrict__ wq, uint32_t* __restrict__ ows, int head, int qt, int lane,
                                          int umask) {
    const int c = lane & 15, g = lane >> 4;
    const float qs = 1.4426950408889634f * 0.08838834764831845f;
    u32x4 qf[4];
#pragma unroll
    for (int u = 0; u < 4; ++u) {
        f32x4 acc0 = {0, 0, 0, 0}, acc1 = {0, 0, 0, 0};
#pragma unroll 1
        for (int ks = 0; ks < 20; ++ks) {
            u32x4 xb = *(const u32x4*)(x + (size_t)(qt * 16 + c) * DM + ks * 32 + g * 8);
            int d0 = 32 * u + 8 * (c >> 2) + (c & 3);
            u32x4 a0, a1;
#pragma unroll
            for (int j = 0; j < 4; ++j) {
                const u16* p = wq + (size_t)(ks * 32 + g * 8 + 2 * j) * (NH * DH) + d0;
                a0[j] = (uint32_t)p[0] | ((uint32_t)p[NH * DH] << 16);
                a1[j] = (uint32_t)p[4] | ((uint32_t)p[NH * DH + 4] << 16);
            }
            acc0 = mfma16(a0, xb, acc0);
            acc1 = mfma16(a1, xb, acc1);
        }
        qf[u][0] = pk2(acc0[0] * qs, acc0[1] * qs);
        qf[u][1] = pk2(acc0[2] * qs, acc0[3] * qs);
        qf[u][2] = pk2(acc1[0] * qs, acc1[1] * qs);
        qf[u][3] = pk2(acc1[2] * qs, acc1[3] * qs);
    }
    f32x4 o[8];
#pragma unroll
    for (int i = 0; i < 8; ++i) o[i] = (f32x4){0, 0, 0, 0};
    float m = -INFINITY, l = 0.f;
#pragma unroll 1
    for (int kb = 0; kb < SK / 32; ++kb) {
        f32x4 s0 = {0, 0, 0, 0}, s1 = {0, 0, 0, 0};
#pragma unroll
        for (int u = 0; u < 4; ++u) {
            u32x4 k0 = *(const u32x4*)(kc + (size_t)(kb * 32 + c) * DH + u * 32 + g * 8);
            u32x4 k1 = *(const u32x4*)(kc + (size_t)(kb * 32 + 16 + c) * DH + u * 32 + g * 8);
            s0 = mfma16(k0, qf[u], s0);
            s1 = mfma16(k1, qf[u], s1);
        }
        float mx = fmaxf(fmaxf(fmaxf(s0[0], s0[1]), fmaxf(s0[2], s0[3])), fmaxf(fmaxf(s1[0], s1[1]), fmaxf(s1[2], s1[3])));
        mx = fmaxf(mx, __shfl_xor(mx, 16));
        mx = fmaxf(mx, __shfl_xor(mx, 32));
        float mn = fmaxf(m, mx);
        float al = __builtin_amdgcn_exp2f(m - mn);
        m = mn;
        float p[8];
#pragma unroll
        for (int j = 0; j < 4; ++j) { p[j] = __builtin_amdgcn_exp2f(s0[j] - mn); p[4 + j] = __builtin_amdgcn_exp2f(s1[j] - mn); }
        u32x4 pf = {pk2(p[0], p[1]), pk2(p[2], p[3]), pk2(p[4], p[5]), pk2(p[6], p[7])};
        float ps = 0.f;
#pragma unroll
        for (int j = 0; j < 4; ++j) ps += __uint_as_float(pf[j] << 16) + __uint_as_float(pf[j] & 0xffff0000u);
        ps += __shfl_xor(ps, 16);
        ps += __shfl_xor(ps, 32);
        l = l * al + ps;
#pragma unroll
        for (int dt = 0; dt < 8; ++dt) {
            u32x4 a;
            const u16* pv = vc + (size_t)(kb * 32 + 4 * g) * DH + dt * 16 + c;
            a[0] = (uint32_t)pv[0] | ((uint32_t)pv[DH] << 16);
            a[1] = (uint32_t)pv[2 * DH] | ((uint32_t)pv[3 * DH] << 16);
            a[2] = (uint32_t)pv[16 * DH] | ((uint32_t)pv[17 * DH] << 16);
            a[3] = (uint32_t)pv[18 * DH] | ((uint32_t)pv[19 * DH] << 16);
            o[dt] = o[dt] * al;
            o[dt] = mfma16(a, pf, o[dt]);
        }
    }
    float r = 1.f / l;
#pragma unroll
    for (int u = 0; u < 4; ++u) {
        u32x4 w = {pk2(o[2 * u][0] * r, o[2 * u][1] * r), pk2(o[2 * u][2] * r, o[2 * u][3] * r),
                   pk2(o[2 * u + 1][0] * r, o[2 * u + 1][1] * r), pk2(o[2 * u + 1][2] * r, o[2 * u + 1][3] * r)};
        uint32_t* p = ows + ((size_t)qt * (TS / 4) + (head * 4 + u) * 256 + lane * 4);
        if ((umask >> u) & 1) asm volatile("global_store_dwordx4 %0, %1, off sc1\n\ts_nop 1" : : "v"(p), "v"(w) : "memory");
    }
}

#define CLOB_V "v0", "v1", "v2", "v3", "v4", "v5", "v6", "v7", "v8", "v9", "v10", "v11", "v12", "v13", "v14", "v15", "v16", "v17", "v18", "v19", "v20", "v21", "v22", "v23", "v24", "v25", "v26", "v27", "v28", "v29", "v30", "v31", "v32", "v33", "v34", "v35", "v36", "v37", "v38", "v39", "v40", "v41", "v42", "v43", "v44", "v45", "v46", "v47", "v48", "v49", "v50", "v51", "v52", "v53", "v54", "v55", "v56", "v57", "v58", "v59", "v60", "v61", "v62", "v63", "v64", "v65", "v66", "v67", "v68", "v69", "v70", "v71", "v72", "v73", "v74", "v75", "v76", "v77", "v78", "v79", "v80", "v81", "v82", "v83", "v84", "v85", "v86", "v87", "v88", "v89", "v90", "v91", "v92", "v93", "v94", "v95", "v96", "v97", "v98", "v99", "v100", "v101", "v102", "v103", "v104", "v105", "v106", "v107", "v108", "v109", "v110", "v111", "v112", "v113", "v114", "v115", "v116", "v117", "v118", "v119", "v120", "v121", "v122", "v123", "v124", "v125", "v126", "v127", "v128", "v129", "v130", "v131", "v132", "v133", "v134", "v135", "v136", "v137", "v138", "v139", "v140", "v141", "v142", "v143", "v144", "v145", "v146", "v147", "v148", "v149", "v150", "v151", "v152", "v153", "v154", "v155", "v156", "v157", "v158", "v159", "v160", "v161", "v162", "v163", "v164", "v165", "v166", "v167", "v168", "v169", "v170", "v171", "v172", "v173", "v174", "v175", "v176", "v177", "v178", "v179", "v180", "v181", "v182", "v183", "v184", "v185", "v186", "v187", "v188", "v189", "v190", "v191", "v192", "v193", "v194", "v195", "v196", "v197", "v198", "v199", "v200", "v201", "v202", "v203", "v204", "v205", "v206", "v207", "v208", "v209", "v210", "v211", "v212", "v213", "v214", "v215", "v216", "v217", "v218", "v219", "v220", "v221", "v222", "v223", "v224", "v225", "v226", "v227", "v228", "v229", "v230", "v231", "v232", "v233", "v234", "v235", "v236", "v237", "v238", "v239", "v240", "v241", "v242", "v243", "v244", "v245", "v246", "v247", "v248", "v249", "v250", "v251", "v252", "v253", "v254", "v255"
#define CLOB_A "a0", "a1", "a2", "a3", "a4", "a5", "a6", "a7", "a8", "a9", "a10", "a11", "a12", "a13", "a14", "a15", "a16", "a17", "a18", "a19", "a20", "a21", "a22", "a23", "a24", "a25", "a26", "a27", "a28", "a29", "a30", "a31", "a32", "a33", "a34", "a35", "a36", "a37", "a38", "a39", "a40", "a41", "a42", "a43", "a44", "a45", "a46", "a47", "a48", "a49", "a50", "a51", "a52", "a53", "a54", "a55", "a56", "a57", "a58", "a59", "a60", "a61", "a62", "a63", "a64", "a65", "a66", "a67", "a68", "a69", "a70", "a71", "a72", "a73", "a74", "a75", "a76", "a77", "a78", "a79", "a80", "a81", "a82", "a83", "a84", "a85", "a86", "a87", "a88", "a89", "a90", "a91", "a92", "a93", "a94", "a95", "a96", "a97", "a98", "a99", "a100", "a101", "a102", "a103", "a104", "a105", "a106", "a107", "a108", "a109", "a110", "a111", "a112", "a113", "a114", "a115", "a116", "a117", "a118", "a119", "a120", "a121", "a122", "a123", "a124", "a125", "a126", "a127", "a128", "a129", "a130", "a131", "a132", "a133", "a134", "a135", "a136", "a137", "a138", "a139", "a140", "a141", "a142", "a143", "a144", "a145", "a146", "a147", "a148", "a149", "a150", "a151", "a152", "a153", "a154", "a155", "a156", "a157", "a158", "a159", "a160", "a161", "a162", "a163", "a164", "a165", "a166", "a167", "a168", "a169", "a170", "a171", "a172", "a173", "a174", "a175", "a176", "a177", "a178", "a179", "a180", "a181", "a182", "a183", "a184", "a185", "a186", "a187", "a188", "a189", "a190", "a191", "a192", "a193", "a194", "a195", "a196", "a197", "a198", "a199", "a200", "a201", "a202", "a203", "a204", "a205", "a206", "a207", "a208", "a209", "a210", "a211", "a212", "a213", "a214", "a215", "a216", "a217", "a218", "a219", "a220", "a221", "a222", "a223", "a224", "a225", "a226", "a227", "a228", "a229", "a230", "a231", "a232", "a233", "a234", "a235", "a236", "a237", "a238", "a239", "a240", "a241", "a242", "a243", "a244", "a245", "a246", "a247", "a248", "a249", "a250", "a251", "a252", "a253", "a254", "a255"
#define CLOB_S "s40", "s41", "s42", "s43", "s44", "s45", "s46", "s47", "s48", "s49", "s50", "s51", "s52", "s53", "s54", "s55", "s56", "s57", "s58", "s59", "s60", "s61", "s62", "s63", "s64", "s65", "s66", "s67", "s68", "s69", "s70", "s71", "s72", "s73", "s74", "s75", "s76", "s77", "s78", "s79", "s80", "s81", "s82", "s83", "s84", "s85", "s86", "s87", "s88", "s89", "s90", "s91", "s92", "s93", "s94", "s95", "s96", "s97", "s98", "s99"

typedef const u16* cptr;
typedef __attribute__((address_space(4))) const cptr* kargs_t;

#define SETUP(ka)                                                                                     \
    const int xcd = wg & 7, idx = wg >> 3;                                                            \
    const int blk = xcd >> 2, b = 4 * (xcd & 1) + (idx & 3), pair = 8 * ((xcd & 3) >> 1) + (idx >> 2); \
    const u16* x = ka[4 * blk] + (size_t)b * SQ * DM;                                                 \
    const u16* kc = ka[4 * blk + 1] + (size_t)(b * NH + 2 * pair) * SK * DH;                          \
    const u16* vc = ka[4 * blk + 2] + (size_t)(b * NH + 2 * pair) * SK * DH;                          \
    const u16* wq = ka[4 * blk + 3] + pair * 256;                                                     \
    const u16* wo = ka[8 + blk];                                                                      \
    char* ws = (char*)ka[10];                                                                         \
    uint32_t* ows = (uint32_t*)(ws + (size_t)(blk * 8 + b) * 18 * TS);                                \
    char* flags = ws + FOFF + (size_t)(blk * 8 + b) * 8192;                                           \
    char* part = ws + POFF + (size_t)((blk * 8 + b) * 4 + (pair & 3)) * 786432;                       \
    const u16* dst = ka[11] + (size_t)b * SQ * 1280 + blk * 640 + (pair & 3) * 160;
#define OPERANDS                                                                                                             \
    : "=s"(flag)                                                                                                             \
    : "s"(x), "s"(wq), "s"(kc), "s"((uintptr_t)vc - 1024), "s"(ows), "s"(wave), "s"(pair), "s"(0), "s"(idx & 3), "n"(0), \
      "s"(wo), "s"((xcd & 3) * 32 + idx), "s"(flags), "s"(part), "s"(dst), "s"((uint32_t)(uintptr_t)lds)                     \
    : "memory", "vcc", "scc", CLOB_S, CLOB_V, CLOB_A

__global__ __launch_bounds__(256, 1) void fused_kernel(cptr xA, cptr kA, cptr vA, cptr wqA, cptr xB, cptr kB, cptr vB, cptr wqB,
                                                       cptr woA, cptr woB, void* wsp, void* out) {
    __shared__ __attribute__((aligned(1024))) char lds[163840];
    kargs_t ka = (kargs_t)__builtin_amdgcn_kernarg_segment_ptr();
    int wg = blockIdx.x;
    int wave = __builtin_amdgcn_readfirstlane(threadIdx.x >> 6);
    uint32_t flag;
    {
        SETUP(ka)
        asm volatile(
        ".Lk_CODE_START:\n"
        "s_getpc_b64 s[60:61]\n"
        "v_mbcnt_lo_u32_b32 v0, -1, 0\n"
        "s_mov_b32 s62, .Lk_CODE_END-.Lk_CODE_START\n"
        "v_mbcnt_hi_u32_b32 v0, -1, v0\n"
        "s_lshl_b32 s66, %6, 13\n"
        "s_mov_b32 s63, 0x20000\n"
        "v_lshl_add_u32 v3, v0, 7, s66\n"
        "s_mov_b32 s92, 0x8000\n"
        "buffer_load_dword a254, v3, s[60:63], 0 offen\n"
        "s_mov_b64 s[96:97], %13\n"
        "s_mov_b32 s98, 8192\n"
        "s_mov_b32 s99, 0x20000\n"
        "buffer_load_dword v37, off, s[96:99], 0 sc1\n"
        "s_mov_b64 s[44:45], %2\n"
        "s_mov_b32 s47, 0x20000\n"
        "s_lshl_b32 s81, %7, 9\n"
        "s_sub_u32 s46, 5242880, s81\n"
        "v_bfe_u32 v255, v0, 2, 2\n"
        "v_lshlrev_b32 v255, 13, v255\n"
        "v_and_b32 v1, 3, v0\n"
        "v_lshl_add_u32 v33, v1, 7, v255\n"
        "s_lshl_b32 s93, %9, 17\n"
        "s_lshl_b32 s81, %6, 15\n"
        "s_add_u32 s93, s93, s81\n"
        "s_mov_b64 s[40:41], %1\n"
        "s_mov_b32 s42, 368640\n"
        "s_mov_b32 s43, 0x20000\n"
        "s_and_b32 s81, %7, 7\n"
        "s_lshl_b32 s81, s81, 2\n"
        "s_add_u32 s81, s81, %6\n"
        "s_mul_i32 s81, s81, 11520\n"
        "v_min_u32 v36, 8, v0\n"
        "v_mul_u32_u24 v36, 1280, v36\n"
        "v_add_u32 v36, s81, v36\n"
        "s_add_u32 s93, s93, 524288\n"
        "v_lshrrev_b32 v1, 5, v0\n"
        "v_and_b32 v2, 31, v0\n"
        "v_lshlrev_b32 v255, 2, v1\n"
        "v_xor_b32 v2, v2, v255\n"
        "v_lshlrev_b32 v2, 4, v2\n"
        "v_lshl_add_u32 v17, v1, 13, v2\n"
        "s_lshl_b32 s81, %6, 17\n"
        "v_add_u32 v17, s81, v17\n"
        "v_xor_b32 v18, 128, v17\n"
        "v_add_u32 v18, 15360, v18\n"
        "v_add_u32 v19, 30720, v17\n"
        "v_add_u32 v20, 30720, v18\n"
        "v_add_u32 v21, 65536, v17\n"
        "v_add_u32 v22, 65536, v18\n"
        "v_add_u32 v23, 96256, v17\n"
        "v_add_u32 v24, 96256, v18\n"
        "s_mov_b32 s79, 0\n"
        "s_add_u32 m0, s66, 0\n"
        "s_nop 0\n"
        "buffer_load_dwordx4 v17, s[44:47], s79 offen offset:0 lds\n"
        "buffer_load_dwordx4 v18, s[44:47], s79 offen offset:1024 lds\n"
        "buffer_load_dwordx4 v19, s[44:47], s79 offen offset:2048 lds\n"
        "buffer_load_dwordx4 v20, s[44:47], s79 offen offset:3072 lds\n"
        "s_add_u32 m0, s66, 4096\n"
        "s_nop 0\n"
        "buffer_load_dwordx4 v21, s[44:47], s79 offen offset:0 lds\n"
        "buffer_load_dwordx4 v22, s[44:47], s79 offen offset:1024 lds\n"
        "buffer_load_dwordx4 v23, s[44:47], s79 offen offset:2048 lds\n"
        "buffer_load_dwordx4 v24, s[44:47], s79 offen offset:3072 lds\n"
        "s_mov_b64 s[40:41], %1\n"
        "s_mov_b32 s42, 368640\n"
        "s_mov_b32 s43, 0x20000\n"
        "s_and_b32 s78, %6, 1\n"
        "s_mul_i32 s90, %6, 9216\n"
        "v_lshrrev_b32 v2, 4, v0\n"
        "v_and_b32 v1, 7, v0\n"
        "v_xor_b32 v1, v1, v2\n"
        "v_lshlrev_b32 v1, 4, v1\n"
        "v_lshrrev_b32 v255, 3, v0\n"
        "v_mul_u32_u24 v255, 1280, v255\n"
        "v_add_u32 v1, v1, v255\n"
        "s_lshl_b32 s81, s78, 6\n"
        "v_xor_b32 v1, s81, v1\n"
        "s_mul_i32 s81, %6, 92160\n"
        "v_add_u32 v4, s81, v1\n"
        "v_xor_b32 v5, 64, v4\n"
        "v_add_u32 v5, 9216, v5\n"
        "v_add_u32 v6, 18432, v4\n"
        "v_add_u32 v7, 18432, v5\n"
        "v_add_u32 v8, 40960, v4\n"
        "v_add_u32 v9, 40960, v5\n"
        "v_add_u32 v10, 59392, v4\n"
        "v_add_u32 v11, 59392, v5\n"
        "v_add_u32 v12, 81920, v4\n"
        "s_mov_b32 s89, 0\n"
        "s_add_u32 m0, s90, 65536\n"
        "s_nop 0\n"
        "buffer_load_dwordx4 v4, s[40:43], s89 offen offset:0 lds\n"
        "buffer_load_dwordx4 v5, s[40:43], s89 offen offset:1024 lds\n"
        "buffer_load_dwordx4 v6, s[40:43], s89 offen offset:2048 lds\n"
        "buffer_load_dwordx4 v7, s[40:43], s89 offen offset:3072 lds\n"
        "s_add_u32 m0, s90, 69632\n"
        "s_nop 0\n"
        "buffer_load_dwordx4 v8, s[40:43], s89 offen offset:0 lds\n"
        "buffer_load_dwordx4 v9, s[40:43], s89 offen offset:1024 lds\n"
        "buffer_load_dwordx4 v10, s[40:43], s89 offen offset:2048 lds\n"
        "buffer_load_dwordx4 v11, s[40:43], s89 offen offset:3072 lds\n"
        "s_add_u32 m0, s90, 73728\n"
        "s_nop 0\n"
        "buffer_load_dwordx4 v12, s[40:43], s89 offen offset:0 lds\n"
        "buffer_load_dword a255, v3, s[60:63], s92 offen\n"
        "v_and_b32 v1, 15, v0\n"
        "s_mov_b64 s[48:49], %3\n"
        "s_mov_b32 s50, 524288\n"
        "s_mov_b32 s51, 0x20000\n"
        "s_mov_b64 s[52:53], %4\n"
        "s_mov_b32 s54, 525312\n"
        "s_mov_b32 s55, 0x20000\n"
        "s_mov_b32 s64, %6\n"
        "s_mov_b32 s80, %7\n"
        "s_lshl_b32 s65, s64, 12\n"
        "v_lshrrev_b32 v253, 1, v1\n"
        "v_xor_b32 v253, v253, v2\n"
        "v_lshlrev_b32 v253, 4, v253\n"
        "v_lshl_add_u32 v253, v1, 7, v253\n"
        "s_add_u32 s81, s66, 65536\n"
        "v_add_u32 v13, s81, v253\n"
        "s_lshr_b32 s85, %7, 2\n"
        "s_add_u32 s72, s85, 0\n"
        "s_and_b32 s72, s72, 3\n"
        "v_lshl_add_u32 v40, s72, 11, v13\n"
        "v_xor_b32 v41, 64, v40\n"
        "s_add_u32 s73, s85, 1\n"
        "s_and_b32 s73, s73, 3\n"
        "v_lshl_add_u32 v42, s73, 11, v13\n"
        "v_xor_b32 v43, 64, v42\n"
        "s_add_u32 s74, s85, 2\n"
        "s_and_b32 s74, s74, 3\n"
        "v_lshl_add_u32 v44, s74, 11, v13\n"
        "v_xor_b32 v45, 64, v44\n"
        "s_add_u32 s75, s85, 3\n"
        "s_and_b32 s75, s75, 3\n"
        "v_lshl_add_u32 v46, s75, 11, v13\n"
        "v_xor_b32 v47, 64, v46\n"
        "s_lshr_b32 s81, s64, 1\n"
        "s_lshl_b32 s81, s81, 11\n"
        "s_add_u32 s81, s81, 98304\n"
        "v_add_u32 v15, s81, v253\n"
        "v_xor_b32 v16, 64, v15\n"
        "v_lshrrev_b32 v253, 2, v1\n"
        "v_lshl_add_u32 v255, v2, 3, v253\n"
        "v_lshlrev_b32 v255, 9, v255\n"
        "v_and_b32 v251, 3, v1\n"
        "v_lshl_add_u32 v255, v251, 4, v255\n"
        "v_lshl_add_u32 v255, v253, 6, v255\n"
        "s_lshl_b32 s81, s78, 7\n"
        "v_xor_b32 v25, s81, v255\n"
        "v_xor_b32 v26, 64, v25\n"
        "v_xor_b32 v27, 128, v25\n"
        "v_xor_b32 v28, 192, v25\n"
        "s_mov_b32 s79, 524288\n"
        "s_add_u32 m0, s66, 32768\n"
        "s_nop 0\n"
        "buffer_load_dwordx4 v17, s[44:47], s79 offen offset:0 lds\n"
        "buffer_load_dwordx4 v18, s[44:47], s79 offen offset:1024 lds\n"
        "buffer_load_dwordx4 v19, s[44:47], s79 offen offset:2048 lds\n"
        "buffer_load_dwordx4 v20, s[44:47], s79 offen offset:3072 lds\n"
        "s_add_u32 m0, s66, 36864\n"
        "s_nop 0\n"
        "buffer_load_dwordx4 v21, s[44:47], s79 offen offset:0 lds\n"
        "buffer_load_dwordx4 v22, s[44:47], s79 offen offset:1024 lds\n"
        "buffer_load_dwordx4 v23, s[44:47], s79 offen offset:2048 lds\n"
        "buffer_load_dwordx4 v24, s[44:47], s79 offen offset:3072 lds\n"
        "v_xor_b32 v253, v1, v2\n"
        "v_lshlrev_b32 v253, 4, v253\n"
        "v_lshl_add_u32 v244, v2, 8, v253\n"
        "v_add_u32 v244, s65, v244\n"
        "v_xor_b32 v245, 64, v244\n"
        "v_xor_b32 v246, 128, v244\n"
        "v_xor_b32 v247, 192, v244\n"
        "v_lshl_add_u32 v253, v1, 8, v253\n"
        "v_xor_b32 v253, s81, v253\n"
        "s_lshl_b32 s81, s78, 13\n"
        "s_add_u32 s81, s81, 65536\n"
        "v_add_u32 v232, s81, v253\n"
        "v_xor_b32 v233, 8192, v232\n"
        "v_xor_b32 v234, 64, v232\n"
        "v_xor_b32 v235, 64, v233\n"
        "v_xor_b32 v236, 128, v232\n"
        "v_xor_b32 v237, 128, v233\n"
        "v_xor_b32 v238, 192, v232\n"
        "v_xor_b32 v239, 192, v233\n"
        "v_lshrrev_b32 v253, 1, v2\n"
        "v_lshlrev_b32 v253, 11, v253\n"
        "v_and_b32 v255, 1, v2\n"
        "v_lshl_add_u32 v253, v255, 8, v253\n"
        "v_lshl_add_u32 v253, v255, 5, v253\n"
        "v_lshrrev_b32 v255, 2, v1\n"
        "v_lshl_add_u32 v253, v255, 6, v253\n"
        "v_and_b32 v255, 3, v1\n"
        "v_lshl_add_u32 v253, v255, 3, v253\n"
        "s_add_u32 s81, s81, 49152\n"
        "v_add_u32 v240, s81, v253\n"
        "v_xor_b32 v241, 32, v240\n"
        "v_xor_b32 v242, 8192, v240\n"
        "v_xor_b32 v243, 8192, v241\n"
        "v_lshrrev_b32 v253, 2, v0\n"
        "v_and_b32 v253, 7, v253\n"
        "v_lshlrev_b32 v253, 8, v253\n"
        "v_and_b32 v255, 1, v2\n"
        "v_lshlrev_b32 v255, 1, v255\n"
        "v_and_b32 v251, 3, v0\n"
        "v_xor_b32 v255, v255, v251\n"
        "v_lshl_add_u32 v253, v255, 4, v253\n"
        "v_lshrrev_b32 v255, 5, v0\n"
        "v_lshl_add_u32 v253, v255, 6, v253\n"
        "v_add_u32 v253, s65, v253\n"
        "v_add_u32 v248, 1024, v253\n"
        "v_add_u32 v249, 128, v253\n"
        "v_lshlrev_b32 v250, 4, v0\n"
        "s_lshl_b32 s81, s64, 14\n"
        "v_add_u32 v251, s81, v250\n"
        "v_xor_b32 v252, 16384, v251\n"
        "s_mov_b64 s[56:57], %11\n"
        "s_mov_b32 s58, 5242880\n"
        "s_mov_b32 s59, 0x20000\n"
        "s_lshl_b32 s81, %12, 2\n"
        "s_add_u32 s81, s81, s64\n"
        "s_mul_i32 s81, s81, 10240\n"
        "v_lshl_add_u32 v35, v1, 7, s81\n"
        "s_lshl_b32 s80, s80, 13\n"
        "s_mul_i32 s81, s64, 540672\n"
        "s_add_u32 s81, s80, s81\n"
        "s_mul_i32 s72, s72, 135168\n"
        "s_add_u32 s72, s72, s81\n"
        "s_mul_i32 s73, s73, 135168\n"
        "s_add_u32 s73, s73, s81\n"
        "s_mul_i32 s74, s74, 135168\n"
        "s_add_u32 s74, s74, s81\n"
        "s_mul_i32 s75, s75, 135168\n"
        "s_add_u32 s75, s75, s81\n"
        "s_lshr_b32 s81, s64, 1\n"
        "s_mul_i32 s81, s81, 135168\n"
        "s_add_u32 s76, s80, s81\n"
        "s_lshl_b32 s81, s78, 11\n"
        "s_add_u32 s76, s76, s81\n"
        "s_add_u32 s76, s76, 2162688\n"
        "s_mov_b32 s70, 0x3e0293ee\n"
        "s_mov_b32 s71, 0x3e0293ee\n"
        "s_mov_b32 s84, 0x71800000\n"
        "v_mov_b32 v253, v3\n"
        "s_mov_b32 s82, .Lk_QEPI-.Lk_CODE_START\n"
        "s_mov_b32 s77, 0\n"
        "s_mov_b32 s69, 0\n"
        "s_waitcnt vmcnt(9)\n"
        "s_barrier\n"
        "ds_read_b64_tr_b16 a[208:209], v25 offset:0\n"
        "ds_read_b64_tr_b16 a[210:211], v25 offset:2048\n"
        "ds_read_b64_tr_b16 a[212:213], v25 offset:16384\n"
        "ds_read_b64_tr_b16 a[214:215], v25 offset:18432\n"
        "ds_read_b128 a[128:131], v40 offset:0\n"
        "ds_read_b128 a[132:135], v41 offset:0\n"
        "ds_read_b128 a[136:139], v42 offset:0\n"
        "ds_read_b128 a[140:143], v43 offset:0\n"
        "ds_read_b128 a[144:147], v44 offset:0\n"
        "ds_read_b128 a[148:151], v45 offset:0\n"
        "ds_read_b128 a[152:155], v46 offset:0\n"
        "ds_read_b128 a[156:159], v47 offset:0\n"
        "ds_read_b128 a[160:163], v15 offset:0\n"
        "ds_read_b128 a[164:167], v16 offset:0\n"
        "ds_read_b64_tr_b16 a[216:217], v25 offset:8\n"
        "ds_read_b64_tr_b16 a[218:219], v25 offset:2056\n"
        "ds_read_b64_tr_b16 a[220:221], v25 offset:16392\n"
        "ds_read_b64_tr_b16 a[222:223], v25 offset:18440\n"
        "ds_read_b64_tr_b16 a[224:225], v26 offset:0\n"
        "ds_read_b64_tr_b16 a[226:227], v26 offset:2048\n"
        "ds_read_b64_tr_b16 a[228:229], v26 offset:16384\n"
        "ds_read_b64_tr_b16 a[230:231], v26 offset:18432\n"
        "ds_read_b64_tr_b16 a[232:233], v26 offset:8\n"
        "ds_read_b64_tr_b16 a[234:235], v26 offset:2056\n"
        "ds_read_b64_tr_b16 a[236:237], v26 offset:16392\n"
        "ds_read_b64_tr_b16 a[238:239], v26 offset:18440\n"
        "s_waitcnt lgkmcnt(15)\n"
        "v_mfma_f32_16x16x32_bf16 v[88:91], a[208:211], a[128:131], 0\n"
        "s_mov_b32 s89, 128\n"
        "s_add_u32 m0, s90, 102400\n"
        "v_mfma_f32_16x16x32_bf16 v[88:91], a[212:215], a[132:135], v[88:91]\n"
        "buffer_load_dwordx4 v4, s[40:43], s89 offen offset:0 lds\n"
        "v_mfma_f32_16x16x32_bf16 v[120:123], a[208:211], a[136:139], 0\n"
        "v_mfma_f32_16x16x32_bf16 v[120:123], a[212:215], a[140:143], v[120:123]\n"
        "v_mfma_f32_16x16x32_bf16 v[152:155], a[208:211], a[144:147], 0\n"
        "v_mfma_f32_16x16x32_bf16 v[152:155], a[212:215], a[148:151], v[152:155]\n"
        "buffer_load_dwordx4 v5, s[40:43], s89 offen offset:1024 lds\n"
        "v_mfma_f32_16x16x32_bf16 v[184:187], a[208:211], a[152:155], 0\n"
        "s_waitcnt lgkmcnt(0)\n"
        "v_mfma_f32_16x16x32_bf16 v[184:187], a[212:215], a[156:159], v[184:187]\n"
        "v_mfma_f32_16x16x32_bf16 v[216:219], a[208:211], a[160:163], 0\n"
        "ds_read_b64_tr_b16 a[208:209], v27 offset:0\n"
        "v_mfma_f32_16x16x32_bf16 v[216:219], a[212:215], a[164:167], v[216:219]\n"
        "ds_read_b64_tr_b16 a[210:211], v27 offset:2048\n"
        "v_mfma_f32_16x16x32_bf16 v[92:95], a[216:219], a[128:131], 0\n"
        "ds_read_b64_tr_b16 a[212:213], v27 offset:16384\n"
        "v_mfma_f32_16x16x32_bf16 v[92:95], a[220:223], a[132:135], v[92:95]\n"
        "ds_read_b64_tr_b16 a[214:215], v27 offset:18432\n"
        "v_mfma_f32_16x16x32_bf16 v[124:127], a[216:219], a[136:139], 0\n"
        "buffer_load_dwordx4 v6, s[40:43], s89 offen offset:2048 lds\n"
        "v_mfma_f32_16x16x32_bf16 v[124:127], a[220:223], a[140:143], v[124:127]\n"
        "v_mfma_f32_16x16x32_bf16 v[156:159], a[216:219], a[144:147], 0\n"
        "v_mfma_f32_16x16x32_bf16 v[156:159], a[220:223], a[148:151], v[156:159]\n"
        "v_mfma_f32_16x16x32_bf16 v[188:191], a[216:219], a[152:155], 0\n"
        "buffer_load_dwordx4 v7, s[40:43], s89 offen offset:3072 lds\n"
        "v_mfma_f32_16x16x32_bf16 v[188:191], a[220:223], a[156:159], v[188:191]\n"
        "s_add_u32 m0, s90, 106496\n"
        "v_mfma_f32_16x16x32_bf16 v[220:223], a[216:219], a[160:163], 0\n"
        "ds_read_b64_tr_b16 a[216:217], v27 offset:8\n"
        "v_mfma_f32_16x16x32_bf16 v[220:223], a[220:223], a[164:167], v[220:223]\n"
        "ds_read_b64_tr_b16 a[218:219], v27 offset:2056\n"
        "v_mfma_f32_16x16x32_bf16 v[96:99], a[224:227], a[128:131], 0\n"
        "s_barrier\n"
        "ds_read_b64_tr_b16 a[220:221], v27 offset:16392\n"
        "v_mfma_f32_16x16x32_bf16 v[96:99], a[228:231], a[132:135], v[96:99]\n"
        "ds_read_b64_tr_b16 a[222:223], v27 offset:18440\n"
        "v_mfma_f32_16x16x32_bf16 v[128:131], a[224:227], a[136:139], 0\n"
        "buffer_load_dwordx4 v8, s[40:43], s89 offen offset:0 lds\n"
        "v_mfma_f32_16x16x32_bf16 v[128:131], a[228:231], a[140:143], v[128:131]\n"
        "v_mfma_f32_16x16x32_bf16 v[160:163], a[224:227], a[144:147], 0\n"
        "v_mfma_f32_16x16x32_bf16 v[160:163], a[228:231], a[148:151], v[160:163]\n"
        "v_mfma_f32_16x16x32_bf16 v[192:195], a[224:227], a[152:155], 0\n"
        "buffer_load_dwordx4 v9, s[40:43], s89 offen offset:1024 lds\n"
        "v_mfma_f32_16x16x32_bf16 v[192:195], a[228:231], a[156:159], v[192:195]\n"
        "v_mfma_f32_16x16x32_bf16 v[224:227], a[224:227], a[160:163], 0\n"
        "ds_read_b64_tr_b16 a[224:225], v28 offset:0\n"
        "v_mfma_f32_16x16x32_bf16 v[224:227], a[228:231], a[164:167], v[224:227]\n"
        "ds_read_b64_tr_b16 a[226:227], v28 offset:2048\n"
        "v_mfma_f32_16x16x32_bf16 v[100:103], a[232:235], a[128:131], 0\n"
        "ds_read_b64_tr_b16 a[228:229], v28 offset:16384\n"
        "v_mfma_f32_16x16x32_bf16 v[100:103], a[236:239], a[132:135], v[100:103]\n"
        "ds_read_b64_tr_b16 a[230:231], v28 offset:18432\n"
        "v_mfma_f32_16x16x32_bf16 v[132:135], a[232:235], a[136:139], 0\n"
        "buffer_load_dwordx4 v10, s[40:43], s89 offen offset:2048 lds\n"
        "v_mfma_f32_16x16x32_bf16 v[132:135], a[236:239], a[140:143], v[132:135]\n"
        "v_mfma_f32_16x16x32_bf16 v[164:167], a[232:235], a[144:147], 0\n"
        "v_mfma_f32_16x16x32_bf16 v[164:167], a[236:239], a[148:151], v[164:167]\n"
        "v_mfma_f32_16x16x32_bf16 v[196:199], a[232:235], a[152:155], 0\n"
        "buffer_load_dwordx4 v11, s[40:43], s89 offen offset:3072 lds\n"
        "v_mfma_f32_16x16x32_bf16 v[196:199], a[236:239], a[156:159], v[196:199]\n"
        "s_add_u32 m0, s90, 110592\n"
        "v_mfma_f32_16x16x32_bf16 v[228:231], a[232:235], a[160:163], 0\n"
        "ds_read_b64_tr_b16 a[232:233], v28 offset:8\n"
        "v_mfma_f32_16x16x32_bf16 v[228:231], a[236:239], a[164:167], v[228:231]\n"
        "s_waitcnt lgkmcnt(1)\n"
        "ds_read_b64_tr_b16 a[234:235], v28 offset:2056\n"
        "v_mfma_f32_16x16x32_bf16 v[104:107], a[208:211], a[128:131], 0\n"
        "ds_read_b64_tr_b16 a[236:237], v28 offset:16392\n"
        "v_mfma_f32_16x16x32_bf16 v[104:107], a[212:215], a[132:135], v[104:107]\n"
        "ds_read_b64_tr_b16 a[238:239], v28 offset:18440\n"
        "v_mfma_f32_16x16x32_bf16 v[136:139], a[208:211], a[136:139], 0\n"
        "buffer_load_dwordx4 v12, s[40:43], s89 offen offset:0 lds\n"
        "v_mfma_f32_16x16x32_bf16 v[136:139], a[212:215], a[140:143], v[136:139]\n"
        "s_add_u32 s93, s93, 524288\n"
        "v_mfma_f32_16x16x32_bf16 v[168:171], a[208:211], a[144:147], 0\n"
        "v_mfma_f32_16x16x32_bf16 v[168:171], a[212:215], a[148:151], v[168:171]\n"
        "v_mfma_f32_16x16x32_bf16 v[200:203], a[208:211], a[152:155], 0\n"
        "ds_read_b64_tr_b16 a[208:209], v25 offset:256\n"
        "v_mfma_f32_16x16x32_bf16 v[200:203], a[212:215], a[156:159], v[200:203]\n"
        "ds_read_b64_tr_b16 a[210:211], v25 offset:2304\n"
        "v_mfma_f32_16x16x32_bf16 v[108:111], a[216:219], a[128:131], 0\n"
        "ds_read_b64_tr_b16 a[212:213], v25 offset:16640\n"
        "v_mfma_f32_16x16x32_bf16 v[108:111], a[220:223], a[132:135], v[108:111]\n"
        "ds_read_b64_tr_b16 a[214:215], v25 offset:18688\n"
        "v_mfma_f32_16x16x32_bf16 v[140:143], a[216:219], a[136:139], 0\n"
        "buffer_load_dword a246, v33, s[44:47], s93 offen\n"
        "v_mfma_f32_16x16x32_bf16 v[140:143], a[220:223], a[140:143], v[140:143]\n"
        "v_mfma_f32_16x16x32_bf16 v[172:175], a[216:219], a[144:147], 0\n"
        "v_mfma_f32_16x16x32_bf16 v[172:175], a[220:223], a[148:151], v[172:175]\n"
        "v_mfma_f32_16x16x32_bf16 v[204:207], a[216:219], a[152:155], 0\n"
        "ds_read_b64_tr_b16 a[216:217], v25 offset:264\n"
        "v_mfma_f32_16x16x32_bf16 v[204:207], a[220:223], a[156:159], v[204:207]\n"
        "ds_read_b64_tr_b16 a[218:219], v25 offset:2312\n"
        "v_mfma_f32_16x16x32_bf16 v[112:115], a[224:227], a[128:131], 0\n"
        "ds_read_b64_tr_b16 a[220:221], v25 offset:16648\n"
        "v_mfma_f32_16x16x32_bf16 v[112:115], a[228:231], a[132:135], v[112:115]\n"
        "ds_read_b64_tr_b16 a[222:223], v25 offset:18696\n"
        "v_mfma_f32_16x16x32_bf16 v[144:147], a[224:227], a[136:139], 0\n"
        "buffer_load_dword a248, v36, s[40:43], 0 offen offset:384\n"
        "v_mfma_f32_16x16x32_bf16 v[144:147], a[228:231], a[140:143], v[144:147]\n"
        "s_add_u32 s93, s93, 524288\n"
        "v_mfma_f32_16x16x32_bf16 v[176:179], a[224:227], a[144:147], 0\n"
        "v_mfma_f32_16x16x32_bf16 v[176:179], a[228:231], a[148:151], v[176:179]\n"
        "v_mfma_f32_16x16x32_bf16 v[208:211], a[224:227], a[152:155], 0\n"
        "ds_read_b64_tr_b16 a[224:225], v26 offset:256\n"
        "v_mfma_f32_16x16x32_bf16 v[208:211], a[228:231], a[156:159], v[208:211]\n"
        "s_waitcnt lgkmcnt(2)\n"
        "ds_read_b64_tr_b16 a[226:227], v26 offset:2304\n"
        "v_mfma_f32_16x16x32_bf16 v[116:119], a[232:235], a[128:131], 0\n"
        "ds_read_b64_tr_b16 a[228:229], v26 offset:16640\n"
        "v_mfma_f32_16x16x32_bf16 v[116:119], a[236:239], a[132:135], v[116:119]\n"
        "ds_read_b64_tr_b16 a[230:231], v26 offset:18688\n"
        "v_mfma_f32_16x16x32_bf16 v[148:151], a[232:235], a[136:139], 0\n"
        "buffer_load_dword a247, v33, s[44:47], s93 offen\n"
        "v_mfma_f32_16x16x32_bf16 v[148:151], a[236:239], a[140:143], v[148:151]\n"
        "v_mfma_f32_16x16x32_bf16 v[180:183], a[232:235], a[144:147], 0\n"
        "v_mfma_f32_16x16x32_bf16 v[180:183], a[236:239], a[148:151], v[180:183]\n"
        "v_mfma_f32_16x16x32_bf16 v[212:215], a[232:235], a[152:155], 0\n"
        "ds_read_b64_tr_b16 a[232:233], v26 offset:264\n"
        "v_mfma_f32_16x16x32_bf16 v[212:215], a[236:239], a[156:159], v[212:215]\n"
        "ds_read_b64_tr_b16 a[234:235], v26 offset:2312\n"
        "v_mfma_f32_16x16x32_bf16 a[0:3], a[208:211], a[128:131], 0\n"
        "ds_read_b64_tr_b16 a[236:237], v26 offset:16648\n"
        "v_mfma_f32_16x16x32_bf16 a[0:3], a[212:215], a[132:135], a[0:3]\n"
        "ds_read_b64_tr_b16 a[238:239], v26 offset:18696\n"
        "v_mfma_f32_16x16x32_bf16 a[32:35], a[208:211], a[136:139], 0\n"
        "buffer_load_dword a249, v36, s[40:43], 0 offen offset:512\n"
        "v_mfma_f32_16x16x32_bf16 a[32:35], a[212:215], a[140:143], a[32:35]\n"
        "s_mov_b32 s89, 256\n"
        "s_add_u32 m0, s90, 65536\n"
        "v_mfma_f32_16x16x32_bf16 a[64:67], a[208:211], a[144:147], 0\n"
        "v_mfma_f32_16x16x32_bf16 a[64:67], a[212:215], a[148:151], a[64:67]\n"
        "v_mfma_f32_16x16x32_bf16 a[96:99], a[208:211], a[152:155], 0\n"
        "buffer_load_dwordx4 v4, s[40:43], s89 offen offset:0 lds\n"
        "v_mfma_f32_16x16x32_bf16 a[96:99], a[212:215], a[156:159], a[96:99]\n"
        "v_mfma_f32_16x16x32_bf16 v[68:71], a[208:211], a[160:163], 0\n"
        "ds_read_b64_tr_b16 a[208:209], v27 offset:256\n"
        "v_mfma_f32_16x16x32_bf16 v[68:71], a[212:215], a[164:167], v[68:71]\n"
        "ds_read_b64_tr_b16 a[210:211], v27 offset:2304\n"
        "v_mfma_f32_16x16x32_bf16 a[4:7], a[216:219], a[128:131], 0\n"
        "s_waitcnt lgkmcnt(2)\n"
        "ds_read_b64_tr_b16 a[212:213], v27 offset:16640\n"
        "v_mfma_f32_16x16x32_bf16 a[4:7], a[220:223], a[132:135], a[4:7]\n"
        "ds_read_b64_tr_b16 a[214:215], v27 offset:18688\n"
        "v_mfma_f32_16x16x32_bf16 a[36:39], a[216:219], a[136:139], 0\n"
        "buffer_load_dwordx4 v5, s[40:43], s89 offen offset:1024 lds\n"
        "v_mfma_f32_16x16x32_bf16 a[36:39], a[220:223], a[140:143], a[36:39]\n"
        "v_mfma_f32_16x16x32_bf16 a[68:71], a[216:219], a[144:147], 0\n"
        "v_mfma_f32_16x16x32_bf16 a[68:71], a[220:223], a[148:151], a[68:71]\n"
        "v_mfma_f32_16x16x32_bf16 a[100:103], a[216:219], a[152:155], 0\n"
        "buffer_load_dwordx4 v6, s[40:43], s89 offen offset:2048 lds\n"
        "v_mfma_f32_16x16x32_bf16 a[100:103], a[220:223], a[156:159], a[100:103]\n"
        "v_mfma_f32_16x16x32_bf16 v[72:75], a[216:219], a[160:163], 0\n"
        "ds_read_b64_tr_b16 a[216:217], v27 offset:264\n"
        "v_mfma_f32_16x16x32_bf16 v[72:75], a[220:223], a[164:167], v[72:75]\n"
        "ds_read_b64_tr_b16 a[218:219], v27 offset:2312\n"
        "v_mfma_f32_16x16x32_bf16 a[8:11], a[224:227], a[128:131], 0\n"
        "ds_read_b64_tr_b16 a[220:221], v27 offset:16648\n"
        "v_mfma_f32_16x16x32_bf16 a[8:11], a[228:231], a[132:135], a[8:11]\n"
        "ds_read_b64_tr_b16 a[222:223], v27 offset:18696\n"
        "v_mfma_f32_16x16x32_bf16 a[40:43], a[224:227], a[136:139], 0\n"
        "buffer_load_dwordx4 v7, s[40:43], s89 offen offset:3072 lds\n"
        "v_mfma_f32_16x16x32_bf16 a[40:43], a[228:231], a[140:143], a[40:43]\n"
        "s_add_u32 m0, s90, 69632\n"
        "v_mfma_f32_16x16x32_bf16 a[72:75], a[224:227], a[144:147], 0\n"
        "v_mfma_f32_16x16x32_bf16 a[72:75], a[228:231], a[148:151], a[72:75]\n"
        "v_mfma_f32_16x16x32_bf16 a[104:107], a[224:227], a[152:155], 0\n"
        "buffer_load_dwordx4 v8, s[40:43], s89 offen offset:0 lds\n"
        "v_mfma_f32_16x16x32_bf16 a[104:107], a[228:231], a[156:159], a[104:107]\n"
        "v_mfma_f32_16x16x32_bf16 v[76:79], a[224:227], a[160:163], 0\n"
        "ds_read_b64_tr_b16 a[224:225], v28 offset:256\n"
        "v_mfma_f32_16x16x32_bf16 v[76:79], a[228:231], a[164:167], v[76:79]\n"
        "ds_read_b64_tr_b16 a[226:227], v28 offset:2304\n"
        "v_mfma_f32_16x16x32_bf16 a[12:15], a[232:235], a[128:131], 0\n"
        "ds_read_b64_tr_b16 a[228:229], v28 offset:16640\n"
        "v_mfma_f32_16x16x32_bf16 a[12:15], a[236:239], a[132:135], a[12:15]\n"
        "ds_read_b64_tr_b16 a[230:231], v28 offset:18688\n"
        "v_mfma_f32_16x16x32_bf16 a[44:47], a[232:235], a[136:139], 0\n"
        "buffer_load_dwordx4 v9, s[40:43], s89 offen offset:1024 lds\n"
        "v_mfma_f32_16x16x32_bf16 a[44:47], a[236:239], a[140:143], a[44:47]\n"
        "v_mfma_f32_16x16x32_bf16 a[76:79], a[232:235], a[144:147], 0\n"
        "v_mfma_f32_16x16x32_bf16 a[76:79], a[236:239], a[148:151], a[76:79]\n"
        "v_mfma_f32_16x16x32_bf16 a[108:111], a[232:235], a[152:155], 0\n"
        "buffer_load_dwordx4 v10, s[40:43], s89 offen offset:2048 lds\n"
        "v_mfma_f32_16x16x32_bf16 a[108:111], a[236:239], a[156:159], a[108:111]\n"
        "v_mfma_f32_16x16x32_bf16 v[80:83], a[232:235], a[160:163], 0\n"
        "ds_read_b64_tr_b16 a[232:233], v28 offset:264\n"
        "v_mfma_f32_16x16x32_bf16 v[80:83], a[236:239], a[164:167], v[80:83]\n"
        "s_waitcnt lgkmcnt(1)\n"
        "ds_read_b64_tr_b16 a[234:235], v28 offset:2312\n"
        "v_mfma_f32_16x16x32_bf16 a[16:19], a[208:211], a[128:131], 0\n"
        "ds_read_b64_tr_b16 a[236:237], v28 offset:16648\n"
        "v_mfma_f32_16x16x32_bf16 a[16:19], a[212:215], a[132:135], a[16:19]\n"
        "ds_read_b64_tr_b16 a[238:239], v28 offset:18696\n"
        "v_mfma_f32_16x16x32_bf16 a[48:51], a[208:211], a[136:139], 0\n"
        "buffer_load_dwordx4 v11, s[40:43], s89 offen offset:3072 lds\n"
        "v_mfma_f32_16x16x32_bf16 a[48:51], a[212:215], a[140:143], a[48:51]\n"
        "s_add_u32 m0, s90, 73728\n"
        "v_mfma_f32_16x16x32_bf16 a[80:83], a[208:211], a[144:147], 0\n"
        "s_waitcnt vmcnt(12)\n"
        "s_barrier\n"
        "v_mfma_f32_16x16x32_bf16 a[80:83], a[212:215], a[148:151], a[80:83]\n"
        "ds_read_b128 a[168:171], v40 offset:36864\n"
        "v_mfma_f32_16x16x32_bf16 a[112:115], a[208:211], a[152:155], 0\n"
        "ds_read_b128 a[172:175], v41 offset:36864\n"
        "v_mfma_f32_16x16x32_bf16 a[112:115], a[212:215], a[156:159], a[112:115]\n"
        "ds_read_b128 a[176:179], v42 offset:36864\n"
        "v_mfma_f32_16x16x32_bf16 a[20:23], a[216:219], a[128:131], 0\n"
        "ds_read_b128 a[180:183], v43 offset:36864\n"
        "v_mfma_f32_16x16x32_bf16 a[20:23], a[220:223], a[132:135], a[20:23]\n"
        "ds_read_b128 a[184:187], v44 offset:36864\n"
        "v_mfma_f32_16x16x32_bf16 a[52:55], a[216:219], a[136:139], 0\n"
        "ds_read_b128 a[188:191], v45 offset:36864\n"
        "v_mfma_f32_16x16x32_bf16 a[52:55], a[220:223], a[140:143], a[52:55]\n"
        "ds_read_b128 a[192:195], v46 offset:36864\n"
        "v_mfma_f32_16x16x32_bf16 a[84:87], a[216:219], a[144:147], 0\n"
        "ds_read_b128 a[196:199], v47 offset:36864\n"
        "v_mfma_f32_16x16x32_bf16 a[84:87], a[220:223], a[148:151], a[84:87]\n"
        "ds_read_b128 a[200:203], v15 offset:36864\n"
        "v_mfma_f32_16x16x32_bf16 a[116:119], a[216:219], a[152:155], 0\n"
        "ds_read_b128 a[204:207], v16 offset:36864\n"
        "v_mfma_f32_16x16x32_bf16 a[116:119], a[220:223], a[156:159], a[116:119]\n"
        "ds_read_b64_tr_b16 a[208:209], v25 offset:32768\n"
        "v_mfma_f32_16x16x32_bf16 a[24:27], a[224:227], a[128:131], 0\n"
        "ds_read_b64_tr_b16 a[210:211], v25 offset:34816\n"
        "v_mfma_f32_16x16x32_bf16 a[24:27], a[228:231], a[132:135], a[24:27]\n"
        "ds_read_b64_tr_b16 a[212:213], v25 offset:49152\n"
        "v_mfma_f32_16x16x32_bf16 a[56:59], a[224:227], a[136:139], 0\n"
        "ds_read_b64_tr_b16 a[214:215], v25 offset:51200\n"
        "v_mfma_f32_16x16x32_bf16 a[56:59], a[228:231], a[140:143], a[56:59]\n"
        "ds_read_b64_tr_b16 a[216:217], v25 offset:32776\n"
        "v_mfma_f32_16x16x32_bf16 a[88:91], a[224:227], a[144:147], 0\n"
        "ds_read_b64_tr_b16 a[218:219], v25 offset:34824\n"
        "v_mfma_f32_16x16x32_bf16 a[88:91], a[228:231], a[148:151], a[88:91]\n"
        "ds_read_b64_tr_b16 a[220:221], v25 offset:49160\n"
        "v_mfma_f32_16x16x32_bf16 a[120:123], a[224:227], a[152:155], 0\n"
        "ds_read_b64_tr_b16 a[222:223], v25 offset:51208\n"
        "v_mfma_f32_16x16x32_bf16 a[120:123], a[228:231], a[156:159], a[120:123]\n"
        "s_waitcnt lgkmcnt(6)\n"
        "ds_read_b64_tr_b16 a[224:225], v26 offset:32768\n"
        "v_mfma_f32_16x16x32_bf16 a[28:31], a[232:235], a[128:131], 0\n"
        "ds_read_b64_tr_b16 a[226:227], v26 offset:34816\n"
        "v_mfma_f32_16x16x32_bf16 a[28:31], a[236:239], a[132:135], a[28:31]\n"
        "ds_read_b64_tr_b16 a[228:229], v26 offset:49152\n"
        "v_mfma_f32_16x16x32_bf16 a[60:63], a[232:235], a[136:139], 0\n"
        "ds_read_b64_tr_b16 a[230:231], v26 offset:51200\n"
        "v_mfma_f32_16x16x32_bf16 a[60:63], a[236:239], a[140:143], a[60:63]\n"
        "buffer_load_dwordx4 v12, s[40:43], s89 offen offset:0 lds\n"
        "v_mfma_f32_16x16x32_bf16 a[92:95], a[232:235], a[144:147], 0\n"
        "s_barrier\n"
        "v_mfma_f32_16x16x32_bf16 a[92:95], a[236:239], a[148:151], a[92:95]\n"
        "s_mov_b32 s79, 1048576\n"
        "s_add_u32 m0, s66, 0\n"
        "v_mfma_f32_16x16x32_bf16 a[124:127], a[232:235], a[152:155], 0\n"
        "ds_read_b64_tr_b16 a[232:233], v26 offset:32776\n"
        "v_mfma_f32_16x16x32_bf16 a[124:127], a[236:239], a[156:159], a[124:127]\n"
        "ds_read_b64_tr_b16 a[234:235], v26 offset:34824\n"
        "v_mfma_f32_16x16x32_bf16 v[88:91], a[208:211], a[168:171], v[88:91]\n"
        "s_waitcnt lgkmcnt(3)\n"
        "ds_read_b64_tr_b16 a[236:237], v26 offset:49160\n"
        "v_mfma_f32_16x16x32_bf16 v[88:91], a[212:215], a[172:175], v[88:91]\n"
        "ds_read_b64_tr_b16 a[238:239], v26 offset:51208\n"
        "v_mfma_f32_16x16x32_bf16 v[120:123], a[208:211], a[176:179], v[120:123]\n"
        "buffer_load_dwordx4 v17, s[44:47], s79 offen offset:0 lds\n"
        "v_mfma_f32_16x16x32_bf16 v[120:123], a[212:215], a[180:183], v[120:123]\n"
        "v_mfma_f32_16x16x32_bf16 v[152:155], a[208:211], a[184:187], v[152:155]\n"
        "v_mfma_f32_16x16x32_bf16 v[152:155], a[212:215], a[188:191], v[152:155]\n"
        "v_mfma_f32_16x16x32_bf16 v[184:187], a[208:211], a[192:195], v[184:187]\n"
        "buffer_load_dwordx4 v18, s[44:47], s79 offen offset:1024 lds\n"
        "v_mfma_f32_16x16x32_bf16 v[184:187], a[212:215], a[196:199], v[184:187]\n"
        "v_mfma_f32_16x16x32_bf16 v[216:219], a[208:211], a[200:203], v[216:219]\n"
        "ds_read_b64_tr_b16 a[208:209], v27 offset:32768\n"
        "v_mfma_f32_16x16x32_bf16 v[216:219], a[212:215], a[204:207], v[216:219]\n"
        "ds_read_b64_tr_b16 a[210:211], v27 offset:34816\n"
        "v_mfma_f32_16x16x32_bf16 v[92:95], a[216:219], a[168:171], v[92:95]\n"
        "ds_read_b64_tr_b16 a[212:213], v27 offset:49152\n"
        "v_mfma_f32_16x16x32_bf16 v[92:95], a[220:223], a[172:175], v[92:95]\n"
        "ds_read_b64_tr_b16 a[214:215], v27 offset:51200\n"
        "v_mfma_f32_16x16x32_bf16 v[124:127], a[216:219], a[176:179], v[124:127]\n"
        "buffer_load_dwordx4 v19, s[44:47], s79 offen offset:2048 lds\n"
        "v_mfma_f32_16x16x32_bf16 v[124:127], a[220:223], a[180:183], v[124:127]\n"
        "v_mfma_f32_16x16x32_bf16 v[156:159], a[216:219], a[184:187], v[156:159]\n"
        "v_mfma_f32_16x16x32_bf16 v[156:159], a[220:223], a[188:191], v[156:159]\n"
        "v_mfma_f32_16x16x32_bf16 v[188:191], a[216:219], a[192:195], v[188:191]\n"
        "buffer_load_dwordx4 v20, s[44:47], s79 offen offset:3072 lds\n"
        "v_mfma_f32_16x16x32_bf16 v[188:191], a[220:223], a[196:199], v[188:191]\n"
        "s_add_u32 m0, s66, 4096\n"
        "v_mfma_f32_16x16x32_bf16 v[220:223], a[216:219], a[200:203], v[220:223]\n"
        "ds_read_b64_tr_b16 a[216:217], v27 offset:32776\n"
        "v_mfma_f32_16x16x32_bf16 v[220:223], a[220:223], a[204:207], v[220:223]\n"
        "ds_read_b64_tr_b16 a[218:219], v27 offset:34824\n"
        "v_mfma_f32_16x16x32_bf16 v[96:99], a[224:227], a[168:171], v[96:99]\n"
        "s_waitcnt lgkmcnt(2)\n"
        "ds_read_b64_tr_b16 a[220:221], v27 offset:49160\n"
        "v_mfma_f32_16x16x32_bf16 v[96:99], a[228:231], a[172:175], v[96:99]\n"
        "ds_read_b64_tr_b16 a[222:223], v27 offset:51208\n"
        "v_mfma_f32_16x16x32_bf16 v[128:131], a[224:227], a[176:179], v[128:131]\n"
        "buffer_load_dwordx4 v21, s[44:47], s79 offen offset:0 lds\n"
        "v_mfma_f32_16x16x32_bf16 v[128:131], a[228:231], a[180:183], v[128:131]\n"
        "v_mfma_f32_16x16x32_bf16 v[160:163], a[224:227], a[184:187], v[160:163]\n"
        "v_mfma_f32_16x16x32_bf16 v[160:163], a[228:231], a[188:191], v[160:163]\n"
        "v_mfma_f32_16x16x32_bf16 v[192:195], a[224:227], a[192:195], v[192:195]\n"
        "buffer_load_dwordx4 v22, s[44:47], s79 offen offset:1024 lds\n"
        "v_mfma_f32_16x16x32_bf16 v[192:195], a[228:231], a[196:199], v[192:195]\n"
        "v_mfma_f32_16x16x32_bf16 v[224:227], a[224:227], a[200:203], v[224:227]\n"
        "ds_read_b64_tr_b16 a[224:225], v28 offset:32768\n"
        "v_mfma_f32_16x16x32_bf16 v[224:227], a[228:231], a[204:207], v[224:227]\n"
        "ds_read_b64_tr_b16 a[226:227], v28 offset:34816\n"
        "v_mfma_f32_16x16x32_bf16 v[100:103], a[232:235], a[168:171], v[100:103]\n"
        "ds_read_b64_tr_b16 a[228:229], v28 offset:49152\n"
        "v_mfma_f32_16x16x32_bf16 v[100:103], a[236:239], a[172:175], v[100:103]\n"
        "ds_read_b64_tr_b16 a[230:231], v28 offset:51200\n"
        "v_mfma_f32_16x16x32_bf16 v[132:135], a[232:235], a[176:179], v[132:135]\n"
        "buffer_load_dwordx4 v23, s[44:47], s79 offen offset:2048 lds\n"
        "v_mfma_f32_16x16x32_bf16 v[132:135], a[236:239], a[180:183], v[132:135]\n"
        "v_mfma_f32_16x16x32_bf16 v[164:167], a[232:235], a[184:187], v[164:167]\n"
        "v_mfma_f32_16x16x32_bf16 v[164:167], a[236:239], a[188:191], v[164:167]\n"
        "v_mfma_f32_16x16x32_bf16 v[196:199], a[232:235], a[192:195], v[196:199]\n"
        "buffer_load_dwordx4 v24, s[44:47], s79 offen offset:3072 lds\n"
        "v_mfma_f32_16x16x32_bf16 v[196:199], a[236:239], a[196:199], v[196:199]\n"
        "s_mov_b32 s89, 384\n"
        "s_add_u32 m0, s90, 102400\n"
        "v_mfma_f32_16x16x32_bf16 v[228:231], a[232:235], a[200:203], v[228:231]\n"
        "ds_read_b64_tr_b16 a[232:233], v28 offset:32776\n"
        "v_mfma_f32_16x16x32_bf16 v[228:231], a[236:239], a[204:207], v[228:231]\n"
        "ds_read_b64_tr_b16 a[234:235], v28 offset:34824\n"
        "v_mfma_f32_16x16x32_bf16 v[104:107], a[208:211], a[168:171], v[104:107]\n"
        "ds_read_b64_tr_b16 a[236:237], v28 offset:49160\n"
        "v_mfma_f32_16x16x32_bf16 v[104:107], a[212:215], a[172:175], v[104:107]\n"
        "ds_read_b64_tr_b16 a[238:239], v28 offset:51208\n"
        "v_mfma_f32_16x16x32_bf16 v[136:139], a[208:211], a[176:179], v[136:139]\n"
        "buffer_load_dwordx4 v4, s[40:43], s89 offen offset:0 lds\n"
        "v_mfma_f32_16x16x32_bf16 v[136:139], a[212:215], a[180:183], v[136:139]\n"
        "v_mfma_f32_16x16x32_bf16 v[168:171], a[208:211], a[184:187], v[168:171]\n"
        "v_mfma_f32_16x16x32_bf16 v[168:171], a[212:215], a[188:191], v[168:171]\n"
        "v_mfma_f32_16x16x32_bf16 v[200:203], a[208:211], a[192:195], v[200:203]\n"
        "ds_read_b64_tr_b16 a[208:209], v25 offset:33024\n"
        "v_mfma_f32_16x16x32_bf16 v[200:203], a[212:215], a[196:199], v[200:203]\n"
        "s_waitcnt lgkmcnt(2)\n"
        "ds_read_b64_tr_b16 a[210:211], v25 offset:35072\n"
        "v_mfma_f32_16x16x32_bf16 v[108:111], a[216:219], a[168:171], v[108:111]\n"
        "ds_read_b64_tr_b16 a[212:213], v25 offset:49408\n"
        "v_mfma_f32_16x16x32_bf16 v[108:111], a[220:223], a[172:175], v[108:111]\n"
        "ds_read_b64_tr_b16 a[214:215], v25 offset:51456\n"
        "v_mfma_f32_16x16x32_bf16 v[140:143], a[216:219], a[176:179], v[140:143]\n"
        "buffer_load_dwordx4 v5, s[40:43], s89 offen offset:1024 lds\n"
        "v_mfma_f32_16x16x32_bf16 v[140:143], a[220:223], a[180:183], v[140:143]\n"
        "v_mfma_f32_16x16x32_bf16 v[172:175], a[216:219], a[184:187], v[172:175]\n"
        "v_mfma_f32_16x16x32_bf16 v[172:175], a[220:223], a[188:191], v[172:175]\n"
        "v_mfma_f32_16x16x32_bf16 v[204:207], a[216:219], a[192:195], v[204:207]\n"
        "ds_read_b64_tr_b16 a[216:217], v25 offset:33032\n"
        "v_mfma_f32_16x16x32_bf16 v[204:207], a[220:223], a[196:199], v[204:207]\n"
        "ds_read_b64_tr_b16 a[218:219], v25 offset:35080\n"
        "v_mfma_f32_16x16x32_bf16 v[112:115], a[224:227], a[168:171], v[112:115]\n"
        "ds_read_b64_tr_b16 a[220:221], v25 offset:49416\n"
        "v_mfma_f32_16x16x32_bf16 v[112:115], a[228:231], a[172:175], v[112:115]\n"
        "ds_read_b64_tr_b16 a[222:223], v25 offset:51464\n"
        "v_mfma_f32_16x16x32_bf16 v[144:147], a[224:227], a[176:179], v[144:147]\n"
        "buffer_load_dwordx4 v6, s[40:43], s89 offen offset:2048 lds\n"
        "v_mfma_f32_16x16x32_bf16 v[144:147], a[228:231], a[180:183], v[144:147]\n"
        "v_mfma_f32_16x16x32_bf16 v[176:179], a[224:227], a[184:187], v[176:179]\n"
        "v_mfma_f32_16x16x32_bf16 v[176:179], a[228:231], a[188:191], v[176:179]\n"
        "v_mfma_f32_16x16x32_bf16 v[208:211], a[224:227], a[192:195], v[208:211]\n"
        "ds_read_b64_tr_b16 a[224:225], v26 offset:33024\n"
        "v_mfma_f32_16x16x32_bf16 v[208:211], a[228:231], a[196:199], v[208:211]\n"
        "ds_read_b64_tr_b16 a[226:227], v26 offset:35072\n"
        "v_mfma_f32_16x16x32_bf16 v[116:119], a[232:235], a[168:171], v[116:119]\n"
        "s_waitcnt lgkmcnt(2)\n"
        "ds_read_b64_tr_b16 a[228:229], v26 offset:49408\n"
        "v_mfma_f32_16x16x32_bf16 v[116:119], a[236:239], a[172:175], v[116:119]\n"
        "ds_read_b64_tr_b16 a[230:231], v26 offset:51456\n"
        "v_mfma_f32_16x16x32_bf16 v[148:151], a[232:235], a[176:179], v[148:151]\n"
        "buffer_load_dwordx4 v7, s[40:43], s89 offen offset:3072 lds\n"
        "v_mfma_f32_16x16x32_bf16 v[148:151], a[236:239], a[180:183], v[148:151]\n"
        "s_add_u32 m0, s90, 106496\n"
        "v_mfma_f32_16x16x32_bf16 v[180:183], a[232:235], a[184:187], v[180:183]\n"
        "v_mfma_f32_16x16x32_bf16 v[180:183], a[236:239], a[188:191], v[180:183]\n"
        "v_mfma_f32_16x16x32_bf16 v[212:215], a[232:235], a[192:195], v[212:215]\n"
        "ds_read_b64_tr_b16 a[232:233], v26 offset:33032\n"
        "v_mfma_f32_16x16x32_bf16 v[212:215], a[236:239], a[196:199], v[212:215]\n"
        "ds_read_b64_tr_b16 a[234:235], v26 offset:35080\n"
        "v_mfma_f32_16x16x32_bf16 a[0:3], a[208:211], a[168:171], a[0:3]\n"
        "ds_read_b64_tr_b16 a[236:237], v26 offset:49416\n"
        "v_mfma_f32_16x16x32_bf16 a[0:3], a[212:215], a[172:175], a[0:3]\n"
        "ds_read_b64_tr_b16 a[238:239], v26 offset:51464\n"
        "v_mfma_f32_16x16x32_bf16 a[32:35], a[208:211], a[176:179], a[32:35]\n"
        "buffer_load_dwordx4 v8, s[40:43], s89 offen offset:0 lds\n"
        "v_mfma_f32_16x16x32_bf16 a[32:35], a[212:215], a[180:183], a[32:35]\n"
        "v_mfma_f32_16x16x32_bf16 a[64:67], a[208:211], a[184:187], a[64:67]\n"
        "v_mfma_f32_16x16x32_bf16 a[64:67], a[212:215], a[188:191], a[64:67]\n"
        "v_mfma_f32_16x16x32_bf16 a[96:99], a[208:211], a[192:195], a[96:99]\n"
        "buffer_load_dwordx4 v9, s[40:43], s89 offen offset:1024 lds\n"
        "v_mfma_f32_16x16x32_bf16 a[96:99], a[212:215], a[196:199], a[96:99]\n"
        "v_mfma_f32_16x16x32_bf16 v[68:71], a[208:211], a[200:203], v[68:71]\n"
        "ds_read_b64_tr_b16 a[208:209], v27 offset:33024\n"
        "v_mfma_f32_16x16x32_bf16 v[68:71], a[212:215], a[204:207], v[68:71]\n"
        "ds_read_b64_tr_b16 a[210:211], v27 offset:35072\n"
        "v_mfma_f32_16x16x32_bf16 a[4:7], a[216:219], a[168:171], a[4:7]\n"
        "ds_read_b64_tr_b16 a[212:213], v27 offset:49408\n"
        "v_mfma_f32_16x16x32_bf16 a[4:7], a[220:223], a[172:175], a[4:7]\n"
        "ds_read_b64_tr_b16 a[214:215], v27 offset:51456\n"
        "v_mfma_f32_16x16x32_bf16 a[36:39], a[216:219], a[176:179], a[36:39]\n"
        "buffer_load_dwordx4 v10, s[40:43], s89 offen offset:2048 lds\n"
        "v_mfma_f32_16x16x32_bf16 a[36:39], a[220:223], a[180:183], a[36:39]\n"
        "v_mfma_f32_16x16x32_bf16 a[68:71], a[216:219], a[184:187], a[68:71]\n"
        "v_mfma_f32_16x16x32_bf16 a[68:71], a[220:223], a[188:191], a[68:71]\n"
        "v_mfma_f32_16x16x32_bf16 a[100:103], a[216:219], a[192:195], a[100:103]\n"
        "buffer_load_dwordx4 v11, s[40:43], s89 offen offset:3072 lds\n"
        "v_mfma_f32_16x16x32_bf16 a[100:103], a[220:223], a[196:199], a[100:103]\n"
        "s_add_u32 m0, s90, 110592\n"
        "v_mfma_f32_16x16x32_bf16 v[72:75], a[216:219], a[200:203], v[72:75]\n"
        "ds_read_b64_tr_b16 a[216:217], v27 offset:33032\n"
        "v_mfma_f32_16x16x32_bf16 v[72:75], a[220:223], a[204:207], v[72:75]\n"
        "s_waitcnt lgkmcnt(1)\n"
        "ds_read_b64_tr_b16 a[218:219], v27 offset:35080\n"
        "v_mfma_f32_16x16x32_bf16 a[8:11], a[224:227], a[168:171], a[8:11]\n"
        "ds_read_b64_tr_b16 a[220:221], v27 offset:49416\n"
        "v_mfma_f32_16x16x32_bf16 a[8:11], a[228:231], a[172:175], a[8:11]\n"
        "ds_read_b64_tr_b16 a[222:223], v27 offset:51464\n"
        "v_mfma_f32_16x16x32_bf16 a[40:43], a[224:227], a[176:179], a[40:43]\n"
        "buffer_load_dwordx4 v12, s[40:43], s89 offen offset:0 lds\n"
        "v_mfma_f32_16x16x32_bf16 a[40:43], a[228:231], a[180:183], a[40:43]\n"
        "s_add_u32 s93, s93, 524288\n"
        "v_mfma_f32_16x16x32_bf16 a[72:75], a[224:227], a[184:187], a[72:75]\n"
        "v_mfma_f32_16x16x32_bf16 a[72:75], a[228:231], a[188:191], a[72:75]\n"
        "v_mfma_f32_16x16x32_bf16 a[104:107], a[224:227], a[192:195], a[104:107]\n"
        "buffer_load_dword a246, v33, s[44:47], s93 offen\n"
        "v_mfma_f32_16x16x32_bf16 a[104:107], a[228:231], a[196:199], a[104:107]\n"
        "v_mfma_f32_16x16x32_bf16 v[76:79], a[224:227], a[200:203], v[76:79]\n"
        "ds_read_b64_tr_b16 a[224:225], v28 offset:33024\n"
        "v_mfma_f32_16x16x32_bf16 v[76:79], a[228:231], a[204:207], v[76:79]\n"
        "ds_read_b64_tr_b16 a[226:227], v28 offset:35072\n"
        "v_mfma_f32_16x16x32_bf16 a[12:15], a[232:235], a[168:171], a[12:15]\n"
        "ds_read_b64_tr_b16 a[228:229], v28 offset:49408\n"
        "v_mfma_f32_16x16x32_bf16 a[12:15], a[236:239], a[172:175], a[12:15]\n"
        "ds_read_b64_tr_b16 a[230:231], v28 offset:51456\n"
        "v_mfma_f32_16x16x32_bf16 a[44:47], a[232:235], a[176:179], a[44:47]\n"
        "buffer_load_dword a248, v36, s[40:43], 0 offen offset:640\n"
        "v_mfma_f32_16x16x32_bf16 a[44:47], a[236:239], a[180:183], a[44:47]\n"
        "v_mfma_f32_16x16x32_bf16 a[76:79], a[232:235], a[184:187], a[76:79]\n"
        "v_mfma_f32_16x16x32_bf16 a[76:79], a[236:239], a[188:191], a[76:79]\n"
        "v_mfma_f32_16x16x32_bf16 a[108:111], a[232:235], a[192:195], a[108:111]\n"
        "v_mfma_f32_16x16x32_bf16 a[108:111], a[236:239], a[196:199], a[108:111]\n"
        "v_mfma_f32_16x16x32_bf16 v[80:83], a[232:235], a[200:203], v[80:83]\n"
        "ds_read_b64_tr_b16 a[232:233], v28 offset:33032\n"
        "v_mfma_f32_16x16x32_bf16 v[80:83], a[236:239], a[204:207], v[80:83]\n"
        "ds_read_b64_tr_b16 a[234:235], v28 offset:35080\n"
        "v_mfma_f32_16x16x32_bf16 a[16:19], a[208:211], a[168:171], a[16:19]\n"
        "ds_read_b64_tr_b16 a[236:237], v28 offset:49416\n"
        "v_mfma_f32_16x16x32_bf16 a[16:19], a[212:215], a[172:175], a[16:19]\n"
        "ds_read_b64_tr_b16 a[238:239], v28 offset:51464\n"
        "v_mfma_f32_16x16x32_bf16 a[48:51], a[208:211], a[176:179], a[48:51]\n"
        "v_mfma_f32_16x16x32_bf16 a[48:51], a[212:215], a[180:183], a[48:51]\n"
        "v_mfma_f32_16x16x32_bf16 a[80:83], a[208:211], a[184:187], a[80:83]\n"
        "s_waitcnt vmcnt(11)\n"
        "s_barrier\n"
        "v_mfma_f32_16x16x32_bf16 a[80:83], a[212:215], a[188:191], a[80:83]\n"
        "ds_read_b128 a[128:131], v40 offset:0\n"
        "v_mfma_f32_16x16x32_bf16 a[112:115], a[208:211], a[192:195], a[112:115]\n"
        "ds_read_b128 a[132:135], v41 offset:0\n"
        "v_mfma_f32_16x16x32_bf16 a[112:115], a[212:215], a[196:199], a[112:115]\n"
        "s_waitcnt lgkmcnt(3)\n"
        "ds_read_b128 a[136:139], v42 offset:0\n"
        "v_mfma_f32_16x16x32_bf16 a[20:23], a[216:219], a[168:171], a[20:23]\n"
        "ds_read_b128 a[140:143], v43 offset:0\n"
        "v_mfma_f32_16x16x32_bf16 a[20:23], a[220:223], a[172:175], a[20:23]\n"
        "ds_read_b128 a[144:147], v44 offset:0\n"
        "v_mfma_f32_16x16x32_bf16 a[52:55], a[216:219], a[176:179], a[52:55]\n"
        "ds_read_b128 a[148:151], v45 offset:0\n"
        "v_mfma_f32_16x16x32_bf16 a[52:55], a[220:223], a[180:183], a[52:55]\n"
        "ds_read_b128 a[152:155], v46 offset:0\n"
        "v_mfma_f32_16x16x32_bf16 a[84:87], a[216:219], a[184:187], a[84:87]\n"
        "ds_read_b128 a[156:159], v47 offset:0\n"
        "v_mfma_f32_16x16x32_bf16 a[84:87], a[220:223], a[188:191], a[84:87]\n"
        "ds_read_b128 a[160:163], v15 offset:0\n"
        "v_mfma_f32_16x16x32_bf16 a[116:119], a[216:219], a[192:195], a[116:119]\n"
        "ds_read_b128 a[164:167], v16 offset:0\n"
        "v_mfma_f32_16x16x32_bf16 a[116:119], a[220:223], a[196:199], a[116:119]\n"
        "ds_read_b64_tr_b16 a[208:209], v25 offset:0\n"
        "v_mfma_f32_16x16x32_bf16 a[24:27], a[224:227], a[168:171], a[24:27]\n"
        "ds_read_b64_tr_b16 a[210:211], v25 offset:2048\n"
        "v_mfma_f32_16x16x32_bf16 a[24:27], a[228:231], a[172:175], a[24:27]\n"
        "ds_read_b64_tr_b16 a[212:213], v25 offset:16384\n"
        "v_mfma_f32_16x16x32_bf16 a[56:59], a[224:227], a[176:179], a[56:59]\n"
        "ds_read_b64_tr_b16 a[214:215], v25 offset:18432\n"
        "v_mfma_f32_16x16x32_bf16 a[56:59], a[228:231], a[180:183], a[56:59]\n"
        "ds_read_b64_tr_b16 a[216:217], v25 offset:8\n"
        "v_mfma_f32_16x16x32_bf16 a[88:91], a[224:227], a[184:187], a[88:91]\n"
        "ds_read_b64_tr_b16 a[218:219], v25 offset:2056\n"
        "v_mfma_f32_16x16x32_bf16 a[88:91], a[228:231], a[188:191], a[88:91]\n"
        "ds_read_b64_tr_b16 a[220:221], v25 offset:16392\n"
        "v_mfma_f32_16x16x32_bf16 a[120:123], a[224:227], a[192:195], a[120:123]\n"
        "ds_read_b64_tr_b16 a[222:223], v25 offset:18440\n"
        "v_mfma_f32_16x16x32_bf16 a[120:123], a[228:231], a[196:199], a[120:123]\n"
        "ds_read_b64_tr_b16 a[224:225], v26 offset:0\n"
        "v_mfma_f32_16x16x32_bf16 a[28:31], a[232:235], a[168:171], a[28:31]\n"
        "s_waitcnt lgkmcnt(6)\n"
        "ds_read_b64_tr_b16 a[226:227], v26 offset:2048\n"
        "v_mfma_f32_16x16x32_bf16 a[28:31], a[236:239], a[172:175], a[28:31]\n"
        "ds_read_b64_tr_b16 a[228:229], v26 offset:16384\n"
        "v_mfma_f32_16x16x32_bf16 a[60:63], a[232:235], a[176:179], a[60:63]\n"
        "ds_read_b64_tr_b16 a[230:231], v26 offset:18432\n"
        "v_mfma_f32_16x16x32_bf16 a[60:63], a[236:239], a[180:183], a[60:63]\n"
        "v_mfma_f32_16x16x32_bf16 a[92:95], a[232:235], a[184:187], a[92:95]\n"
        "s_barrier\n"
        "v_mfma_f32_16x16x32_bf16 a[92:95], a[236:239], a[188:191], a[92:95]\n"
        "s_mov_b32 s79, 1572864\n"
        "s_add_u32 m0, s66, 32768\n"
        "v_mfma_f32_16x16x32_bf16 a[124:127], a[232:235], a[192:195], a[124:127]\n"
        "ds_read_b64_tr_b16 a[232:233], v26 offset:8\n"
        "v_mfma_f32_16x16x32_bf16 a[124:127], a[236:239], a[196:199], a[124:127]\n"
        "ds_read_b64_tr_b16 a[234:235], v26 offset:2056\n"
        "v_mfma_f32_16x16x32_bf16 v[88:91], a[208:211], a[128:131], v[88:91]\n"
        "s_waitcnt lgkmcnt(3)\n"
        "ds_read_b64_tr_b16 a[236:237], v26 offset:16392\n"
        "v_mfma_f32_16x16x32_bf16 v[88:91], a[212:215], a[132:135], v[88:91]\n"
        "ds_read_b64_tr_b16 a[238:239], v26 offset:18440\n"
        "v_mfma_f32_16x16x32_bf16 v[120:123], a[208:211], a[136:139], v[120:123]\n"
        "buffer_load_dwordx4 v17, s[44:47], s79 offen offset:0 lds\n"
        "v_mfma_f32_16x16x32_bf16 v[120:123], a[212:215], a[140:143], v[120:123]\n"
        "v_mfma_f32_16x16x32_bf16 v[152:155], a[208:211], a[144:147], v[152:155]\n"
        "v_mfma_f32_16x16x32_bf16 v[152:155], a[212:215], a[148:151], v[152:155]\n"
        "v_mfma_f32_16x16x32_bf16 v[184:187], a[208:211], a[152:155], v[184:187]\n"
        "buffer_load_dwordx4 v18, s[44:47], s79 offen offset:1024 lds\n"
        "v_mfma_f32_16x16x32_bf16 v[184:187], a[212:215], a[156:159], v[184:187]\n"
        "v_mfma_f32_16x16x32_bf16 v[216:219], a[208:211], a[160:163], v[216:219]\n"
        "ds_read_b64_tr_b16 a[208:209], v27 offset:0\n"
        "v_mfma_f32_16x16x32_bf16 v[216:219], a[212:215], a[164:167], v[216:219]\n"
        "ds_read_b64_tr_b16 a[210:211], v27 offset:2048\n"
        "v_mfma_f32_16x16x32_bf16 v[92:95], a[216:219], a[128:131], v[92:95]\n"
        "ds_read_b64_tr_b16 a[212:213], v27 offset:16384\n"
        "v_mfma_f32_16x16x32_bf16 v[92:95], a[220:223], a[132:135], v[92:95]\n"
        "ds_read_b64_tr_b16 a[214:215], v27 offset:18432\n"
        "v_mfma_f32_16x16x32_bf16 v[124:127], a[216:219], a[136:139], v[124:127]\n"
        "buffer_load_dwordx4 v19, s[44:47], s79 offen offset:2048 lds\n"
        "v_mfma_f32_16x16x32_bf16 v[124:127], a[220:223], a[140:143], v[124:127]\n"
        "v_mfma_f32_16x16x32_bf16 v[156:159], a[216:219], a[144:147], v[156:159]\n"
        "v_mfma_f32_16x16x32_bf16 v[156:159], a[220:223], a[148:151], v[156:159]\n"
        "v_mfma_f32_16x16x32_bf16 v[188:191], a[216:219], a[152:155], v[188:191]\n"
        "buffer_load_dwordx4 v20, s[44:47], s79 offen offset:3072 lds\n"
        "v_mfma_f32_16x16x32_bf16 v[188:191], a[220:223], a[156:159], v[188:191]\n"
        "s_add_u32 m0, s66, 36864\n"
        "v_mfma_f32_16x16x32_bf16 v[220:223], a[216:219], a[160:163], v[220:223]\n"
        "ds_read_b64_tr_b16 a[216:217], v27 offset:8\n"
        "v_mfma_f32_16x16x32_bf16 v[220:223], a[220:223], a[164:167], v[220:223]\n"
        "ds_read_b64_tr_b16 a[218:219], v27 offset:2056\n"
        "v_mfma_f32_16x16x32_bf16 v[96:99], a[224:227], a[128:131], v[96:99]\n"
        "s_waitcnt lgkmcnt(2)\n"
        "ds_read_b64_tr_b16 a[220:221], v27 offset:16392\n"
        "v_mfma_f32_16x16x32_bf16 v[96:99], a[228:231], a[132:135], v[96:99]\n"
        "ds_read_b64_tr_b16 a[222:223], v27 offset:18440\n"
        "v_mfma_f32_16x16x32_bf16 v[128:131], a[224:227], a[136:139], v[128:131]\n"
        "buffer_load_dwordx4 v21, s[44:47], s79 offen offset:0 lds\n"
        "v_mfma_f32_16x16x32_bf16 v[128:131], a[228:231], a[140:143], v[128:131]\n"
        "v_mfma_f32_16x16x32_bf16 v[160:163], a[224:227], a[144:147], v[160:163]\n"
        "v_mfma_f32_16x16x32_bf16 v[160:163], a[228:231], a[148:151], v[160:163]\n"
        "v_mfma_f32_16x16x32_bf16 v[192:195], a[224:227], a[152:155], v[192:195]\n"
        "buffer_load_dwordx4 v22, s[44:47], s79 offen offset:1024 lds\n"
        "v_mfma_f32_16x16x32_bf16 v[192:195], a[228:231], a[156:159], v[192:195]\n"
        "v_mfma_f32_16x16x32_bf16 v[224:227], a[224:227], a[160:163], v[224:227]\n"
        "ds_read_b64_tr_b16 a[224:225], v28 offset:0\n"
        "v_mfma_f32_16x16x32_bf16 v[224:227], a[228:231], a[164:167], v[224:227]\n"
        "ds_read_b64_tr_b16 a[226:227], v28 offset:2048\n"
        "v_mfma_f32_16x16x32_bf16 v[100:103], a[232:235], a[128:131], v[100:103]\n"
        "ds_read_b64_tr_b16 a[228:229], v28 offset:16384\n"
        "v_mfma_f32_16x16x32_bf16 v[100:103], a[236:239], a[132:135], v[100:103]\n"
        "ds_read_b64_tr_b16 a[230:231], v28 offset:18432\n"
        "v_mfma_f32_16x16x32_bf16 v[132:135], a[232:235], a[136:139], v[132:135]\n"
        "buffer_load_dwordx4 v23, s[44:47], s79 offen offset:2048 lds\n"
        "v_mfma_f32_16x16x32_bf16 v[132:135], a[236:239], a[140:143], v[132:135]\n"
        "v_mfma_f32_16x16x32_bf16 v[164:167], a[232:235], a[144:147], v[164:167]\n"
        "v_mfma_f32_16x16x32_bf16 v[164:167], a[236:239], a[148:151], v[164:167]\n"
        "v_mfma_f32_16x16x32_bf16 v[196:199], a[232:235], a[152:155], v[196:199]\n"
        "buffer_load_dwordx4 v24, s[44:47], s79 offen offset:3072 lds\n"
        "v_mfma_f32_16x16x32_bf16 v[196:199], a[236:239], a[156:159], v[196:199]\n"
        "s_mov_b32 s89, 512\n"
        "s_add_u32 m0, s90, 65536\n"
        "v_mfma_f32_16x16x32_bf16 v[228:231], a[232:235], a[160:163], v[228:231]\n"
        "ds_read_b64_tr_b16 a[232:233], v28 offset:8\n"
        "v_mfma_f32_16x16x32_bf16 v[228:231], a[236:239], a[164:167], v[228:231]\n"
        "ds_read_b64_tr_b16 a[234:235], v28 offset:2056\n"
        "v_mfma_f32_16x16x32_bf16 v[104:107], a[208:211], a[128:131], v[104:107]\n"
        "ds_read_b64_tr_b16 a[236:237], v28 offset:16392\n"
        "v_mfma_f32_16x16x32_bf16 v[104:107], a[212:215], a[132:135], v[104:107]\n"
        "ds_read_b64_tr_b16 a[238:239], v28 offset:18440\n"
        "v_mfma_f32_16x16x32_bf16 v[136:139], a[208:211], a[136:139], v[136:139]\n"
        "buffer_load_dwordx4 v4, s[40:43], s89 offen offset:0 lds\n"
        "v_mfma_f32_16x16x32_bf16 v[136:139], a[212:215], a[140:143], v[136:139]\n"
        "v_mfma_f32_16x16x32_bf16 v[168:171], a[208:211], a[144:147], v[168:171]\n"
        "v_mfma_f32_16x16x32_bf16 v[168:171], a[212:215], a[148:151], v[168:171]\n"
        "v_mfma_f32_16x16x32_bf16 v[200:203], a[208:211], a[152:155], v[200:203]\n"
        "ds_read_b64_tr_b16 a[208:209], v25 offset:256\n"
        "v_mfma_f32_16x16x32_bf16 v[200:203], a[212:215], a[156:159], v[200:203]\n"
        "s_waitcnt lgkmcnt(2)\n"
        "ds_read_b64_tr_b16 a[210:211], v25 offset:2304\n"
        "v_mfma_f32_16x16x32_bf16 v[108:111], a[216:219], a[128:131], v[108:111]\n"
        "ds_read_b64_tr_b16 a[212:213], v25 offset:16640\n"
        "v_mfma_f32_16x16x32_bf16 v[108:111], a[220:223], a[132:135], v[108:111]\n"
        "ds_read_b64_tr_b16 a[214:215], v25 offset:18688\n"
        "v_mfma_f32_16x16x32_bf16 v[140:143], a[216:219], a[136:139], v[140:143]\n"
        "buffer_load_dwordx4 v5, s[40:43], s89 offen offset:1024 lds\n"
        "v_mfma_f32_16x16x32_bf16 v[140:143], a[220:223], a[140:143], v[140:143]\n"
        "v_mfma_f32_16x16x32_bf16 v[172:175], a[216:219], a[144:147], v[172:175]\n"
        "v_mfma_f32_16x16x32_bf16 v[172:175], a[220:223], a[148:151], v[172:175]\n"
        "v_mfma_f32_16x16x32_bf16 v[204:207], a[216:219], a[152:155], v[204:207]\n"
        "ds_read_b64_tr_b16 a[216:217], v25 offset:264\n"
        "v_mfma_f32_16x16x32_bf16 v[204:207], a[220:223], a[156:159], v[204:207]\n"
        "ds_read_b64_tr_b16 a[218:219], v25 offset:2312\n"
        "v_mfma_f32_16x16x32_bf16 v[112:115], a[224:227], a[128:131], v[112:115]\n"
        "ds_read_b64_tr_b16 a[220:221], v25 offset:16648\n"
        "v_mfma_f32_16x16x32_bf16 v[112:115], a[228:231], a[132:135], v[112:115]\n"
        "ds_read_b64_tr_b16 a[222:223], v25 offset:18696\n"
        "v_mfma_f32_16x16x32_bf16 v[144:147], a[224:227], a[136:139], v[144:147]\n"
        "buffer_load_dwordx4 v6, s[40:43], s89 offen offset:2048 lds\n"
        "v_mfma_f32_16x16x32_bf16 v[144:147], a[228:231], a[140:143], v[144:147]\n"
        "v_mfma_f32_16x16x32_bf16 v[176:179], a[224:227], a[144:147], v[176:179]\n"
        "v_mfma_f32_16x16x32_bf16 v[176:179], a[228:231], a[148:151], v[176:179]\n"
        "v_mfma_f32_16x16x32_bf16 v[208:211], a[224:227], a[152:155], v[208:211]\n"
        "ds_read_b64_tr_b16 a[224:225], v26 offset:256\n"
        "v_mfma_f32_16x16x32_bf16 v[208:211], a[228:231], a[156:159], v[208:211]\n"
        "ds_read_b64_tr_b16 a[226:227], v26 offset:2304\n"
        "v_mfma_f32_16x16x32_bf16 v[116:119], a[232:235], a[128:131], v[116:119]\n"
        "s_waitcnt lgkmcnt(2)\n"
        "ds_read_b64_tr_b16 a[228:229], v26 offset:16640\n"
        "v_mfma_f32_16x16x32_bf16 v[116:119], a[236:239], a[132:135], v[116:119]\n"
        "ds_read_b64_tr_b16 a[230:231], v26 offset:18688\n"
        "v_mfma_f32_16x16x32_bf16 v[148:151], a[232:235], a[136:139], v[148:151]\n"
        "buffer_load_dwordx4 v7, s[40:43], s89 offen offset:3072 lds\n"
        "v_mfma_f32_16x16x32_bf16 v[148:151], a[236:239], a[140:143], v[148:151]\n"
        "s_add_u32 m0, s90, 69632\n"
        "v_mfma_f32_16x16x32_bf16 v[180:183], a[232:235], a[144:147], v[180:183]\n"
        "v_mfma_f32_16x16x32_bf16 v[180:183], a[236:239], a[148:151], v[180:183]\n"
        "v_mfma_f32_16x16x32_bf16 v[212:215], a[232:235], a[152:155], v[212:215]\n"
        "ds_read_b64_tr_b16 a[232:233], v26 offset:264\n"
        "v_mfma_f32_16x16x32_bf16 v[212:215], a[236:239], a[156:159], v[212:215]\n"
        "ds_read_b64_tr_b16 a[234:235], v26 offset:2312\n"
        "v_mfma_f32_16x16x32_bf16 a[0:3], a[208:211], a[128:131], a[0:3]\n"
        "ds_read_b64_tr_b16 a[236:237], v26 offset:16648\n"
        "v_mfma_f32_16x16x32_bf16 a[0:3], a[212:215], a[132:135], a[0:3]\n"
        "ds_read_b64_tr_b16 a[238:239], v26 offset:18696\n"
        "v_mfma_f32_16x16x32_bf16 a[32:35], a[208:211], a[136:139], a[32:35]\n"
        "buffer_load_dwordx4 v8, s[40:43], s89 offen offset:0 lds\n"
        "v_mfma_f32_16x16x32_bf16 a[32:35], a[212:215], a[140:143], a[32:35]\n"
        "v_mfma_f32_16x16x32_bf16 a[64:67], a[208:211], a[144:147], a[64:67]\n"
        "v_mfma_f32_16x16x32_bf16 a[64:67], a[212:215], a[148:151], a[64:67]\n"
        "v_mfma_f32_16x16x32_bf16 a[96:99], a[208:211], a[152:155], a[96:99]\n"
        "buffer_load_dwordx4 v9, s[40:43], s89 offen offset:1024 lds\n"
        "v_mfma_f32_16x16x32_bf16 a[96:99], a[212:215], a[156:159], a[96:99]\n"
        "v_mfma_f32_16x16x32_bf16 v[68:71], a[208:211], a[160:163], v[68:71]\n"
        "ds_read_b64_tr_b16 a[208:209], v27 offset:256\n"
        "v_mfma_f32_16x16x32_bf16 v[68:71], a[212:215], a[164:167], v[68:71]\n"
        "ds_read_b64_tr_b16 a[210:211], v27 offset:2304\n"
        "v_mfma_f32_16x16x32_bf16 a[4:7], a[216:219], a[128:131], a[4:7]\n"
        "ds_read_b64_tr_b16 a[212:213], v27 offset:16640\n"
        "v_mfma_f32_16x16x32_bf16 a[4:7], a[220:223], a[132:135], a[4:7]\n"
        "ds_read_b64_tr_b16 a[214:215], v27 offset:18688\n"
        "v_mfma_f32_16x16x32_bf16 a[36:39], a[216:219], a[136:139], a[36:39]\n"
        "buffer_load_dwordx4 v10, s[40:43], s89 offen offset:2048 lds\n"
        "v_mfma_f32_16x16x32_bf16 a[36:39], a[220:223], a[140:143], a[36:39]\n"
        "v_mfma_f32_16x16x32_bf16 a[68:71], a[216:219], a[144:147], a[68:71]\n"
        "v_mfma_f32_16x16x32_bf16 a[68:71], a[220:223], a[148:151], a[68:71]\n"
        "v_mfma_f32_16x16x32_bf16 a[100:103], a[216:219], a[152:155], a[100:103]\n"
        "buffer_load_dwordx4 v11, s[40:43], s89 offen offset:3072 lds\n"
        "v_mfma_f32_16x16x32_bf16 a[100:103], a[220:223], a[156:159], a[100:103]\n"
        "s_add_u32 m0, s90, 73728\n"
        "v_mfma_f32_16x16x32_bf16 v[72:75], a[216:219], a[160:163], v[72:75]\n"
        "ds_read_b64_tr_b16 a[216:217], v27 offset:264\n"
        "v_mfma_f32_16x16x32_bf16 v[72:75], a[220:223], a[164:167], v[72:75]\n"
        "s_waitcnt lgkmcnt(1)\n"
        "ds_read_b64_tr_b16 a[218:219], v27 offset:2312\n"
        "v_mfma_f32_16x16x32_bf16 a[8:11], a[224:227], a[128:131], a[8:11]\n"
        "ds_read_b64_tr_b16 a[220:221], v27 offset:16648\n"
        "v_mfma_f32_16x16x32_bf16 a[8:11], a[228:231], a[132:135], a[8:11]\n"
        "ds_read_b64_tr_b16 a[222:223], v27 offset:18696\n"
        "v_mfma_f32_16x16x32_bf16 a[40:43], a[224:227], a[136:139], a[40:43]\n"
        "buffer_load_dwordx4 v12, s[40:43], s89 offen offset:0 lds\n"
        "v_mfma_f32_16x16x32_bf16 a[40:43], a[228:231], a[140:143], a[40:43]\n"
        "s_add_u32 s93, s93, 524288\n"
        "v_mfma_f32_16x16x32_bf16 a[72:75], a[224:227], a[144:147], a[72:75]\n"
        "v_mfma_f32_16x16x32_bf16 a[72:75], a[228:231], a[148:151], a[72:75]\n"
        "v_mfma_f32_16x16x32_bf16 a[104:107], a[224:227], a[152:155], a[104:107]\n"
        "buffer_load_dword a247, v33, s[44:47], s93 offen\n"
        "v_mfma_f32_16x16x32_bf16 a[104:107], a[228:231], a[156:159], a[104:107]\n"
        "v_mfma_f32_16x16x32_bf16 v[76:79], a[224:227], a[160:163], v[76:79]\n"
        "ds_read_b64_tr_b16 a[224:225], v28 offset:256\n"
        "v_mfma_f32_16x16x32_bf16 v[76:79], a[228:231], a[164:167], v[76:79]\n"
        "ds_read_b64_tr_b16 a[226:227], v28 offset:2304\n"
        "v_mfma_f32_16x16x32_bf16 a[12:15], a[232:235], a[128:131], a[12:15]\n"
        "ds_read_b64_tr_b16 a[228:229], v28 offset:16640\n"
        "v_mfma_f32_16x16x32_bf16 a[12:15], a[236:239], a[132:135], a[12:15]\n"
        "ds_read_b64_tr_b16 a[230:231], v28 offset:18688\n"
        "v_mfma_f32_16x16x32_bf16 a[44:47], a[232:235], a[136:139], a[44:47]\n"
        "buffer_load_dword a249, v36, s[40:43], 0 offen offset:768\n"
        "v_mfma_f32_16x16x32_bf16 a[44:47], a[236:239], a[140:143], a[44:47]\n"
        "s_mov_b32 s67, 0\n"
        "s_add_u32 m0, s65, 147456\n"
        "v_mfma_f32_16x16x32_bf16 a[76:79], a[232:235], a[144:147], a[76:79]\n"
        "v_mfma_f32_16x16x32_bf16 a[76:79], a[236:239], a[148:151], a[76:79]\n"
        "v_mfma_f32_16x16x32_bf16 a[108:111], a[232:235], a[152:155], a[108:111]\n"
        "buffer_load_dwordx4 v248, s[52:55], s67 offen offset:0 nt lds\n"
        "v_mfma_f32_16x16x32_bf16 a[108:111], a[236:239], a[156:159], a[108:111]\n"
        "v_mfma_f32_16x16x32_bf16 v[80:83], a[232:235], a[160:163], v[80:83]\n"
        "ds_read_b64_tr_b16 a[232:233], v28 offset:264\n"
        "v_mfma_f32_16x16x32_bf16 v[80:83], a[236:239], a[164:167], v[80:83]\n"
        "ds_read_b64_tr_b16 a[234:235], v28 offset:2312\n"
        "v_mfma_f32_16x16x32_bf16 a[16:19], a[208:211], a[128:131], a[16:19]\n"
        "ds_read_b64_tr_b16 a[236:237], v28 offset:16648\n"
        "v_mfma_f32_16x16x32_bf16 a[16:19], a[212:215], a[132:135], a[16:19]\n"
        "ds_read_b64_tr_b16 a[238:239], v28 offset:18696\n"
        "v_mfma_f32_16x16x32_bf16 a[48:51], a[208:211], a[136:139], a[48:51]\n"
        "buffer_load_dwordx4 v249, s[52:55], s67 offen offset:1024 nt lds\n"
        "v_mfma_f32_16x16x32_bf16 a[48:51], a[212:215], a[140:143], a[48:51]\n"
        "v_mfma_f32_16x16x32_bf16 a[80:83], a[208:211], a[144:147], a[80:83]\n"
        "s_waitcnt vmcnt(13)\n"
        "s_barrier\n"
        "v_mfma_f32_16x16x32_bf16 a[80:83], a[212:215], a[148:151], a[80:83]\n"
        "ds_read_b128 a[168:171], v40 offset:36864\n"
        "v_mfma_f32_16x16x32_bf16 a[112:115], a[208:211], a[152:155], a[112:115]\n"
        "ds_read_b128 a[172:175], v41 offset:36864\n"
        "v_mfma_f32_16x16x32_bf16 a[112:115], a[212:215], a[156:159], a[112:115]\n"
        "s_waitcnt lgkmcnt(3)\n"
        "ds_read_b128 a[176:179], v42 offset:36864\n"
        "v_mfma_f32_16x16x32_bf16 a[20:23], a[216:219], a[128:131], a[20:23]\n"
        "ds_read_b128 a[180:183], v43 offset:36864\n"
        "v_mfma_f32_16x16x32_bf16 a[20:23], a[220:223], a[132:135], a[20:23]\n"
        "ds_read_b128 a[184:187], v44 offset:36864\n"
        "v_mfma_f32_16x16x32_bf16 a[52:55], a[216:219], a[136:139], a[52:55]\n"
        "ds_read_b128 a[188:191], v45 offset:36864\n"
        "v_mfma_f32_16x16x32_bf16 a[52:55], a[220:223], a[140:143], a[52:55]\n"
        "ds_read_b128 a[192:195], v46 offset:36864\n"
        "v_mfma_f32_16x16x32_bf16 a[84:87], a[216:219], a[144:147], a[84:87]\n"
        "ds_read_b128 a[196:199], v47 offset:36864\n"
        "v_mfma_f32_16x16x32_bf16 a[84:87], a[220:223], a[148:151], a[84:87]\n"
        "ds_read_b128 a[200:203], v15 offset:36864\n"
        "v_mfma_f32_16x16x32_bf16 a[116:119], a[216:219], a[152:155], a[116:119]\n"
        "ds_read_b128 a[204:207], v16 offset:36864\n"
        "v_mfma_f32_16x16x32_bf16 a[116:119], a[220:223], a[156:159], a[116:119]\n"
        "ds_read_b64_tr_b16 a[208:209], v25 offset:32768\n"
        "v_mfma_f32_16x16x32_bf16 a[24:27], a[224:227], a[128:131], a[24:27]\n"
        "ds_read_b64_tr_b16 a[210:211], v25 offset:34816\n"
        "v_mfma_f32_16x16x32_bf16 a[24:27], a[228:231], a[132:135], a[24:27]\n"
        "ds_read_b64_tr_b16 a[212:213], v25 offset:49152\n"
        "v_mfma_f32_16x16x32_bf16 a[56:59], a[224:227], a[136:139], a[56:59]\n"
        "ds_read_b64_tr_b16 a[214:215], v25 offset:51200\n"
        "v_mfma_f32_16x16x32_bf16 a[56:59], a[228:231], a[140:143], a[56:59]\n"
        "ds_read_b64_tr_b16 a[216:217], v25 offset:32776\n"
        "v_mfma_f32_16x16x32_bf16 a[88:91], a[224:227], a[144:147], a[88:91]\n"
        "ds_read_b64_tr_b16 a[218:219], v25 offset:34824\n"
        "v_mfma_f32_16x16x32_bf16 a[88:91], a[228:231], a[148:151], a[88:91]\n"
        "ds_read_b64_tr_b16 a[220:221], v25 offset:49160\n"
        "v_mfma_f32_16x16x32_bf16 a[120:123], a[224:227], a[152:155], a[120:123]\n"
        "ds_read_b64_tr_b16 a[222:223], v25 offset:51208\n"
        "v_mfma_f32_16x16x32_bf16 a[120:123], a[228:231], a[156:159], a[120:123]\n"
        "buffer_load_dwordx4 v248, s[52:55], s67 offen offset:2048 nt lds\n"
        "v_mfma_f32_16x16x32_bf16 a[28:31], a[232:235], a[128:131], a[28:31]\n"
        "s_waitcnt lgkmcnt(5)\n"
        "ds_read_b64_tr_b16 a[224:225], v26 offset:32768\n"
        "v_mfma_f32_16x16x32_bf16 a[28:31], a[236:239], a[132:135], a[28:31]\n"
        "ds_read_b64_tr_b16 a[226:227], v26 offset:34816\n"
        "v_mfma_f32_16x16x32_bf16 a[60:63], a[232:235], a[136:139], a[60:63]\n"
        "ds_read_b64_tr_b16 a[228:229], v26 offset:49152\n"
        "v_mfma_f32_16x16x32_bf16 a[60:63], a[236:239], a[140:143], a[60:63]\n"
        "buffer_load_dwordx4 v249, s[52:55], s67 offen offset:3072 nt lds\n"
        "v_mfma_f32_16x16x32_bf16 a[92:95], a[232:235], a[144:147], a[92:95]\n"
        "s_barrier\n"
        "ds_read_b64_tr_b16 a[230:231], v26 offset:51200\n"
        "v_mfma_f32_16x16x32_bf16 a[92:95], a[236:239], a[148:151], a[92:95]\n"
        "s_mov_b32 s79, 2097152\n"
        "s_add_u32 m0, s66, 0\n"
        "v_mfma_f32_16x16x32_bf16 a[124:127], a[232:235], a[152:155], a[124:127]\n"
        "ds_read_b64_tr_b16 a[232:233], v26 offset:32776\n"
        "v_mfma_f32_16x16x32_bf16 a[124:127], a[236:239], a[156:159], a[124:127]\n"
        "ds_read_b64_tr_b16 a[234:235], v26 offset:34824\n"
        "v_mfma_f32_16x16x32_bf16 v[88:91], a[208:211], a[168:171], v[88:91]\n"
        "s_waitcnt lgkmcnt(4)\n"
        "ds_read_b64_tr_b16 a[236:237], v26 offset:49160\n"
        "v_mfma_f32_16x16x32_bf16 v[88:91], a[212:215], a[172:175], v[88:91]\n"
        "ds_read_b64_tr_b16 a[238:239], v26 offset:51208\n"
        "v_mfma_f32_16x16x32_bf16 v[120:123], a[208:211], a[176:179], v[120:123]\n"
        "buffer_load_dwordx4 v17, s[44:47], s79 offen offset:0 lds\n"
        "v_mfma_f32_16x16x32_bf16 v[120:123], a[212:215], a[180:183], v[120:123]\n"
        "v_mfma_f32_16x16x32_bf16 v[152:155], a[208:211], a[184:187], v[152:155]\n"
        "v_mfma_f32_16x16x32_bf16 v[152:155], a[212:215], a[188:191], v[152:155]\n"
        "v_mfma_f32_16x16x32_bf16 v[184:187], a[208:211], a[192:195], v[184:187]\n"
        "buffer_load_dwordx4 v18, s[44:47], s79 offen offset:1024 lds\n"
        "v_mfma_f32_16x16x32_bf16 v[184:187], a[212:215], a[196:199], v[184:187]\n"
        "v_mfma_f32_16x16x32_bf16 v[216:219], a[208:211], a[200:203], v[216:219]\n"
        "ds_read_b64_tr_b16 a[208:209], v27 offset:32768\n"
        "v_mfma_f32_16x16x32_bf16 v[216:219], a[212:215], a[204:207], v[216:219]\n"
        "ds_read_b64_tr_b16 a[210:211], v27 offset:34816\n"
        "v_mfma_f32_16x16x32_bf16 v[92:95], a[216:219], a[168:171], v[92:95]\n"
        "ds_read_b64_tr_b16 a[212:213], v27 offset:49152\n"
        "v_mfma_f32_16x16x32_bf16 v[92:95], a[220:223], a[172:175], v[92:95]\n"
        "ds_read_b64_tr_b16 a[214:215], v27 offset:51200\n"
        "v_mfma_f32_16x16x32_bf16 v[124:127], a[216:219], a[176:179], v[124:127]\n"
        "buffer_load_dwordx4 v19, s[44:47], s79 offen offset:2048 lds\n"
        "v_mfma_f32_16x16x32_bf16 v[124:127], a[220:223], a[180:183], v[124:127]\n"
        "v_mfma_f32_16x16x32_bf16 v[156:159], a[216:219], a[184:187], v[156:159]\n"
        "v_mfma_f32_16x16x32_bf16 v[156:159], a[220:223], a[188:191], v[156:159]\n"
        "v_mfma_f32_16x16x32_bf16 v[188:191], a[216:219], a[192:195], v[188:191]\n"
        "buffer_load_dwordx4 v20, s[44:47], s79 offen offset:3072 lds\n"
        "v_mfma_f32_16x16x32_bf16 v[188:191], a[220:223], a[196:199], v[188:191]\n"
        "s_add_u32 m0, s66, 4096\n"
        "v_mfma_f32_16x16x32_bf16 v[220:223], a[216:219], a[200:203], v[220:223]\n"
        "ds_read_b64_tr_b16 a[216:217], v27 offset:32776\n"
        "v_mfma_f32_16x16x32_bf16 v[220:223], a[220:223], a[204:207], v[220:223]\n"
        "ds_read_b64_tr_b16 a[218:219], v27 offset:34824\n"
        "v_mfma_f32_16x16x32_bf16 v[96:99], a[224:227], a[168:171], v[96:99]\n"
        "s_waitcnt lgkmcnt(2)\n"
        "ds_read_b64_tr_b16 a[220:221], v27 offset:49160\n"
        "v_mfma_f32_16x16x32_bf16 v[96:99], a[228:231], a[172:175], v[96:99]\n"
        "ds_read_b64_tr_b16 a[222:223], v27 offset:51208\n"
        "v_mfma_f32_16x16x32_bf16 v[128:131], a[224:227], a[176:179], v[128:131]\n"
        "buffer_load_dwordx4 v21, s[44:47], s79 offen offset:0 lds\n"
        "v_mfma_f32_16x16x32_bf16 v[128:131], a[228:231], a[180:183], v[128:131]\n"
        "v_mfma_f32_16x16x32_bf16 v[160:163], a[224:227], a[184:187], v[160:163]\n"
        "v_mfma_f32_16x16x32_bf16 v[160:163], a[228:231], a[188:191], v[160:163]\n"
        "v_mfma_f32_16x16x32_bf16 v[192:195], a[224:227], a[192:195], v[192:195]\n"
        "buffer_load_dwordx4 v22, s[44:47], s79 offen offset:1024 lds\n"
        "v_mfma_f32_16x16x32_bf16 v[192:195], a[228:231], a[196:199], v[192:195]\n"
        "v_mfma_f32_16x16x32_bf16 v[224:227], a[224:227], a[200:203], v[224:227]\n"
        "ds_read_b64_tr_b16 a[224:225], v28 offset:32768\n"
        "v_mfma_f32_16x16x32_bf16 v[224:227], a[228:231], a[204:207], v[224:227]\n"
        "ds_read_b64_tr_b16 a[226:227], v28 offset:34816\n"
        "v_mfma_f32_16x16x32_bf16 v[100:103], a[232:235], a[168:171], v[100:103]\n"
        "ds_read_b64_tr_b16 a[228:229], v28 offset:49152\n"
        "v_mfma_f32_16x16x32_bf16 v[100:103], a[236:239], a[172:175], v[100:103]\n"
        "ds_read_b64_tr_b16 a[230:231], v28 offset:51200\n"
        "v_mfma_f32_16x16x32_bf16 v[132:135], a[232:235], a[176:179], v[132:135]\n"
        "buffer_load_dwordx4 v23, s[44:47], s79 offen offset:2048 lds\n"
        "v_mfma_f32_16x16x32_bf16 v[132:135], a[236:239], a[180:183], v[132:135]\n"
        "v_mfma_f32_16x16x32_bf16 v[164:167], a[232:235], a[184:187], v[164:167]\n"
        "v_mfma_f32_16x16x32_bf16 v[164:167], a[236:239], a[188:191], v[164:167]\n"
        "v_mfma_f32_16x16x32_bf16 v[196:199], a[232:235], a[192:195], v[196:199]\n"
        "buffer_load_dwordx4 v24, s[44:47], s79 offen offset:3072 lds\n"
        "v_mfma_f32_16x16x32_bf16 v[196:199], a[236:239], a[196:199], v[196:199]\n"
        "s_mov_b32 s89, 640\n"
        "s_add_u32 m0, s90, 102400\n"
        "v_mfma_f32_16x16x32_bf16 v[228:231], a[232:235], a[200:203], v[228:231]\n"
        "ds_read_b64_tr_b16 a[232:233], v28 offset:32776\n"
        "v_mfma_f32_16x16x32_bf16 v[228:231], a[236:239], a[204:207], v[228:231]\n"
        "ds_read_b64_tr_b16 a[234:235], v28 offset:34824\n"
        "v_mfma_f32_16x16x32_bf16 v[104:107], a[208:211], a[168:171], v[104:107]\n"
        "ds_read_b64_tr_b16 a[236:237], v28 offset:49160\n"
        "v_mfma_f32_16x16x32_bf16 v[104:107], a[212:215], a[172:175], v[104:107]\n"
        "ds_read_b64_tr_b16 a[238:239], v28 offset:51208\n"
        "v_mfma_f32_16x16x32_bf16 v[136:139], a[208:211], a[176:179], v[136:139]\n"
        "buffer_load_dwordx4 v4, s[40:43], s89 offen offset:0 lds\n"
        "v_mfma_f32_16x16x32_bf16 v[136:139], a[212:215], a[180:183], v[136:139]\n"
        "v_mfma_f32_16x16x32_bf16 v[168:171], a[208:211], a[184:187], v[168:171]\n"
        "v_mfma_f32_16x16x32_bf16 v[168:171], a[212:215], a[188:191], v[168:171]\n"
        "v_mfma_f32_16x16x32_bf16 v[200:203], a[208:211], a[192:195], v[200:203]\n"
        "ds_read_b64_tr_b16 a[208:209], v25 offset:33024\n"
        "v_mfma_f32_16x16x32_bf16 v[200:203], a[212:215], a[196:199], v[200:203]\n"
        "s_waitcnt lgkmcnt(2)\n"
        "ds_read_b64_tr_b16 a[210:211], v25 offset:35072\n"
        "v_mfma_f32_16x16x32_bf16 v[108:111], a[216:219], a[168:171], v[108:111]\n"
        "ds_read_b64_tr_b16 a[212:213], v25 offset:49408\n"
        "v_mfma_f32_16x16x32_bf16 v[108:111], a[220:223], a[172:175], v[108:111]\n"
        "ds_read_b64_tr_b16 a[214:215], v25 offset:51456\n"
        "v_mfma_f32_16x16x32_bf16 v[140:143], a[216:219], a[176:179], v[140:143]\n"
        "buffer_load_dwordx4 v5, s[40:43], s89 offen offset:1024 lds\n"
        "v_mfma_f32_16x16x32_bf16 v[140:143], a[220:223], a[180:183], v[140:143]\n"
        "v_mfma_f32_16x16x32_bf16 v[172:175], a[216:219], a[184:187], v[172:175]\n"
        "v_mfma_f32_16x16x32_bf16 v[172:175], a[220:223], a[188:191], v[172:175]\n"
        "v_mfma_f32_16x16x32_bf16 v[204:207], a[216:219], a[192:195], v[204:207]\n"
        "ds_read_b64_tr_b16 a[216:217], v25 offset:33032\n"
        "v_mfma_f32_16x16x32_bf16 v[204:207], a[220:223], a[196:199], v[204:207]\n"
        "ds_read_b64_tr_b16 a[218:219], v25 offset:35080\n"
        "v_mfma_f32_16x16x32_bf16 v[112:115], a[224:227], a[168:171], v[112:115]\n"
        "ds_read_b64_tr_b16 a[220:221], v25 offset:49416\n"
        "v_mfma_f32_16x16x32_bf16 v[112:115], a[228:231], a[172:175], v[112:115]\n"
        "ds_read_b64_tr_b16 a[222:223], v25 offset:51464\n"
        "v_mfma_f32_16x16x32_bf16 v[144:147], a[224:227], a[176:179], v[144:147]\n"
        "buffer_load_dwordx4 v6, s[40:43], s89 offen offset:2048 lds\n"
        "v_mfma_f32_16x16x32_bf16 v[144:147], a[228:231], a[180:183], v[144:147]\n"
        "v_mfma_f32_16x16x32_bf16 v[176:179], a[224:227], a[184:187], v[176:179]\n"
        "v_mfma_f32_16x16x32_bf16 v[176:179], a[228:231], a[188:191], v[176:179]\n"
        "v_mfma_f32_16x16x32_bf16 v[208:211], a[224:227], a[192:195], v[208:211]\n"
        "ds_read_b64_tr_b16 a[224:225], v26 offset:33024\n"
        "v_mfma_f32_16x16x32_bf16 v[208:211], a[228:231], a[196:199], v[208:211]\n"
        "ds_read_b64_tr_b16 a[226:227], v26 offset:35072\n"
        "v_mfma_f32_16x16x32_bf16 v[116:119], a[232:235], a[168:171], v[116:119]\n"
        "s_waitcnt lgkmcnt(2)\n"
        "ds_read_b64_tr_b16 a[228:229], v26 offset:49408\n"
        "v_mfma_f32_16x16x32_bf16 v[116:119], a[236:239], a[172:175], v[116:119]\n"
        "ds_read_b64_tr_b16 a[230:231], v26 offset:51456\n"
        "v_mfma_f32_16x16x32_bf16 v[148:151], a[232:235], a[176:179], v[148:151]\n"
        "buffer_load_dwordx4 v7, s[40:43], s89 offen offset:3072 lds\n"
        "v_mfma_f32_16x16x32_bf16 v[148:151], a[236:239], a[180:183], v[148:151]\n"
        "s_add_u32 m0, s90, 106496\n"
        "v_mfma_f32_16x16x32_bf16 v[180:183], a[232:235], a[184:187], v[180:183]\n"
        "v_mfma_f32_16x16x32_bf16 v[180:183], a[236:239], a[188:191], v[180:183]\n"
        "v_mfma_f32_16x16x32_bf16 v[212:215], a[232:235], a[192:195], v[212:215]\n"
        "ds_read_b64_tr_b16 a[232:233], v26 offset:33032\n"
        "v_mfma_f32_16x16x32_bf16 v[212:215], a[236:239], a[196:199], v[212:215]\n"
        "ds_read_b64_tr_b16 a[234:235], v26 offset:35080\n"
        "v_mfma_f32_16x16x32_bf16 a[0:3], a[208:211], a[168:171], a[0:3]\n"
        "ds_read_b64_tr_b16 a[236:237], v26 offset:49416\n"
        "v_mfma_f32_16x16x32_bf16 a[0:3], a[212:215], a[172:175], a[0:3]\n"
        "ds_read_b64_tr_b16 a[238:239], v26 offset:51464\n"
        "v_mfma_f32_16x16x32_bf16 a[32:35], a[208:211], a[176:179], a[32:35]\n"
        "buffer_load_dwordx4 v8, s[40:43], s89 offen offset:0 lds\n"
        "v_mfma_f32_16x16x32_bf16 a[32:35], a[212:215], a[180:183], a[32:35]\n"
        "v_mfma_f32_16x16x32_bf16 a[64:67], a[208:211], a[184:187], a[64:67]\n"
        "v_mfma_f32_16x16x32_bf16 a[64:67], a[212:215], a[188:191], a[64:67]\n"
        "v_mfma_f32_16x16x32_bf16 a[96:99], a[208:211], a[192:195], a[96:99]\n"
        "buffer_load_dwordx4 v9, s[40:43], s89 offen offset:1024 lds\n"
        "v_mfma_f32_16x16x32_bf16 a[96:99], a[212:215], a[196:199], a[96:99]\n"
        "v_mfma_f32_16x16x32_bf16 v[68:71], a[208:211], a[200:203], v[68:71]\n"
        "ds_read_b64_tr_b16 a[208:209], v27 offset:33024\n"
        "v_mfma_f32_16x16x32_bf16 v[68:71], a[212:215], a[204:207], v[68:71]\n"
        "ds_read_b64_tr_b16 a[210:211], v27 offset:35072\n"
        "v_mfma_f32_16x16x32_bf16 a[4:7], a[216:219], a[168:171], a[4:7]\n"
        "ds_read_b64_tr_b16 a[212:213], v27 offset:49408\n"
        "v_mfma_f32_16x16x32_bf16 a[4:7], a[220:223], a[172:175], a[4:7]\n"
        "ds_read_b64_tr_b16 a[214:215], v27 offset:51456\n"
        "v_mfma_f32_16x16x32_bf16 a[36:39], a[216:219], a[176:179], a[36:39]\n"
        "buffer_load_dwordx4 v10, s[40:43], s89 offen offset:2048 lds\n"
        "v_mfma_f32_16x16x32_bf16 a[36:39], a[220:223], a[180:183], a[36:39]\n"
        "v_mfma_f32_16x16x32_bf16 a[68:71], a[216:219], a[184:187], a[68:71]\n"
        "v_mfma_f32_16x16x32_bf16 a[68:71], a[220:223], a[188:191], a[68:71]\n"
        "v_mfma_f32_16x16x32_bf16 a[100:103], a[216:219], a[192:195], a[100:103]\n"
        "buffer_load_dwordx4 v11, s[40:43], s89 offen offset:3072 lds\n"
        "v_mfma_f32_16x16x32_bf16 a[100:103], a[220:223], a[196:199], a[100:103]\n"
        "s_add_u32 m0, s90, 110592\n"
        "v_mfma_f32_16x16x32_bf16 v[72:75], a[216:219], a[200:203], v[72:75]\n"
        "ds_read_b64_tr_b16 a[216:217], v27 offset:33032\n"
        "v_mfma_f32_16x16x32_bf16 v[72:75], a[220:223], a[204:207], v[72:75]\n"
        "s_waitcnt lgkmcnt(1)\n"
        "ds_read_b64_tr_b16 a[218:219], v27 offset:35080\n"
        "v_mfma_f32_16x16x32_bf16 a[8:11], a[224:227], a[168:171], a[8:11]\n"
        "ds_read_b64_tr_b16 a[220:221], v27 offset:49416\n"
        "v_mfma_f32_16x16x32_bf16 a[8:11], a[228:231], a[172:175], a[8:11]\n"
        "ds_read_b64_tr_b16 a[222:223], v27 offset:51464\n"
        "v_mfma_f32_16x16x32_bf16 a[40:43], a[224:227], a[176:179], a[40:43]\n"
        "buffer_load_dwordx4 v12, s[40:43], s89 offen offset:0 lds\n"
        "v_mfma_f32_16x16x32_bf16 a[40:43], a[228:231], a[180:183], a[40:43]\n"
        "s_add_u32 s93, s93, 524288\n"
        "v_mfma_f32_16x16x32_bf16 a[72:75], a[224:227], a[184:187], a[72:75]\n"
        "v_mfma_f32_16x16x32_bf16 a[72:75], a[228:231], a[188:191], a[72:75]\n"
        "v_mfma_f32_16x16x32_bf16 a[104:107], a[224:227], a[192:195], a[104:107]\n"
        "buffer_load_dword a246, v33, s[44:47], s93 offen\n"
        "v_mfma_f32_16x16x32_bf16 a[104:107], a[228:231], a[196:199], a[104:107]\n"
        "v_mfma_f32_16x16x32_bf16 v[76:79], a[224:227], a[200:203], v[76:79]\n"
        "ds_read_b64_tr_b16 a[224:225], v28 offset:33024\n"
        "v_mfma_f32_16x16x32_bf16 v[76:79], a[228:231], a[204:207], v[76:79]\n"
        "ds_read_b64_tr_b16 a[226:227], v28 offset:35072\n"
        "v_mfma_f32_16x16x32_bf16 a[12:15], a[232:235], a[168:171], a[12:15]\n"
        "ds_read_b64_tr_b16 a[228:229], v28 offset:49408\n"
        "v_mfma_f32_16x16x32_bf16 a[12:15], a[236:239], a[172:175], a[12:15]\n"
        "ds_read_b64_tr_b16 a[230:231], v28 offset:51456\n"
        "v_mfma_f32_16x16x32_bf16 a[44:47], a[232:235], a[176:179], a[44:47]\n"
        "buffer_load_dword a248, v36, s[40:43], 0 offen offset:896\n"
        "v_mfma_f32_16x16x32_bf16 a[44:47], a[236:239], a[180:183], a[44:47]\n"
        "v_mfma_f32_16x16x32_bf16 a[76:79], a[232:235], a[184:187], a[76:79]\n"
        "v_mfma_f32_16x16x32_bf16 a[76:79], a[236:239], a[188:191], a[76:79]\n"
        "v_mfma_f32_16x16x32_bf16 a[108:111], a[232:235], a[192:195], a[108:111]\n"
        "v_mfma_f32_16x16x32_bf16 a[108:111], a[236:239], a[196:199], a[108:111]\n"
        "v_mfma_f32_16x16x32_bf16 v[80:83], a[232:235], a[200:203], v[80:83]\n"
        "ds_read_b64_tr_b16 a[232:233], v28 offset:33032\n"
        "v_mfma_f32_16x16x32_bf16 v[80:83], a[236:239], a[204:207], v[80:83]\n"
        "ds_read_b64_tr_b16 a[234:235], v28 offset:35080\n"
        "v_mfma_f32_16x16x32_bf16 a[16:19], a[208:211], a[168:171], a[16:19]\n"
        "ds_read_b64_tr_b16 a[236:237], v28 offset:49416\n"
        "v_mfma_f32_16x16x32_bf16 a[16:19], a[212:215], a[172:175], a[16:19]\n"
        "ds_read_b64_tr_b16 a[238:239], v28 offset:51464\n"
        "v_mfma_f32_16x16x32_bf16 a[48:51], a[208:211], a[176:179], a[48:51]\n"
        "v_mfma_f32_16x16x32_bf16 a[48:51], a[212:215], a[180:183], a[48:51]\n"
        "v_mfma_f32_16x16x32_bf16 a[80:83], a[208:211], a[184:187], a[80:83]\n"
        "s_waitcnt vmcnt(11)\n"
        "s_barrier\n"
        "v_mfma_f32_16x16x32_bf16 a[80:83], a[212:215], a[188:191], a[80:83]\n"
        "ds_read_b128 a[128:131], v40 offset:0\n"
        "v_mfma_f32_16x16x32_bf16 a[112:115], a[208:211], a[192:195], a[112:115]\n"
        "ds_read_b128 a[132:135], v41 offset:0\n"
        "v_mfma_f32_16x16x32_bf16 a[112:115], a[212:215], a[196:199], a[112:115]\n"
        "s_waitcnt lgkmcnt(3)\n"
        "ds_read_b128 a[136:139], v42 offset:0\n"
        "v_mfma_f32_16x16x32_bf16 a[20:23], a[216:219], a[168:171], a[20:23]\n"
        "ds_read_b128 a[140:143], v43 offset:0\n"
        "v_mfma_f32_16x16x32_bf16 a[20:23], a[220:223], a[172:175], a[20:23]\n"
        "ds_read_b128 a[144:147], v44 offset:0\n"
        "v_mfma_f32_16x16x32_bf16 a[52:55], a[216:219], a[176:179], a[52:55]\n"
        "ds_read_b128 a[148:151], v45 offset:0\n"
        "v_mfma_f32_16x16x32_bf16 a[52:55], a[220:223], a[180:183], a[52:55]\n"
        "ds_read_b128 a[152:155], v46 offset:0\n"
        "v_mfma_f32_16x16x32_bf16 a[84:87], a[216:219], a[184:187], a[84:87]\n"
        "ds_read_b128 a[156:159], v47 offset:0\n"
        "v_mfma_f32_16x16x32_bf16 a[84:87], a[220:223], a[188:191], a[84:87]\n"
        "ds_read_b128 a[160:163], v15 offset:0\n"
        "v_mfma_f32_16x16x32_bf16 a[116:119], a[216:219], a[192:195], a[116:119]\n"
        "ds_read_b128 a[164:167], v16 offset:0\n"
        "v_mfma_f32_16x16x32_bf16 a[116:119], a[220:223], a[196:199], a[116:119]\n"
        "ds_read_b64_tr_b16 a[208:209], v25 offset:0\n"
        "v_mfma_f32_16x16x32_bf16 a[24:27], a[224:227], a[168:171], a[24:27]\n"
        "ds_read_b64_tr_b16 a[210:211], v25 offset:2048\n"
        "v_mfma_f32_16x16x32_bf16 a[24:27], a[228:231], a[172:175], a[24:27]\n"
        "ds_read_b64_tr_b16 a[212:213], v25 offset:16384\n"
        "v_mfma_f32_16x16x32_bf16 a[56:59], a[224:227], a[176:179], a[56:59]\n"
        "ds_read_b64_tr_b16 a[214:215], v25 offset:18432\n"
        "v_mfma_f32_16x16x32_bf16 a[56:59], a[228:231], a[180:183], a[56:59]\n"
        "ds_read_b64_tr_b16 a[216:217], v25 offset:8\n"
        "v_mfma_f32_16x16x32_bf16 a[88:91], a[224:227], a[184:187], a[88:91]\n"
        "ds_read_b64_tr_b16 a[218:219], v25 offset:2056\n"
        "v_mfma_f32_16x16x32_bf16 a[88:91], a[228:231], a[188:191], a[88:91]\n"
        "ds_read_b64_tr_b16 a[220:221], v25 offset:16392\n"
        "v_mfma_f32_16x16x32_bf16 a[120:123], a[224:227], a[192:195], a[120:123]\n"
        "ds_read_b64_tr_b16 a[222:223], v25 offset:18440\n"
        "v_mfma_f32_16x16x32_bf16 a[120:123], a[228:231], a[196:199], a[120:123]\n"
        "ds_read_b64_tr_b16 a[224:225], v26 offset:0\n"
        "v_mfma_f32_16x16x32_bf16 a[28:31], a[232:235], a[168:171], a[28:31]\n"
        "s_waitcnt lgkmcnt(6)\n"
        "ds_read_b64_tr_b16 a[226:227], v26 offset:2048\n"
        "v_mfma_f32_16x16x32_bf16 a[28:31], a[236:239], a[172:175], a[28:31]\n"
        "ds_read_b64_tr_b16 a[228:229], v26 offset:16384\n"
        "v_mfma_f32_16x16x32_bf16 a[60:63], a[232:235], a[176:179], a[60:63]\n"
        "ds_read_b64_tr_b16 a[230:231], v26 offset:18432\n"
        "v_mfma_f32_16x16x32_bf16 a[60:63], a[236:239], a[180:183], a[60:63]\n"
        "v_mfma_f32_16x16x32_bf16 a[92:95], a[232:235], a[184:187], a[92:95]\n"
        "s_barrier\n"
        "v_mfma_f32_16x16x32_bf16 a[92:95], a[236:239], a[188:191], a[92:95]\n"
        "s_mov_b32 s79, 2621440\n"
        "s_add_u32 m0, s66, 32768\n"
        "v_mfma_f32_16x16x32_bf16 a[124:127], a[232:235], a[192:195], a[124:127]\n"
        "ds_read_b64_tr_b16 a[232:233], v26 offset:8\n"
        "v_mfma_f32_16x16x32_bf16 a[124:127], a[236:239], a[196:199], a[124:127]\n"
        "ds_read_b64_tr_b16 a[234:235], v26 offset:2056\n"
        "v_mfma_f32_16x16x32_bf16 v[88:91], a[208:211], a[128:131], v[88:91]\n"
        "s_waitcnt lgkmcnt(3)\n"
        "ds_read_b64_tr_b16 a[236:237], v26 offset:16392\n"
        "v_mfma_f32_16x16x32_bf16 v[88:91], a[212:215], a[132:135], v[88:91]\n"
        "ds_read_b64_tr_b16 a[238:239], v26 offset:18440\n"
        "v_mfma_f32_16x16x32_bf16 v[120:123], a[208:211], a[136:139], v[120:123]\n"
        "buffer_load_dwordx4 v17, s[44:47], s79 offen offset:0 lds\n"
        "v_mfma_f32_16x16x32_bf16 v[120:123], a[212:215], a[140:143], v[120:123]\n"
        "v_mfma_f32_16x16x32_bf16 v[152:155], a[208:211], a[144:147], v[152:155]\n"
        "v_mfma_f32_16x16x32_bf16 v[152:155], a[212:215], a[148:151], v[152:155]\n"
        "v_mfma_f32_16x16x32_bf16 v[184:187], a[208:211], a[152:155], v[184:187]\n"
        "buffer_load_dwordx4 v18, s[44:47], s79 offen offset:1024 lds\n"
        "v_mfma_f32_16x16x32_bf16 v[184:187], a[212:215], a[156:159], v[184:187]\n"
        "v_mfma_f32_16x16x32_bf16 v[216:219], a[208:211], a[160:163], v[216:219]\n"
        "ds_read_b64_tr_b16 a[208:209], v27 offset:0\n"
        "v_mfma_f32_16x16x32_bf16 v[216:219], a[212:215], a[164:167], v[216:219]\n"
        "ds_read_b64_tr_b16 a[210:211], v27 offset:2048\n"
        "v_mfma_f32_16x16x32_bf16 v[92:95], a[216:219], a[128:131], v[92:95]\n"
        "ds_read_b64_tr_b16 a[212:213], v27 offset:16384\n"
        "v_mfma_f32_16x16x32_bf16 v[92:95], a[220:223], a[132:135], v[92:95]\n"
        "ds_read_b64_tr_b16 a[214:215], v27 offset:18432\n"
        "v_mfma_f32_16x16x32_bf16 v[124:127], a[216:219], a[136:139], v[124:127]\n"
        "buffer_load_dwordx4 v19, s[44:47], s79 offen offset:2048 lds\n"
        "v_mfma_f32_16x16x32_bf16 v[124:127], a[220:223], a[140:143], v[124:127]\n"
        "v_mfma_f32_16x16x32_bf16 v[156:159], a[216:219], a[144:147], v[156:159]\n"
        "v_mfma_f32_16x16x32_bf16 v[156:159], a[220:223], a[148:151], v[156:159]\n"
        "v_mfma_f32_16x16x32_bf16 v[188:191], a[216:219], a[152:155], v[188:191]\n"
        "buffer_load_dwordx4 v20, s[44:47], s79 offen offset:3072 lds\n"
        "v_mfma_f32_16x16x32_bf16 v[188:191], a[220:223], a[156:159], v[188:191]\n"
        "s_add_u32 m0, s66, 36864\n"
        "v_mfma_f32_16x16x32_bf16 v[220:223], a[216:219], a[160:163], v[220:223]\n"
        "ds_read_b64_tr_b16 a[216:217], v27 offset:8\n"
        "v_mfma_f32_16x16x32_bf16 v[220:223], a[220:223], a[164:167], v[220:223]\n"
        "ds_read_b64_tr_b16 a[218:219], v27 offset:2056\n"
        "v_mfma_f32_16x16x32_bf16 v[96:99], a[224:227], a[128:131], v[96:99]\n"
        "s_waitcnt lgkmcnt(2)\n"
        "ds_read_b64_tr_b16 a[220:221], v27 offset:16392\n"
        "v_mfma_f32_16x16x32_bf16 v[96:99], a[228:231], a[132:135], v[96:99]\n"
        "ds_read_b64_tr_b16 a[222:223], v27 offset:18440\n"
        "v_mfma_f32_16x16x32_bf16 v[128:131], a[224:227], a[136:139], v[128:131]\n"
        "buffer_load_dwordx4 v21, s[44:47], s79 offen offset:0 lds\n"
        "v_mfma_f32_16x16x32_bf16 v[128:131], a[228:231], a[140:143], v[128:131]\n"
        "v_mfma_f32_16x16x32_bf16 v[160:163], a[224:227], a[144:147], v[160:163]\n"
        "v_mfma_f32_16x16x32_bf16 v[160:163], a[228:231], a[148:151], v[160:163]\n"
        "v_mfma_f32_16x16x32_bf16 v[192:195], a[224:227], a[152:155], v[192:195]\n"
        "buffer_load_dwordx4 v22, s[44:47], s79 offen offset:1024 lds\n"
        "v_mfma_f32_16x16x32_bf16 v[192:195], a[228:231], a[156:159], v[192:195]\n"
        "v_mfma_f32_16x16x32_bf16 v[224:227], a[224:227], a[160:163], v[224:227]\n"
        "ds_read_b64_tr_b16 a[224:225], v28 offset:0\n"
        "v_mfma_f32_16x16x32_bf16 v[224:227], a[228:231], a[164:167], v[224:227]\n"
        "ds_read_b64_tr_b16 a[226:227], v28 offset:2048\n"
        "v_mfma_f32_16x16x32_bf16 v[100:103], a[232:235], a[128:131], v[100:103]\n"
        "ds_read_b64_tr_b16 a[228:229], v28 offset:16384\n"
        "v_mfma_f32_16x16x32_bf16 v[100:103], a[236:239], a[132:135], v[100:103]\n"
        "ds_read_b64_tr_b16 a[230:231], v28 offset:18432\n"
        "v_mfma_f32_16x16x32_bf16 v[132:135], a[232:235], a[136:139], v[132:135]\n"
        "buffer_load_dwordx4 v23, s[44:47], s79 offen offset:2048 lds\n"
        "v_mfma_f32_16x16x32_bf16 v[132:135], a[236:239], a[140:143], v[132:135]\n"
        "v_mfma_f32_16x16x32_bf16 v[164:167], a[232:235], a[144:147], v[164:167]\n"
        "v_mfma_f32_16x16x32_bf16 v[164:167], a[236:239], a[148:151], v[164:167]\n"
        "v_mfma_f32_16x16x32_bf16 v[196:199], a[232:235], a[152:155], v[196:199]\n"
        "buffer_load_dwordx4 v24, s[44:47], s79 offen offset:3072 lds\n"
        "v_mfma_f32_16x16x32_bf16 v[196:199], a[236:239], a[156:159], v[196:199]\n"
        "s_mov_b32 s89, 768\n"
        "s_add_u32 m0, s90, 65536\n"
        "v_mfma_f32_16x16x32_bf16 v[228:231], a[232:235], a[160:163], v[228:231]\n"
        "ds_read_b64_tr_b16 a[232:233], v28 offset:8\n"
        "v_mfma_f32_16x16x32_bf16 v[228:231], a[236:239], a[164:167], v[228:231]\n"
        "ds_read_b64_tr_b16 a[234:235], v28 offset:2056\n"
        "v_mfma_f32_16x16x32_bf16 v[104:107], a[208:211], a[128:131], v[104:107]\n"
        "ds_read_b64_tr_b16 a[236:237], v28 offset:16392\n"
        "v_mfma_f32_16x16x32_bf16 v[104:107], a[212:215], a[132:135], v[104:107]\n"
        "ds_read_b64_tr_b16 a[238:239], v28 offset:18440\n"
        "v_mfma_f32_16x16x32_bf16 v[136:139], a[208:211], a[136:139], v[136:139]\n"
        "buffer_load_dwordx4 v4, s[40:43], s89 offen offset:0 lds\n"
        "v_mfma_f32_16x16x32_bf16 v[136:139], a[212:215], a[140:143], v[136:139]\n"
        "v_mfma_f32_16x16x32_bf16 v[168:171], a[208:211], a[144:147], v[168:171]\n"
        "v_mfma_f32_16x16x32_bf16 v[168:171], a[212:215], a[148:151], v[168:171]\n"
        "v_mfma_f32_16x16x32_bf16 v[200:203], a[208:211], a[152:155], v[200:203]\n"
        "ds_read_b64_tr_b16 a[208:209], v25 offset:256\n"
        "v_mfma_f32_16x16x32_bf16 v[200:203], a[212:215], a[156:159], v[200:203]\n"
        "s_waitcnt lgkmcnt(2)\n"
        "ds_read_b64_tr_b16 a[210:211], v25 offset:2304\n"
        "v_mfma_f32_16x16x32_bf16 v[108:111], a[216:219], a[128:131], v[108:111]\n"
        "ds_read_b64_tr_b16 a[212:213], v25 offset:16640\n"
        "v_mfma_f32_16x16x32_bf16 v[108:111], a[220:223], a[132:135], v[108:111]\n"
        "ds_read_b64_tr_b16 a[214:215], v25 offset:18688\n"
        "v_mfma_f32_16x16x32_bf16 v[140:143], a[216:219], a[136:139], v[140:143]\n"
        "buffer_load_dwordx4 v5, s[40:43], s89 offen offset:1024 lds\n"
        "v_mfma_f32_16x16x32_bf16 v[140:143], a[220:223], a[140:143], v[140:143]\n"
        "v_mfma_f32_16x16x32_bf16 v[172:175], a[216:219], a[144:147], v[172:175]\n"
        "v_mfma_f32_16x16x32_bf16 v[172:175], a[220:223], a[148:151], v[172:175]\n"
        "v_mfma_f32_16x16x32_bf16 v[204:207], a[216:219], a[152:155], v[204:207]\n"
        "ds_read_b64_tr_b16 a[216:217], v25 offset:264\n"
        "v_mfma_f32_16x16x32_bf16 v[204:207], a[220:223], a[156:159], v[204:207]\n"
        "ds_read_b64_tr_b16 a[218:219], v25 offset:2312\n"
        "v_mfma_f32_16x16x32_bf16 v[112:115], a[224:227], a[128:131], v[112:115]\n"
        "ds_read_b64_tr_b16 a[220:221], v25 offset:16648\n"
        "v_mfma_f32_16x16x32_bf16 v[112:115], a[228:231], a[132:135], v[112:115]\n"
        "ds_read_b64_tr_b16 a[222:223], v25 offset:18696\n"
        "v_mfma_f32_16x16x32_bf16 v[144:147], a[224:227], a[136:139], v[144:147]\n"
        "buffer_load_dwordx4 v6, s[40:43], s89 offen offset:2048 lds\n"
        "v_mfma_f32_16x16x32_bf16 v[144:147], a[228:231], a[140:143], v[144:147]\n"
        "v_mfma_f32_16x16x32_bf16 v[176:179], a[224:227], a[144:147], v[176:179]\n"
        "v_mfma_f32_16x16x32_bf16 v[176:179], a[228:231], a[148:151], v[176:179]\n"
        "v_mfma_f32_16x16x32_bf16 v[208:211], a[224:227], a[152:155], v[208:211]\n"
        "ds_read_b64_tr_b16 a[224:225], v26 offset:256\n"
        "v_mfma_f32_16x16x32_bf16 v[208:211], a[228:231], a[156:159], v[208:211]\n"
        "ds_read_b64_tr_b16 a[226:227], v26 offset:2304\n"
        "v_mfma_f32_16x16x32_bf16 v[116:119], a[232:235], a[128:131], v[116:119]\n"
        "s_waitcnt lgkmcnt(2)\n"
        "ds_read_b64_tr_b16 a[228:229], v26 offset:16640\n"
        "v_mfma_f32_16x16x32_bf16 v[116:119], a[236:239], a[132:135], v[116:119]\n"
        "ds_read_b64_tr_b16 a[230:231], v26 offset:18688\n"
        "v_mfma_f32_16x16x32_bf16 v[148:151], a[232:235], a[136:139], v[148:151]\n"
        "buffer_load_dwordx4 v7, s[40:43], s89 offen offset:3072 lds\n"
        "v_mfma_f32_16x16x32_bf16 v[148:151], a[236:239], a[140:143], v[148:151]\n"
        "s_add_u32 m0, s90, 69632\n"
        "v_mfma_f32_16x16x32_bf16 v[180:183], a[232:235], a[144:147], v[180:183]\n"
        "v_mfma_f32_16x16x32_bf16 v[180:183], a[236:239], a[148:151], v[180:183]\n"
        "v_mfma_f32_16x16x32_bf16 v[212:215], a[232:235], a[152:155], v[212:215]\n"
        "ds_read_b64_tr_b16 a[232:233], v26 offset:264\n"
        "v_mfma_f32_16x16x32_bf16 v[212:215], a[236:239], a[156:159], v[212:215]\n"
        "ds_read_b64_tr_b16 a[234:235], v26 offset:2312\n"
        "v_mfma_f32_16x16x32_bf16 a[0:3], a[208:211], a[128:131], a[0:3]\n"
        "ds_read_b64_tr_b16 a[236:237], v26 offset:16648\n"
        "v_mfma_f32_16x16x32_bf16 a[0:3], a[212:215], a[132:135], a[0:3]\n"
        "ds_read_b64_tr_b16 a[238:239], v26 offset:18696\n"
        "v_mfma_f32_16x16x32_bf16 a[32:35], a[208:211], a[136:139], a[32:35]\n"
        "buffer_load_dwordx4 v8, s[40:43], s89 offen offset:0 lds\n"
        "v_mfma_f32_16x16x32_bf16 a[32:35], a[212:215], a[140:143], a[32:35]\n"
        "v_mfma_f32_16x16x32_bf16 a[64:67], a[208:211], a[144:147], a[64:67]\n"
        "v_mfma_f32_16x16x32_bf16 a[64:67], a[212:215], a[148:151], a[64:67]\n"
        "v_mfma_f32_16x16x32_bf16 a[96:99], a[208:211], a[152:155], a[96:99]\n"
        "buffer_load_dwordx4 v9, s[40:43], s89 offen offset:1024 lds\n"
        "v_mfma_f32_16x16x32_bf16 a[96:99], a[212:215], a[156:159], a[96:99]\n"
        "v_mfma_f32_16x16x32_bf16 v[68:71], a[208:211], a[160:163], v[68:71]\n"
        "ds_read_b64_tr_b16 a[208:209], v27 offset:256\n"
        "v_mfma_f32_16x16x32_bf16 v[68:71], a[212:215], a[164:167], v[68:71]\n"
        "ds_read_b64_tr_b16 a[210:211], v27 offset:2304\n"
        "v_mfma_f32_16x16x32_bf16 a[4:7], a[216:219], a[128:131], a[4:7]\n"
        "ds_read_b64_tr_b16 a[212:213], v27 offset:16640\n"
        "v_mfma_f32_16x16x32_bf16 a[4:7], a[220:223], a[132:135], a[4:7]\n"
        "ds_read_b64_tr_b16 a[214:215], v27 offset:18688\n"
        "v_mfma_f32_16x16x32_bf16 a[36:39], a[216:219], a[136:139], a[36:39]\n"
        "buffer_load_dwordx4 v10, s[40:43], s89 offen offset:2048 lds\n"
        "v_mfma_f32_16x16x32_bf16 a[36:39], a[220:223], a[140:143], a[36:39]\n"
        "v_mfma_f32_16x16x32_bf16 a[68:71], a[216:219], a[144:147], a[68:71]\n"
        "v_mfma_f32_16x16x32_bf16 a[68:71], a[220:223], a[148:151], a[68:71]\n"
        "v_mfma_f32_16x16x32_bf16 a[100:103], a[216:219], a[152:155], a[100:103]\n"
        "buffer_load_dwordx4 v11, s[40:43], s89 offen offset:3072 lds\n"
        "v_mfma_f32_16x16x32_bf16 a[100:103], a[220:223], a[156:159], a[100:103]\n"
        "s_add_u32 m0, s90, 73728\n"
        "v_mfma_f32_16x16x32_bf16 v[72:75], a[216:219], a[160:163], v[72:75]\n"
        "ds_read_b64_tr_b16 a[216:217], v27 offset:264\n"
        "v_mfma_f32_16x16x32_bf16 v[72:75], a[220:223], a[164:167], v[72:75]\n"
        "s_waitcnt lgkmcnt(1)\n"
        "ds_read_b64_tr_b16 a[218:219], v27 offset:2312\n"
        "v_mfma_f32_16x16x32_bf16 a[8:11], a[224:227], a[128:131], a[8:11]\n"
        "ds_read_b64_tr_b16 a[220:221], v27 offset:16648\n"
        "v_mfma_f32_16x16x32_bf16 a[8:11], a[228:231], a[132:135], a[8:11]\n"
        "ds_read_b64_tr_b16 a[222:223], v27 offset:18696\n"
        "v_mfma_f32_16x16x32_bf16 a[40:43], a[224:227], a[136:139], a[40:43]\n"
        "buffer_load_dwordx4 v12, s[40:43], s89 offen offset:0 lds\n"
        "v_mfma_f32_16x16x32_bf16 a[40:43], a[228:231], a[140:143], a[40:43]\n"
        "s_add_u32 s93, s93, 524288\n"
        "v_mfma_f32_16x16x32_bf16 a[72:75], a[224:227], a[144:147], a[72:75]\n"
        "v_mfma_f32_16x16x32_bf16 a[72:75], a[228:231], a[148:151], a[72:75]\n"
        "v_mfma_f32_16x16x32_bf16 a[104:107], a[224:227], a[152:155], a[104:107]\n"
        "buffer_load_dword a247, v33, s[44:47], s93 offen\n"
        "v_mfma_f32_16x16x32_bf16 a[104:107], a[228:231], a[156:159], a[104:107]\n"
        "v_mfma_f32_16x16x32_bf16 v[76:79], a[224:227], a[160:163], v[76:79]\n"
        "ds_read_b64_tr_b16 a[224:225], v28 offset:256\n"
        "v_mfma_f32_16x16x32_bf16 v[76:79], a[228:231], a[164:167], v[76:79]\n"
        "ds_read_b64_tr_b16 a[226:227], v28 offset:2304\n"
        "v_mfma_f32_16x16x32_bf16 a[12:15], a[232:235], a[128:131], a[12:15]\n"
        "ds_read_b64_tr_b16 a[228:229], v28 offset:16640\n"
        "v_mfma_f32_16x16x32_bf16 a[12:15], a[236:239], a[132:135], a[12:15]\n"
        "ds_read_b64_tr_b16 a[230:231], v28 offset:18688\n"
        "v_mfma_f32_16x16x32_bf16 a[44:47], a[232:235], a[136:139], a[44:47]\n"
        "buffer_load_dword a249, v36, s[40:43], 0 offen offset:1024\n"
        "v_mfma_f32_16x16x32_bf16 a[44:47], a[236:239], a[140:143], a[44:47]\n"
        "v_mfma_f32_16x16x32_bf16 a[76:79], a[232:235], a[144:147], a[76:79]\n"
        "v_mfma_f32_16x16x32_bf16 a[76:79], a[236:239], a[148:151], a[76:79]\n"
        "v_mfma_f32_16x16x32_bf16 a[108:111], a[232:235], a[152:155], a[108:111]\n"
        "v_mfma_f32_16x16x32_bf16 a[108:111], a[236:239], a[156:159], a[108:111]\n"
        "v_mfma_f32_16x16x32_bf16 v[80:83], a[232:235], a[160:163], v[80:83]\n"
        "ds_read_b64_tr_b16 a[232:233], v28 offset:264\n"
        "v_mfma_f32_16x16x32_bf16 v[80:83], a[236:239], a[164:167], v[80:83]\n"
        "ds_read_b64_tr_b16 a[234:235], v28 offset:2312\n"
        "v_mfma_f32_16x16x32_bf16 a[16:19], a[208:211], a[128:131], a[16:19]\n"
        "ds_read_b64_tr_b16 a[236:237], v28 offset:16648\n"
        "v_mfma_f32_16x16x32_bf16 a[16:19], a[212:215], a[132:135], a[16:19]\n"
        "ds_read_b64_tr_b16 a[238:239], v28 offset:18696\n"
        "v_mfma_f32_16x16x32_bf16 a[48:51], a[208:211], a[136:139], a[48:51]\n"
        "v_mfma_f32_16x16x32_bf16 a[48:51], a[212:215], a[140:143], a[48:51]\n"
        "v_mfma_f32_16x16x32_bf16 a[80:83], a[208:211], a[144:147], a[80:83]\n"
        "s_waitcnt vmcnt(11)\n"
        "s_barrier\n"
        "v_mfma_f32_16x16x32_bf16 a[80:83], a[212:215], a[148:151], a[80:83]\n"
        "ds_read_b128 a[168:171], v40 offset:36864\n"
        "v_mfma_f32_16x16x32_bf16 a[112:115], a[208:211], a[152:155], a[112:115]\n"
        "ds_read_b128 a[172:175], v41 offset:36864\n"
        "v_mfma_f32_16x16x32_bf16 a[112:115], a[212:215], a[156:159], a[112:115]\n"
        "s_waitcnt lgkmcnt(3)\n"
        "ds_read_b128 a[176:179], v42 offset:36864\n"
        "v_mfma_f32_16x16x32_bf16 a[20:23], a[216:219], a[128:131], a[20:23]\n"
        "ds_read_b128 a[180:183], v43 offset:36864\n"
        "v_mfma_f32_16x16x32_bf16 a[20:23], a[220:223], a[132:135], a[20:23]\n"
        "ds_read_b128 a[184:187], v44 offset:36864\n"
        "v_mfma_f32_16x16x32_bf16 a[52:55], a[216:219], a[136:139], a[52:55]\n"
        "ds_read_b128 a[188:191], v45 offset:36864\n"
        "v_mfma_f32_16x16x32_bf16 a[52:55], a[220:223], a[140:143], a[52:55]\n"
        "ds_read_b128 a[192:195], v46 offset:36864\n"
        "v_mfma_f32_16x16x32_bf16 a[84:87], a[216:219], a[144:147], a[84:87]\n"
        "ds_read_b128 a[196:199], v47 offset:36864\n"
        "v_mfma_f32_16x16x32_bf16 a[84:87], a[220:223], a[148:151], a[84:87]\n"
        "ds_read_b128 a[200:203], v15 offset:36864\n"
        "v_mfma_f32_16x16x32_bf16 a[116:119], a[216:219], a[152:155], a[116:119]\n"
        "ds_read_b128 a[204:207], v16 offset:36864\n"
        "v_mfma_f32_16x16x32_bf16 a[116:119], a[220:223], a[156:159], a[116:119]\n"
        "ds_read_b64_tr_b16 a[208:209], v25 offset:32768\n"
        "v_mfma_f32_16x16x32_bf16 a[24:27], a[224:227], a[128:131], a[24:27]\n"
        "ds_read_b64_tr_b16 a[210:211], v25 offset:34816\n"
        "v_mfma_f32_16x16x32_bf16 a[24:27], a[228:231], a[132:135], a[24:27]\n"
        "ds_read_b64_tr_b16 a[212:213], v25 offset:49152\n"
        "v_mfma_f32_16x16x32_bf16 a[56:59], a[224:227], a[136:139], a[56:59]\n"
        "ds_read_b64_tr_b16 a[214:215], v25 offset:51200\n"
        "v_mfma_f32_16x16x32_bf16 a[56:59], a[228:231], a[140:143], a[56:59]\n"
        "ds_read_b64_tr_b16 a[216:217], v25 offset:32776\n"
        "v_mfma_f32_16x16x32_bf16 a[88:91], a[224:227], a[144:147], a[88:91]\n"
        "ds_read_b64_tr_b16 a[218:219], v25 offset:34824\n"
        "v_mfma_f32_16x16x32_bf16 a[88:91], a[228:231], a[148:151], a[88:91]\n"
        "ds_read_b64_tr_b16 a[220:221], v25 offset:49160\n"
        "v_mfma_f32_16x16x32_bf16 a[120:123], a[224:227], a[152:155], a[120:123]\n"
        "ds_read_b64_tr_b16 a[222:223], v25 offset:51208\n"
        "v_mfma_f32_16x16x32_bf16 a[120:123], a[228:231], a[156:159], a[120:123]\n"
        "ds_read_b64_tr_b16 a[224:225], v26 offset:32768\n"
        "v_mfma_f32_16x16x32_bf16 a[28:31], a[232:235], a[128:131], a[28:31]\n"
        "s_waitcnt lgkmcnt(6)\n"
        "ds_read_b64_tr_b16 a[226:227], v26 offset:34816\n"
        "v_mfma_f32_16x16x32_bf16 a[28:31], a[236:239], a[132:135], a[28:31]\n"
        "ds_read_b64_tr_b16 a[228:229], v26 offset:49152\n"
        "v_mfma_f32_16x16x32_bf16 a[60:63], a[232:235], a[136:139], a[60:63]\n"
        "ds_read_b64_tr_b16 a[230:231], v26 offset:51200\n"
        "v_mfma_f32_16x16x32_bf16 a[60:63], a[236:239], a[140:143], a[60:63]\n"
        "v_mfma_f32_16x16x32_bf16 a[92:95], a[232:235], a[144:147], a[92:95]\n"
        "s_barrier\n"
        "v_mfma_f32_16x16x32_bf16 a[92:95], a[236:239], a[148:151], a[92:95]\n"
        "s_mov_b32 s79, 3145728\n"
        "s_add_u32 m0, s66, 0\n"
        "v_mfma_f32_16x16x32_bf16 a[124:127], a[232:235], a[152:155], a[124:127]\n"
        "ds_read_b64_tr_b16 a[232:233], v26 offset:32776\n"
        "v_mfma_f32_16x16x32_bf16 a[124:127], a[236:239], a[156:159], a[124:127]\n"
        "ds_read_b64_tr_b16 a[234:235], v26 offset:34824\n"
        "v_mfma_f32_16x16x32_bf16 v[88:91], a[208:211], a[168:171], v[88:91]\n"
        "s_waitcnt lgkmcnt(3)\n"
        "ds_read_b64_tr_b16 a[236:237], v26 offset:49160\n"
        "v_mfma_f32_16x16x32_bf16 v[88:91], a[212:215], a[172:175], v[88:91]\n"
        "ds_read_b64_tr_b16 a[238:239], v26 offset:51208\n"
        "v_mfma_f32_16x16x32_bf16 v[120:123], a[208:211], a[176:179], v[120:123]\n"
        "buffer_load_dwordx4 v17, s[44:47], s79 offen offset:0 lds\n"
        "v_mfma_f32_16x16x32_bf16 v[120:123], a[212:215], a[180:183], v[120:123]\n"
        "v_mfma_f32_16x16x32_bf16 v[152:155], a[208:211], a[184:187], v[152:155]\n"
        "v_mfma_f32_16x16x32_bf16 v[152:155], a[212:215], a[188:191], v[152:155]\n"
        "v_mfma_f32_16x16x32_bf16 v[184:187], a[208:211], a[192:195], v[184:187]\n"
        "buffer_load_dwordx4 v18, s[44:47], s79 offen offset:1024 lds\n"
        "v_mfma_f32_16x16x32_bf16 v[184:187], a[212:215], a[196:199], v[184:187]\n"
        "v_mfma_f32_16x16x32_bf16 v[216:219], a[208:211], a[200:203], v[216:219]\n"
        "ds_read_b64_tr_b16 a[208:209], v27 offset:32768\n"
        "v_mfma_f32_16x16x32_bf16 v[216:219], a[212:215], a[204:207], v[216:219]\n"
        "ds_read_b64_tr_b16 a[210:211], v27 offset:34816\n"
        "v_mfma_f32_16x16x32_bf16 v[92:95], a[216:219], a[168:171], v[92:95]\n"
        "ds_read_b64_tr_b16 a[212:213], v27 offset:49152\n"
        "v_mfma_f32_16x16x32_bf16 v[92:95], a[220:223], a[172:175], v[92:95]\n"
        "ds_read_b64_tr_b16 a[214:215], v27 offset:51200\n"
        "v_mfma_f32_16x16x32_bf16 v[124:127], a[216:219], a[176:179], v[124:127]\n"
        "buffer_load_dwordx4 v19, s[44:47], s79 offen offset:2048 lds\n"
        "v_mfma_f32_16x16x32_bf16 v[124:127], a[220:223], a[180:183], v[124:127]\n"
        "v_mfma_f32_16x16x32_bf16 v[156:159], a[216:219], a[184:187], v[156:159]\n"
        "v_mfma_f32_16x16x32_bf16 v[156:159], a[220:223], a[188:191], v[156:159]\n"
        "v_mfma_f32_16x16x32_bf16 v[188:191], a[216:219], a[192:195], v[188:191]\n"
        "buffer_load_dwordx4 v20, s[44:47], s79 offen offset:3072 lds\n"
        "v_mfma_f32_16x16x32_bf16 v[188:191], a[220:223], a[196:199], v[188:191]\n"
        "s_add_u32 m0, s66, 4096\n"
        "v_mfma_f32_16x16x32_bf16 v[220:223], a[216:219], a[200:203], v[220:223]\n"
        "ds_read_b64_tr_b16 a[216:217], v27 offset:32776\n"
        "v_mfma_f32_16x16x32_bf16 v[220:223], a[220:223], a[204:207], v[220:223]\n"
        "ds_read_b64_tr_b16 a[218:219], v27 offset:34824\n"
        "v_mfma_f32_16x16x32_bf16 v[96:99], a[224:227], a[168:171], v[96:99]\n"
        "s_waitcnt lgkmcnt(2)\n"
        "ds_read_b64_tr_b16 a[220:221], v27 offset:49160\n"
        "v_mfma_f32_16x16x32_bf16 v[96:99], a[228:231], a[172:175], v[96:99]\n"
        "ds_read_b64_tr_b16 a[222:223], v27 offset:51208\n"
        "v_mfma_f32_16x16x32_bf16 v[128:131], a[224:227], a[176:179], v[128:131]\n"
        "buffer_load_dwordx4 v21, s[44:47], s79 offen offset:0 lds\n"
        "v_mfma_f32_16x16x32_bf16 v[128:131], a[228:231], a[180:183], v[128:131]\n"
        "v_mfma_f32_16x16x32_bf16 v[160:163], a[224:227], a[184:187], v[160:163]\n"
        "v_mfma_f32_16x16x32_bf16 v[160:163], a[228:231], a[188:191], v[160:163]\n"
        "v_mfma_f32_16x16x32_bf16 v[192:195], a[224:227], a[192:195], v[192:195]\n"
        "buffer_load_dwordx4 v22, s[44:47], s79 offen offset:1024 lds\n"
        "v_mfma_f32_16x16x32_bf16 v[192:195], a[228:231], a[196:199], v[192:195]\n"
        "v_mfma_f32_16x16x32_bf16 v[224:227], a[224:227], a[200:203], v[224:227]\n"
        "ds_read_b64_tr_b16 a[224:225], v28 offset:32768\n"
        "v_mfma_f32_16x16x32_bf16 v[224:227], a[228:231], a[204:207], v[224:227]\n"
        "ds_read_b64_tr_b16 a[226:227], v28 offset:34816\n"
        "v_mfma_f32_16x16x32_bf16 v[100:103], a[232:235], a[168:171], v[100:103]\n"
        "ds_read_b64_tr_b16 a[228:229], v28 offset:49152\n"
        "v_mfma_f32_16x16x32_bf16 v[100:103], a[236:239], a[172:175], v[100:103]\n"
        "ds_read_b64_tr_b16 a[230:231], v28 offset:51200\n"
        "v_mfma_f32_16x16x32_bf16 v[132:135], a[232:235], a[176:179], v[132:135]\n"
        "buffer_load_dwordx4 v23, s[44:47], s79 offen offset:2048 lds\n"
        "v_mfma_f32_16x16x32_bf16 v[132:135], a[236:239], a[180:183], v[132:135]\n"
        "v_mfma_f32_16x16x32_bf16 v[164:167], a[232:235], a[184:187], v[164:167]\n"
        "v_mfma_f32_16x16x32_bf16 v[164:167], a[236:239], a[188:191], v[164:167]\n"
        "v_mfma_f32_16x16x32_bf16 v[196:199], a[232:235], a[192:195], v[196:199]\n"
        "buffer_load_dwordx4 v24, s[44:47], s79 offen offset:3072 lds\n"
        "v_mfma_f32_16x16x32_bf16 v[196:199], a[236:239], a[196:199], v[196:199]\n"
        "s_mov_b32 s89, 896\n"
        "s_add_u32 m0, s90, 102400\n"
        "v_mfma_f32_16x16x32_bf16 v[228:231], a[232:235], a[200:203], v[228:231]\n"
        "ds_read_b64_tr_b16 a[232:233], v28 offset:32776\n"
        "v_mfma_f32_16x16x32_bf16 v[228:231], a[236:239], a[204:207], v[228:231]\n"
        "ds_read_b64_tr_b16 a[234:235], v28 offset:34824\n"
        "v_mfma_f32_16x16x32_bf16 v[104:107], a[208:211], a[168:171], v[104:107]\n"
        "ds_read_b64_tr_b16 a[236:237], v28 offset:49160\n"
        "v_mfma_f32_16x16x32_bf16 v[104:107], a[212:215], a[172:175], v[104:107]\n"
        "ds_read_b64_tr_b16 a[238:239], v28 offset:51208\n"
        "v_mfma_f32_16x16x32_bf16 v[136:139], a[208:211], a[176:179], v[136:139]\n"
        "buffer_load_dwordx4 v4, s[40:43], s89 offen offset:0 lds\n"
        "v_mfma_f32_16x16x32_bf16 v[136:139], a[212:215], a[180:183], v[136:139]\n"
        "v_mfma_f32_16x16x32_bf16 v[168:171], a[208:211], a[184:187], v[168:171]\n"
        "v_mfma_f32_16x16x32_bf16 v[168:171], a[212:215], a[188:191], v[168:171]\n"
        "v_mfma_f32_16x16x32_bf16 v[200:203], a[208:211], a[192:195], v[200:203]\n"
        "ds_read_b64_tr_b16 a[208:209], v25 offset:33024\n"
        "v_mfma_f32_16x16x32_bf16 v[200:203], a[212:215], a[196:199], v[200:203]\n"
        "s_waitcnt lgkmcnt(2)\n"
        "ds_read_b64_tr_b16 a[210:211], v25 offset:35072\n"
        "v_mfma_f32_16x16x32_bf16 v[108:111], a[216:219], a[168:171], v[108:111]\n"
        "ds_read_b64_tr_b16 a[212:213], v25 offset:49408\n"
        "v_mfma_f32_16x16x32_bf16 v[108:111], a[220:223], a[172:175], v[108:111]\n"
        "ds_read_b64_tr_b16 a[214:215], v25 offset:51456\n"
        "v_mfma_f32_16x16x32_bf16 v[140:143], a[216:219], a[176:179], v[140:143]\n"
        "buffer_load_dwordx4 v5, s[40:43], s89 offen offset:1024 lds\n"
        "v_mfma_f32_16x16x32_bf16 v[140:143], a[220:223], a[180:183], v[140:143]\n"
        "v_mfma_f32_16x16x32_bf16 v[172:175], a[216:219], a[184:187], v[172:175]\n"
        "v_mfma_f32_16x16x32_bf16 v[172:175], a[220:223], a[188:191], v[172:175]\n"
        "v_mfma_f32_16x16x32_bf16 v[204:207], a[216:219], a[192:195], v[204:207]\n"
        "ds_read_b64_tr_b16 a[216:217], v25 offset:33032\n"
        "v_mfma_f32_16x16x32_bf16 v[204:207], a[220:223], a[196:199], v[204:207]\n"
        "ds_read_b64_tr_b16 a[218:219], v25 offset:35080\n"
        "v_mfma_f32_16x16x32_bf16 v[112:115], a[224:227], a[168:171], v[112:115]\n"
        "ds_read_b64_tr_b16 a[220:221], v25 offset:49416\n"
        "v_mfma_f32_16x16x32_bf16 v[112:115], a[228:231], a[172:175], v[112:115]\n"
        "ds_read_b64_tr_b16 a[222:223], v25 offset:51464\n"
        "v_mfma_f32_16x16x32_bf16 v[144:147], a[224:227], a[176:179], v[144:147]\n"
        "buffer_load_dwordx4 v6, s[40:43], s89 offen offset:2048 lds\n"
        "v_mfma_f32_16x16x32_bf16 v[144:147], a[228:231], a[180:183], v[144:147]\n"
        "v_mfma_f32_16x16x32_bf16 v[176:179], a[224:227], a[184:187], v[176:179]\n"
        "v_mfma_f32_16x16x32_bf16 v[176:179], a[228:231], a[188:191], v[176:179]\n"
        "v_mfma_f32_16x16x32_bf16 v[208:211], a[224:227], a[192:195], v[208:211]\n"
        "ds_read_b64_tr_b16 a[224:225], v26 offset:33024\n"
        "v_mfma_f32_16x16x32_bf16 v[208:211], a[228:231], a[196:199], v[208:211]\n"
        "ds_read_b64_tr_b16 a[226:227], v26 offset:35072\n"
        "v_mfma_f32_16x16x32_bf16 v[116:119], a[232:235], a[168:171], v[116:119]\n"
        "s_waitcnt lgkmcnt(2)\n"
        "ds_read_b64_tr_b16 a[228:229], v26 offset:49408\n"
        "v_mfma_f32_16x16x32_bf16 v[116:119], a[236:239], a[172:175], v[116:119]\n"
        "ds_read_b64_tr_b16 a[230:231], v26 offset:51456\n"
        "v_mfma_f32_16x16x32_bf16 v[148:151], a[232:235], a[176:179], v[148:151]\n"
        "buffer_load_dwordx4 v7, s[40:43], s89 offen offset:3072 lds\n"
        "v_mfma_f32_16x16x32_bf16 v[148:151], a[236:239], a[180:183], v[148:151]\n"
        "s_add_u32 m0, s90, 106496\n"
        "v_mfma_f32_16x16x32_bf16 v[180:183], a[232:235], a[184:187], v[180:183]\n"
        "v_mfma_f32_16x16x32_bf16 v[180:183], a[236:239], a[188:191], v[180:183]\n"
        "v_mfma_f32_16x16x32_bf16 v[212:215], a[232:235], a[192:195], v[212:215]\n"
        "ds_read_b64_tr_b16 a[232:233], v26 offset:33032\n"
        "v_mfma_f32_16x16x32_bf16 v[212:215], a[236:239], a[196:199], v[212:215]\n"
        "ds_read_b64_tr_b16 a[234:235], v26 offset:35080\n"
        "v_mfma_f32_16x16x32_bf16 a[0:3], a[208:211], a[168:171], a[0:3]\n"
        "ds_read_b64_tr_b16 a[236:237], v26 offset:49416\n"
        "v_mfma_f32_16x16x32_bf16 a[0:3], a[212:215], a[172:175], a[0:3]\n"
        "ds_read_b64_tr_b16 a[238:239], v26 offset:51464\n"
        "v_mfma_f32_16x16x32_bf16 a[32:35], a[208:211], a[176:179], a[32:35]\n"
        "buffer_load_dwordx4 v8, s[40:43], s89 offen offset:0 lds\n"
        "v_mfma_f32_16x16x32_bf16 a[32:35], a[212:215], a[180:183], a[32:35]\n"
        "v_mfma_f32_16x16x32_bf16 a[64:67], a[208:211], a[184:187], a[64:67]\n"
        "v_mfma_f32_16x16x32_bf16 a[64:67], a[212:215], a[188:191], a[64:67]\n"
        "v_mfma_f32_16x16x32_bf16 a[96:99], a[208:211], a[192:195], a[96:99]\n"
        "buffer_load_dwordx4 v9, s[40:43], s89 offen offset:1024 lds\n"
        "v_mfma_f32_16x16x32_bf16 a[96:99], a[212:215], a[196:199], a[96:99]\n"
        "v_mfma_f32_16x16x32_bf16 v[68:71], a[208:211], a[200:203], v[68:71]\n"
        "ds_read_b64_tr_b16 a[208:209], v27 offset:33024\n"
        "v_mfma_f32_16x16x32_bf16 v[68:71], a[212:215], a[204:207], v[68:71]\n"
        "ds_read_b64_tr_b16 a[210:211], v27 offset:35072\n"
        "v_mfma_f32_16x16x32_bf16 a[4:7], a[216:219], a[168:171], a[4:7]\n"
        "ds_read_b64_tr_b16 a[212:213], v27 offset:49408\n"
        "v_mfma_f32_16x16x32_bf16 a[4:7], a[220:223], a[172:175], a[4:7]\n"
        "ds_read_b64_tr_b16 a[214:215], v27 offset:51456\n"
        "v_mfma_f32_16x16x32_bf16 a[36:39], a[216:219], a[176:179], a[36:39]\n"
        "buffer_load_dwordx4 v10, s[40:43], s89 offen offset:2048 lds\n"
        "v_mfma_f32_16x16x32_bf16 a[36:39], a[220:223], a[180:183], a[36:39]\n"
        "v_mfma_f32_16x16x32_bf16 a[68:71], a[216:219], a[184:187], a[68:71]\n"
        "v_mfma_f32_16x16x32_bf16 a[68:71], a[220:223], a[188:191], a[68:71]\n"
        "v_mfma_f32_16x16x32_bf16 a[100:103], a[216:219], a[192:195], a[100:103]\n"
        "buffer_load_dwordx4 v11, s[40:43], s89 offen offset:3072 lds\n"
        "v_mfma_f32_16x16x32_bf16 a[100:103], a[220:223], a[196:199], a[100:103]\n"
        "s_add_u32 m0, s90, 110592\n"
        "v_mfma_f32_16x16x32_bf16 v[72:75], a[216:219], a[200:203], v[72:75]\n"
        "ds_read_b64_tr_b16 a[216:217], v27 offset:33032\n"
        "v_mfma_f32_16x16x32_bf16 v[72:75], a[220:223], a[204:207], v[72:75]\n"
        "s_waitcnt lgkmcnt(1)\n"
        "ds_read_b64_tr_b16 a[218:219], v27 offset:35080\n"
        "v_mfma_f32_16x16x32_bf16 a[8:11], a[224:227], a[168:171], a[8:11]\n"
        "ds_read_b64_tr_b16 a[220:221], v27 offset:49416\n"
        "v_mfma_f32_16x16x32_bf16 a[8:11], a[228:231], a[172:175], a[8:11]\n"
        "ds_read_b64_tr_b16 a[222:223], v27 offset:51464\n"
        "v_mfma_f32_16x16x32_bf16 a[40:43], a[224:227], a[176:179], a[40:43]\n"
        "buffer_load_dwordx4 v12, s[40:43], s89 offen offset:0 lds\n"
        "v_mfma_f32_16x16x32_bf16 a[40:43], a[228:231], a[180:183], a[40:43]\n"
        "s_add_u32 s93, s93, 524288\n"
        "v_mfma_f32_16x16x32_bf16 a[72:75], a[224:227], a[184:187], a[72:75]\n"
        "v_mfma_f32_16x16x32_bf16 a[72:75], a[228:231], a[188:191], a[72:75]\n"
        "v_mfma_f32_16x16x32_bf16 a[104:107], a[224:227], a[192:195], a[104:107]\n"
        "buffer_load_dword a246, v33, s[44:47], s93 offen\n"
        "v_mfma_f32_16x16x32_bf16 a[104:107], a[228:231], a[196:199], a[104:107]\n"
        "v_mfma_f32_16x16x32_bf16 v[76:79], a[224:227], a[200:203], v[76:79]\n"
        "ds_read_b64_tr_b16 a[224:225], v28 offset:33024\n"
        "v_mfma_f32_16x16x32_bf16 v[76:79], a[228:231], a[204:207], v[76:79]\n"
        "ds_read_b64_tr_b16 a[226:227], v28 offset:35072\n"
        "v_mfma_f32_16x16x32_bf16 a[12:15], a[232:235], a[168:171], a[12:15]\n"
        "ds_read_b64_tr_b16 a[228:229], v28 offset:49408\n"
        "v_mfma_f32_16x16x32_bf16 a[12:15], a[236:239], a[172:175], a[12:15]\n"
        "ds_read_b64_tr_b16 a[230:231], v28 offset:51456\n"
        "v_mfma_f32_16x16x32_bf16 a[44:47], a[232:235], a[176:179], a[44:47]\n"
        "buffer_load_dword a248, v36, s[40:43], 0 offen offset:1152\n"
        "v_mfma_f32_16x16x32_bf16 a[44:47], a[236:239], a[180:183], a[44:47]\n"
        "v_mfma_f32_16x16x32_bf16 a[76:79], a[232:235], a[184:187], a[76:79]\n"
        "v_mfma_f32_16x16x32_bf16 a[76:79], a[236:239], a[188:191], a[76:79]\n"
        "v_mfma_f32_16x16x32_bf16 a[108:111], a[232:235], a[192:195], a[108:111]\n"
        "v_mfma_f32_16x16x32_bf16 a[108:111], a[236:239], a[196:199], a[108:111]\n"
        "v_mfma_f32_16x16x32_bf16 v[80:83], a[232:235], a[200:203], v[80:83]\n"
        "ds_read_b64_tr_b16 a[232:233], v28 offset:33032\n"
        "v_mfma_f32_16x16x32_bf16 v[80:83], a[236:239], a[204:207], v[80:83]\n"
        "ds_read_b64_tr_b16 a[234:235], v28 offset:35080\n"
        "v_mfma_f32_16x16x32_bf16 a[16:19], a[208:211], a[168:171], a[16:19]\n"
        "ds_read_b64_tr_b16 a[236:237], v28 offset:49416\n"
        "v_mfma_f32_16x16x32_bf16 a[16:19], a[212:215], a[172:175], a[16:19]\n"
        "ds_read_b64_tr_b16 a[238:239], v28 offset:51464\n"
        "v_mfma_f32_16x16x32_bf16 a[48:51], a[208:211], a[176:179], a[48:51]\n"
        "v_mfma_f32_16x16x32_bf16 a[48:51], a[212:215], a[180:183], a[48:51]\n"
        "v_mfma_f32_16x16x32_bf16 a[80:83], a[208:211], a[184:187], a[80:83]\n"
        "s_waitcnt vmcnt(11)\n"
        "s_barrier\n"
        "v_mfma_f32_16x16x32_bf16 a[80:83], a[212:215], a[188:191], a[80:83]\n"
        "ds_read_b128 a[128:131], v40 offset:0\n"
        "v_mfma_f32_16x16x32_bf16 a[112:115], a[208:211], a[192:195], a[112:115]\n"
        "ds_read_b128 a[132:135], v41 offset:0\n"
        "v_mfma_f32_16x16x32_bf16 a[112:115], a[212:215], a[196:199], a[112:115]\n"
        "s_waitcnt lgkmcnt(3)\n"
        "ds_read_b128 a[136:139], v42 offset:0\n"
        "v_mfma_f32_16x16x32_bf16 a[20:23], a[216:219], a[168:171], a[20:23]\n"
        "ds_read_b128 a[140:143], v43 offset:0\n"
        "v_mfma_f32_16x16x32_bf16 a[20:23], a[220:223], a[172:175], a[20:23]\n"
        "ds_read_b128 a[144:147], v44 offset:0\n"
        "v_mfma_f32_16x16x32_bf16 a[52:55], a[216:219], a[176:179], a[52:55]\n"
        "ds_read_b128 a[148:151], v45 offset:0\n"
        "v_mfma_f32_16x16x32_bf16 a[52:55], a[220:223], a[180:183], a[52:55]\n"
        "ds_read_b128 a[152:155], v46 offset:0\n"
        "v_mfma_f32_16x16x32_bf16 a[84:87], a[216:219], a[184:187], a[84:87]\n"
        "ds_read_b128 a[156:159], v47 offset:0\n"
        "v_mfma_f32_16x16x32_bf16 a[84:87], a[220:223], a[188:191], a[84:87]\n"
        "ds_read_b128 a[160:163], v15 offset:0\n"
        "v_mfma_f32_16x16x32_bf16 a[116:119], a[216:219], a[192:195], a[116:119]\n"
        "ds_read_b128 a[164:167], v16 offset:0\n"
        "v_mfma_f32_16x16x32_bf16 a[116:119], a[220:223], a[196:199], a[116:119]\n"
        "ds_read_b64_tr_b16 a[208:209], v25 offset:0\n"
        "v_mfma_f32_16x16x32_bf16 a[24:27], a[224:227], a[168:171], a[24:27]\n"
        "ds_read_b64_tr_b16 a[210:211], v25 offset:2048\n"
        "v_mfma_f32_16x16x32_bf16 a[24:27], a[228:231], a[172:175], a[24:27]\n"
        "ds_read_b64_tr_b16 a[212:213], v25 offset:16384\n"
        "v_mfma_f32_16x16x32_bf16 a[56:59], a[224:227], a[176:179], a[56:59]\n"
        "ds_read_b64_tr_b16 a[214:215], v25 offset:18432\n"
        "v_mfma_f32_16x16x32_bf16 a[56:59], a[228:231], a[180:183], a[56:59]\n"
        "ds_read_b64_tr_b16 a[216:217], v25 offset:8\n"
        "v_mfma_f32_16x16x32_bf16 a[88:91], a[224:227], a[184:187], a[88:91]\n"
        "ds_read_b64_tr_b16 a[218:219], v25 offset:2056\n"
        "v_mfma_f32_16x16x32_bf16 a[88:91], a[228:231], a[188:191], a[88:91]\n"
        "ds_read_b64_tr_b16 a[220:221], v25 offset:16392\n"
        "v_mfma_f32_16x16x32_bf16 a[120:123], a[224:227], a[192:195], a[120:123]\n"
        "ds_read_b64_tr_b16 a[222:223], v25 offset:18440\n"
        "v_mfma_f32_16x16x32_bf16 a[120:123], a[228:231], a[196:199], a[120:123]\n"
        "ds_read_b64_tr_b16 a[224:225], v26 offset:0\n"
        "v_mfma_f32_16x16x32_bf16 a[28:31], a[232:235], a[168:171], a[28:31]\n"
        "s_waitcnt lgkmcnt(6)\n"
        "ds_read_b64_tr_b16 a[226:227], v26 offset:2048\n"
        "v_mfma_f32_16x16x32_bf16 a[28:31], a[236:239], a[172:175], a[28:31]\n"
        "ds_read_b64_tr_b16 a[228:229], v26 offset:16384\n"
        "v_mfma_f32_16x16x32_bf16 a[60:63], a[232:235], a[176:179], a[60:63]\n"
        "ds_read_b64_tr_b16 a[230:231], v26 offset:18432\n"
        "v_mfma_f32_16x16x32_bf16 a[60:63], a[236:239], a[180:183], a[60:63]\n"
        "v_mfma_f32_16x16x32_bf16 a[92:95], a[232:235], a[184:187], a[92:95]\n"
        "s_barrier\n"
        "v_mfma_f32_16x16x32_bf16 a[92:95], a[236:239], a[188:191], a[92:95]\n"
        "s_mov_b32 s79, 3670016\n"
        "s_add_u32 m0, s66, 32768\n"
        "v_mfma_f32_16x16x32_bf16 a[124:127], a[232:235], a[192:195], a[124:127]\n"
        "ds_read_b64_tr_b16 a[232:233], v26 offset:8\n"
        "v_mfma_f32_16x16x32_bf16 a[124:127], a[236:239], a[196:199], a[124:127]\n"
        "ds_read_b64_tr_b16 a[234:235], v26 offset:2056\n"
        "v_mfma_f32_16x16x32_bf16 v[88:91], a[208:211], a[128:131], v[88:91]\n"
        "s_waitcnt lgkmcnt(3)\n"
        "ds_read_b64_tr_b16 a[236:237], v26 offset:16392\n"
        "v_mfma_f32_16x16x32_bf16 v[88:91], a[212:215], a[132:135], v[88:91]\n"
        "ds_read_b64_tr_b16 a[238:239], v26 offset:18440\n"
        "v_mfma_f32_16x16x32_bf16 v[120:123], a[208:211], a[136:139], v[120:123]\n"
        "buffer_load_dwordx4 v17, s[44:47], s79 offen offset:0 lds\n"
        "v_mfma_f32_16x16x32_bf16 v[120:123], a[212:215], a[140:143], v[120:123]\n"
        "v_mfma_f32_16x16x32_bf16 v[152:155], a[208:211], a[144:147], v[152:155]\n"
        "v_mfma_f32_16x16x32_bf16 v[152:155], a[212:215], a[148:151], v[152:155]\n"
        "v_mfma_f32_16x16x32_bf16 v[184:187], a[208:211], a[152:155], v[184:187]\n"
        "buffer_load_dwordx4 v18, s[44:47], s79 offen offset:1024 lds\n"
        "v_mfma_f32_16x16x32_bf16 v[184:187], a[212:215], a[156:159], v[184:187]\n"
        "v_mfma_f32_16x16x32_bf16 v[216:219], a[208:211], a[160:163], v[216:219]\n"
        "ds_read_b64_tr_b16 a[208:209], v27 offset:0\n"
        "v_mfma_f32_16x16x32_bf16 v[216:219], a[212:215], a[164:167], v[216:219]\n"
        "ds_read_b64_tr_b16 a[210:211], v27 offset:2048\n"
        "v_mfma_f32_16x16x32_bf16 v[92:95], a[216:219], a[128:131], v[92:95]\n"
        "ds_read_b64_tr_b16 a[212:213], v27 offset:16384\n"
        "v_mfma_f32_16x16x32_bf16 v[92:95], a[220:223], a[132:135], v[92:95]\n"
        "ds_read_b64_tr_b16 a[214:215], v27 offset:18432\n"
        "v_mfma_f32_16x16x32_bf16 v[124:127], a[216:219], a[136:139], v[124:127]\n"
        "buffer_load_dwordx4 v19, s[44:47], s79 offen offset:2048 lds\n"
        "v_mfma_f32_16x16x32_bf16 v[124:127], a[220:223], a[140:143], v[124:127]\n"
        "v_mfma_f32_16x16x32_bf16 v[156:159], a[216:219], a[144:147], v[156:159]\n"
        "v_mfma_f32_16x16x32_bf16 v[156:159], a[220:223], a[148:151], v[156:159]\n"
        "v_mfma_f32_16x16x32_bf16 v[188:191], a[216:219], a[152:155], v[188:191]\n"
        "buffer_load_dwordx4 v20, s[44:47], s79 offen offset:3072 lds\n"
        "v_mfma_f32_16x16x32_bf16 v[188:191], a[220:223], a[156:159], v[188:191]\n"
        "s_add_u32 m0, s66, 36864\n"
        "v_mfma_f32_16x16x32_bf16 v[220:223], a[216:219], a[160:163], v[220:223]\n"
        "ds_read_b64_tr_b16 a[216:217], v27 offset:8\n"
        "v_mfma_f32_16x16x32_bf16 v[220:223], a[220:223], a[164:167], v[220:223]\n"
        "ds_read_b64_tr_b16 a[218:219], v27 offset:2056\n"
        "v_mfma_f32_16x16x32_bf16 v[96:99], a[224:227], a[128:131], v[96:99]\n"
        "s_waitcnt lgkmcnt(2)\n"
        "ds_read_b64_tr_b16 a[220:221], v27 offset:16392\n"
        "v_mfma_f32_16x16x32_bf16 v[96:99], a[228:231], a[132:135], v[96:99]\n"
        "ds_read_b64_tr_b16 a[222:223], v27 offset:18440\n"
        "v_mfma_f32_16x16x32_bf16 v[128:131], a[224:227], a[136:139], v[128:131]\n"
        "buffer_load_dwordx4 v21, s[44:47], s79 offen offset:0 lds\n"
        "v_mfma_f32_16x16x32_bf16 v[128:131], a[228:231], a[140:143], v[128:131]\n"
        "v_mfma_f32_16x16x32_bf16 v[160:163], a[224:227], a[144:147], v[160:163]\n"
        "v_mfma_f32_16x16x32_bf16 v[160:163], a[228:231], a[148:151], v[160:163]\n"
        "v_mfma_f32_16x16x32_bf16 v[192:195], a[224:227], a[152:155], v[192:195]\n"
        "buffer_load_dwordx4 v22, s[44:47], s79 offen offset:1024 lds\n"
        "v_mfma_f32_16x16x32_bf16 v[192:195], a[228:231], a[156:159], v[192:195]\n"
        "v_mfma_f32_16x16x32_bf16 v[224:227], a[224:227], a[160:163], v[224:227]\n"
        "ds_read_b64_tr_b16 a[224:225], v28 offset:0\n"
        "v_mfma_f32_16x16x32_bf16 v[224:227], a[228:231], a[164:167], v[224:227]\n"
        "ds_read_b64_tr_b16 a[226:227], v28 offset:2048\n"
        "v_mfma_f32_16x16x32_bf16 v[100:103], a[232:235], a[128:131], v[100:103]\n"
        "ds_read_b64_tr_b16 a[228:229], v28 offset:16384\n"
        "v_mfma_f32_16x16x32_bf16 v[100:103], a[236:239], a[132:135], v[100:103]\n"
        "ds_read_b64_tr_b16 a[230:231], v28 offset:18432\n"
        "v_mfma_f32_16x16x32_bf16 v[132:135], a[232:235], a[136:139], v[132:135]\n"
        "buffer_load_dwordx4 v23, s[44:47], s79 offen offset:2048 lds\n"
        "v_mfma_f32_16x16x32_bf16 v[132:135], a[236:239], a[140:143], v[132:135]\n"
        "v_mfma_f32_16x16x32_bf16 v[164:167], a[232:235], a[144:147], v[164:167]\n"
        "v_mfma_f32_16x16x32_bf16 v[164:167], a[236:239], a[148:151], v[164:167]\n"
        "v_mfma_f32_16x16x32_bf16 v[196:199], a[232:235], a[152:155], v[196:199]\n"
        "buffer_load_dwordx4 v24, s[44:47], s79 offen offset:3072 lds\n"
        "v_mfma_f32_16x16x32_bf16 v[196:199], a[236:239], a[156:159], v[196:199]\n"
        "s_mov_b32 s89, 1024\n"
        "s_add_u32 m0, s90, 65536\n"
        "v_mfma_f32_16x16x32_bf16 v[228:231], a[232:235], a[160:163], v[228:231]\n"
        "ds_read_b64_tr_b16 a[232:233], v28 offset:8\n"
        "v_mfma_f32_16x16x32_bf16 v[228:231], a[236:239], a[164:167], v[228:231]\n"
        "ds_read_b64_tr_b16 a[234:235], v28 offset:2056\n"
        "v_mfma_f32_16x16x32_bf16 v[104:107], a[208:211], a[128:131], v[104:107]\n"
        "ds_read_b64_tr_b16 a[236:237], v28 offset:16392\n"
        "v_mfma_f32_16x16x32_bf16 v[104:107], a[212:215], a[132:135], v[104:107]\n"
        "ds_read_b64_tr_b16 a[238:239], v28 offset:18440\n"
        "v_mfma_f32_16x16x32_bf16 v[136:139], a[208:211], a[136:139], v[136:139]\n"
        "buffer_load_dwordx4 v4, s[40:43], s89 offen offset:0 lds\n"
        "v_mfma_f32_16x16x32_bf16 v[136:139], a[212:215], a[140:143], v[136:139]\n"
        "v_mfma_f32_16x16x32_bf16 v[168:171], a[208:211], a[144:147], v[168:171]\n"
        "v_mfma_f32_16x16x32_bf16 v[168:171], a[212:215], a[148:151], v[168:171]\n"
        "v_mfma_f32_16x16x32_bf16 v[200:203], a[208:211], a[152:155], v[200:203]\n"
        "ds_read_b64_tr_b16 a[208:209], v25 offset:256\n"
        "v_mfma_f32_16x16x32_bf16 v[200:203], a[212:215], a[156:159], v[200:203]\n"
        "s_waitcnt lgkmcnt(2)\n"
        "ds_read_b64_tr_b16 a[210:211], v25 offset:2304\n"
        "v_mfma_f32_16x16x32_bf16 v[108:111], a[216:219], a[128:131], v[108:111]\n"
        "ds_read_b64_tr_b16 a[212:213], v25 offset:16640\n"
        "v_mfma_f32_16x16x32_bf16 v[108:111], a[220:223], a[132:135], v[108:111]\n"
        "ds_read_b64_tr_b16 a[214:215], v25 offset:18688\n"
        "v_mfma_f32_16x16x32_bf16 v[140:143], a[216:219], a[136:139], v[140:143]\n"
        "buffer_load_dwordx4 v5, s[40:43], s89 offen offset:1024 lds\n"
        "v_mfma_f32_16x16x32_bf16 v[140:143], a[220:223], a[140:143], v[140:143]\n"
        "v_mfma_f32_16x16x32_bf16 v[172:175], a[216:219], a[144:147], v[172:175]\n"
        "v_mfma_f32_16x16x32_bf16 v[172:175], a[220:223], a[148:151], v[172:175]\n"
        "v_mfma_f32_16x16x32_bf16 v[204:207], a[216:219], a[152:155], v[204:207]\n"
        "ds_read_b64_tr_b16 a[216:217], v25 offset:264\n"
        "v_mfma_f32_16x16x32_bf16 v[204:207], a[220:223], a[156:159], v[204:207]\n"
        "ds_read_b64_tr_b16 a[218:219], v25 offset:2312\n"
        "v_mfma_f32_16x16x32_bf16 v[112:115], a[224:227], a[128:131], v[112:115]\n"
        "ds_read_b64_tr_b16 a[220:221], v25 offset:16648\n"
        "v_mfma_f32_16x16x32_bf16 v[112:115], a[228:231], a[132:135], v[112:115]\n"
        "ds_read_b64_tr_b16 a[222:223], v25 offset:18696\n"
        "v_mfma_f32_16x16x32_bf16 v[144:147], a[224:227], a[136:139], v[144:147]\n"
        "buffer_load_dwordx4 v6, s[40:43], s89 offen offset:2048 lds\n"
        "v_mfma_f32_16x16x32_bf16 v[144:147], a[228:231], a[140:143], v[144:147]\n"
        "v_mfma_f32_16x16x32_bf16 v[176:179], a[224:227], a[144:147], v[176:179]\n"
        "v_mfma_f32_16x16x32_bf16 v[176:179], a[228:231], a[148:151], v[176:179]\n"
        "v_mfma_f32_16x16x32_bf16 v[208:211], a[224:227], a[152:155], v[208:211]\n"
        "ds_read_b64_tr_b16 a[224:225], v26 offset:256\n"
        "v_mfma_f32_16x16x32_bf16 v[208:211], a[228:231], a[156:159], v[208:211]\n"
        "ds_read_b64_tr_b16 a[226:227], v26 offset:2304\n"
        "v_mfma_f32_16x16x32_bf16 v[116:119], a[232:235], a[128:131], v[116:119]\n"
        "s_waitcnt lgkmcnt(2)\n"
        "ds_read_b64_tr_b16 a[228:229], v26 offset:16640\n"
        "v_mfma_f32_16x16x32_bf16 v[116:119], a[236:239], a[132:135], v[116:119]\n"
        "ds_read_b64_tr_b16 a[230:231], v26 offset:18688\n"
        "v_mfma_f32_16x16x32_bf16 v[148:151], a[232:235], a[136:139], v[148:151]\n"
        "buffer_load_dwordx4 v7, s[40:43], s89 offen offset:3072 lds\n"
        "v_mfma_f32_16x16x32_bf16 v[148:151], a[236:239], a[140:143], v[148:151]\n"
        "s_add_u32 m0, s90, 69632\n"
        "v_mfma_f32_16x16x32_bf16 v[180:183], a[232:235], a[144:147], v[180:183]\n"
        "v_mfma_f32_16x16x32_bf16 v[180:183], a[236:239], a[148:151], v[180:183]\n"
        "v_mfma_f32_16x16x32_bf16 v[212:215], a[232:235], a[152:155], v[212:215]\n"
        "ds_read_b64_tr_b16 a[232:233], v26 offset:264\n"
        "v_mfma_f32_16x16x32_bf16 v[212:215], a[236:239], a[156:159], v[212:215]\n"
        "ds_read_b64_tr_b16 a[234:235], v26 offset:2312\n"
        "v_mfma_f32_16x16x32_bf16 a[0:3], a[208:211], a[128:131], a[0:3]\n"
        "ds_read_b64_tr_b16 a[236:237], v26 offset:16648\n"
        "v_mfma_f32_16x16x32_bf16 a[0:3], a[212:215], a[132:135], a[0:3]\n"
        "ds_read_b64_tr_b16 a[238:239], v26 offset:18696\n"
        "v_mfma_f32_16x16x32_bf16 a[32:35], a[208:211], a[136:139], a[32:35]\n"
        "buffer_load_dwordx4 v8, s[40:43], s89 offen offset:0 lds\n"
        "v_mfma_f32_16x16x32_bf16 a[32:35], a[212:215], a[140:143], a[32:35]\n"
        "v_mfma_f32_16x16x32_bf16 a[64:67], a[208:211], a[144:147], a[64:67]\n"
        "v_mfma_f32_16x16x32_bf16 a[64:67], a[212:215], a[148:151], a[64:67]\n"
        "v_mfma_f32_16x16x32_bf16 a[96:99], a[208:211], a[152:155], a[96:99]\n"
        "buffer_load_dwordx4 v9, s[40:43], s89 offen offset:1024 lds\n"
        "v_mfma_f32_16x16x32_bf16 a[96:99], a[212:215], a[156:159], a[96:99]\n"
        "v_mfma_f32_16x16x32_bf16 v[68:71], a[208:211], a[160:163], v[68:71]\n"
        "ds_read_b64_tr_b16 a[208:209], v27 offset:256\n"
        "v_mfma_f32_16x16x32_bf16 v[68:71], a[212:215], a[164:167], v[68:71]\n"
        "ds_read_b64_tr_b16 a[210:211], v27 offset:2304\n"
        "v_mfma_f32_16x16x32_bf16 a[4:7], a[216:219], a[128:131], a[4:7]\n"
        "ds_read_b64_tr_b16 a[212:213], v27 offset:16640\n"
        "v_mfma_f32_16x16x32_bf16 a[4:7], a[220:223], a[132:135], a[4:7]\n"
        "ds_read_b64_tr_b16 a[214:215], v27 offset:18688\n"
        "v_mfma_f32_16x16x32_bf16 a[36:39], a[216:219], a[136:139], a[36:39]\n"
        "buffer_load_dwordx4 v10, s[40:43], s89 offen offset:2048 lds\n"
        "v_mfma_f32_16x16x32_bf16 a[36:39], a[220:223], a[140:143], a[36:39]\n"
        "v_mfma_f32_16x16x32_bf16 a[68:71], a[216:219], a[144:147], a[68:71]\n"
        "v_mfma_f32_16x16x32_bf16 a[68:71], a[220:223], a[148:151], a[68:71]\n"
        "v_mfma_f32_16x16x32_bf16 a[100:103], a[216:219], a[152:155], a[100:103]\n"
        "buffer_load_dwordx4 v11, s[40:43], s89 offen offset:3072 lds\n"
        "v_mfma_f32_16x16x32_bf16 a[100:103], a[220:223], a[156:159], a[100:103]\n"
        "s_add_u32 m0, s90, 73728\n"
        "v_mfma_f32_16x16x32_bf16 v[72:75], a[216:219], a[160:163], v[72:75]\n"
        "ds_read_b64_tr_b16 a[216:217], v27 offset:264\n"
        "v_mfma_f32_16x16x32_bf16 v[72:75], a[220:223], a[164:167], v[72:75]\n"
        "s_waitcnt lgkmcnt(1)\n"
        "ds_read_b64_tr_b16 a[218:219], v27 offset:2312\n"
        "v_mfma_f32_16x16x32_bf16 a[8:11], a[224:227], a[128:131], a[8:11]\n"
        "ds_read_b64_tr_b16 a[220:221], v27 offset:16648\n"
        "v_mfma_f32_16x16x32_bf16 a[8:11], a[228:231], a[132:135], a[8:11]\n"
        "ds_read_b64_tr_b16 a[222:223], v27 offset:18696\n"
        "v_mfma_f32_16x16x32_bf16 a[40:43], a[224:227], a[136:139], a[40:43]\n"
        "buffer_load_dwordx4 v12, s[40:43], s89 offen offset:0 lds\n"
        "v_mfma_f32_16x16x32_bf16 a[40:43], a[228:231], a[140:143], a[40:43]\n"
        "s_add_u32 s93, s93, 524288\n"
        "v_mfma_f32_16x16x32_bf16 a[72:75], a[224:227], a[144:147], a[72:75]\n"
        "v_mfma_f32_16x16x32_bf16 a[72:75], a[228:231], a[148:151], a[72:75]\n"
        "v_mfma_f32_16x16x32_bf16 a[104:107], a[224:227], a[152:155], a[104:107]\n"
        "buffer_load_dword a247, v33, s[44:47], s93 offen\n"
        "v_mfma_f32_16x16x32_bf16 a[104:107], a[228:231], a[156:159], a[104:107]\n"
        "v_mfma_f32_16x16x32_bf16 v[76:79], a[224:227], a[160:163], v[76:79]\n"
        "ds_read_b64_tr_b16 a[224:225], v28 offset:256\n"
        "v_mfma_f32_16x16x32_bf16 v[76:79], a[228:231], a[164:167], v[76:79]\n"
        "ds_read_b64_tr_b16 a[226:227], v28 offset:2304\n"
        "v_mfma_f32_16x16x32_bf16 a[12:15], a[232:235], a[128:131], a[12:15]\n"
        "ds_read_b64_tr_b16 a[228:229], v28 offset:16640\n"
        "v_mfma_f32_16x16x32_bf16 a[12:15], a[236:239], a[132:135], a[12:15]\n"
        "ds_read_b64_tr_b16 a[230:231], v28 offset:18688\n"
        "v_mfma_f32_16x16x32_bf16 a[44:47], a[232:235], a[136:139], a[44:47]\n"
        "v_mfma_f32_16x16x32_bf16 a[44:47], a[236:239], a[140:143], a[44:47]\n"
        "v_mfma_f32_16x16x32_bf16 a[76:79], a[232:235], a[144:147], a[76:79]\n"
        "v_mfma_f32_16x16x32_bf16 a[76:79], a[236:239], a[148:151], a[76:79]\n"
        "v_mfma_f32_16x16x32_bf16 a[108:111], a[232:235], a[152:155], a[108:111]\n"
        "v_mfma_f32_16x16x32_bf16 a[108:111], a[236:239], a[156:159], a[108:111]\n"
        "v_mfma_f32_16x16x32_bf16 v[80:83], a[232:235], a[160:163], v[80:83]\n"
        "ds_read_b64_tr_b16 a[232:233], v28 offset:264\n"
        "v_mfma_f32_16x16x32_bf16 v[80:83], a[236:239], a[164:167], v[80:83]\n"
        "ds_read_b64_tr_b16 a[234:235], v28 offset:2312\n"
        "v_mfma_f32_16x16x32_bf16 a[16:19], a[208:211], a[128:131], a[16:19]\n"
        "ds_read_b64_tr_b16 a[236:237], v28 offset:16648\n"
        "v_mfma_f32_16x16x32_bf16 a[16:19], a[212:215], a[132:135], a[16:19]\n"
        "ds_read_b64_tr_b16 a[238:239], v28 offset:18696\n"
        "v_mfma_f32_16x16x32_bf16 a[48:51], a[208:211], a[136:139], a[48:51]\n"
        "v_mfma_f32_16x16x32_bf16 a[48:51], a[212:215], a[140:143], a[48:51]\n"
        "v_mfma_f32_16x16x32_bf16 a[80:83], a[208:211], a[144:147], a[80:83]\n"
        "s_waitcnt vmcnt(10)\n"
        "s_barrier\n"
        "v_mfma_f32_16x16x32_bf16 a[80:83], a[212:215], a[148:151], a[80:83]\n"
        "ds_read_b128 a[168:171], v40 offset:36864\n"
        "v_mfma_f32_16x16x32_bf16 a[112:115], a[208:211], a[152:155], a[112:115]\n"
        "ds_read_b128 a[172:175], v41 offset:36864\n"
        "v_mfma_f32_16x16x32_bf16 a[112:115], a[212:215], a[156:159], a[112:115]\n"
        "s_waitcnt lgkmcnt(3)\n"
        "ds_read_b128 a[176:179], v42 offset:36864\n"
        "v_mfma_f32_16x16x32_bf16 a[20:23], a[216:219], a[128:131], a[20:23]\n"
        "ds_read_b128 a[180:183], v43 offset:36864\n"
        "v_mfma_f32_16x16x32_bf16 a[20:23], a[220:223], a[132:135], a[20:23]\n"
        "ds_read_b128 a[184:187], v44 offset:36864\n"
        "v_mfma_f32_16x16x32_bf16 a[52:55], a[216:219], a[136:139], a[52:55]\n"
        "ds_read_b128 a[188:191], v45 offset:36864\n"
        "v_mfma_f32_16x16x32_bf16 a[52:55], a[220:223], a[140:143], a[52:55]\n"
        "ds_read_b128 a[192:195], v46 offset:36864\n"
        "v_mfma_f32_16x16x32_bf16 a[84:87], a[216:219], a[144:147], a[84:87]\n"
        "ds_read_b128 a[196:199], v47 offset:36864\n"
        "v_mfma_f32_16x16x32_bf16 a[84:87], a[220:223], a[148:151], a[84:87]\n"
        "ds_read_b128 a[200:203], v15 offset:36864\n"
        "v_mfma_f32_16x16x32_bf16 a[116:119], a[216:219], a[152:155], a[116:119]\n"
        "ds_read_b128 a[204:207], v16 offset:36864\n"
        "v_mfma_f32_16x16x32_bf16 a[116:119], a[220:223], a[156:159], a[116:119]\n"
        "ds_read_b64_tr_b16 a[208:209], v25 offset:32768\n"
        "v_mfma_f32_16x16x32_bf16 a[24:27], a[224:227], a[128:131], a[24:27]\n"
        "ds_read_b64_tr_b16 a[210:211], v25 offset:34816\n"
        "v_mfma_f32_16x16x32_bf16 a[24:27], a[228:231], a[132:135], a[24:27]\n"
        "ds_read_b64_tr_b16 a[212:213], v25 offset:49152\n"
        "v_mfma_f32_16x16x32_bf16 a[56:59], a[224:227], a[136:139], a[56:59]\n"
        "ds_read_b64_tr_b16 a[214:215], v25 offset:51200\n"
        "v_mfma_f32_16x16x32_bf16 a[56:59], a[228:231], a[140:143], a[56:59]\n"
        "ds_read_b64_tr_b16 a[216:217], v25 offset:32776\n"
        "v_mfma_f32_16x16x32_bf16 a[88:91], a[224:227], a[144:147], a[88:91]\n"
        "ds_read_b64_tr_b16 a[218:219], v25 offset:34824\n"
        "v_mfma_f32_16x16x32_bf16 a[88:91], a[228:231], a[148:151], a[88:91]\n"
        "ds_read_b64_tr_b16 a[220:221], v25 offset:49160\n"
        "v_mfma_f32_16x16x32_bf16 a[120:123], a[224:227], a[152:155], a[120:123]\n"
        "ds_read_b64_tr_b16 a[222:223], v25 offset:51208\n"
        "v_mfma_f32_16x16x32_bf16 a[120:123], a[228:231], a[156:159], a[120:123]\n"
        "ds_read_b64_tr_b16 a[224:225], v26 offset:32768\n"
        "v_mfma_f32_16x16x32_bf16 a[28:31], a[232:235], a[128:131], a[28:31]\n"
        "s_waitcnt lgkmcnt(6)\n"
        "ds_read_b64_tr_b16 a[226:227], v26 offset:34816\n"
        "v_mfma_f32_16x16x32_bf16 a[28:31], a[236:239], a[132:135], a[28:31]\n"
        "ds_read_b64_tr_b16 a[228:229], v26 offset:49152\n"
        "v_mfma_f32_16x16x32_bf16 a[60:63], a[232:235], a[136:139], a[60:63]\n"
        "ds_read_b64_tr_b16 a[230:231], v26 offset:51200\n"
        "v_mfma_f32_16x16x32_bf16 a[60:63], a[236:239], a[140:143], a[60:63]\n"
        "v_mfma_f32_16x16x32_bf16 a[92:95], a[232:235], a[144:147], a[92:95]\n"
        "s_barrier\n"
        "v_mfma_f32_16x16x32_bf16 a[92:95], a[236:239], a[148:151], a[92:95]\n"
        "s_mov_b32 s79, 4194304\n"
        "s_add_u32 m0, s66, 0\n"
        "v_mfma_f32_16x16x32_bf16 a[124:127], a[232:235], a[152:155], a[124:127]\n"
        "ds_read_b64_tr_b16 a[232:233], v26 offset:32776\n"
        "v_mfma_f32_16x16x32_bf16 a[124:127], a[236:239], a[156:159], a[124:127]\n"
        "ds_read_b64_tr_b16 a[234:235], v26 offset:34824\n"
        "v_mfma_f32_16x16x32_bf16 v[88:91], a[208:211], a[168:171], v[88:91]\n"
        "s_waitcnt lgkmcnt(3)\n"
        "ds_read_b64_tr_b16 a[236:237], v26 offset:49160\n"
        "v_mfma_f32_16x16x32_bf16 v[88:91], a[212:215], a[172:175], v[88:91]\n"
        "ds_read_b64_tr_b16 a[238:239], v26 offset:51208\n"
        "v_mfma_f32_16x16x32_bf16 v[120:123], a[208:211], a[176:179], v[120:123]\n"
        "buffer_load_dwordx4 v17, s[44:47], s79 offen offset:0 lds\n"
        "v_mfma_f32_16x16x32_bf16 v[120:123], a[212:215], a[180:183], v[120:123]\n"
        "v_mfma_f32_16x16x32_bf16 v[152:155], a[208:211], a[184:187], v[152:155]\n"
        "v_mfma_f32_16x16x32_bf16 v[152:155], a[212:215], a[188:191], v[152:155]\n"
        "v_mfma_f32_16x16x32_bf16 v[184:187], a[208:211], a[192:195], v[184:187]\n"
        "buffer_load_dwordx4 v18, s[44:47], s79 offen offset:1024 lds\n"
        "v_mfma_f32_16x16x32_bf16 v[184:187], a[212:215], a[196:199], v[184:187]\n"
        "v_mfma_f32_16x16x32_bf16 v[216:219], a[208:211], a[200:203], v[216:219]\n"
        "ds_read_b64_tr_b16 a[208:209], v27 offset:32768\n"
        "v_mfma_f32_16x16x32_bf16 v[216:219], a[212:215], a[204:207], v[216:219]\n"
        "ds_read_b64_tr_b16 a[210:211], v27 offset:34816\n"
        "v_mfma_f32_16x16x32_bf16 v[92:95], a[216:219], a[168:171], v[92:95]\n"
        "ds_read_b64_tr_b16 a[212:213], v27 offset:49152\n"
        "v_mfma_f32_16x16x32_bf16 v[92:95], a[220:223], a[172:175], v[92:95]\n"
        "ds_read_b64_tr_b16 a[214:215], v27 offset:51200\n"
        "v_mfma_f32_16x16x32_bf16 v[124:127], a[216:219], a[176:179], v[124:127]\n"
        "buffer_load_dwordx4 v19, s[44:47], s79 offen offset:2048 lds\n"
        "v_mfma_f32_16x16x32_bf16 v[124:127], a[220:223], a[180:183], v[124:127]\n"
        "v_mfma_f32_16x16x32_bf16 v[156:159], a[216:219], a[184:187], v[156:159]\n"
        "v_mfma_f32_16x16x32_bf16 v[156:159], a[220:223], a[188:191], v[156:159]\n"
        "v_mfma_f32_16x16x32_bf16 v[188:191], a[216:219], a[192:195], v[188:191]\n"
        "buffer_load_dwordx4 v20, s[44:47], s79 offen offset:3072 lds\n"
        "v_mfma_f32_16x16x32_bf16 v[188:191], a[220:223], a[196:199], v[188:191]\n"
        "s_add_u32 m0, s66, 4096\n"
        "v_mfma_f32_16x16x32_bf16 v[220:223], a[216:219], a[200:203], v[220:223]\n"
        "ds_read_b64_tr_b16 a[216:217], v27 offset:32776\n"
        "v_mfma_f32_16x16x32_bf16 v[220:223], a[220:223], a[204:207], v[220:223]\n"
        "ds_read_b64_tr_b16 a[218:219], v27 offset:34824\n"
        "v_mfma_f32_16x16x32_bf16 v[96:99], a[224:227], a[168:171], v[96:99]\n"
        "s_waitcnt lgkmcnt(2)\n"
        "ds_read_b64_tr_b16 a[220:221], v27 offset:49160\n"
        "v_mfma_f32_16x16x32_bf16 v[96:99], a[228:231], a[172:175], v[96:99]\n"
        "ds_read_b64_tr_b16 a[222:223], v27 offset:51208\n"
        "v_mfma_f32_16x16x32_bf16 v[128:131], a[224:227], a[176:179], v[128:131]\n"
        "buffer_load_dwordx4 v21, s[44:47], s79 offen offset:0 lds\n"
        "v_mfma_f32_16x16x32_bf16 v[128:131], a[228:231], a[180:183], v[128:131]\n"
        "v_mfma_f32_16x16x32_bf16 v[160:163], a[224:227], a[184:187], v[160:163]\n"
        "v_mfma_f32_16x16x32_bf16 v[160:163], a[228:231], a[188:191], v[160:163]\n"
        "v_mfma_f32_16x16x32_bf16 v[192:195], a[224:227], a[192:195], v[192:195]\n"
        "buffer_load_dwordx4 v22, s[44:47], s79 offen offset:1024 lds\n"
        "v_mfma_f32_16x16x32_bf16 v[192:195], a[228:231], a[196:199], v[192:195]\n"
        "v_mfma_f32_16x16x32_bf16 v[224:227], a[224:227], a[200:203], v[224:227]\n"
        "ds_read_b64_tr_b16 a[224:225], v28 offset:32768\n"
        "v_mfma_f32_16x16x32_bf16 v[224:227], a[228:231], a[204:207], v[224:227]\n"
        "ds_read_b64_tr_b16 a[226:227], v28 offset:34816\n"
        "v_mfma_f32_16x16x32_bf16 v[100:103], a[232:235], a[168:171], v[100:103]\n"
        "ds_read_b64_tr_b16 a[228:229], v28 offset:49152\n"
        "v_mfma_f32_16x16x32_bf16 v[100:103], a[236:239], a[172:175], v[100:103]\n"
        "ds_read_b64_tr_b16 a[230:231], v28 offset:51200\n"
        "v_mfma_f32_16x16x32_bf16 v[132:135], a[232:235], a[176:179], v[132:135]\n"
        "buffer_load_dwordx4 v23, s[44:47], s79 offen offset:2048 lds\n"
        "v_mfma_f32_16x16x32_bf16 v[132:135], a[236:239], a[180:183], v[132:135]\n"
        "v_mfma_f32_16x16x32_bf16 v[164:167], a[232:235], a[184:187], v[164:167]\n"
        "v_mfma_f32_16x16x32_bf16 v[164:167], a[236:239], a[188:191], v[164:167]\n"
        "v_mfma_f32_16x16x32_bf16 v[196:199], a[232:235], a[192:195], v[196:199]\n"
        "buffer_load_dwordx4 v24, s[44:47], s79 offen offset:3072 lds\n"
        "v_mfma_f32_16x16x32_bf16 v[196:199], a[236:239], a[196:199], v[196:199]\n"
        "s_mov_b32 s89, 1152\n"
        "s_add_u32 m0, s90, 102400\n"
        "v_mfma_f32_16x16x32_bf16 v[228:231], a[232:235], a[200:203], v[228:231]\n"
        "ds_read_b64_tr_b16 a[232:233], v28 offset:32776\n"
        "v_mfma_f32_16x16x32_bf16 v[228:231], a[236:239], a[204:207], v[228:231]\n"
        "ds_read_b64_tr_b16 a[234:235], v28 offset:34824\n"
        "v_mfma_f32_16x16x32_bf16 v[104:107], a[208:211], a[168:171], v[104:107]\n"
        "ds_read_b64_tr_b16 a[236:237], v28 offset:49160\n"
        "v_mfma_f32_16x16x32_bf16 v[104:107], a[212:215], a[172:175], v[104:107]\n"
        "ds_read_b64_tr_b16 a[238:239], v28 offset:51208\n"
        "v_mfma_f32_16x16x32_bf16 v[136:139], a[208:211], a[176:179], v[136:139]\n"
        "buffer_load_dwordx4 v4, s[40:43], s89 offen offset:0 lds\n"
        "v_mfma_f32_16x16x32_bf16 v[136:139], a[212:215], a[180:183], v[136:139]\n"
        "v_mfma_f32_16x16x32_bf16 v[168:171], a[208:211], a[184:187], v[168:171]\n"
        "v_mfma_f32_16x16x32_bf16 v[168:171], a[212:215], a[188:191], v[168:171]\n"
        "v_mfma_f32_16x16x32_bf16 v[200:203], a[208:211], a[192:195], v[200:203]\n"
        "ds_read_b64_tr_b16 a[208:209], v25 offset:33024\n"
        "v_mfma_f32_16x16x32_bf16 v[200:203], a[212:215], a[196:199], v[200:203]\n"
        "s_waitcnt lgkmcnt(2)\n"
        "ds_read_b64_tr_b16 a[210:211], v25 offset:35072\n"
        "v_mfma_f32_16x16x32_bf16 v[108:111], a[216:219], a[168:171], v[108:111]\n"
        "ds_read_b64_tr_b16 a[212:213], v25 offset:49408\n"
        "v_mfma_f32_16x16x32_bf16 v[108:111], a[220:223], a[172:175], v[108:111]\n"
        "ds_read_b64_tr_b16 a[214:215], v25 offset:51456\n"
        "v_mfma_f32_16x16x32_bf16 v[140:143], a[216:219], a[176:179], v[140:143]\n"
        "buffer_load_dwordx4 v5, s[40:43], s89 offen offset:1024 lds\n"
        "v_mfma_f32_16x16x32_bf16 v[140:143], a[220:223], a[180:183], v[140:143]\n"
        "v_mfma_f32_16x16x32_bf16 v[172:175], a[216:219], a[184:187], v[172:175]\n"
        "v_mfma_f32_16x16x32_bf16 v[172:175], a[220:223], a[188:191], v[172:175]\n"
        "v_mfma_f32_16x16x32_bf16 v[204:207], a[216:219], a[192:195], v[204:207]\n"
        "ds_read_b64_tr_b16 a[216:217], v25 offset:33032\n"
        "v_mfma_f32_16x16x32_bf16 v[204:207], a[220:223], a[196:199], v[204:207]\n"
        "ds_read_b64_tr_b16 a[218:219], v25 offset:35080\n"
        "v_mfma_f32_16x16x32_bf16 v[112:115], a[224:227], a[168:171], v[112:115]\n"
        "ds_read_b64_tr_b16 a[220:221], v25 offset:49416\n"
        "v_mfma_f32_16x16x32_bf16 v[112:115], a[228:231], a[172:175], v[112:115]\n"
        "ds_read_b64_tr_b16 a[222:223], v25 offset:51464\n"
        "v_mfma_f32_16x16x32_bf16 v[144:147], a[224:227], a[176:179], v[144:147]\n"
        "buffer_load_dwordx4 v6, s[40:43], s89 offen offset:2048 lds\n"
        "v_mfma_f32_16x16x32_bf16 v[144:147], a[228:231], a[180:183], v[144:147]\n"
        "v_mfma_f32_16x16x32_bf16 v[176:179], a[224:227], a[184:187], v[176:179]\n"
        "v_mfma_f32_16x16x32_bf16 v[176:179], a[228:231], a[188:191], v[176:179]\n"
        "v_mfma_f32_16x16x32_bf16 v[208:211], a[224:227], a[192:195], v[208:211]\n"
        "ds_read_b64_tr_b16 a[224:225], v26 offset:33024\n"
        "v_mfma_f32_16x16x32_bf16 v[208:211], a[228:231], a[196:199], v[208:211]\n"
        "ds_read_b64_tr_b16 a[226:227], v26 offset:35072\n"
        "v_mfma_f32_16x16x32_bf16 v[116:119], a[232:235], a[168:171], v[116:119]\n"
        "s_waitcnt lgkmcnt(2)\n"
        "ds_read_b64_tr_b16 a[228:229], v26 offset:49408\n"
        "v_mfma_f32_16x16x32_bf16 v[116:119], a[236:239], a[172:175], v[116:119]\n"
        "ds_read_b64_tr_b16 a[230:231], v26 offset:51456\n"
        "v_mfma_f32_16x16x32_bf16 v[148:151], a[232:235], a[176:179], v[148:151]\n"
        "buffer_load_dwordx4 v7, s[40:43], s89 offen offset:3072 lds\n"
        "v_mfma_f32_16x16x32_bf16 v[148:151], a[236:239], a[180:183], v[148:151]\n"
        "s_add_u32 m0, s90, 106496\n"
        "v_mfma_f32_16x16x32_bf16 v[180:183], a[232:235], a[184:187], v[180:183]\n"
        "v_mfma_f32_16x16x32_bf16 v[180:183], a[236:239], a[188:191], v[180:183]\n"
        "v_mfma_f32_16x16x32_bf16 v[212:215], a[232:235], a[192:195], v[212:215]\n"
        "ds_read_b64_tr_b16 a[232:233], v26 offset:33032\n"
        "v_mfma_f32_16x16x32_bf16 v[212:215], a[236:239], a[196:199], v[212:215]\n"
        "ds_read_b64_tr_b16 a[234:235], v26 offset:35080\n"
        "v_mfma_f32_16x16x32_bf16 a[0:3], a[208:211], a[168:171], a[0:3]\n"
        "ds_read_b64_tr_b16 a[236:237], v26 offset:49416\n"
        "v_mfma_f32_16x16x32_bf16 a[0:3], a[212:215], a[172:175], a[0:3]\n"
        "ds_read_b64_tr_b16 a[238:239], v26 offset:51464\n"
        "v_mfma_f32_16x16x32_bf16 a[32:35], a[208:211], a[176:179], a[32:35]\n"
        "buffer_load_dwordx4 v8, s[40:43], s89 offen offset:0 lds\n"
        "v_mfma_f32_16x16x32_bf16 a[32:35], a[212:215], a[180:183], a[32:35]\n"
        "v_mfma_f32_16x16x32_bf16 a[64:67], a[208:211], a[184:187], a[64:67]\n"
        "v_mfma_f32_16x16x32_bf16 a[64:67], a[212:215], a[188:191], a[64:67]\n"
        "v_mfma_f32_16x16x32_bf16 a[96:99], a[208:211], a[192:195], a[96:99]\n"
        "buffer_load_dwordx4 v9, s[40:43], s89 offen offset:1024 lds\n"
        "v_mfma_f32_16x16x32_bf16 a[96:99], a[212:215], a[196:199], a[96:99]\n"
        "v_mfma_f32_16x16x32_bf16 v[68:71], a[208:211], a[200:203], v[68:71]\n"
        "ds_read_b64_tr_b16 a[208:209], v27 offset:33024\n"
        "v_mfma_f32_16x16x32_bf16 v[68:71], a[212:215], a[204:207], v[68:71]\n"
        "ds_read_b64_tr_b16 a[210:211], v27 offset:35072\n"
        "v_mfma_f32_16x16x32_bf16 a[4:7], a[216:219], a[168:171], a[4:7]\n"
        "ds_read_b64_tr_b16 a[212:213], v27 offset:49408\n"
        "v_mfma_f32_16x16x32_bf16 a[4:7], a[220:223], a[172:175], a[4:7]\n"
        "ds_read_b64_tr_b16 a[214:215], v27 offset:51456\n"
        "v_mfma_f32_16x16x32_bf16 a[36:39], a[216:219], a[176:179], a[36:39]\n"
        "buffer_load_dwordx4 v10, s[40:43], s89 offen offset:2048 lds\n"
        "v_mfma_f32_16x16x32_bf16 a[36:39], a[220:223], a[180:183], a[36:39]\n"
        "v_mfma_f32_16x16x32_bf16 a[68:71], a[216:219], a[184:187], a[68:71]\n"
        "v_mfma_f32_16x16x32_bf16 a[68:71], a[220:223], a[188:191], a[68:71]\n"
        "v_mfma_f32_16x16x32_bf16 a[100:103], a[216:219], a[192:195], a[100:103]\n"
        "buffer_load_dwordx4 v11, s[40:43], s89 offen offset:3072 lds\n"
        "v_mfma_f32_16x16x32_bf16 a[100:103], a[220:223], a[196:199], a[100:103]\n"
        "s_add_u32 m0, s90, 110592\n"
        "v_mfma_f32_16x16x32_bf16 v[72:75], a[216:219], a[200:203], v[72:75]\n"
        "ds_read_b64_tr_b16 a[216:217], v27 offset:33032\n"
        "v_mfma_f32_16x16x32_bf16 v[72:75], a[220:223], a[204:207], v[72:75]\n"
        "s_waitcnt lgkmcnt(1)\n"
        "ds_read_b64_tr_b16 a[218:219], v27 offset:35080\n"
        "v_mfma_f32_16x16x32_bf16 a[8:11], a[224:227], a[168:171], a[8:11]\n"
        "ds_read_b64_tr_b16 a[220:221], v27 offset:49416\n"
        "v_mfma_f32_16x16x32_bf16 a[8:11], a[228:231], a[172:175], a[8:11]\n"
        "ds_read_b64_tr_b16 a[222:223], v27 offset:51464\n"
        "v_mfma_f32_16x16x32_bf16 a[40:43], a[224:227], a[176:179], a[40:43]\n"
        "buffer_load_dwordx4 v12, s[40:43], s89 offen offset:0 lds\n"
        "v_mfma_f32_16x16x32_bf16 a[40:43], a[228:231], a[180:183], a[40:43]\n"
        "v_mfma_f32_16x16x32_bf16 a[72:75], a[224:227], a[184:187], a[72:75]\n"
        "v_mfma_f32_16x16x32_bf16 a[72:75], a[228:231], a[188:191], a[72:75]\n"
        "v_mfma_f32_16x16x32_bf16 a[104:107], a[224:227], a[192:195], a[104:107]\n"
        "v_mfma_f32_16x16x32_bf16 a[104:107], a[228:231], a[196:199], a[104:107]\n"
        "v_mfma_f32_16x16x32_bf16 v[76:79], a[224:227], a[200:203], v[76:79]\n"
        "ds_read_b64_tr_b16 a[224:225], v28 offset:33024\n"
        "v_mfma_f32_16x16x32_bf16 v[76:79], a[228:231], a[204:207], v[76:79]\n"
        "ds_read_b64_tr_b16 a[226:227], v28 offset:35072\n"
        "v_mfma_f32_16x16x32_bf16 a[12:15], a[232:235], a[168:171], a[12:15]\n"
        "ds_read_b64_tr_b16 a[228:229], v28 offset:49408\n"
        "v_mfma_f32_16x16x32_bf16 a[12:15], a[236:239], a[172:175], a[12:15]\n"
        "ds_read_b64_tr_b16 a[230:231], v28 offset:51456\n"
        "v_mfma_f32_16x16x32_bf16 a[44:47], a[232:235], a[176:179], a[44:47]\n"
        "v_mfma_f32_16x16x32_bf16 a[44:47], a[236:239], a[180:183], a[44:47]\n"
        "v_mfma_f32_16x16x32_bf16 a[76:79], a[232:235], a[184:187], a[76:79]\n"
        "v_mfma_f32_16x16x32_bf16 a[76:79], a[236:239], a[188:191], a[76:79]\n"
        "v_mfma_f32_16x16x32_bf16 a[108:111], a[232:235], a[192:195], a[108:111]\n"
        "v_mfma_f32_16x16x32_bf16 a[108:111], a[236:239], a[196:199], a[108:111]\n"
        "v_mfma_f32_16x16x32_bf16 v[80:83], a[232:235], a[200:203], v[80:83]\n"
        "ds_read_b64_tr_b16 a[232:233], v28 offset:33032\n"
        "v_mfma_f32_16x16x32_bf16 v[80:83], a[236:239], a[204:207], v[80:83]\n"
        "ds_read_b64_tr_b16 a[234:235], v28 offset:35080\n"
        "v_mfma_f32_16x16x32_bf16 a[16:19], a[208:211], a[168:171], a[16:19]\n"
        "ds_read_b64_tr_b16 a[236:237], v28 offset:49416\n"
        "v_mfma_f32_16x16x32_bf16 a[16:19], a[212:215], a[172:175], a[16:19]\n"
        "ds_read_b64_tr_b16 a[238:239], v28 offset:51464\n"
        "v_mfma_f32_16x16x32_bf16 a[48:51], a[208:211], a[176:179], a[48:51]\n"
        "v_mfma_f32_16x16x32_bf16 a[48:51], a[212:215], a[180:183], a[48:51]\n"
        "v_mfma_f32_16x16x32_bf16 a[80:83], a[208:211], a[184:187], a[80:83]\n"
        "s_waitcnt vmcnt(9)\n"
        "s_barrier\n"
        "v_mfma_f32_16x16x32_bf16 a[80:83], a[212:215], a[188:191], a[80:83]\n"
        "ds_read_b128 a[128:131], v40 offset:0\n"
        "v_mfma_f32_16x16x32_bf16 a[112:115], a[208:211], a[192:195], a[112:115]\n"
        "ds_read_b128 a[132:135], v41 offset:0\n"
        "v_mfma_f32_16x16x32_bf16 a[112:115], a[212:215], a[196:199], a[112:115]\n"
        "s_waitcnt lgkmcnt(3)\n"
        "ds_read_b128 a[136:139], v42 offset:0\n"
        "v_mfma_f32_16x16x32_bf16 a[20:23], a[216:219], a[168:171], a[20:23]\n"
        "ds_read_b128 a[140:143], v43 offset:0\n"
        "v_mfma_f32_16x16x32_bf16 a[20:23], a[220:223], a[172:175], a[20:23]\n"
        "ds_read_b128 a[144:147], v44 offset:0\n"
        "v_mfma_f32_16x16x32_bf16 a[52:55], a[216:219], a[176:179], a[52:55]\n"
        "ds_read_b128 a[148:151], v45 offset:0\n"
        "v_mfma_f32_16x16x32_bf16 a[52:55], a[220:223], a[180:183], a[52:55]\n"
        "ds_read_b128 a[152:155], v46 offset:0\n"
        "v_mfma_f32_16x16x32_bf16 a[84:87], a[216:219], a[184:187], a[84:87]\n"
        "ds_read_b128 a[156:159], v47 offset:0\n"
        "v_mfma_f32_16x16x32_bf16 a[84:87], a[220:223], a[188:191], a[84:87]\n"
        "ds_read_b128 a[160:163], v15 offset:0\n"
        "v_mfma_f32_16x16x32_bf16 a[116:119], a[216:219], a[192:195], a[116:119]\n"
        "ds_read_b128 a[164:167], v16 offset:0\n"
        "v_mfma_f32_16x16x32_bf16 a[116:119], a[220:223], a[196:199], a[116:119]\n"
        "ds_read_b64_tr_b16 a[208:209], v25 offset:0\n"
        "v_mfma_f32_16x16x32_bf16 a[24:27], a[224:227], a[168:171], a[24:27]\n"
        "ds_read_b64_tr_b16 a[210:211], v25 offset:2048\n"
        "v_mfma_f32_16x16x32_bf16 a[24:27], a[228:231], a[172:175], a[24:27]\n"
        "ds_read_b64_tr_b16 a[212:213], v25 offset:16384\n"
        "v_mfma_f32_16x16x32_bf16 a[56:59], a[224:227], a[176:179], a[56:59]\n"
        "ds_read_b64_tr_b16 a[214:215], v25 offset:18432\n"
        "v_mfma_f32_16x16x32_bf16 a[56:59], a[228:231], a[180:183], a[56:59]\n"
        "ds_read_b64_tr_b16 a[216:217], v25 offset:8\n"
        "v_mfma_f32_16x16x32_bf16 a[88:91], a[224:227], a[184:187], a[88:91]\n"
        "ds_read_b64_tr_b16 a[218:219], v25 offset:2056\n"
        "v_mfma_f32_16x16x32_bf16 a[88:91], a[228:231], a[188:191], a[88:91]\n"
        "ds_read_b64_tr_b16 a[220:221], v25 offset:16392\n"
        "v_mfma_f32_16x16x32_bf16 a[120:123], a[224:227], a[192:195], a[120:123]\n"
        "ds_read_b64_tr_b16 a[222:223], v25 offset:18440\n"
        "v_mfma_f32_16x16x32_bf16 a[120:123], a[228:231], a[196:199], a[120:123]\n"
        "ds_read_b64_tr_b16 a[224:225], v26 offset:0\n"
        "v_mfma_f32_16x16x32_bf16 a[28:31], a[232:235], a[168:171], a[28:31]\n"
        "s_waitcnt lgkmcnt(6)\n"
        "ds_read_b64_tr_b16 a[226:227], v26 offset:2048\n"
        "v_mfma_f32_16x16x32_bf16 a[28:31], a[236:239], a[172:175], a[28:31]\n"
        "ds_read_b64_tr_b16 a[228:229], v26 offset:16384\n"
        "v_mfma_f32_16x16x32_bf16 a[60:63], a[232:235], a[176:179], a[60:63]\n"
        "ds_read_b64_tr_b16 a[230:231], v26 offset:18432\n"
        "v_mfma_f32_16x16x32_bf16 a[60:63], a[236:239], a[180:183], a[60:63]\n"
        "v_mfma_f32_16x16x32_bf16 a[92:95], a[232:235], a[184:187], a[92:95]\n"
        "s_barrier\n"
        "v_mfma_f32_16x16x32_bf16 a[92:95], a[236:239], a[188:191], a[92:95]\n"
        "s_mov_b32 s79, 4718592\n"
        "s_add_u32 m0, s66, 32768\n"
        "v_mfma_f32_16x16x32_bf16 a[124:127], a[232:235], a[192:195], a[124:127]\n"
        "ds_read_b64_tr_b16 a[232:233], v26 offset:8\n"
        "v_mfma_f32_16x16x32_bf16 a[124:127], a[236:239], a[196:199], a[124:127]\n"
        "ds_read_b64_tr_b16 a[234:235], v26 offset:2056\n"
        "v_mfma_f32_16x16x32_bf16 v[88:91], a[208:211], a[128:131], v[88:91]\n"
        "s_waitcnt lgkmcnt(3)\n"
        "ds_read_b64_tr_b16 a[236:237], v26 offset:16392\n"
        "v_mfma_f32_16x16x32_bf16 v[88:91], a[212:215], a[132:135], v[88:91]\n"
        "ds_read_b64_tr_b16 a[238:239], v26 offset:18440\n"
        "v_mfma_f32_16x16x32_bf16 v[120:123], a[208:211], a[136:139], v[120:123]\n"
        "buffer_load_dwordx4 v17, s[44:47], s79 offen offset:0 lds\n"
        "v_mfma_f32_16x16x32_bf16 v[120:123], a[212:215], a[140:143], v[120:123]\n"
        "v_mfma_f32_16x16x32_bf16 v[152:155], a[208:211], a[144:147], v[152:155]\n"
        "v_mfma_f32_16x16x32_bf16 v[152:155], a[212:215], a[148:151], v[152:155]\n"
        "v_mfma_f32_16x16x32_bf16 v[184:187], a[208:211], a[152:155], v[184:187]\n"
        "buffer_load_dwordx4 v18, s[44:47], s79 offen offset:1024 lds\n"
        "v_mfma_f32_16x16x32_bf16 v[184:187], a[212:215], a[156:159], v[184:187]\n"
        "v_mfma_f32_16x16x32_bf16 v[216:219], a[208:211], a[160:163], v[216:219]\n"
        "ds_read_b64_tr_b16 a[208:209], v27 offset:0\n"
        "v_mfma_f32_16x16x32_bf16 v[216:219], a[212:215], a[164:167], v[216:219]\n"
        "ds_read_b64_tr_b16 a[210:211], v27 offset:2048\n"
        "v_mfma_f32_16x16x32_bf16 v[92:95], a[216:219], a[128:131], v[92:95]\n"
        "ds_read_b64_tr_b16 a[212:213], v27 offset:16384\n"
        "v_mfma_f32_16x16x32_bf16 v[92:95], a[220:223], a[132:135], v[92:95]\n"
        "ds_read_b64_tr_b16 a[214:215], v27 offset:18432\n"
        "v_mfma_f32_16x16x32_bf16 v[124:127], a[216:219], a[136:139], v[124:127]\n"
        "buffer_load_dwordx4 v19, s[44:47], s79 offen offset:2048 lds\n"
        "v_mfma_f32_16x16x32_bf16 v[124:127], a[220:223], a[140:143], v[124:127]\n"
        "v_mfma_f32_16x16x32_bf16 v[156:159], a[216:219], a[144:147], v[156:159]\n"
        "v_mfma_f32_16x16x32_bf16 v[156:159], a[220:223], a[148:151], v[156:159]\n"
        "v_mfma_f32_16x16x32_bf16 v[188:191], a[216:219], a[152:155], v[188:191]\n"
        "buffer_load_dwordx4 v20, s[44:47], s79 offen offset:3072 lds\n"
        "v_mfma_f32_16x16x32_bf16 v[188:191], a[220:223], a[156:159], v[188:191]\n"
        "s_add_u32 m0, s66, 36864\n"
        "v_mfma_f32_16x16x32_bf16 v[220:223], a[216:219], a[160:163], v[220:223]\n"
        "ds_read_b64_tr_b16 a[216:217], v27 offset:8\n"
        "v_mfma_f32_16x16x32_bf16 v[220:223], a[220:223], a[164:167], v[220:223]\n"
        "ds_read_b64_tr_b16 a[218:219], v27 offset:2056\n"
        "v_mfma_f32_16x16x32_bf16 v[96:99], a[224:227], a[128:131], v[96:99]\n"
        "s_waitcnt lgkmcnt(2)\n"
        "ds_read_b64_tr_b16 a[220:221], v27 offset:16392\n"
        "v_mfma_f32_16x16x32_bf16 v[96:99], a[228:231], a[132:135], v[96:99]\n"
        "ds_read_b64_tr_b16 a[222:223], v27 offset:18440\n"
        "v_mfma_f32_16x16x32_bf16 v[128:131], a[224:227], a[136:139], v[128:131]\n"
        "buffer_load_dwordx4 v21, s[44:47], s79 offen offset:0 lds\n"
        "v_mfma_f32_16x16x32_bf16 v[128:131], a[228:231], a[140:143], v[128:131]\n"
        "v_mfma_f32_16x16x32_bf16 v[160:163], a[224:227], a[144:147], v[160:163]\n"
        "v_mfma_f32_16x16x32_bf16 v[160:163], a[228:231], a[148:151], v[160:163]\n"
        "v_mfma_f32_16x16x32_bf16 v[192:195], a[224:227], a[152:155], v[192:195]\n"
        "buffer_load_dwordx4 v22, s[44:47], s79 offen offset:1024 lds\n"
        "v_mfma_f32_16x16x32_bf16 v[192:195], a[228:231], a[156:159], v[192:195]\n"
        "v_mfma_f32_16x16x32_bf16 v[224:227], a[224:227], a[160:163], v[224:227]\n"
        "ds_read_b64_tr_b16 a[224:225], v28 offset:0\n"
        "v_mfma_f32_16x16x32_bf16 v[224:227], a[228:231], a[164:167], v[224:227]\n"
        "ds_read_b64_tr_b16 a[226:227], v28 offset:2048\n"
        "v_mfma_f32_16x16x32_bf16 v[100:103], a[232:235], a[128:131], v[100:103]\n"
        "ds_read_b64_tr_b16 a[228:229], v28 offset:16384\n"
        "v_mfma_f32_16x16x32_bf16 v[100:103], a[236:239], a[132:135], v[100:103]\n"
        "ds_read_b64_tr_b16 a[230:231], v28 offset:18432\n"
        "v_mfma_f32_16x16x32_bf16 v[132:135], a[232:235], a[136:139], v[132:135]\n"
        "buffer_load_dwordx4 v23, s[44:47], s79 offen offset:2048 lds\n"
        "v_mfma_f32_16x16x32_bf16 v[132:135], a[236:239], a[140:143], v[132:135]\n"
        "v_mfma_f32_16x16x32_bf16 v[164:167], a[232:235], a[144:147], v[164:167]\n"
        "v_mfma_f32_16x16x32_bf16 v[164:167], a[236:239], a[148:151], v[164:167]\n"
        "v_mfma_f32_16x16x32_bf16 v[196:199], a[232:235], a[152:155], v[196:199]\n"
        "buffer_load_dwordx4 v24, s[44:47], s79 offen offset:3072 lds\n"
        "v_mfma_f32_16x16x32_bf16 v[196:199], a[236:239], a[156:159], v[196:199]\n"
        "s_mov_b32 s67, 0\n"
        "s_add_u32 m0, s65, 65536\n"
        "v_mfma_f32_16x16x32_bf16 v[228:231], a[232:235], a[160:163], v[228:231]\n"
        "ds_read_b64_tr_b16 a[232:233], v28 offset:8\n"
        "v_mfma_f32_16x16x32_bf16 v[228:231], a[236:239], a[164:167], v[228:231]\n"
        "ds_read_b64_tr_b16 a[234:235], v28 offset:2056\n"
        "v_mfma_f32_16x16x32_bf16 v[104:107], a[208:211], a[128:131], v[104:107]\n"
        "ds_read_b64_tr_b16 a[236:237], v28 offset:16392\n"
        "v_mfma_f32_16x16x32_bf16 v[104:107], a[212:215], a[132:135], v[104:107]\n"
        "ds_read_b64_tr_b16 a[238:239], v28 offset:18440\n"
        "v_mfma_f32_16x16x32_bf16 v[136:139], a[208:211], a[136:139], v[136:139]\n"
        "buffer_load_dwordx4 v244, s[48:51], s67 offen offset:0 nt lds\n"
        "v_mfma_f32_16x16x32_bf16 v[136:139], a[212:215], a[140:143], v[136:139]\n"
        "v_mfma_f32_16x16x32_bf16 v[168:171], a[208:211], a[144:147], v[168:171]\n"
        "v_mfma_f32_16x16x32_bf16 v[168:171], a[212:215], a[148:151], v[168:171]\n"
        "v_mfma_f32_16x16x32_bf16 v[200:203], a[208:211], a[152:155], v[200:203]\n"
        "ds_read_b64_tr_b16 a[208:209], v25 offset:256\n"
        "v_mfma_f32_16x16x32_bf16 v[200:203], a[212:215], a[156:159], v[200:203]\n"
        "s_waitcnt lgkmcnt(2)\n"
        "ds_read_b64_tr_b16 a[210:211], v25 offset:2304\n"
        "v_mfma_f32_16x16x32_bf16 v[108:111], a[216:219], a[128:131], v[108:111]\n"
        "ds_read_b64_tr_b16 a[212:213], v25 offset:16640\n"
        "v_mfma_f32_16x16x32_bf16 v[108:111], a[220:223], a[132:135], v[108:111]\n"
        "ds_read_b64_tr_b16 a[214:215], v25 offset:18688\n"
        "v_mfma_f32_16x16x32_bf16 v[140:143], a[216:219], a[136:139], v[140:143]\n"
        "buffer_load_dwordx4 v245, s[48:51], s67 offen offset:1024 nt lds\n"
        "v_mfma_f32_16x16x32_bf16 v[140:143], a[220:223], a[140:143], v[140:143]\n"
        "v_mfma_f32_16x16x32_bf16 v[172:175], a[216:219], a[144:147], v[172:175]\n"
        "v_mfma_f32_16x16x32_bf16 v[172:175], a[220:223], a[148:151], v[172:175]\n"
        "v_mfma_f32_16x16x32_bf16 v[204:207], a[216:219], a[152:155], v[204:207]\n"
        "ds_read_b64_tr_b16 a[216:217], v25 offset:264\n"
        "v_mfma_f32_16x16x32_bf16 v[204:207], a[220:223], a[156:159], v[204:207]\n"
        "ds_read_b64_tr_b16 a[218:219], v25 offset:2312\n"
        "v_mfma_f32_16x16x32_bf16 v[112:115], a[224:227], a[128:131], v[112:115]\n"
        "ds_read_b64_tr_b16 a[220:221], v25 offset:16648\n"
        "v_mfma_f32_16x16x32_bf16 v[112:115], a[228:231], a[132:135], v[112:115]\n"
        "ds_read_b64_tr_b16 a[222:223], v25 offset:18696\n"
        "v_mfma_f32_16x16x32_bf16 v[144:147], a[224:227], a[136:139], v[144:147]\n"
        "buffer_load_dwordx4 v246, s[48:51], s67 offen offset:2048 nt lds\n"
        "v_mfma_f32_16x16x32_bf16 v[144:147], a[228:231], a[140:143], v[144:147]\n"
        "v_mfma_f32_16x16x32_bf16 v[176:179], a[224:227], a[144:147], v[176:179]\n"
        "v_mfma_f32_16x16x32_bf16 v[176:179], a[228:231], a[148:151], v[176:179]\n"
        "v_mfma_f32_16x16x32_bf16 v[208:211], a[224:227], a[152:155], v[208:211]\n"
        "ds_read_b64_tr_b16 a[224:225], v26 offset:256\n"
        "v_mfma_f32_16x16x32_bf16 v[208:211], a[228:231], a[156:159], v[208:211]\n"
        "ds_read_b64_tr_b16 a[226:227], v26 offset:2304\n"
        "v_mfma_f32_16x16x32_bf16 v[116:119], a[232:235], a[128:131], v[116:119]\n"
        "s_waitcnt lgkmcnt(2)\n"
        "ds_read_b64_tr_b16 a[228:229], v26 offset:16640\n"
        "v_mfma_f32_16x16x32_bf16 v[116:119], a[236:239], a[132:135], v[116:119]\n"
        "ds_read_b64_tr_b16 a[230:231], v26 offset:18688\n"
        "v_mfma_f32_16x16x32_bf16 v[148:151], a[232:235], a[136:139], v[148:151]\n"
        "buffer_load_dwordx4 v247, s[48:51], s67 offen offset:3072 nt lds\n"
        "v_mfma_f32_16x16x32_bf16 v[148:151], a[236:239], a[140:143], v[148:151]\n"
        "s_mov_b32 s67, 16384\n"
        "s_add_u32 m0, s65, 81920\n"
        "v_mfma_f32_16x16x32_bf16 v[180:183], a[232:235], a[144:147], v[180:183]\n"
        "v_mfma_f32_16x16x32_bf16 v[180:183], a[236:239], a[148:151], v[180:183]\n"
        "v_mfma_f32_16x16x32_bf16 v[212:215], a[232:235], a[152:155], v[212:215]\n"
        "ds_read_b64_tr_b16 a[232:233], v26 offset:264\n"
        "v_mfma_f32_16x16x32_bf16 v[212:215], a[236:239], a[156:159], v[212:215]\n"
        "ds_read_b64_tr_b16 a[234:235], v26 offset:2312\n"
        "v_mfma_f32_16x16x32_bf16 a[0:3], a[208:211], a[128:131], a[0:3]\n"
        "ds_read_b64_tr_b16 a[236:237], v26 offset:16648\n"
        "v_mfma_f32_16x16x32_bf16 a[0:3], a[212:215], a[132:135], a[0:3]\n"
        "ds_read_b64_tr_b16 a[238:239], v26 offset:18696\n"
        "v_mfma_f32_16x16x32_bf16 a[32:35], a[208:211], a[136:139], a[32:35]\n"
        "buffer_load_dwordx4 v244, s[48:51], s67 offen offset:0 nt lds\n"
        "v_mfma_f32_16x16x32_bf16 a[32:35], a[212:215], a[140:143], a[32:35]\n"
        "v_mfma_f32_16x16x32_bf16 a[64:67], a[208:211], a[144:147], a[64:67]\n"
        "v_mfma_f32_16x16x32_bf16 a[64:67], a[212:215], a[148:151], a[64:67]\n"
        "v_mfma_f32_16x16x32_bf16 a[96:99], a[208:211], a[152:155], a[96:99]\n"
        "buffer_load_dwordx4 v245, s[48:51], s67 offen offset:1024 nt lds\n"
        "v_mfma_f32_16x16x32_bf16 a[96:99], a[212:215], a[156:159], a[96:99]\n"
        "v_mfma_f32_16x16x32_bf16 v[68:71], a[208:211], a[160:163], v[68:71]\n"
        "ds_read_b64_tr_b16 a[208:209], v27 offset:256\n"
        "v_mfma_f32_16x16x32_bf16 v[68:71], a[212:215], a[164:167], v[68:71]\n"
        "ds_read_b64_tr_b16 a[210:211], v27 offset:2304\n"
        "v_mfma_f32_16x16x32_bf16 a[4:7], a[216:219], a[128:131], a[4:7]\n"
        "ds_read_b64_tr_b16 a[212:213], v27 offset:16640\n"
        "v_mfma_f32_16x16x32_bf16 a[4:7], a[220:223], a[132:135], a[4:7]\n"
        "ds_read_b64_tr_b16 a[214:215], v27 offset:18688\n"
        "v_mfma_f32_16x16x32_bf16 a[36:39], a[216:219], a[136:139], a[36:39]\n"
        "buffer_load_dwordx4 v246, s[48:51], s67 offen offset:2048 nt lds\n"
        "v_mfma_f32_16x16x32_bf16 a[36:39], a[220:223], a[140:143], a[36:39]\n"
        "v_mfma_f32_16x16x32_bf16 a[68:71], a[216:219], a[144:147], a[68:71]\n"
        "v_mfma_f32_16x16x32_bf16 a[68:71], a[220:223], a[148:151], a[68:71]\n"
        "v_mfma_f32_16x16x32_bf16 a[100:103], a[216:219], a[152:155], a[100:103]\n"
        "buffer_load_dwordx4 v247, s[48:51], s67 offen offset:3072 nt lds\n"
        "v_mfma_f32_16x16x32_bf16 a[100:103], a[220:223], a[156:159], a[100:103]\n"
        "v_mfma_f32_16x16x32_bf16 v[72:75], a[216:219], a[160:163], v[72:75]\n"
        "ds_read_b64_tr_b16 a[216:217], v27 offset:264\n"
        "v_mfma_f32_16x16x32_bf16 v[72:75], a[220:223], a[164:167], v[72:75]\n"
        "s_waitcnt lgkmcnt(1)\n"
        "ds_read_b64_tr_b16 a[218:219], v27 offset:2312\n"
        "v_mfma_f32_16x16x32_bf16 a[8:11], a[224:227], a[128:131], a[8:11]\n"
        "ds_read_b64_tr_b16 a[220:221], v27 offset:16648\n"
        "v_mfma_f32_16x16x32_bf16 a[8:11], a[228:231], a[132:135], a[8:11]\n"
        "ds_read_b64_tr_b16 a[222:223], v27 offset:18696\n"
        "v_mfma_f32_16x16x32_bf16 a[40:43], a[224:227], a[136:139], a[40:43]\n"
        "buffer_load_dword a255, v253, s[60:63], s82 offen\n"
        "v_mfma_f32_16x16x32_bf16 a[40:43], a[228:231], a[140:143], a[40:43]\n"
        "v_mfma_f32_16x16x32_bf16 a[72:75], a[224:227], a[144:147], a[72:75]\n"
        "v_mfma_f32_16x16x32_bf16 a[72:75], a[228:231], a[148:151], a[72:75]\n"
        "v_mfma_f32_16x16x32_bf16 a[104:107], a[224:227], a[152:155], a[104:107]\n"
        "v_mfma_f32_16x16x32_bf16 a[104:107], a[228:231], a[156:159], a[104:107]\n"
        "v_mfma_f32_16x16x32_bf16 v[76:79], a[224:227], a[160:163], v[76:79]\n"
        "ds_read_b64_tr_b16 a[224:225], v28 offset:256\n"
        "v_mfma_f32_16x16x32_bf16 v[76:79], a[228:231], a[164:167], v[76:79]\n"
        "ds_read_b64_tr_b16 a[226:227], v28 offset:2304\n"
        "v_mfma_f32_16x16x32_bf16 a[12:15], a[232:235], a[128:131], a[12:15]\n"
        "ds_read_b64_tr_b16 a[228:229], v28 offset:16640\n"
        "v_mfma_f32_16x16x32_bf16 a[12:15], a[236:239], a[132:135], a[12:15]\n"
        "ds_read_b64_tr_b16 a[230:231], v28 offset:18688\n"
        "v_mfma_f32_16x16x32_bf16 a[44:47], a[232:235], a[136:139], a[44:47]\n"
        "v_mfma_f32_16x16x32_bf16 a[44:47], a[236:239], a[140:143], a[44:47]\n"
        "v_mfma_f32_16x16x32_bf16 a[76:79], a[232:235], a[144:147], a[76:79]\n"
        "v_mfma_f32_16x16x32_bf16 a[76:79], a[236:239], a[148:151], a[76:79]\n"
        "v_mfma_f32_16x16x32_bf16 a[108:111], a[232:235], a[152:155], a[108:111]\n"
        "v_mfma_f32_16x16x32_bf16 a[108:111], a[236:239], a[156:159], a[108:111]\n"
        "v_mfma_f32_16x16x32_bf16 v[80:83], a[232:235], a[160:163], v[80:83]\n"
        "ds_read_b64_tr_b16 a[232:233], v28 offset:264\n"
        "v_mfma_f32_16x16x32_bf16 v[80:83], a[236:239], a[164:167], v[80:83]\n"
        "ds_read_b64_tr_b16 a[234:235], v28 offset:2312\n"
        "v_mfma_f32_16x16x32_bf16 a[16:19], a[208:211], a[128:131], a[16:19]\n"
        "ds_read_b64_tr_b16 a[236:237], v28 offset:16648\n"
        "v_mfma_f32_16x16x32_bf16 a[16:19], a[212:215], a[132:135], a[16:19]\n"
        "ds_read_b64_tr_b16 a[238:239], v28 offset:18696\n"
        "v_mfma_f32_16x16x32_bf16 a[48:51], a[208:211], a[136:139], a[48:51]\n"
        "v_mfma_f32_16x16x32_bf16 a[48:51], a[212:215], a[140:143], a[48:51]\n"
        "v_mfma_f32_16x16x32_bf16 a[80:83], a[208:211], a[144:147], a[80:83]\n"
        "s_waitcnt vmcnt(9)\n"
        "s_barrier\n"
        "v_mfma_f32_16x16x32_bf16 a[80:83], a[212:215], a[148:151], a[80:83]\n"
        "ds_read_b128 a[168:171], v40 offset:36864\n"
        "v_mfma_f32_16x16x32_bf16 a[112:115], a[208:211], a[152:155], a[112:115]\n"
        "ds_read_b128 a[172:175], v41 offset:36864\n"
        "v_mfma_f32_16x16x32_bf16 a[112:115], a[212:215], a[156:159], a[112:115]\n"
        "s_waitcnt lgkmcnt(3)\n"
        "ds_read_b128 a[176:179], v42 offset:36864\n"
        "v_mfma_f32_16x16x32_bf16 a[20:23], a[216:219], a[128:131], a[20:23]\n"
        "ds_read_b128 a[180:183], v43 offset:36864\n"
        "v_mfma_f32_16x16x32_bf16 a[20:23], a[220:223], a[132:135], a[20:23]\n"
        "ds_read_b128 a[184:187], v44 offset:36864\n"
        "v_mfma_f32_16x16x32_bf16 a[52:55], a[216:219], a[136:139], a[52:55]\n"
        "ds_read_b128 a[188:191], v45 offset:36864\n"
        "v_mfma_f32_16x16x32_bf16 a[52:55], a[220:223], a[140:143], a[52:55]\n"
        "ds_read_b128 a[192:195], v46 offset:36864\n"
        "v_mfma_f32_16x16x32_bf16 a[84:87], a[216:219], a[144:147], a[84:87]\n"
        "ds_read_b128 a[196:199], v47 offset:36864\n"
        "v_mfma_f32_16x16x32_bf16 a[84:87], a[220:223], a[148:151], a[84:87]\n"
        "ds_read_b128 a[200:203], v15 offset:36864\n"
        "v_mfma_f32_16x16x32_bf16 a[116:119], a[216:219], a[152:155], a[116:119]\n"
        "ds_read_b128 a[204:207], v16 offset:36864\n"
        "v_mfma_f32_16x16x32_bf16 a[116:119], a[220:223], a[156:159], a[116:119]\n"
        "ds_read_b64_tr_b16 a[208:209], v25 offset:32768\n"
        "v_mfma_f32_16x16x32_bf16 a[24:27], a[224:227], a[128:131], a[24:27]\n"
        "ds_read_b64_tr_b16 a[210:211], v25 offset:34816\n"
        "v_mfma_f32_16x16x32_bf16 a[24:27], a[228:231], a[132:135], a[24:27]\n"
        "ds_read_b64_tr_b16 a[212:213], v25 offset:49152\n"
        "v_mfma_f32_16x16x32_bf16 a[56:59], a[224:227], a[136:139], a[56:59]\n"
        "ds_read_b64_tr_b16 a[214:215], v25 offset:51200\n"
        "v_mfma_f32_16x16x32_bf16 a[56:59], a[228:231], a[140:143], a[56:59]\n"
        "ds_read_b64_tr_b16 a[216:217], v25 offset:32776\n"
        "v_mfma_f32_16x16x32_bf16 a[88:91], a[224:227], a[144:147], a[88:91]\n"
        "ds_read_b64_tr_b16 a[218:219], v25 offset:34824\n"
        "v_mfma_f32_16x16x32_bf16 a[88:91], a[228:231], a[148:151], a[88:91]\n"
        "ds_read_b64_tr_b16 a[220:221], v25 offset:49160\n"
        "v_mfma_f32_16x16x32_bf16 a[120:123], a[224:227], a[152:155], a[120:123]\n"
        "ds_read_b64_tr_b16 a[222:223], v25 offset:51208\n"
        "v_mfma_f32_16x16x32_bf16 a[120:123], a[228:231], a[156:159], a[120:123]\n"
        "ds_read_b64_tr_b16 a[224:225], v26 offset:32768\n"
        "v_mfma_f32_16x16x32_bf16 a[28:31], a[232:235], a[128:131], a[28:31]\n"
        "s_waitcnt lgkmcnt(6)\n"
        "ds_read_b64_tr_b16 a[226:227], v26 offset:34816\n"
        "v_mfma_f32_16x16x32_bf16 a[28:31], a[236:239], a[132:135], a[28:31]\n"
        "ds_read_b64_tr_b16 a[228:229], v26 offset:49152\n"
        "v_mfma_f32_16x16x32_bf16 a[60:63], a[232:235], a[136:139], a[60:63]\n"
        "ds_read_b64_tr_b16 a[230:231], v26 offset:51200\n"
        "v_mfma_f32_16x16x32_bf16 a[60:63], a[236:239], a[140:143], a[60:63]\n"
        "v_mfma_f32_16x16x32_bf16 a[92:95], a[232:235], a[144:147], a[92:95]\n"
        "s_barrier\n"
        "v_mfma_f32_16x16x32_bf16 a[92:95], a[236:239], a[148:151], a[92:95]\n"
        "s_mov_b32 s67, 16384\n"
        "s_add_u32 m0, s65, 114688\n"
        "v_mfma_f32_16x16x32_bf16 a[124:127], a[232:235], a[152:155], a[124:127]\n"
        "ds_read_b64_tr_b16 a[232:233], v26 offset:32776\n"
        "v_mfma_f32_16x16x32_bf16 a[124:127], a[236:239], a[156:159], a[124:127]\n"
        "ds_read_b64_tr_b16 a[234:235], v26 offset:34824\n"
        "v_mfma_f32_16x16x32_bf16 v[88:91], a[208:211], a[168:171], v[88:91]\n"
        "s_waitcnt lgkmcnt(3)\n"
        "ds_read_b64_tr_b16 a[236:237], v26 offset:49160\n"
        "v_mfma_f32_16x16x32_bf16 v[88:91], a[212:215], a[172:175], v[88:91]\n"
        "ds_read_b64_tr_b16 a[238:239], v26 offset:51208\n"
        "v_mfma_f32_16x16x32_bf16 v[120:123], a[208:211], a[176:179], v[120:123]\n"
        "buffer_load_dwordx4 v248, s[52:55], s67 offen offset:0 nt lds\n"
        "v_mfma_f32_16x16x32_bf16 v[120:123], a[212:215], a[180:183], v[120:123]\n"
        "v_mfma_f32_16x16x32_bf16 v[152:155], a[208:211], a[184:187], v[152:155]\n"
        "v_mfma_f32_16x16x32_bf16 v[152:155], a[212:215], a[188:191], v[152:155]\n"
        "v_mfma_f32_16x16x32_bf16 v[184:187], a[208:211], a[192:195], v[184:187]\n"
        "buffer_load_dwordx4 v249, s[52:55], s67 offen offset:1024 nt lds\n"
        "v_mfma_f32_16x16x32_bf16 v[184:187], a[212:215], a[196:199], v[184:187]\n"
        "v_mul_f32 v88, s70, v88\n"
        "v_mul_f32 v89, s70, v89\n"
        "v_mfma_f32_16x16x32_bf16 v[216:219], a[208:211], a[200:203], v[216:219]\n"
        "ds_read_b64_tr_b16 a[208:209], v27 offset:32768\n"
        "v_cvt_pk_bf16_f32 v88, v88, v89\n"
        "v_mfma_f32_16x16x32_bf16 v[216:219], a[212:215], a[204:207], v[216:219]\n"
        "ds_read_b64_tr_b16 a[210:211], v27 offset:34816\n"
        "v_mul_f32 v90, s70, v90\n"
        "v_mfma_f32_16x16x32_bf16 v[92:95], a[216:219], a[168:171], v[92:95]\n"
        "ds_read_b64_tr_b16 a[212:213], v27 offset:49152\n"
        "v_mul_f32 v91, s70, v91\n"
        "v_mfma_f32_16x16x32_bf16 v[92:95], a[220:223], a[172:175], v[92:95]\n"
        "ds_read_b64_tr_b16 a[214:215], v27 offset:51200\n"
        "v_cvt_pk_bf16_f32 v89, v90, v91\n"
        "v_mfma_f32_16x16x32_bf16 v[124:127], a[216:219], a[176:179], v[124:127]\n"
        "buffer_load_dwordx4 v248, s[52:55], s67 offen offset:2048 nt lds\n"
        "v_mfma_f32_16x16x32_bf16 v[124:127], a[220:223], a[180:183], v[124:127]\n"
        "v_mul_f32 v120, s70, v120\n"
        "v_mul_f32 v121, s70, v121\n"
        "v_mfma_f32_16x16x32_bf16 v[156:159], a[216:219], a[184:187], v[156:159]\n"
        "v_cvt_pk_bf16_f32 v120, v120, v121\n"
        "v_mul_f32 v122, s70, v122\n"
        "v_mfma_f32_16x16x32_bf16 v[156:159], a[220:223], a[188:191], v[156:159]\n"
        "v_mul_f32 v123, s70, v123\n"
        "v_mul_f32 v152, s70, v152\n"
        "v_mfma_f32_16x16x32_bf16 v[188:191], a[216:219], a[192:195], v[188:191]\n"
        "buffer_load_dwordx4 v249, s[52:55], s67 offen offset:3072 nt lds\n"
        "v_mfma_f32_16x16x32_bf16 v[188:191], a[220:223], a[196:199], v[188:191]\n"
        "v_cvt_pk_bf16_f32 v121, v122, v123\n"
        "v_mul_f32 v153, s70, v153\n"
        "v_mfma_f32_16x16x32_bf16 v[220:223], a[216:219], a[200:203], v[220:223]\n"
        "ds_read_b64_tr_b16 a[216:217], v27 offset:32776\n"
        "v_cvt_pk_bf16_f32 v152, v152, v153\n"
        "v_mfma_f32_16x16x32_bf16 v[220:223], a[220:223], a[204:207], v[220:223]\n"
        "ds_read_b64_tr_b16 a[218:219], v27 offset:34824\n"
        "v_mul_f32 v154, s70, v154\n"
        "v_mfma_f32_16x16x32_bf16 v[96:99], a[224:227], a[168:171], v[96:99]\n"
        "s_waitcnt lgkmcnt(2)\n"
        "ds_read_b64_tr_b16 a[220:221], v27 offset:49160\n"
        "v_mul_f32 v155, s70, v155\n"
        "v_mfma_f32_16x16x32_bf16 v[96:99], a[228:231], a[172:175], v[96:99]\n"
        "ds_read_b64_tr_b16 a[222:223], v27 offset:51208\n"
        "v_cvt_pk_bf16_f32 v153, v154, v155\n"
        "v_mfma_f32_16x16x32_bf16 v[128:131], a[224:227], a[176:179], v[128:131]\n"
        "v_mul_f32 v184, s70, v184\n"
        "v_mul_f32 v185, s70, v185\n"
        "v_mfma_f32_16x16x32_bf16 v[128:131], a[228:231], a[180:183], v[128:131]\n"
        "v_cvt_pk_bf16_f32 v184, v184, v185\n"
        "v_mul_f32 v186, s70, v186\n"
        "v_mfma_f32_16x16x32_bf16 v[160:163], a[224:227], a[184:187], v[160:163]\n"
        "v_mul_f32 v187, s70, v187\n"
        "v_mul_f32 v216, s70, v216\n"
        "v_mfma_f32_16x16x32_bf16 v[160:163], a[228:231], a[188:191], v[160:163]\n"
        "v_cvt_pk_bf16_f32 v185, v186, v187\n"
        "v_mul_f32 v217, s70, v217\n"
        "v_mfma_f32_16x16x32_bf16 v[192:195], a[224:227], a[192:195], v[192:195]\n"
        "v_cvt_pk_bf16_f32 v216, v216, v217\n"
        "v_mul_f32 v218, s70, v218\n"
        "v_mfma_f32_16x16x32_bf16 v[192:195], a[228:231], a[196:199], v[192:195]\n"
        "v_mul_f32 v219, s70, v219\n"
        "v_mul_f32 v92, s70, v92\n"
        "v_mfma_f32_16x16x32_bf16 v[224:227], a[224:227], a[200:203], v[224:227]\n"
        "ds_read_b64_tr_b16 a[224:225], v28 offset:32768\n"
        "v_cvt_pk_bf16_f32 v217, v218, v219\n"
        "v_mfma_f32_16x16x32_bf16 v[224:227], a[228:231], a[204:207], v[224:227]\n"
        "ds_read_b64_tr_b16 a[226:227], v28 offset:34816\n"
        "v_mul_f32 v93, s70, v93\n"
        "v_mfma_f32_16x16x32_bf16 v[100:103], a[232:235], a[168:171], v[100:103]\n"
        "ds_read_b64_tr_b16 a[228:229], v28 offset:49152\n"
        "v_cvt_pk_bf16_f32 v90, v92, v93\n"
        "v_mfma_f32_16x16x32_bf16 v[100:103], a[236:239], a[172:175], v[100:103]\n"
        "ds_read_b64_tr_b16 a[230:231], v28 offset:51200\n"
        "v_mul_f32 v94, s70, v94\n"
        "v_mfma_f32_16x16x32_bf16 v[132:135], a[232:235], a[176:179], v[132:135]\n"
        "v_mul_f32 v95, s70, v95\n"
        "v_mul_f32 v124, s70, v124\n"
        "v_mfma_f32_16x16x32_bf16 v[132:135], a[236:239], a[180:183], v[132:135]\n"
        "v_cvt_pk_bf16_f32 v91, v94, v95\n"
        "v_mul_f32 v125, s70, v125\n"
        "v_mfma_f32_16x16x32_bf16 v[164:167], a[232:235], a[184:187], v[164:167]\n"
        "v_cvt_pk_bf16_f32 v122, v124, v125\n"
        "v_mul_f32 v126, s70, v126\n"
        "v_mfma_f32_16x16x32_bf16 v[164:167], a[236:239], a[188:191], v[164:167]\n"
        "v_mul_f32 v127, s70, v127\n"
        "v_mul_f32 v156, s70, v156\n"
        "v_mfma_f32_16x16x32_bf16 v[196:199], a[232:235], a[192:195], v[196:199]\n"
        "v_cvt_pk_bf16_f32 v123, v126, v127\n"
        "v_mul_f32 v157, s70, v157\n"
        "v_mfma_f32_16x16x32_bf16 v[196:199], a[236:239], a[196:199], v[196:199]\n"
        "v_cvt_pk_bf16_f32 v154, v156, v157\n"
        "v_mul_f32 v158, s70, v158\n"
        "v_mfma_f32_16x16x32_bf16 v[228:231], a[232:235], a[200:203], v[228:231]\n"
        "ds_read_b64_tr_b16 a[232:233], v28 offset:32776\n"
        "v_mul_f32 v159, s70, v159\n"
        "v_mfma_f32_16x16x32_bf16 v[228:231], a[236:239], a[204:207], v[228:231]\n"
        "ds_read_b64_tr_b16 a[234:235], v28 offset:34824\n"
        "v_cvt_pk_bf16_f32 v155, v158, v159\n"
        "v_mfma_f32_16x16x32_bf16 v[104:107], a[208:211], a[168:171], v[104:107]\n"
        "ds_read_b64_tr_b16 a[236:237], v28 offset:49160\n"
        "v_mul_f32 v188, s70, v188\n"
        "v_mfma_f32_16x16x32_bf16 v[104:107], a[212:215], a[172:175], v[104:107]\n"
        "ds_read_b64_tr_b16 a[238:239], v28 offset:51208\n"
        "v_mul_f32 v189, s70, v189\n"
        "v_mfma_f32_16x16x32_bf16 v[136:139], a[208:211], a[176:179], v[136:139]\n"
        "v_cvt_pk_bf16_f32 v186, v188, v189\n"
        "v_mul_f32 v190, s70, v190\n"
        "v_mfma_f32_16x16x32_bf16 v[136:139], a[212:215], a[180:183], v[136:139]\n"
        "v_mul_f32 v191, s70, v191\n"
        "v_mul_f32 v220, s70, v220\n"
        "v_mfma_f32_16x16x32_bf16 v[168:171], a[208:211], a[184:187], v[168:171]\n"
        "v_cvt_pk_bf16_f32 v187, v190, v191\n"
        "v_mul_f32 v221, s70, v221\n"
        "v_mfma_f32_16x16x32_bf16 v[168:171], a[212:215], a[188:191], v[168:171]\n"
        "v_cvt_pk_bf16_f32 v218, v220, v221\n"
        "v_mul_f32 v222, s70, v222\n"
        "v_mfma_f32_16x16x32_bf16 v[200:203], a[208:211], a[192:195], v[200:203]\n"
        "ds_read_b64_tr_b16 a[208:209], v25 offset:33024\n"
        "v_mul_f32 v223, s70, v223\n"
        "v_mfma_f32_16x16x32_bf16 v[200:203], a[212:215], a[196:199], v[200:203]\n"
        "s_waitcnt lgkmcnt(2)\n"
        "ds_read_b64_tr_b16 a[210:211], v25 offset:35072\n"
        "v_cvt_pk_bf16_f32 v219, v222, v223\n"
        "v_mfma_f32_16x16x32_bf16 v[108:111], a[216:219], a[168:171], v[108:111]\n"
        "ds_read_b64_tr_b16 a[212:213], v25 offset:49408\n"
        "v_mul_f32 v96, s70, v96\n"
        "v_mfma_f32_16x16x32_bf16 v[108:111], a[220:223], a[172:175], v[108:111]\n"
        "ds_read_b64_tr_b16 a[214:215], v25 offset:51456\n"
        "v_mul_f32 v97, s70, v97\n"
        "v_mfma_f32_16x16x32_bf16 v[140:143], a[216:219], a[176:179], v[140:143]\n"
        "v_cvt_pk_bf16_f32 v96, v96, v97\n"
        "v_mul_f32 v98, s70, v98\n"
        "v_mfma_f32_16x16x32_bf16 v[140:143], a[220:223], a[180:183], v[140:143]\n"
        "v_mul_f32 v99, s70, v99\n"
        "v_mul_f32 v128, s70, v128\n"
        "v_mfma_f32_16x16x32_bf16 v[172:175], a[216:219], a[184:187], v[172:175]\n"
        "v_cvt_pk_bf16_f32 v97, v98, v99\n"
        "v_mul_f32 v129, s70, v129\n"
        "v_mfma_f32_16x16x32_bf16 v[172:175], a[220:223], a[188:191], v[172:175]\n"
        "v_cvt_pk_bf16_f32 v128, v128, v129\n"
        "v_mul_f32 v130, s70, v130\n"
        "v_mfma_f32_16x16x32_bf16 v[204:207], a[216:219], a[192:195], v[204:207]\n"
        "ds_read_b64_tr_b16 a[216:217], v25 offset:33032\n"
        "v_mul_f32 v131, s70, v131\n"
        "v_mfma_f32_16x16x32_bf16 v[204:207], a[220:223], a[196:199], v[204:207]\n"
        "ds_read_b64_tr_b16 a[218:219], v25 offset:35080\n"
        "v_cvt_pk_bf16_f32 v129, v130, v131\n"
        "v_mfma_f32_16x16x32_bf16 v[112:115], a[224:227], a[168:171], v[112:115]\n"
        "ds_read_b64_tr_b16 a[220:221], v25 offset:49416\n"
        "v_mul_f32 v160, s70, v160\n"
        "v_mfma_f32_16x16x32_bf16 v[112:115], a[228:231], a[172:175], v[112:115]\n"
        "ds_read_b64_tr_b16 a[222:223], v25 offset:51464\n"
        "v_mul_f32 v161, s70, v161\n"
        "v_mfma_f32_16x16x32_bf16 v[144:147], a[224:227], a[176:179], v[144:147]\n"
        "v_cvt_pk_bf16_f32 v160, v160, v161\n"
        "v_mul_f32 v162, s70, v162\n"
        "v_mfma_f32_16x16x32_bf16 v[144:147], a[228:231], a[180:183], v[144:147]\n"
        "v_mul_f32 v163, s70, v163\n"
        "v_mul_f32 v192, s70, v192\n"
        "v_mfma_f32_16x16x32_bf16 v[176:179], a[224:227], a[184:187], v[176:179]\n"
        "v_cvt_pk_bf16_f32 v161, v162, v163\n"
        "v_mul_f32 v193, s70, v193\n"
        "v_mfma_f32_16x16x32_bf16 v[176:179], a[228:231], a[188:191], v[176:179]\n"
        "v_cvt_pk_bf16_f32 v192, v192, v193\n"
        "v_mul_f32 v194, s70, v194\n"
        "v_mfma_f32_16x16x32_bf16 v[208:211], a[224:227], a[192:195], v[208:211]\n"
        "ds_read_b64_tr_b16 a[224:225], v26 offset:33024\n"
        "v_mul_f32 v195, s70, v195\n"
        "v_mfma_f32_16x16x32_bf16 v[208:211], a[228:231], a[196:199], v[208:211]\n"
        "ds_read_b64_tr_b16 a[226:227], v26 offset:35072\n"
        "v_cvt_pk_bf16_f32 v193, v194, v195\n"
        "v_mfma_f32_16x16x32_bf16 v[116:119], a[232:235], a[168:171], v[116:119]\n"
        "s_waitcnt lgkmcnt(2)\n"
        "ds_read_b64_tr_b16 a[228:229], v26 offset:49408\n"
        "v_mul_f32 v224, s70, v224\n"
        "v_mfma_f32_16x16x32_bf16 v[116:119], a[236:239], a[172:175], v[116:119]\n"
        "ds_read_b64_tr_b16 a[230:231], v26 offset:51456\n"
        "v_mul_f32 v225, s70, v225\n"
        "v_mfma_f32_16x16x32_bf16 v[148:151], a[232:235], a[176:179], v[148:151]\n"
        "v_cvt_pk_bf16_f32 v220, v224, v225\n"
        "v_mul_f32 v226, s70, v226\n"
        "v_mfma_f32_16x16x32_bf16 v[148:151], a[236:239], a[180:183], v[148:151]\n"
        "v_mul_f32 v227, s70, v227\n"
        "v_mul_f32 v100, s70, v100\n"
        "v_mfma_f32_16x16x32_bf16 v[180:183], a[232:235], a[184:187], v[180:183]\n"
        "v_cvt_pk_bf16_f32 v221, v226, v227\n"
        "v_mul_f32 v101, s70, v101\n"
        "v_mfma_f32_16x16x32_bf16 v[180:183], a[236:239], a[188:191], v[180:183]\n"
        "v_cvt_pk_bf16_f32 v98, v100, v101\n"
        "v_mul_f32 v102, s70, v102\n"
        "v_mfma_f32_16x16x32_bf16 v[212:215], a[232:235], a[192:195], v[212:215]\n"
        "ds_read_b64_tr_b16 a[232:233], v26 offset:33032\n"
        "v_mul_f32 v103, s70, v103\n"
        "v_mfma_f32_16x16x32_bf16 v[212:215], a[236:239], a[196:199], v[212:215]\n"
        "ds_read_b64_tr_b16 a[234:235], v26 offset:35080\n"
        "v_cvt_pk_bf16_f32 v99, v102, v103\n"
        "v_mfma_f32_16x16x32_bf16 a[0:3], a[208:211], a[168:171], a[0:3]\n"
        "ds_read_b64_tr_b16 a[236:237], v26 offset:49416\n"
        "v_mul_f32 v132, s70, v132\n"
        "v_mfma_f32_16x16x32_bf16 a[0:3], a[212:215], a[172:175], a[0:3]\n"
        "ds_read_b64_tr_b16 a[238:239], v26 offset:51464\n"
        "v_mul_f32 v133, s70, v133\n"
        "v_mfma_f32_16x16x32_bf16 a[32:35], a[208:211], a[176:179], a[32:35]\n"
        "v_cvt_pk_bf16_f32 v130, v132, v133\n"
        "v_mul_f32 v134, s70, v134\n"
        "v_mfma_f32_16x16x32_bf16 a[32:35], a[212:215], a[180:183], a[32:35]\n"
        "v_mul_f32 v135, s70, v135\n"
        "v_mul_f32 v164, s70, v164\n"
        "v_mfma_f32_16x16x32_bf16 a[64:67], a[208:211], a[184:187], a[64:67]\n"
        "v_cvt_pk_bf16_f32 v131, v134, v135\n"
        "v_mul_f32 v165, s70, v165\n"
        "v_mfma_f32_16x16x32_bf16 a[64:67], a[212:215], a[188:191], a[64:67]\n"
        "v_cvt_pk_bf16_f32 v162, v164, v165\n"
        "v_mul_f32 v166, s70, v166\n"
        "v_mfma_f32_16x16x32_bf16 a[96:99], a[208:211], a[192:195], a[96:99]\n"
        "v_mul_f32 v167, s70, v167\n"
        "v_mul_f32 v196, s70, v196\n"
        "v_mfma_f32_16x16x32_bf16 a[96:99], a[212:215], a[196:199], a[96:99]\n"
        "v_cvt_pk_bf16_f32 v163, v166, v167\n"
        "v_mul_f32 v197, s70, v197\n"
        "v_mfma_f32_16x16x32_bf16 v[68:71], a[208:211], a[200:203], v[68:71]\n"
        "ds_read_b64_tr_b16 a[208:209], v27 offset:33024\n"
        "v_cvt_pk_bf16_f32 v194, v196, v197\n"
        "v_mfma_f32_16x16x32_bf16 v[68:71], a[212:215], a[204:207], v[68:71]\n"
        "ds_read_b64_tr_b16 a[210:211], v27 offset:35072\n"
        "v_mul_f32 v198, s70, v198\n"
        "v_mfma_f32_16x16x32_bf16 a[4:7], a[216:219], a[168:171], a[4:7]\n"
        "ds_read_b64_tr_b16 a[212:213], v27 offset:49408\n"
        "v_mul_f32 v199, s70, v199\n"
        "v_mfma_f32_16x16x32_bf16 a[4:7], a[220:223], a[172:175], a[4:7]\n"
        "ds_read_b64_tr_b16 a[214:215], v27 offset:51456\n"
        "v_cvt_pk_bf16_f32 v195, v198, v199\n"
        "v_mfma_f32_16x16x32_bf16 a[36:39], a[216:219], a[176:179], a[36:39]\n"
        "v_mul_f32 v228, s70, v228\n"
        "v_mul_f32 v229, s70, v229\n"
        "v_mfma_f32_16x16x32_bf16 a[36:39], a[220:223], a[180:183], a[36:39]\n"
        "v_cvt_pk_bf16_f32 v222, v228, v229\n"
        "v_mul_f32 v230, s70, v230\n"
        "v_mfma_f32_16x16x32_bf16 a[68:71], a[216:219], a[184:187], a[68:71]\n"
        "v_mul_f32 v231, s70, v231\n"
        "v_mul_f32 v104, s70, v104\n"
        "v_mfma_f32_16x16x32_bf16 a[68:71], a[220:223], a[188:191], a[68:71]\n"
        "v_cvt_pk_bf16_f32 v223, v230, v231\n"
        "v_mul_f32 v105, s70, v105\n"
        "v_mfma_f32_16x16x32_bf16 a[100:103], a[216:219], a[192:195], a[100:103]\n"
        "v_cvt_pk_bf16_f32 v104, v104, v105\n"
        "v_mul_f32 v106, s70, v106\n"
        "v_mfma_f32_16x16x32_bf16 a[100:103], a[220:223], a[196:199], a[100:103]\n"
        "v_mul_f32 v107, s70, v107\n"
        "v_mul_f32 v136, s70, v136\n"
        "v_mfma_f32_16x16x32_bf16 v[72:75], a[216:219], a[200:203], v[72:75]\n"
        "ds_read_b64_tr_b16 a[216:217], v27 offset:33032\n"
        "v_cvt_pk_bf16_f32 v105, v106, v107\n"
        "v_mfma_f32_16x16x32_bf16 v[72:75], a[220:223], a[204:207], v[72:75]\n"
        "s_waitcnt lgkmcnt(1)\n"
        "ds_read_b64_tr_b16 a[218:219], v27 offset:35080\n"
        "v_mul_f32 v137, s70, v137\n"
        "v_mfma_f32_16x16x32_bf16 a[8:11], a[224:227], a[168:171], a[8:11]\n"
        "ds_read_b64_tr_b16 a[220:221], v27 offset:49416\n"
        "v_cvt_pk_bf16_f32 v136, v136, v137\n"
        "v_mfma_f32_16x16x32_bf16 a[8:11], a[228:231], a[172:175], a[8:11]\n"
        "ds_read_b64_tr_b16 a[222:223], v27 offset:51464\n"
        "v_mul_f32 v138, s70, v138\n"
        "v_mfma_f32_16x16x32_bf16 a[40:43], a[224:227], a[176:179], a[40:43]\n"
        "v_mul_f32 v139, s70, v139\n"
        "v_mul_f32 v168, s70, v168\n"
        "v_mfma_f32_16x16x32_bf16 a[40:43], a[228:231], a[180:183], a[40:43]\n"
        "v_cvt_pk_bf16_f32 v137, v138, v139\n"
        "v_mul_f32 v169, s70, v169\n"
        "v_mfma_f32_16x16x32_bf16 a[72:75], a[224:227], a[184:187], a[72:75]\n"
        "v_cvt_pk_bf16_f32 v168, v168, v169\n"
        "v_mul_f32 v170, s70, v170\n"
        "v_mfma_f32_16x16x32_bf16 a[72:75], a[228:231], a[188:191], a[72:75]\n"
        "v_mul_f32 v171, s70, v171\n"
        "v_mul_f32 v200, s70, v200\n"
        "v_mfma_f32_16x16x32_bf16 a[104:107], a[224:227], a[192:195], a[104:107]\n"
        "v_cvt_pk_bf16_f32 v169, v170, v171\n"
        "v_mul_f32 v201, s70, v201\n"
        "v_mfma_f32_16x16x32_bf16 a[104:107], a[228:231], a[196:199], a[104:107]\n"
        "v_cvt_pk_bf16_f32 v200, v200, v201\n"
        "v_mul_f32 v202, s70, v202\n"
        "v_mfma_f32_16x16x32_bf16 v[76:79], a[224:227], a[200:203], v[76:79]\n"
        "ds_read_b64_tr_b16 a[224:225], v28 offset:33024\n"
        "v_mul_f32 v203, s70, v203\n"
        "v_mfma_f32_16x16x32_bf16 v[76:79], a[228:231], a[204:207], v[76:79]\n"
        "ds_read_b64_tr_b16 a[226:227], v28 offset:35072\n"
        "v_cvt_pk_bf16_f32 v201, v202, v203\n"
        "v_mfma_f32_16x16x32_bf16 a[12:15], a[232:235], a[168:171], a[12:15]\n"
        "ds_read_b64_tr_b16 a[228:229], v28 offset:49408\n"
        "v_mul_f32 v108, s70, v108\n"
        "v_mfma_f32_16x16x32_bf16 a[12:15], a[236:239], a[172:175], a[12:15]\n"
        "ds_read_b64_tr_b16 a[230:231], v28 offset:51456\n"
        "v_mul_f32 v109, s70, v109\n"
        "v_mfma_f32_16x16x32_bf16 a[44:47], a[232:235], a[176:179], a[44:47]\n"
        "v_cvt_pk_bf16_f32 v106, v108, v109\n"
        "v_mul_f32 v110, s70, v110\n"
        "v_mfma_f32_16x16x32_bf16 a[44:47], a[236:239], a[180:183], a[44:47]\n"
        "v_mul_f32 v111, s70, v111\n"
        "v_mul_f32 v140, s70, v140\n"
        "v_mfma_f32_16x16x32_bf16 a[76:79], a[232:235], a[184:187], a[76:79]\n"
        "v_cvt_pk_bf16_f32 v107, v110, v111\n"
        "v_mul_f32 v141, s70, v141\n"
        "v_mfma_f32_16x16x32_bf16 a[76:79], a[236:239], a[188:191], a[76:79]\n"
        "v_cvt_pk_bf16_f32 v138, v140, v141\n"
        "v_mul_f32 v142, s70, v142\n"
        "v_mfma_f32_16x16x32_bf16 a[108:111], a[232:235], a[192:195], a[108:111]\n"
        "v_mul_f32 v143, s70, v143\n"
        "v_mul_f32 v172, s70, v172\n"
        "v_mfma_f32_16x16x32_bf16 a[108:111], a[236:239], a[196:199], a[108:111]\n"
        "v_cvt_pk_bf16_f32 v139, v142, v143\n"
        "v_mul_f32 v173, s70, v173\n"
        "v_mfma_f32_16x16x32_bf16 v[80:83], a[232:235], a[200:203], v[80:83]\n"
        "ds_read_b64_tr_b16 a[232:233], v28 offset:33032\n"
        "v_cvt_pk_bf16_f32 v170, v172, v173\n"
        "v_mfma_f32_16x16x32_bf16 v[80:83], a[236:239], a[204:207], v[80:83]\n"
        "ds_read_b64_tr_b16 a[234:235], v28 offset:35080\n"
        "v_mul_f32 v174, s70, v174\n"
        "v_mfma_f32_16x16x32_bf16 a[16:19], a[208:211], a[168:171], a[16:19]\n"
        "ds_read_b64_tr_b16 a[236:237], v28 offset:49416\n"
        "v_mul_f32 v175, s70, v175\n"
        "v_mfma_f32_16x16x32_bf16 a[16:19], a[212:215], a[172:175], a[16:19]\n"
        "ds_read_b64_tr_b16 a[238:239], v28 offset:51464\n"
        "v_cvt_pk_bf16_f32 v171, v174, v175\n"
        "v_mfma_f32_16x16x32_bf16 a[48:51], a[208:211], a[176:179], a[48:51]\n"
        "v_mul_f32 v204, s70, v204\n"
        "v_mul_f32 v205, s70, v205\n"
        "v_mfma_f32_16x16x32_bf16 a[48:51], a[212:215], a[180:183], a[48:51]\n"
        "v_cvt_pk_bf16_f32 v202, v204, v205\n"
        "v_mul_f32 v206, s70, v206\n"
        "v_mfma_f32_16x16x32_bf16 a[80:83], a[208:211], a[184:187], a[80:83]\n"
        "v_mul_f32 v207, s70, v207\n"
        "v_mul_f32 v112, s70, v112\n"
        "v_mfma_f32_16x16x32_bf16 a[80:83], a[212:215], a[188:191], a[80:83]\n"
        "v_cvt_pk_bf16_f32 v203, v206, v207\n"
        "v_mul_f32 v113, s70, v113\n"
        "v_mfma_f32_16x16x32_bf16 a[112:115], a[208:211], a[192:195], a[112:115]\n"
        "v_cvt_pk_bf16_f32 v112, v112, v113\n"
        "v_mul_f32 v114, s70, v114\n"
        "v_mfma_f32_16x16x32_bf16 a[112:115], a[212:215], a[196:199], a[112:115]\n"
        "s_waitcnt lgkmcnt(1)\n"
        "v_mul_f32 v115, s70, v115\n"
        "v_mul_f32 v144, s70, v144\n"
        "v_mfma_f32_16x16x32_bf16 a[20:23], a[216:219], a[168:171], a[20:23]\n"
        "v_cvt_pk_bf16_f32 v113, v114, v115\n"
        "v_mul_f32 v145, s70, v145\n"
        "v_mfma_f32_16x16x32_bf16 a[20:23], a[220:223], a[172:175], a[20:23]\n"
        "v_cvt_pk_bf16_f32 v144, v144, v145\n"
        "v_mul_f32 v146, s70, v146\n"
        "v_mfma_f32_16x16x32_bf16 a[52:55], a[216:219], a[176:179], a[52:55]\n"
        "v_mul_f32 v147, s70, v147\n"
        "v_mul_f32 v116, s70, v116\n"
        "v_mfma_f32_16x16x32_bf16 a[52:55], a[220:223], a[180:183], a[52:55]\n"
        "v_cvt_pk_bf16_f32 v145, v146, v147\n"
        "v_mul_f32 v117, s70, v117\n"
        "v_mfma_f32_16x16x32_bf16 a[84:87], a[216:219], a[184:187], a[84:87]\n"
        "v_cvt_pk_bf16_f32 v114, v116, v117\n"
        "v_mul_f32 v118, s70, v118\n"
        "v_mfma_f32_16x16x32_bf16 a[84:87], a[220:223], a[188:191], a[84:87]\n"
        "v_mul_f32 v119, s70, v119\n"
        "v_mul_f32 v148, s70, v148\n"
        "v_mfma_f32_16x16x32_bf16 a[116:119], a[216:219], a[192:195], a[116:119]\n"
        "v_cvt_pk_bf16_f32 v115, v118, v119\n"
        "v_mul_f32 v149, s70, v149\n"
        "v_mfma_f32_16x16x32_bf16 a[116:119], a[220:223], a[196:199], a[116:119]\n"
        "v_cvt_pk_bf16_f32 v146, v148, v149\n"
        "v_mul_f32 v150, s70, v150\n"
        "v_mfma_f32_16x16x32_bf16 a[24:27], a[224:227], a[168:171], a[24:27]\n"
        "v_mul_f32 v151, s70, v151\n"
        "v_mfma_f32_16x16x32_bf16 a[24:27], a[228:231], a[172:175], a[24:27]\n"
        "v_cvt_pk_bf16_f32 v147, v150, v151\n"
        "v_mfma_f32_16x16x32_bf16 a[56:59], a[224:227], a[176:179], a[56:59]\n"
        "v_mfma_f32_16x16x32_bf16 a[56:59], a[228:231], a[180:183], a[56:59]\n"
        "v_mfma_f32_16x16x32_bf16 a[88:91], a[224:227], a[184:187], a[88:91]\n"
        "v_mfma_f32_16x16x32_bf16 a[88:91], a[228:231], a[188:191], a[88:91]\n"
        "v_mfma_f32_16x16x32_bf16 a[120:123], a[224:227], a[192:195], a[120:123]\n"
        "v_mfma_f32_16x16x32_bf16 a[120:123], a[228:231], a[196:199], a[120:123]\n"
        "v_mfma_f32_16x16x32_bf16 a[28:31], a[232:235], a[168:171], a[28:31]\n"
        "s_waitcnt lgkmcnt(0)\n"
        "v_mfma_f32_16x16x32_bf16 a[28:31], a[236:239], a[172:175], a[28:31]\n"
        "v_mfma_f32_16x16x32_bf16 a[60:63], a[232:235], a[176:179], a[60:63]\n"
        "v_mfma_f32_16x16x32_bf16 a[60:63], a[236:239], a[180:183], a[60:63]\n"
        "v_mfma_f32_16x16x32_bf16 a[92:95], a[232:235], a[184:187], a[92:95]\n"
        "s_waitcnt vmcnt(9)\n"
        "s_barrier\n"
        "v_mfma_f32_16x16x32_bf16 a[92:95], a[236:239], a[188:191], a[92:95]\n"
        "v_mfma_f32_16x16x32_bf16 a[124:127], a[232:235], a[192:195], a[124:127]\n"
        "v_mfma_f32_16x16x32_bf16 a[124:127], a[236:239], a[196:199], a[124:127]\n"
        ".Lk_QEPI:\n"
        "v_readfirstlane_b32 s94, v37\n"
        "v_mov_b32 v84, 0\n"
        "v_mov_b32 v85, 0\n"
        "v_pk_mul_f32 v[176:177], v[176:177], s[70:71]\n"
        "v_pk_mul_f32 v[178:179], v[178:179], s[70:71]\n"
        "v_pk_mul_f32 v[180:181], v[180:181], s[70:71]\n"
        "v_pk_mul_f32 v[182:183], v[182:183], s[70:71]\n"
        "v_pk_mul_f32 v[208:209], v[208:209], s[70:71]\n"
        "v_pk_mul_f32 v[210:211], v[210:211], s[70:71]\n"
        "v_pk_mul_f32 v[212:213], v[212:213], s[70:71]\n"
        "v_pk_mul_f32 v[214:215], v[214:215], s[70:71]\n"
        "v_pk_mul_f32 v[68:69], v[68:69], s[70:71]\n"
        "v_pk_mul_f32 v[70:71], v[70:71], s[70:71]\n"
        "v_pk_mul_f32 v[72:73], v[72:73], s[70:71]\n"
        "v_pk_mul_f32 v[74:75], v[74:75], s[70:71]\n"
        "v_pk_mul_f32 v[76:77], v[76:77], s[70:71]\n"
        "v_pk_mul_f32 v[78:79], v[78:79], s[70:71]\n"
        "v_pk_mul_f32 v[80:81], v[80:81], s[70:71]\n"
        "v_pk_mul_f32 v[82:83], v[82:83], s[70:71]\n"
        "s_mul_i32 s95, s94, 0x9e3779b1\n"
        "s_add_u32 s95, s95, 0x7f4a7c15\n"
        "v_mov_b32 v86, 0\n"
        "v_mov_b32 v87, 0\n"
        "ds_read_b128 a[180:183], v232 offset:0\n"
        "v_cvt_pk_bf16_f32 v176, v176, v177\n"
        "v_mfma_f32_16x16x32_bf16 a[128:131], v[84:87], v[84:87], 0\n"
        "ds_read_b128 a[184:187], v234 offset:0\n"
        "v_mfma_f32_16x16x32_bf16 a[132:135], v[84:87], v[84:87], 0\n"
        "v_cvt_pk_bf16_f32 v177, v178, v179\n"
        "ds_read_b128 a[188:191], v236 offset:0\n"
        "v_mfma_f32_16x16x32_bf16 a[136:139], v[84:87], v[84:87], 0\n"
        "v_cvt_pk_bf16_f32 v178, v180, v181\n"
        "ds_read_b128 a[192:195], v238 offset:0\n"
        "v_mfma_f32_16x16x32_bf16 a[140:143], v[84:87], v[84:87], 0\n"
        "v_cvt_pk_bf16_f32 v179, v182, v183\n"
        "ds_read_b128 a[212:215], v233 offset:0\n"
        "v_mfma_f32_16x16x32_bf16 a[144:147], v[84:87], v[84:87], 0\n"
        "v_accvgpr_read_b32 v52, a0\n"
        "ds_read_b128 a[216:219], v235 offset:0\n"
        "v_mfma_f32_16x16x32_bf16 a[148:151], v[84:87], v[84:87], 0\n"
        "v_accvgpr_read_b32 v53, a1\n"
        "ds_read_b128 a[220:223], v237 offset:0\n"
        "v_mfma_f32_16x16x32_bf16 a[152:155], v[84:87], v[84:87], 0\n"
        "v_accvgpr_read_b32 v54, a2\n"
        "ds_read_b128 a[224:227], v239 offset:0\n"
        "v_mfma_f32_16x16x32_bf16 a[156:159], v[84:87], v[84:87], 0\n"
        "v_accvgpr_read_b32 v55, a3\n"
        "v_mfma_f32_16x16x32_bf16 a[160:163], v[84:87], v[84:87], 0\n"
        "ds_read_b128 a[196:199], v232 offset:4096\n"
        "v_accvgpr_read_b32 v56, a4\n"
        "v_mfma_f32_16x16x32_bf16 a[164:167], v[84:87], v[84:87], 0\n"
        "ds_read_b128 a[200:203], v234 offset:4096\n"
        "v_accvgpr_read_b32 v57, a5\n"
        "s_waitcnt lgkmcnt(9)\n"
        "v_mfma_f32_16x16x32_bf16 v[44:47], a[180:183], v[216:219], 0\n"
        "ds_read_b128 a[204:207], v236 offset:4096\n"
        "v_accvgpr_read_b32 v58, a6\n"
        "v_mfma_f32_16x16x32_bf16 v[28:31], a[180:183], v[88:91], 0\n"
        "ds_read_b128 a[208:211], v238 offset:4096\n"
        "v_accvgpr_read_b32 v59, a7\n"
        "v_mfma_f32_16x16x32_bf16 v[32:35], a[180:183], v[120:123], 0\n"
        "ds_read_b128 a[228:231], v233 offset:4096\n"
        "v_accvgpr_read_b32 v60, a8\n"
        "v_mfma_f32_16x16x32_bf16 v[36:39], a[180:183], v[152:155], 0\n"
        "ds_read_b128 a[232:235], v235 offset:4096\n"
        "v_accvgpr_read_b32 v61, a9\n"
        "s_waitcnt lgkmcnt(9)\n"
        "v_mfma_f32_16x16x32_bf16 v[48:51], a[212:215], v[216:219], 0\n"
        "ds_read_b128 a[236:239], v237 offset:4096\n"
        "v_mfma_f32_16x16x32_bf16 v[40:43], a[180:183], v[184:187], 0\n"
        "v_accvgpr_read_b32 v62, a10\n"
        "ds_read_b128 a[240:243], v239 offset:4096\n"
        "v_mfma_f32_16x16x32_bf16 v[44:47], a[184:187], v[220:223], v[44:47]\n"
        "v_accvgpr_read_b32 v63, a11\n"
        "ds_write_b128 v252, v[216:219] offset:12288\n"
        "s_waitcnt lgkmcnt(11)\n"
        "v_mfma_f32_16x16x32_bf16 v[48:51], a[216:219], v[220:223], v[48:51]\n"
        "s_mov_b32 s67, 32768\n"
        "s_add_u32 m0, s65, 98304\n"
        "s_nop 0\n"
        "buffer_load_dwordx4 v244, s[48:51], s67 offen offset:0 nt lds\n"
        "v_mfma_f32_16x16x32_bf16 v[28:31], a[184:187], v[96:99], v[28:31]\n"
        "v_accvgpr_read_b32 v64, a12\n"
        "v_mfma_f32_16x16x32_bf16 v[32:35], a[184:187], v[128:131], v[32:35]\n"
        "v_accvgpr_read_b32 v65, a13\n"
        "ds_write_b128 v252, v[220:223] offset:13312\n"
        "v_mfma_f32_16x16x32_bf16 v[36:39], a[184:187], v[160:163], v[36:39]\n"
        "v_accvgpr_read_b32 v66, a14\n"
        "v_accvgpr_read_b32 v67, a15\n"
        "v_cvt_pk_bf16_f32 v208, v208, v209\n"
        "v_pk_mul_f32 v[52:53], v[52:53], s[70:71]\n"
        "v_pk_mul_f32 v[54:55], v[54:55], s[70:71]\n"
        "v_pk_mul_f32 v[56:57], v[56:57], s[70:71]\n"
        "v_pk_mul_f32 v[58:59], v[58:59], s[70:71]\n"
        "v_pk_mul_f32 v[60:61], v[60:61], s[70:71]\n"
        "v_pk_mul_f32 v[62:63], v[62:63], s[70:71]\n"
        "v_pk_mul_f32 v[64:65], v[64:65], s[70:71]\n"
        "v_pk_mul_f32 v[66:67], v[66:67], s[70:71]\n"
        "v_cvt_pk_bf16_f32 v209, v210, v211\n"
        "v_mfma_f32_16x16x32_bf16 v[40:43], a[184:187], v[192:195], v[40:43]\n"
        "v_cvt_pk_bf16_f32 v210, v212, v213\n"
        "v_mfma_f32_16x16x32_bf16 v[28:31], a[188:191], v[104:107], v[28:31]\n"
        "v_cvt_pk_bf16_f32 v211, v214, v215\n"
        "v_cvt_pk_bf16_f32 v52, v52, v53\n"
        "v_mfma_f32_16x16x32_bf16 v[32:35], a[188:191], v[136:139], v[32:35]\n"
        "v_cvt_pk_bf16_f32 v53, v54, v55\n"
        "v_cvt_pk_bf16_f32 v54, v56, v57\n"
        "v_mfma_f32_16x16x32_bf16 v[36:39], a[188:191], v[168:171], v[36:39]\n"
        "v_cvt_pk_bf16_f32 v55, v58, v59\n"
        "v_cvt_pk_bf16_f32 v68, v68, v69\n"
        "v_mfma_f32_16x16x32_bf16 v[40:43], a[188:191], v[200:203], v[40:43]\n"
        "v_cvt_pk_bf16_f32 v69, v70, v71\n"
        "s_waitcnt lgkmcnt(0)\n"
        "s_barrier\n"
        "v_mfma_f32_16x16x32_bf16 v[28:31], a[192:195], v[112:115], v[28:31]\n"
        "ds_read_b128 v[224:227], v251 offset:12288\n"
        "v_cvt_pk_bf16_f32 v70, v72, v73\n"
        "v_mfma_f32_16x16x32_bf16 v[32:35], a[192:195], v[144:147], v[32:35]\n"
        "ds_read_b128 v[228:231], v251 offset:13312\n"
        "v_cvt_pk_bf16_f32 v71, v74, v75\n"
        "v_mfma_f32_16x16x32_bf16 v[36:39], a[192:195], v[176:179], v[36:39]\n"
        "ds_read_b64_tr_b16 v[92:93], v240 offset:32768\n"
        "v_cvt_pk_bf16_f32 v72, v76, v77\n"
        "v_mfma_f32_16x16x32_bf16 v[40:43], a[192:195], v[208:211], v[40:43]\n"
        "ds_read_b64_tr_b16 v[94:95], v240 offset:36864\n"
        "v_cvt_pk_bf16_f32 v73, v78, v79\n"
        "v_mfma_f32_16x16x32_bf16 a[168:171], v[84:87], v[84:87], 0\n"
        "ds_read_b64_tr_b16 v[108:109], v241 offset:32768\n"
        "v_mfma_f32_16x16x32_bf16 a[172:175], v[84:87], v[84:87], 0\n"
        "ds_read_b64_tr_b16 v[110:111], v241 offset:36864\n"
        "v_cvt_pk_bf16_f32 v74, v80, v81\n"
        "v_mfma_f32_16x16x32_bf16 a[176:179], v[84:87], v[84:87], 0\n"
        "buffer_load_dwordx4 v245, s[48:51], s67 offen offset:1024 nt lds\n"
        "ds_read_b64_tr_b16 v[100:101], v242 offset:32768\n"
        "v_mfma_f32_16x16x32_bf16 a[0:3], v[84:87], v[84:87], 0\n"
        "ds_read_b64_tr_b16 v[102:103], v242 offset:36864\n"
        "v_cvt_pk_bf16_f32 v75, v82, v83\n"
        "v_mfma_f32_16x16x32_bf16 a[4:7], v[84:87], v[84:87], 0\n"
        "ds_read_b64_tr_b16 v[116:117], v243 offset:32768\n"
        "v_accvgpr_write_b32 a244, v68\n"
        "v_mfma_f32_16x16x32_bf16 a[8:11], v[84:87], v[84:87], 0\n"
        "ds_read_b64_tr_b16 v[118:119], v243 offset:36864\n"
        "v_accvgpr_write_b32 a245, v69\n"
        "v_mfma_f32_16x16x32_bf16 a[12:15], v[84:87], v[84:87], 0\n"
        "ds_read_b64_tr_b16 v[124:125], v240 offset:33280\n"
        "v_accvgpr_write_b32 a246, v70\n"
        "s_waitcnt lgkmcnt(10)\n"
        "v_mfma_f32_16x16x32_bf16 v[44:47], a[188:191], v[224:227], v[44:47]\n"
        "ds_read_b64_tr_b16 v[126:127], v240 offset:37376\n"
        "v_mfma_f32_16x16x32_bf16 v[48:51], a[220:223], v[224:227], v[48:51]\n"
        "ds_read_b64_tr_b16 v[140:141], v241 offset:33280\n"
        "v_accvgpr_write_b32 a247, v71\n"
        "s_waitcnt lgkmcnt(11)\n"
        "v_mfma_f32_16x16x32_bf16 v[44:47], a[192:195], v[228:231], v[44:47]\n"
        "ds_read_b64_tr_b16 v[142:143], v241 offset:37376\n"
        "v_accvgpr_write_b32 a248, v72\n"
        "v_mfma_f32_16x16x32_bf16 v[48:51], a[224:227], v[228:231], v[48:51]\n"
        "ds_read_b64_tr_b16 v[132:133], v242 offset:33280\n"
        "v_accvgpr_write_b32 a249, v73\n"
        "ds_read_b64_tr_b16 v[134:135], v242 offset:37376\n"
        "v_accvgpr_write_b32 a250, v74\n"
        "ds_read_b64_tr_b16 v[148:149], v243 offset:33280\n"
        "v_accvgpr_write_b32 a251, v75\n"
        "ds_read_b64_tr_b16 v[150:151], v243 offset:37376\n"
        "v_accvgpr_read_b32 v68, a16\n"
        "ds_read_b64_tr_b16 v[156:157], v240 offset:33792\n"
        "v_accvgpr_read_b32 v69, a17\n"
        "ds_read_b64_tr_b16 v[158:159], v240 offset:37888\n"
        "v_accvgpr_read_b32 v70, a18\n"
        "ds_read_b64_tr_b16 v[172:173], v241 offset:33792\n"
        "v_accvgpr_read_b32 v71, a19\n"
        "v_mfma_f32_16x16x32_bf16 a[16:19], v[84:87], v[84:87], 0\n"
        "ds_read_b64_tr_b16 v[174:175], v241 offset:37888\n"
        "v_accvgpr_read_b32 v72, a20\n"
        "ds_read_b64_tr_b16 v[164:165], v242 offset:33792\n"
        "v_accvgpr_read_b32 v73, a21\n"
        "ds_read_b64_tr_b16 v[166:167], v242 offset:37888\n"
        "v_accvgpr_read_b32 v74, a22\n"
        "ds_read_b64_tr_b16 v[180:181], v243 offset:33792\n"
        "v_accvgpr_read_b32 v75, a23\n"
        "v_mfma_f32_16x16x32_bf16 a[20:23], v[84:87], v[84:87], 0\n"
        "ds_read_b64_tr_b16 v[182:183], v243 offset:37888\n"
        "v_accvgpr_read_b32 v76, a24\n"
        "ds_read_b64_tr_b16 v[188:189], v240 offset:34304\n"
        "v_accvgpr_read_b32 v77, a25\n"
        "ds_read_b64_tr_b16 v[190:191], v240 offset:38400\n"
        "v_accvgpr_read_b32 v78, a26\n"
        "buffer_load_dwordx4 v246, s[48:51], s67 offen offset:2048 nt lds\n"
        "ds_read_b64_tr_b16 v[204:205], v241 offset:34304\n"
        "v_accvgpr_read_b32 v79, a27\n"
        "v_mfma_f32_16x16x32_bf16 a[24:27], v[84:87], v[84:87], 0\n"
        "ds_read_b64_tr_b16 v[206:207], v241 offset:38400\n"
        "v_accvgpr_read_b32 v80, a28\n"
        "ds_read_b64_tr_b16 v[196:197], v242 offset:34304\n"
        "v_accvgpr_read_b32 v81, a29\n"
        "ds_read_b64_tr_b16 v[198:199], v242 offset:38400\n"
        "v_accvgpr_read_b32 v82, a30\n"
        "ds_read_b64_tr_b16 v[212:213], v243 offset:34304\n"
        "v_accvgpr_read_b32 v83, a31\n"
        "v_pk_mul_f32 v[68:69], v[68:69], s[70:71]\n"
        "v_pk_mul_f32 v[70:71], v[70:71], s[70:71]\n"
        "v_pk_mul_f32 v[72:73], v[72:73], s[70:71]\n"
        "v_pk_mul_f32 v[74:75], v[74:75], s[70:71]\n"
        "v_pk_mul_f32 v[76:77], v[76:77], s[70:71]\n"
        "v_pk_mul_f32 v[78:79], v[78:79], s[70:71]\n"
        "v_pk_mul_f32 v[80:81], v[80:81], s[70:71]\n"
        "v_pk_mul_f32 v[82:83], v[82:83], s[70:71]\n"
        "ds_read_b64_tr_b16 v[214:215], v243 offset:38400\n"
        "v_mfma_f32_16x16x32_bf16 a[28:31], v[84:87], v[84:87], 0\n"
        "ds_write_b128 v251, v[52:55] offset:0\n"
        "v_cvt_pk_bf16_f32 v56, v60, v61\n"
        "v_cvt_pk_bf16_f32 v57, v62, v63\n"
        "v_cvt_pk_bf16_f32 v58, v64, v65\n"
        "v_cvt_pk_bf16_f32 v59, v66, v67\n"
        "v_cvt_pk_bf16_f32 v68, v68, v69\n"
        "v_cvt_pk_bf16_f32 v69, v70, v71\n"
        "v_cvt_pk_bf16_f32 v70, v72, v73\n"
        "v_cvt_pk_bf16_f32 v71, v74, v75\n"
        "ds_write_b128 v251, v[56:59] offset:1024\n"
        "v_accvgpr_read_b32 v52, a32\n"
        "v_accvgpr_read_b32 v53, a33\n"
        "v_accvgpr_read_b32 v54, a34\n"
        "v_accvgpr_read_b32 v55, a35\n"
        "v_mfma_f32_16x16x32_bf16 a[32:35], v[84:87], v[84:87], 0\n"
        "v_accvgpr_read_b32 v56, a36\n"
        "v_accvgpr_read_b32 v57, a37\n"
        "v_accvgpr_read_b32 v58, a38\n"
        "ds_write_b128 v251, v[68:71] offset:2048\n"
        "v_accvgpr_read_b32 v59, a39\n"
        "v_mfma_f32_16x16x32_bf16 a[36:39], v[84:87], v[84:87], 0\n"
        "v_accvgpr_read_b32 v60, a40\n"
        "v_accvgpr_read_b32 v61, a41\n"
        "v_accvgpr_read_b32 v62, a42\n"
        "v_accvgpr_read_b32 v63, a43\n"
        "v_mfma_f32_16x16x32_bf16 a[40:43], v[84:87], v[84:87], 0\n"
        "v_accvgpr_read_b32 v64, a44\n"
        "v_accvgpr_read_b32 v65, a45\n"
        "v_accvgpr_read_b32 v66, a46\n"
        "v_accvgpr_read_b32 v67, a47\n"
        "v_pk_mul_f32 v[52:53], v[52:53], s[70:71]\n"
        "v_pk_mul_f32 v[54:55], v[54:55], s[70:71]\n"
        "v_pk_mul_f32 v[56:57], v[56:57], s[70:71]\n"
        "v_pk_mul_f32 v[58:59], v[58:59], s[70:71]\n"
        "v_pk_mul_f32 v[60:61], v[60:61], s[70:71]\n"
        "v_pk_mul_f32 v[62:63], v[62:63], s[70:71]\n"
        "v_pk_mul_f32 v[64:65], v[64:65], s[70:71]\n"
        "v_pk_mul_f32 v[66:67], v[66:67], s[70:71]\n"
        "buffer_load_dwordx4 v247, s[48:51], s67 offen offset:3072 nt lds\n"
        "v_mfma_f32_16x16x32_bf16 a[44:47], v[84:87], v[84:87], 0\n"
        "v_cvt_pk_bf16_f32 v72, v76, v77\n"
        "v_cvt_pk_bf16_f32 v73, v78, v79\n"
        "v_cvt_pk_bf16_f32 v74, v80, v81\n"
        "v_cvt_pk_bf16_f32 v75, v82, v83\n"
        "ds_write_b128 v251, v[72:75] offset:3072\n"
        "v_cvt_pk_bf16_f32 v52, v52, v53\n"
        "v_cvt_pk_bf16_f32 v53, v54, v55\n"
        "v_cvt_pk_bf16_f32 v54, v56, v57\n"
        "v_cvt_pk_bf16_f32 v55, v58, v59\n"
        "v_accvgpr_read_b32 v68, a48\n"
        "v_accvgpr_read_b32 v69, a49\n"
        "v_accvgpr_read_b32 v70, a50\n"
        "v_accvgpr_read_b32 v71, a51\n"
        "v_mfma_f32_16x16x32_bf16 a[48:51], v[84:87], v[84:87], 0\n"
        "ds_write_b128 v251, v[52:55] offset:4096\n"
        "v_accvgpr_read_b32 v72, a52\n"
        "v_accvgpr_read_b32 v73, a53\n"
        "v_accvgpr_read_b32 v74, a54\n"
        "v_accvgpr_read_b32 v75, a55\n"
        "v_mfma_f32_16x16x32_bf16 a[52:55], v[84:87], v[84:87], 0\n"
        "v_accvgpr_read_b32 v76, a56\n"
        "v_accvgpr_read_b32 v77, a57\n"
        "v_accvgpr_read_b32 v78, a58\n"
        "v_accvgpr_read_b32 v79, a59\n"
        "v_mfma_f32_16x16x32_bf16 a[56:59], v[84:87], v[84:87], 0\n"
        "v_accvgpr_read_b32 v80, a60\n"
        "v_accvgpr_read_b32 v81, a61\n"
        "v_accvgpr_read_b32 v82, a62\n"
        "v_accvgpr_read_b32 v83, a63\n"
        "v_pk_mul_f32 v[68:69], v[68:69], s[70:71]\n"
        "v_pk_mul_f32 v[70:71], v[70:71], s[70:71]\n"
        "v_pk_mul_f32 v[72:73], v[72:73], s[70:71]\n"
        "v_pk_mul_f32 v[74:75], v[74:75], s[70:71]\n"
        "v_pk_mul_f32 v[76:77], v[76:77], s[70:71]\n"
        "v_pk_mul_f32 v[78:79], v[78:79], s[70:71]\n"
        "v_pk_mul_f32 v[80:81], v[80:81], s[70:71]\n"
        "v_pk_mul_f32 v[82:83], v[82:83], s[70:71]\n"
        "v_cvt_pk_bf16_f32 v56, v60, v61\n"
        "v_mfma_f32_16x16x32_bf16 a[60:63], v[84:87], v[84:87], 0\n"
        "v_cvt_pk_bf16_f32 v57, v62, v63\n"
        "v_cvt_pk_bf16_f32 v58, v64, v65\n"
        "v_cvt_pk_bf16_f32 v59, v66, v67\n"
        "ds_write_b128 v251, v[56:59] offset:5120\n"
        "v_cvt_pk_bf16_f32 v68, v68, v69\n"
        "v_cvt_pk_bf16_f32 v69, v70, v71\n"
        "s_mov_b32 s67, 32768\n"
        "s_add_u32 m0, s65, 131072\n"
        "s_nop 0\n"
        "buffer_load_dwordx4 v248, s[52:55], s67 offen offset:0 nt lds\n"
        "v_cvt_pk_bf16_f32 v70, v72, v73\n"
        "v_cvt_pk_bf16_f32 v71, v74, v75\n"
        "v_accvgpr_read_b32 v52, a64\n"
        "ds_write_b128 v251, v[68:71] offset:6144\n"
        "v_accvgpr_read_b32 v53, a65\n"
        "v_accvgpr_read_b32 v54, a66\n"
        "v_accvgpr_read_b32 v55, a67\n"
        "v_mfma_f32_16x16x32_bf16 a[64:67], v[84:87], v[84:87], 0\n"
        "v_accvgpr_read_b32 v56, a68\n"
        "v_accvgpr_read_b32 v57, a69\n"
        "v_accvgpr_read_b32 v58, a70\n"
        "v_accvgpr_read_b32 v59, a71\n"
        "v_mfma_f32_16x16x32_bf16 a[68:71], v[84:87], v[84:87], 0\n"
        "v_accvgpr_read_b32 v60, a72\n"
        "v_accvgpr_read_b32 v61, a73\n"
        "v_accvgpr_read_b32 v62, a74\n"
        "v_accvgpr_read_b32 v63, a75\n"
        "v_mfma_f32_16x16x32_bf16 a[72:75], v[84:87], v[84:87], 0\n"
        "v_accvgpr_read_b32 v64, a76\n"
        "v_accvgpr_read_b32 v65, a77\n"
        "v_accvgpr_read_b32 v66, a78\n"
        "v_accvgpr_read_b32 v67, a79\n"
        "v_pk_mul_f32 v[52:53], v[52:53], s[70:71]\n"
        "v_pk_mul_f32 v[54:55], v[54:55], s[70:71]\n"
        "v_pk_mul_f32 v[56:57], v[56:57], s[70:71]\n"
        "v_pk_mul_f32 v[58:59], v[58:59], s[70:71]\n"
        "v_pk_mul_f32 v[60:61], v[60:61], s[70:71]\n"
        "v_pk_mul_f32 v[62:63], v[62:63], s[70:71]\n"
        "v_pk_mul_f32 v[64:65], v[64:65], s[70:71]\n"
        "v_pk_mul_f32 v[66:67], v[66:67], s[70:71]\n"
        "v_cvt_pk_bf16_f32 v72, v76, v77\n"
        "v_mfma_f32_16x16x32_bf16 a[76:79], v[84:87], v[84:87], 0\n"
        "v_cvt_pk_bf16_f32 v73, v78, v79\n"
        "v_cvt_pk_bf16_f32 v74, v80, v81\n"
        "v_cvt_pk_bf16_f32 v75, v82, v83\n"
        "ds_write_b128 v251, v[72:75] offset:7168\n"
        "v_cvt_pk_bf16_f32 v52, v52, v53\n"
        "v_cvt_pk_bf16_f32 v53, v54, v55\n"
        "v_cvt_pk_bf16_f32 v54, v56, v57\n"
        "v_cvt_pk_bf16_f32 v55, v58, v59\n"
        "v_accvgpr_read_b32 v68, a80\n"
        "v_accvgpr_read_b32 v69, a81\n"
        "v_accvgpr_read_b32 v70, a82\n"
        "v_accvgpr_read_b32 v71, a83\n"
        "v_mfma_f32_16x16x32_bf16 a[80:83], v[84:87], v[84:87], 0\n"
        "ds_write_b128 v251, v[52:55] offset:8192\n"
        "v_accvgpr_read_b32 v72, a84\n"
        "v_accvgpr_read_b32 v73, a85\n"
        "v_accvgpr_read_b32 v74, a86\n"
        "v_accvgpr_read_b32 v75, a87\n"
        "v_mfma_f32_16x16x32_bf16 a[84:87], v[84:87], v[84:87], 0\n"
        "buffer_load_dwordx4 v249, s[52:55], s67 offen offset:1024 nt lds\n"
        "v_accvgpr_read_b32 v76, a88\n"
        "v_accvgpr_read_b32 v77, a89\n"
        "v_accvgpr_read_b32 v78, a90\n"
        "v_accvgpr_read_b32 v79, a91\n"
        "v_mfma_f32_16x16x32_bf16 a[88:91], v[84:87], v[84:87], 0\n"
        "v_accvgpr_read_b32 v80, a92\n"
        "v_accvgpr_read_b32 v81, a93\n"
        "v_accvgpr_read_b32 v82, a94\n"
        "v_accvgpr_read_b32 v83, a95\n"
        "v_pk_mul_f32 v[68:69], v[68:69], s[70:71]\n"
        "v_pk_mul_f32 v[70:71], v[70:71], s[70:71]\n"
        "v_pk_mul_f32 v[72:73], v[72:73], s[70:71]\n"
        "v_pk_mul_f32 v[74:75], v[74:75], s[70:71]\n"
        "v_pk_mul_f32 v[76:77], v[76:77], s[70:71]\n"
        "v_pk_mul_f32 v[78:79], v[78:79], s[70:71]\n"
        "v_pk_mul_f32 v[80:81], v[80:81], s[70:71]\n"
        "v_pk_mul_f32 v[82:83], v[82:83], s[70:71]\n"
        "v_cvt_pk_bf16_f32 v56, v60, v61\n"
        "v_mfma_f32_16x16x32_bf16 a[92:95], v[84:87], v[84:87], 0\n"
        "v_cvt_pk_bf16_f32 v57, v62, v63\n"
        "v_cvt_pk_bf16_f32 v58, v64, v65\n"
        "v_cvt_pk_bf16_f32 v59, v66, v67\n"
        "ds_write_b128 v251, v[56:59] offset:9216\n"
        "v_cvt_pk_bf16_f32 v68, v68, v69\n"
        "v_cvt_pk_bf16_f32 v69, v70, v71\n"
        "v_cvt_pk_bf16_f32 v70, v72, v73\n"
        "v_cvt_pk_bf16_f32 v71, v74, v75\n"
        "v_accvgpr_read_b32 v52, a96\n"
        "v_accvgpr_read_b32 v53, a97\n"
        "v_accvgpr_read_b32 v54, a98\n"
        "v_accvgpr_read_b32 v55, a99\n"
        "v_mfma_f32_16x16x32_bf16 a[96:99], v[84:87], v[84:87], 0\n"
        "ds_write_b128 v251, v[68:71] offset:10240\n"
        "v_accvgpr_read_b32 v56, a100\n"
        "v_accvgpr_read_b32 v57, a101\n"
        "v_accvgpr_read_b32 v58, a102\n"
        "v_accvgpr_read_b32 v59, a103\n"
        "v_mfma_f32_16x16x32_bf16 a[100:103], v[84:87], v[84:87], 0\n"
        "v_accvgpr_read_b32 v60, a104\n"
        "v_accvgpr_read_b32 v61, a105\n"
        "v_accvgpr_read_b32 v62, a106\n"
        "v_accvgpr_read_b32 v63, a107\n"
        "v_mfma_f32_16x16x32_bf16 a[104:107], v[84:87], v[84:87], 0\n"
        "v_accvgpr_read_b32 v64, a108\n"
        "v_accvgpr_read_b32 v65, a109\n"
        "v_accvgpr_read_b32 v66, a110\n"
        "v_accvgpr_read_b32 v67, a111\n"
        "v_pk_mul_f32 v[52:53], v[52:53], s[70:71]\n"
        "v_pk_mul_f32 v[54:55], v[54:55], s[70:71]\n"
        "v_pk_mul_f32 v[56:57], v[56:57], s[70:71]\n"
        "v_pk_mul_f32 v[58:59], v[58:59], s[70:71]\n"
        "v_pk_mul_f32 v[60:61], v[60:61], s[70:71]\n"
        "v_pk_mul_f32 v[62:63], v[62:63], s[70:71]\n"
        "v_pk_mul_f32 v[64:65], v[64:65], s[70:71]\n"
        "v_pk_mul_f32 v[66:67], v[66:67], s[70:71]\n"
        "buffer_load_dwordx4 v248, s[52:55], s67 offen offset:2048 nt lds\n"
        "v_mfma_f32_16x16x32_bf16 a[108:111], v[84:87], v[84:87], 0\n"
        "v_cvt_pk_bf16_f32 v72, v76, v77\n"
        "v_cvt_pk_bf16_f32 v73, v78, v79\n"
        "v_cvt_pk_bf16_f32 v74, v80, v81\n"
        "v_cvt_pk_bf16_f32 v75, v82, v83\n"
        "ds_write_b128 v251, v[72:75] offset:11264\n"
        "v_cvt_pk_bf16_f32 v52, v52, v53\n"
        "v_cvt_pk_bf16_f32 v53, v54, v55\n"
        "v_cvt_pk_bf16_f32 v54, v56, v57\n"
        "v_cvt_pk_bf16_f32 v55, v58, v59\n"
        "v_accvgpr_read_b32 v68, a112\n"
        "v_accvgpr_read_b32 v69, a113\n"
        "v_accvgpr_read_b32 v70, a114\n"
        "v_accvgpr_read_b32 v71, a115\n"
        "v_mfma_f32_16x16x32_bf16 a[112:115], v[84:87], v[84:87], 0\n"
        "ds_write_b128 v251, v[52:55] offset:12288\n"
        "v_accvgpr_read_b32 v72, a116\n"
        "v_accvgpr_read_b32 v73, a117\n"
        "v_accvgpr_read_b32 v74, a118\n"
        "v_accvgpr_read_b32 v75, a119\n"
        "v_mfma_f32_16x16x32_bf16 a[116:119], v[84:87], v[84:87], 0\n"
        "v_accvgpr_read_b32 v76, a120\n"
        "v_accvgpr_read_b32 v77, a121\n"
        "v_accvgpr_read_b32 v78, a122\n"
        "v_accvgpr_read_b32 v79, a123\n"
        "v_mfma_f32_16x16x32_bf16 a[120:123], v[84:87], v[84:87], 0\n"
        "v_accvgpr_read_b32 v80, a124\n"
        "v_accvgpr_read_b32 v81, a125\n"
        "v_accvgpr_read_b32 v82, a126\n"
        "v_accvgpr_read_b32 v83, a127\n"
        "v_pk_mul_f32 v[68:69], v[68:69], s[70:71]\n"
        "v_pk_mul_f32 v[70:71], v[70:71], s[70:71]\n"
        "v_pk_mul_f32 v[72:73], v[72:73], s[70:71]\n"
        "v_pk_mul_f32 v[74:75], v[74:75], s[70:71]\n"
        "v_pk_mul_f32 v[76:77], v[76:77], s[70:71]\n"
        "v_pk_mul_f32 v[78:79], v[78:79], s[70:71]\n"
        "v_pk_mul_f32 v[80:81], v[80:81], s[70:71]\n"
        "v_pk_mul_f32 v[82:83], v[82:83], s[70:71]\n"
        "v_cvt_pk_bf16_f32 v56, v60, v61\n"
        "v_mfma_f32_16x16x32_bf16 a[124:127], v[84:87], v[84:87], 0\n"
        "v_cvt_pk_bf16_f32 v57, v62, v63\n"
        "v_cvt_pk_bf16_f32 v58, v64, v65\n"
        "v_cvt_pk_bf16_f32 v59, v66, v67\n"
        "ds_write_b128 v251, v[56:59] offset:13312\n"
        "v_cvt_pk_bf16_f32 v68, v68, v69\n"
        "v_cvt_pk_bf16_f32 v69, v70, v71\n"
        "v_cvt_pk_bf16_f32 v70, v72, v73\n"
        "v_cvt_pk_bf16_f32 v71, v74, v75\n"
        "v_max3_f32 v44, v44, v48, v49\n"
        "buffer_load_dwordx4 v249, s[52:55], s67 offen offset:3072 nt lds\n"
        "v_max3_f32 v45, v45, v50, v51\n"
        "v_max3_f32 v28, v28, v29, v30\n"
        "ds_write_b128 v251, v[68:71] offset:14336\n"
        "v_max3_f32 v32, v32, v33, v34\n"
        "v_max3_f32 v36, v36, v37, v38\n"
        "v_max3_f32 v40, v40, v41, v42\n"
        "v_max3_f32 v44, v44, v45, v46\n"
        "v_max_f32 v28, v28, v31\n"
        "v_max_f32 v32, v32, v35\n"
        "v_max_f32 v36, v36, v39\n"
        "v_max_f32 v40, v40, v43\n"
        "v_max_f32 v44, v44, v47\n"
        "v_mov_b32 v29, v28\n"
        "v_mov_b32 v33, v32\n"
        "v_mov_b32 v37, v36\n"
        "v_mov_b32 v41, v40\n"
        "v_mov_b32 v45, v44\n"
        "v_permlane32_swap_b32 v28, v29\n"
        "v_permlane32_swap_b32 v32, v33\n"
        "v_permlane32_swap_b32 v36, v37\n"
        "v_permlane32_swap_b32 v40, v41\n"
        "v_permlane32_swap_b32 v44, v45\n"
        "v_max_f32 v28, v28, v29\n"
        "v_max_f32 v32, v32, v33\n"
        "v_max_f32 v36, v36, v37\n"
        "v_max_f32 v40, v40, v41\n"
        "v_max_f32 v44, v44, v45\n"
        "v_and_b32 v3, 2, v0\n"
        "v_mov_b32 v29, v28\n"
        "v_mov_b32 v33, v32\n"
        "v_mov_b32 v37, v36\n"
        "v_mov_b32 v41, v40\n"
        "v_mov_b32 v45, v44\n"
        "v_cmp_eq_u32 vcc, 0, v3\n"
        "v_permlane16_swap_b32 v28, v29\n"
        "v_permlane16_swap_b32 v32, v33\n"
        "v_permlane16_swap_b32 v36, v37\n"
        "v_permlane16_swap_b32 v40, v41\n"
        "v_permlane16_swap_b32 v44, v45\n"
        "v_and_b32 v253, 1, v0\n"
        "v_mov_b32 v3, 0x3f803f80\n"
        "v_mul_u32_u24 v253, 0xaaaa, v253\n"
        "v_cvt_pk_bf16_f32 v72, v76, v77\n"
        "v_cvt_pk_bf16_f32 v73, v78, v79\n"
        "v_cvt_pk_bf16_f32 v74, v80, v81\n"
        "v_cvt_pk_bf16_f32 v75, v82, v83\n"
        "v_max_f32 v28, v28, v29\n"
        "v_max_f32 v32, v32, v33\n"
        "v_max_f32 v36, v36, v37\n"
        "v_max_f32 v40, v40, v41\n"
        "v_max_f32 v44, v44, v45\n"
        "v_cndmask_b32 v4, 0, v3, vcc\n"
        "v_cndmask_b32 v5, 0, v3, vcc\n"
        "v_cndmask_b32 v6, 0, v3, vcc\n"
        "v_cndmask_b32 v7, 0, v3, vcc\n"
        "v_or_b32 v253, 0x4444, v253\n"
        "ds_write_b128 v251, v[72:75] offset:15360\n"
        "v_xor_b32 v8, 0x80000000, v28\n"
        "v_xor_b32 v9, 0x80000000, v28\n"
        "v_xor_b32 v10, 0x80000000, v28\n"
        "v_xor_b32 v11, 0x80000000, v28\n"
        "v_xor_b32 v12, 0x80000000, v32\n"
        "v_xor_b32 v13, 0x80000000, v32\n"
        "v_xor_b32 v14, 0x80000000, v32\n"
        "s_memrealtime s[60:61]\n"
        "v_xor_b32 v15, 0x80000000, v32\n"
        "v_xor_b32 v16, 0x80000000, v36\n"
        "v_xor_b32 v17, 0x80000000, v36\n"
        "v_xor_b32 v18, 0x80000000, v36\n"
        "v_xor_b32 v19, 0x80000000, v36\n"
        "v_xor_b32 v20, 0x80000000, v40\n"
        "v_xor_b32 v21, 0x80000000, v40\n"
        "v_xor_b32 v22, 0x80000000, v40\n"
        "v_xor_b32 v23, 0x80000000, v40\n"
        "v_xor_b32 v24, 0x80000000, v44\n"
        "v_xor_b32 v25, 0x80000000, v44\n"
        "v_xor_b32 v26, 0x80000000, v44\n"
        "v_xor_b32 v27, 0x80000000, v44\n"
        "v_mov_b32 v255, s84\n"
        "s_mov_b64 s[56:57], %5\n"
        "s_mov_b32 s58, 2433024\n"
        "s_mov_b32 s89, s65\n"
        "s_mov_b64 vcc, 0\n"
        "s_mov_b32 s67, 49152\n"
        "s_mov_b32 s68, 0\n"
        "s_mov_b32 s91, 0\n"
        "s_mov_b32 s90, 0\n"
        "s_waitcnt lgkmcnt(0)\n"
        "s_nop 0\n"
        "s_waitcnt lgkmcnt(0)\n"
        "s_add_u32 s83, s60, 32\n"
        "s_mov_b32 s79, 149\n"
        "s_mov_b32 s63, -1\n"
        "s_bitcmp1_b32 %12, 0\n"
        "s_cbranch_scc0 .Lk_RSP\n"
        "s_sleep 8\n"
        "s_add_u32 s83, s83, 18\n"
        ".Lk_RSP:\n"
        ".p2align 5\n"
        "s_nop 0\n"
        "s_nop 0\n"
        "s_nop 0\n"
        "s_nop 0\n"
        "s_nop 0\n"
        "s_nop 0\n"
        ".Lk_T0:\n"
        "v_mfma_f32_16x16x32_bf16 v[28:31], a[180:183], v[88:91], v[8:11]\n"
        "ds_read_b64_tr_b16 v[142:143], v241 offset:37376\n"
        "v_mfma_f32_16x16x32_bf16 v[28:31], a[184:187], v[96:99], v[28:31]\n"
        "ds_read_b64_tr_b16 v[156:157], v240 offset:33792\n"
        "v_mfma_f32_16x16x32_bf16 v[28:31], a[188:191], v[104:107], v[28:31]\n"
        "ds_read_b64_tr_b16 v[158:159], v240 offset:37888\n"
        "v_mfma_f32_16x16x32_bf16 v[28:31], a[192:195], v[112:115], v[28:31]\n"
        "ds_read_b64_tr_b16 v[172:173], v241 offset:33792\n"
        "v_mfma_f32_16x16x32_bf16 v[32:35], a[196:199], v[88:91], v[8:11]\n"
        "ds_read_b64_tr_b16 v[174:175], v241 offset:37888\n"
        "v_mfma_f32_16x16x32_bf16 v[32:35], a[200:203], v[96:99], v[32:35]\n"
        "ds_read_b64_tr_b16 v[188:189], v240 offset:34304\n"
        "v_mfma_f32_16x16x32_bf16 v[32:35], a[204:207], v[104:107], v[32:35]\n"
        "ds_read_b64_tr_b16 v[190:191], v240 offset:38400\n"
        "v_mfma_f32_16x16x32_bf16 v[32:35], a[208:211], v[112:115], v[32:35]\n"
        "v_exp_f32 v28, v28\n"
        "v_mfma_f32_16x16x32_bf16 v[36:39], a[212:215], v[88:91], v[8:11]\n"
        "v_exp_f32 v29, v29\n"
        "v_mfma_f32_16x16x32_bf16 v[36:39], a[216:219], v[96:99], v[36:39]\n"
        "v_exp_f32 v30, v30\n"
        "v_mfma_f32_16x16x32_bf16 v[36:39], a[220:223], v[104:107], v[36:39]\n"
        "ds_read_b64_tr_b16 v[204:205], v241 offset:34304\n"
        "v_cvt_pk_bf16_f32 v68, v28, v29\n"
        "v_mfma_f32_16x16x32_bf16 v[36:39], a[224:227], v[112:115], v[36:39]\n"
        "v_exp_f32 v31, v31\n"
        "v_mfma_f32_16x16x32_bf16 v[40:43], a[228:231], v[88:91], v[8:11]\n"
        "v_exp_f32 v32, v32\n"
        "v_mfma_f32_16x16x32_bf16 v[40:43], a[232:235], v[96:99], v[40:43]\n"
        "ds_read_b64_tr_b16 v[206:207], v241 offset:38400\n"
        "v_cvt_pk_bf16_f32 v69, v30, v31\n"
        "v_mfma_f32_16x16x32_bf16 v[40:43], a[236:239], v[104:107], v[40:43]\n"
        "v_exp_f32 v33, v33\n"
        "v_mfma_f32_16x16x32_bf16 v[40:43], a[240:243], v[112:115], v[40:43]\n"
        "v_exp_f32 v34, v34\n"
        "v_mfma_f32_16x16x32_bf16 a[128:131], v[100:103], v[84:87], a[128:131]\n"
        "ds_read_b64_tr_b16 v[100:101], v242 offset:32768\n"
        "v_cvt_pk_bf16_f32 v70, v32, v33\n"
        "v_mfma_f32_16x16x32_bf16 a[132:135], v[116:119], v[84:87], a[132:135]\n"
        "v_exp_f32 v35, v35\n"
        "v_mfma_f32_16x16x32_bf16 a[136:139], v[132:135], v[84:87], a[136:139]\n"
        "v_exp_f32 v36, v36\n"
        "v_mfma_f32_16x16x32_bf16 a[140:143], v[148:151], v[84:87], a[140:143]\n"
        "ds_read_b64_tr_b16 v[102:103], v242 offset:36864\n"
        "v_cvt_pk_bf16_f32 v71, v34, v35\n"
        "v_mfma_f32_16x16x32_bf16 a[144:147], v[164:167], v[84:87], a[144:147]\n"
        "v_exp_f32 v37, v37\n"
        "v_mfma_f32_16x16x32_bf16 a[148:151], v[180:183], v[84:87], a[148:151]\n"
        "v_exp_f32 v38, v38\n"
        "v_mfma_f32_16x16x32_bf16 a[152:155], v[196:199], v[84:87], a[152:155]\n"
        "ds_read_b64_tr_b16 v[116:117], v243 offset:32768\n"
        "v_cvt_pk_bf16_f32 v72, v36, v37\n"
        "v_mfma_f32_16x16x32_bf16 a[156:159], v[212:215], v[84:87], a[156:159]\n"
        "v_exp_f32 v39, v39\n"
        "v_mfma_f32_16x16x32_bf16 a[176:179], v[4:7], v[84:87], a[176:179]\n"
        "v_exp_f32 v40, v40\n"
        "v_mfma_f32_16x16x32_bf16 v[44:47], a[180:183], v[120:123], v[12:15]\n"
        "ds_read_b64_tr_b16 v[118:119], v243 offset:36864\n"
        "v_cvt_pk_bf16_f32 v73, v38, v39\n"
        "v_mfma_f32_16x16x32_bf16 v[44:47], a[184:187], v[128:131], v[44:47]\n"
        "v_exp_f32 v41, v41\n"
        "v_mfma_f32_16x16x32_bf16 v[44:47], a[188:191], v[136:139], v[44:47]\n"
        "v_exp_f32 v42, v42\n"
        "v_mfma_f32_16x16x32_bf16 v[44:47], a[192:195], v[144:147], v[44:47]\n"
        "ds_read_b64_tr_b16 v[132:133], v242 offset:33280\n"
        "v_cvt_pk_bf16_f32 v74, v40, v41\n"
        "v_mfma_f32_16x16x32_bf16 v[48:51], a[196:199], v[120:123], v[12:15]\n"
        "ds_read_b64_tr_b16 v[134:135], v242 offset:37376\n"
        "v_mfma_f32_16x16x32_bf16 v[48:51], a[200:203], v[128:131], v[48:51]\n"
        "v_exp_f32 v43, v43\n"
        "v_mfma_f32_16x16x32_bf16 v[48:51], a[204:207], v[136:139], v[48:51]\n"
        "ds_read_b64_tr_b16 v[148:149], v243 offset:33280\n"
        "v_cvt_pk_bf16_f32 v75, v42, v43\n"
        "v_mfma_f32_16x16x32_bf16 v[48:51], a[208:211], v[144:147], v[48:51]\n"
        "ds_read_b64_tr_b16 v[150:151], v243 offset:37376\n"
        "v_mfma_f32_16x16x32_bf16 v[52:55], a[212:215], v[120:123], v[12:15]\n"
        "ds_read_b64_tr_b16 v[164:165], v242 offset:33792\n"
        "v_mfma_f32_16x16x32_bf16 v[52:55], a[216:219], v[128:131], v[52:55]\n"
        "ds_read_b64_tr_b16 v[166:167], v242 offset:37888\n"
        "v_mfma_f32_16x16x32_bf16 v[52:55], a[220:223], v[136:139], v[52:55]\n"
        "ds_read_b64_tr_b16 v[180:181], v243 offset:33792\n"
        "v_mfma_f32_16x16x32_bf16 v[52:55], a[224:227], v[144:147], v[52:55]\n"
        "ds_read_b64_tr_b16 v[182:183], v243 offset:37888\n"
        "v_mfma_f32_16x16x32_bf16 v[56:59], a[228:231], v[120:123], v[12:15]\n"
        "ds_read_b64_tr_b16 v[196:197], v242 offset:34304\n"
        "v_mfma_f32_16x16x32_bf16 v[56:59], a[232:235], v[128:131], v[56:59]\n"
        "ds_read_b64_tr_b16 v[198:199], v242 offset:38400\n"
        "v_mfma_f32_16x16x32_bf16 v[56:59], a[236:239], v[136:139], v[56:59]\n"
        "ds_read_b64_tr_b16 v[212:213], v243 offset:34304\n"
        "v_mfma_f32_16x16x32_bf16 v[56:59], a[240:243], v[144:147], v[56:59]\n"
        "s_waitcnt lgkmcnt(6)\n"
        "ds_read_b64_tr_b16 v[214:215], v243 offset:38400\n"
        "v_mfma_f32_16x16x32_bf16 a[0:3], v[92:95], v[68:71], a[0:3]\n"
        "v_exp_f32 v44, v44\n"
        "v_mfma_f32_16x16x32_bf16 a[0:3], v[100:103], v[72:75], a[0:3]\n"
        "v_exp_f32 v45, v45\n"
        "v_mfma_f32_16x16x32_bf16 a[4:7], v[108:111], v[68:71], a[4:7]\n"
        "v_exp_f32 v46, v46\n"
        "v_mfma_f32_16x16x32_bf16 a[4:7], v[116:119], v[72:75], a[4:7]\n"
        "v_cvt_pk_bf16_f32 v76, v44, v45\n"
        "v_mfma_f32_16x16x32_bf16 a[8:11], v[124:127], v[68:71], a[8:11]\n"
        "v_exp_f32 v47, v47\n"
        "v_mfma_f32_16x16x32_bf16 a[8:11], v[132:135], v[72:75], a[8:11]\n"
        "v_exp_f32 v48, v48\n"
        "v_mfma_f32_16x16x32_bf16 a[12:15], v[140:143], v[68:71], a[12:15]\n"
        "s_waitcnt lgkmcnt(0)\n"
        "v_cvt_pk_bf16_f32 v77, v46, v47\n"
        "v_mfma_f32_16x16x32_bf16 a[12:15], v[148:151], v[72:75], a[12:15]\n"
        "v_exp_f32 v49, v49\n"
        "v_mfma_f32_16x16x32_bf16 a[16:19], v[156:159], v[68:71], a[16:19]\n"
        "v_exp_f32 v50, v50\n"
        "v_mfma_f32_16x16x32_bf16 a[16:19], v[164:167], v[72:75], a[16:19]\n"
        "v_cvt_pk_bf16_f32 v78, v48, v49\n"
        "s_memrealtime s[60:61]\n"
        "v_mfma_f32_16x16x32_bf16 a[20:23], v[172:175], v[68:71], a[20:23]\n"
        "v_exp_f32 v51, v51\n"
        "v_mfma_f32_16x16x32_bf16 a[20:23], v[180:183], v[72:75], a[20:23]\n"
        "v_exp_f32 v52, v52\n"
        "v_mfma_f32_16x16x32_bf16 a[24:27], v[188:191], v[68:71], a[24:27]\n"
        "v_cvt_pk_bf16_f32 v79, v50, v51\n"
        "v_mfma_f32_16x16x32_bf16 a[24:27], v[196:199], v[72:75], a[24:27]\n"
        "v_exp_f32 v53, v53\n"
        "v_mfma_f32_16x16x32_bf16 a[28:31], v[204:207], v[68:71], a[28:31]\n"
        "v_exp_f32 v54, v54\n"
        "v_mfma_f32_16x16x32_bf16 a[28:31], v[212:215], v[72:75], a[28:31]\n"
        "s_waitcnt vmcnt(8)\n"
        "v_cvt_pk_bf16_f32 v80, v52, v53\n"
        "v_smfmac_f32_16x16x64_bf16 a[160:163], v[4:7], v[68:75], v253\n"
        "v_exp_f32 v55, v55\n"
        "v_mfma_f32_16x16x32_bf16 v[28:31], a[180:183], v[152:155], v[16:19]\n"
        "s_barrier\n"
        "v_cvt_pk_bf16_f32 v81, v54, v55\n"
        "v_mfma_f32_16x16x32_bf16 v[28:31], a[184:187], v[160:163], v[28:31]\n"
        "s_add_u32 m0, s65, 65536\n"
        "v_mfma_f32_16x16x32_bf16 v[28:31], a[188:191], v[168:171], v[28:31]\n"
        "buffer_load_dwordx4 v244, s[48:51], s67 offen offset:0 nt lds\n"
        "v_mfma_f32_16x16x32_bf16 v[28:31], a[192:195], v[176:179], v[28:31]\n"
        "v_exp_f32 v56, v56\n"
        "v_mfma_f32_16x16x32_bf16 v[32:35], a[196:199], v[152:155], v[16:19]\n"
        "v_exp_f32 v57, v57\n"
        "v_mfma_f32_16x16x32_bf16 v[32:35], a[200:203], v[160:163], v[32:35]\n"
        "v_exp_f32 v58, v58\n"
        "v_mfma_f32_16x16x32_bf16 v[32:35], a[204:207], v[168:171], v[32:35]\n"
        "v_cvt_pk_bf16_f32 v82, v56, v57\n"
        "s_waitcnt lgkmcnt(0)\n"
        "s_sub_u32 s62, s60, s83\n"
        "v_mfma_f32_16x16x32_bf16 v[32:35], a[208:211], v[176:179], v[32:35]\n"
        "v_exp_f32 v59, v59\n"
        "v_mfma_f32_16x16x32_bf16 v[36:39], a[212:215], v[152:155], v[16:19]\n"
        "v_cvt_pk_bf16_f32 v83, v58, v59\n"
        "s_bitcmp1_b32 s62, 31\n"
        "s_cbranch_scc1 .Lk_PACE0\n"
        ".Lk_PACER0:\n"
        "v_mfma_f32_16x16x32_bf16 v[36:39], a[216:219], v[160:163], v[36:39]\n"
        "v_exp_f32 v28, v28\n"
        "v_mfma_f32_16x16x32_bf16 v[36:39], a[220:223], v[168:171], v[36:39]\n"
        "buffer_load_dwordx4 v245, s[48:51], s67 offen offset:1024 nt lds\n"
        "v_mfma_f32_16x16x32_bf16 v[36:39], a[224:227], v[176:179], v[36:39]\n"
        "v_exp_f32 v29, v29\n"
        "v_mfma_f32_16x16x32_bf16 v[40:43], a[228:231], v[152:155], v[16:19]\n"
        "v_exp_f32 v30, v30\n"
        "v_mfma_f32_16x16x32_bf16 v[40:43], a[232:235], v[160:163], v[40:43]\n"
        "v_cvt_pk_bf16_f32 v68, v28, v29\n"
        "s_sub_u32 s79, s79, 1\n"
        "v_mfma_f32_16x16x32_bf16 v[40:43], a[236:239], v[168:171], v[40:43]\n"
        "v_exp_f32 v31, v31\n"
        "v_mfma_f32_16x16x32_bf16 v[40:43], a[240:243], v[176:179], v[40:43]\n"
        "v_exp_f32 v32, v32\n"
        "v_mfma_f32_16x16x32_bf16 a[32:35], v[92:95], v[76:79], a[32:35]\n"
        "v_cvt_pk_bf16_f32 v69, v30, v31\n"
        "s_max_u32 s79, s79, 130\n"
        "v_mfma_f32_16x16x32_bf16 a[32:35], v[100:103], v[80:83], a[32:35]\n"
        "v_exp_f32 v33, v33\n"
        "v_mfma_f32_16x16x32_bf16 a[36:39], v[108:111], v[76:79], a[36:39]\n"
        "buffer_load_dwordx4 v246, s[48:51], s67 offen offset:2048 nt lds\n"
        "v_mfma_f32_16x16x32_bf16 a[36:39], v[116:119], v[80:83], a[36:39]\n"
        "v_exp_f32 v34, v34\n"
        "v_mfma_f32_16x16x32_bf16 a[40:43], v[124:127], v[76:79], a[40:43]\n"
        "v_cvt_pk_bf16_f32 v70, v32, v33\n"
        "s_cmp_gt_u32 s62, s63\n"
        "v_mfma_f32_16x16x32_bf16 a[40:43], v[132:135], v[80:83], a[40:43]\n"
        "v_exp_f32 v35, v35\n"
        "v_mfma_f32_16x16x32_bf16 a[44:47], v[140:143], v[76:79], a[44:47]\n"
        "v_exp_f32 v36, v36\n"
        "v_mfma_f32_16x16x32_bf16 a[44:47], v[148:151], v[80:83], a[44:47]\n"
        "v_cvt_pk_bf16_f32 v71, v34, v35\n"
        "s_cselect_b32 s79, 152, s79\n"
        "v_mfma_f32_16x16x32_bf16 a[48:51], v[156:159], v[76:79], a[48:51]\n"
        "v_exp_f32 v37, v37\n"
        "v_mfma_f32_16x16x32_bf16 a[48:51], v[164:167], v[80:83], a[48:51]\n"
        "v_exp_f32 v38, v38\n"
        "v_mfma_f32_16x16x32_bf16 a[52:55], v[172:175], v[76:79], a[52:55]\n"
        "buffer_load_dwordx4 v247, s[48:51], s67 offen offset:3072 nt lds\n"
        "v_mfma_f32_16x16x32_bf16 a[52:55], v[180:183], v[80:83], a[52:55]\n"
        "v_cvt_pk_bf16_f32 v72, v36, v37\n"
        "s_min_u32 s62, s62, 8\n"
        "v_mfma_f32_16x16x32_bf16 a[56:59], v[188:191], v[76:79], a[56:59]\n"
        "v_exp_f32 v39, v39\n"
        "v_mfma_f32_16x16x32_bf16 a[56:59], v[196:199], v[80:83], a[56:59]\n"
        "v_exp_f32 v40, v40\n"
        "v_mfma_f32_16x16x32_bf16 a[60:63], v[204:207], v[76:79], a[60:63]\n"
        "v_cvt_pk_bf16_f32 v73, v38, v39\n"
        "s_sub_u32 s60, s60, s62\n"
        "v_mfma_f32_16x16x32_bf16 a[60:63], v[212:215], v[80:83], a[60:63]\n"
        "v_exp_f32 v41, v41\n"
        "v_smfmac_f32_16x16x64_bf16 a[164:167], v[4:7], v[76:83], v253\n"
        "v_exp_f32 v42, v42\n"
        "v_mfma_f32_16x16x32_bf16 v[44:47], a[180:183], v[184:187], v[20:23]\n"
        "s_add_u32 m0, s89, 147456\n"
        "ds_read_b128 a[180:183], v232 offset:16384\n"
        "v_cvt_pk_bf16_f32 v74, v40, v41\n"
        "v_mfma_f32_16x16x32_bf16 v[44:47], a[184:187], v[192:195], v[44:47]\n"
        "buffer_load_dwordx4 v248, s[52:55], s67 offen offset:0 nt lds\n"
        "v_mfma_f32_16x16x32_bf16 v[44:47], a[188:191], v[200:203], v[44:47]\n"
        "v_exp_f32 v43, v43\n"
        "v_mfma_f32_16x16x32_bf16 v[44:47], a[192:195], v[208:211], v[44:47]\n"
        "ds_read_b128 a[184:187], v234 offset:16384\n"
        "v_cvt_pk_bf16_f32 v75, v42, v43\n"
        "v_mfma_f32_16x16x32_bf16 v[48:51], a[196:199], v[184:187], v[20:23]\n"
        "ds_read_b128 a[188:191], v236 offset:16384\n"
        "v_mfma_f32_16x16x32_bf16 v[48:51], a[200:203], v[192:195], v[48:51]\n"
        "ds_read_b128 a[192:195], v238 offset:16384\n"
        "v_mfma_f32_16x16x32_bf16 v[48:51], a[204:207], v[200:203], v[48:51]\n"
        "ds_read_b128 a[196:199], v232 offset:20480\n"
        "v_mfma_f32_16x16x32_bf16 v[48:51], a[208:211], v[208:211], v[48:51]\n"
        "v_exp_f32 v44, v44\n"
        "v_mfma_f32_16x16x32_bf16 v[52:55], a[212:215], v[184:187], v[20:23]\n"
        "v_exp_f32 v45, v45\n"
        "v_mfma_f32_16x16x32_bf16 v[52:55], a[216:219], v[192:195], v[52:55]\n"
        "buffer_load_dwordx4 v249, s[52:55], s67 offen offset:1024 nt lds\n"
        "v_mfma_f32_16x16x32_bf16 v[52:55], a[220:223], v[200:203], v[52:55]\n"
        "v_exp_f32 v46, v46\n"
        "v_mfma_f32_16x16x32_bf16 v[52:55], a[224:227], v[208:211], v[52:55]\n"
        "ds_read_b128 a[200:203], v234 offset:20480\n"
        "v_cvt_pk_bf16_f32 v76, v44, v45\n"
        "v_mfma_f32_16x16x32_bf16 v[56:59], a[228:231], v[184:187], v[20:23]\n"
        "v_exp_f32 v47, v47\n"
        "v_mfma_f32_16x16x32_bf16 v[56:59], a[232:235], v[192:195], v[56:59]\n"
        "v_exp_f32 v48, v48\n"
        "v_mfma_f32_16x16x32_bf16 v[56:59], a[236:239], v[200:203], v[56:59]\n"
        "ds_read_b128 a[204:207], v236 offset:20480\n"
        "v_cvt_pk_bf16_f32 v77, v46, v47\n"
        "v_mfma_f32_16x16x32_bf16 v[56:59], a[240:243], v[208:211], v[56:59]\n"
        "v_exp_f32 v49, v49\n"
        "v_mfma_f32_16x16x32_bf16 a[64:67], v[92:95], v[68:71], a[64:67]\n"
        "v_exp_f32 v50, v50\n"
        "v_mfma_f32_16x16x32_bf16 a[64:67], v[100:103], v[72:75], a[64:67]\n"
        "buffer_load_dwordx4 v248, s[52:55], s67 offen offset:2048 nt lds\n"
        "v_mfma_f32_16x16x32_bf16 a[68:71], v[108:111], v[68:71], a[68:71]\n"
        "ds_read_b128 a[208:211], v238 offset:20480\n"
        "v_cvt_pk_bf16_f32 v78, v48, v49\n"
        "v_mfma_f32_16x16x32_bf16 a[68:71], v[116:119], v[72:75], a[68:71]\n"
        "v_exp_f32 v51, v51\n"
        "v_mfma_f32_16x16x32_bf16 a[72:75], v[124:127], v[68:71], a[72:75]\n"
        "v_exp_f32 v52, v52\n"
        "v_mfma_f32_16x16x32_bf16 a[72:75], v[132:135], v[72:75], a[72:75]\n"
        "v_cvt_pk_bf16_f32 v79, v50, v51\n"
        "s_add_u32 s83, s60, s79\n"
        "v_mfma_f32_16x16x32_bf16 a[76:79], v[140:143], v[68:71], a[76:79]\n"
        "v_exp_f32 v53, v53\n"
        "v_mfma_f32_16x16x32_bf16 a[76:79], v[148:151], v[72:75], a[76:79]\n"
        "v_exp_f32 v54, v54\n"
        "v_mfma_f32_16x16x32_bf16 a[80:83], v[156:159], v[68:71], a[80:83]\n"
        "v_cvt_pk_bf16_f32 v80, v52, v53\n"
        "v_mfma_f32_16x16x32_bf16 a[80:83], v[164:167], v[72:75], a[80:83]\n"
        "buffer_load_dwordx4 v249, s[52:55], s67 offen offset:3072 nt lds\n"
        "v_mfma_f32_16x16x32_bf16 a[84:87], v[172:175], v[68:71], a[84:87]\n"
        "s_add_u32 s67, s67, 16384\n"
        "v_mfma_f32_16x16x32_bf16 a[84:87], v[180:183], v[72:75], a[84:87]\n"
        "v_exp_f32 v55, v55\n"
        "v_mfma_f32_16x16x32_bf16 a[88:91], v[188:191], v[68:71], a[88:91]\n"
        "v_exp_f32 v56, v56\n"
        "v_mfma_f32_16x16x32_bf16 a[88:91], v[196:199], v[72:75], a[88:91]\n"
        "v_cvt_pk_bf16_f32 v81, v54, v55\n"
        "v_mfma_f32_16x16x32_bf16 a[92:95], v[204:207], v[68:71], a[92:95]\n"
        "v_exp_f32 v57, v57\n"
        "v_mfma_f32_16x16x32_bf16 a[92:95], v[212:215], v[72:75], a[92:95]\n"
        "v_exp_f32 v58, v58\n"
        "v_smfmac_f32_16x16x64_bf16 a[168:171], v[4:7], v[68:75], v253\n"
        "s_sub_u32 s68, s68, 1\n"
        "v_cvt_pk_bf16_f32 v82, v56, v57\n"
        "v_mfma_f32_16x16x32_bf16 v[60:63], a[212:215], v[216:219], v[24:27]\n"
        "v_exp_f32 v59, v59\n"
        "v_mfma_f32_16x16x32_bf16 v[60:63], a[216:219], v[220:223], v[60:63]\n"
        "ds_read_b128 a[212:215], v233 offset:16384\n"
        "v_cvt_pk_bf16_f32 v83, v58, v59\n"
        "v_mfma_f32_16x16x32_bf16 v[60:63], a[220:223], v[224:227], v[60:63]\n"
        "ds_read_b128 a[216:219], v235 offset:16384\n"
        "v_mfma_f32_16x16x32_bf16 v[60:63], a[224:227], v[228:231], v[60:63]\n"
        "ds_read_b128 a[220:223], v237 offset:16384\n"
        "v_mfma_f32_16x16x32_bf16 v[64:67], a[228:231], v[216:219], v[24:27]\n"
        "ds_read_b128 a[224:227], v239 offset:16384\n"
        "v_mfma_f32_16x16x32_bf16 v[64:67], a[232:235], v[220:223], v[64:67]\n"
        "ds_read_b128 a[228:231], v233 offset:20480\n"
        "v_mfma_f32_16x16x32_bf16 v[64:67], a[236:239], v[224:227], v[64:67]\n"
        "ds_read_b128 a[232:235], v235 offset:20480\n"
        "v_mfma_f32_16x16x32_bf16 v[64:67], a[240:243], v[228:231], v[64:67]\n"
        "v_exp_f32 v60, v60\n"
        "v_mfma_f32_16x16x32_bf16 a[96:99], v[92:95], v[76:79], a[96:99]\n"
        "v_exp_f32 v61, v61\n"
        "v_mfma_f32_16x16x32_bf16 a[96:99], v[100:103], v[80:83], a[96:99]\n"
        "v_exp_f32 v62, v62\n"
        "v_mfma_f32_16x16x32_bf16 a[100:103], v[108:111], v[76:79], a[100:103]\n"
        "ds_read_b128 a[236:239], v237 offset:20480\n"
        "v_cvt_pk_bf16_f32 v84, v60, v61\n"
        "v_mfma_f32_16x16x32_bf16 a[100:103], v[116:119], v[80:83], a[100:103]\n"
        "v_exp_f32 v63, v63\n"
        "v_mfma_f32_16x16x32_bf16 a[104:107], v[124:127], v[76:79], a[104:107]\n"
        "v_exp_f32 v64, v64\n"
        "v_mfma_f32_16x16x32_bf16 a[104:107], v[132:135], v[80:83], a[104:107]\n"
        "ds_read_b128 a[240:243], v239 offset:20480\n"
        "v_cvt_pk_bf16_f32 v85, v62, v63\n"
        "v_mfma_f32_16x16x32_bf16 a[108:111], v[140:143], v[76:79], a[108:111]\n"
        "v_exp_f32 v65, v65\n"
        "v_mfma_f32_16x16x32_bf16 a[108:111], v[148:151], v[80:83], a[108:111]\n"
        "v_exp_f32 v66, v66\n"
        "v_mfma_f32_16x16x32_bf16 a[112:115], v[156:159], v[76:79], a[112:115]\n"
        "ds_read_b64_tr_b16 v[92:93], v240 offset:0\n"
        "v_cvt_pk_bf16_f32 v86, v64, v65\n"
        "v_mfma_f32_16x16x32_bf16 a[112:115], v[164:167], v[80:83], a[112:115]\n"
        "v_exp_f32 v67, v67\n"
        "v_mfma_f32_16x16x32_bf16 a[116:119], v[172:175], v[76:79], a[116:119]\n"
        "ds_read_b64_tr_b16 v[94:95], v240 offset:4096\n"
        "v_cvt_pk_bf16_f32 v87, v66, v67\n"
        "v_mfma_f32_16x16x32_bf16 a[116:119], v[180:183], v[80:83], a[116:119]\n"
        "ds_read_b64_tr_b16 v[108:109], v241 offset:0\n"
        "v_mfma_f32_16x16x32_bf16 a[120:123], v[188:191], v[76:79], a[120:123]\n"
        "ds_read_b64_tr_b16 v[110:111], v241 offset:4096\n"
        "v_mfma_f32_16x16x32_bf16 a[120:123], v[196:199], v[80:83], a[120:123]\n"
        "ds_read_b64_tr_b16 v[124:125], v240 offset:512\n"
        "v_mfma_f32_16x16x32_bf16 a[124:127], v[204:207], v[76:79], a[124:127]\n"
        "ds_read_b64_tr_b16 v[126:127], v240 offset:4608\n"
        "v_mfma_f32_16x16x32_bf16 a[124:127], v[212:215], v[80:83], a[124:127]\n"
        "s_waitcnt lgkmcnt(5)\n"
        "ds_read_b64_tr_b16 v[140:141], v241 offset:512\n"
        "v_smfmac_f32_16x16x64_bf16 a[172:175], v[4:7], v[76:83], v253\n"
        "s_cbranch_scc1 .Lk_END0\n"
        ".Lk_T1:\n"
        "v_mfma_f32_16x16x32_bf16 v[28:31], a[180:183], v[88:91], v[8:11]\n"
        "ds_read_b64_tr_b16 v[142:143], v241 offset:4608\n"
        "v_mfma_f32_16x16x32_bf16 v[28:31], a[184:187], v[96:99], v[28:31]\n"
        "ds_read_b64_tr_b16 v[156:157], v240 offset:1024\n"
        "v_mfma_f32_16x16x32_bf16 v[28:31], a[188:191], v[104:107], v[28:31]\n"
        "ds_read_b64_tr_b16 v[158:159], v240 offset:5120\n"
        "v_mfma_f32_16x16x32_bf16 v[28:31], a[192:195], v[112:115], v[28:31]\n"
        "ds_read_b64_tr_b16 v[172:173], v241 offset:1024\n"
        "v_mfma_f32_16x16x32_bf16 v[32:35], a[196:199], v[88:91], v[8:11]\n"
        "ds_read_b64_tr_b16 v[174:175], v241 offset:5120\n"
        "v_mfma_f32_16x16x32_bf16 v[32:35], a[200:203], v[96:99], v[32:35]\n"
        "ds_read_b64_tr_b16 v[188:189], v240 offset:1536\n"
        "v_mfma_f32_16x16x32_bf16 v[32:35], a[204:207], v[104:107], v[32:35]\n"
        "ds_read_b64_tr_b16 v[190:191], v240 offset:5632\n"
        "v_mfma_f32_16x16x32_bf16 v[32:35], a[208:211], v[112:115], v[32:35]\n"
        "v_exp_f32 v28, v28\n"
        "v_mfma_f32_16x16x32_bf16 v[36:39], a[212:215], v[88:91], v[8:11]\n"
        "v_exp_f32 v29, v29\n"
        "v_mfma_f32_16x16x32_bf16 v[36:39], a[216:219], v[96:99], v[36:39]\n"
        "v_exp_f32 v30, v30\n"
        "v_mfma_f32_16x16x32_bf16 v[36:39], a[220:223], v[104:107], v[36:39]\n"
        "ds_read_b64_tr_b16 v[204:205], v241 offset:1536\n"
        "v_cvt_pk_bf16_f32 v68, v28, v29\n"
        "v_mfma_f32_16x16x32_bf16 v[36:39], a[224:227], v[112:115], v[36:39]\n"
        "v_exp_f32 v31, v31\n"
        "v_mfma_f32_16x16x32_bf16 v[40:43], a[228:231], v[88:91], v[8:11]\n"
        "v_exp_f32 v32, v32\n"
        "v_mfma_f32_16x16x32_bf16 v[40:43], a[232:235], v[96:99], v[40:43]\n"
        "ds_read_b64_tr_b16 v[206:207], v241 offset:5632\n"
        "v_cvt_pk_bf16_f32 v69, v30, v31\n"
        "v_mfma_f32_16x16x32_bf16 v[40:43], a[236:239], v[104:107], v[40:43]\n"
        "v_exp_f32 v33, v33\n"
        "v_mfma_f32_16x16x32_bf16 v[40:43], a[240:243], v[112:115], v[40:43]\n"
        "v_exp_f32 v34, v34\n"
        "v_mfma_f32_16x16x32_bf16 a[128:131], v[100:103], v[84:87], a[128:131]\n"
        "ds_read_b64_tr_b16 v[100:101], v242 offset:0\n"
        "v_cvt_pk_bf16_f32 v70, v32, v33\n"
        "v_mfma_f32_16x16x32_bf16 a[132:135], v[116:119], v[84:87], a[132:135]\n"
        "v_exp_f32 v35, v35\n"
        "v_mfma_f32_16x16x32_bf16 a[136:139], v[132:135], v[84:87], a[136:139]\n"
        "v_exp_f32 v36, v36\n"
        "v_mfma_f32_16x16x32_bf16 a[140:143], v[148:151], v[84:87], a[140:143]\n"
        "ds_read_b64_tr_b16 v[102:103], v242 offset:4096\n"
        "v_cvt_pk_bf16_f32 v71, v34, v35\n"
        "v_mfma_f32_16x16x32_bf16 a[144:147], v[164:167], v[84:87], a[144:147]\n"
        "v_exp_f32 v37, v37\n"
        "v_mfma_f32_16x16x32_bf16 a[148:151], v[180:183], v[84:87], a[148:151]\n"
        "v_exp_f32 v38, v38\n"
        "v_mfma_f32_16x16x32_bf16 a[152:155], v[196:199], v[84:87], a[152:155]\n"
        "ds_read_b64_tr_b16 v[116:117], v243 offset:0\n"
        "v_cvt_pk_bf16_f32 v72, v36, v37\n"
        "v_mfma_f32_16x16x32_bf16 a[156:159], v[212:215], v[84:87], a[156:159]\n"
        "v_exp_f32 v39, v39\n"
        "v_mfma_f32_16x16x32_bf16 a[176:179], v[4:7], v[84:87], a[176:179]\n"
        "v_exp_f32 v40, v40\n"
        "v_mfma_f32_16x16x32_bf16 v[44:47], a[180:183], v[120:123], v[12:15]\n"
        "ds_read_b64_tr_b16 v[118:119], v243 offset:4096\n"
        "v_cvt_pk_bf16_f32 v73, v38, v39\n"
        "v_mfma_f32_16x16x32_bf16 v[44:47], a[184:187], v[128:131], v[44:47]\n"
        "v_exp_f32 v41, v41\n"
        "v_mfma_f32_16x16x32_bf16 v[44:47], a[188:191], v[136:139], v[44:47]\n"
        "v_exp_f32 v42, v42\n"
        "v_mfma_f32_16x16x32_bf16 v[44:47], a[192:195], v[144:147], v[44:47]\n"
        "ds_read_b64_tr_b16 v[132:133], v242 offset:512\n"
        "v_cvt_pk_bf16_f32 v74, v40, v41\n"
        "v_mfma_f32_16x16x32_bf16 v[48:51], a[196:199], v[120:123], v[12:15]\n"
        "ds_read_b64_tr_b16 v[134:135], v242 offset:4608\n"
        "v_mfma_f32_16x16x32_bf16 v[48:51], a[200:203], v[128:131], v[48:51]\n"
        "v_exp_f32 v43, v43\n"
        "v_mfma_f32_16x16x32_bf16 v[48:51], a[204:207], v[136:139], v[48:51]\n"
        "ds_read_b64_tr_b16 v[148:149], v243 offset:512\n"
        "v_cvt_pk_bf16_f32 v75, v42, v43\n"
        "v_mfma_f32_16x16x32_bf16 v[48:51], a[208:211], v[144:147], v[48:51]\n"
        "ds_read_b64_tr_b16 v[150:151], v243 offset:4608\n"
        "v_mfma_f32_16x16x32_bf16 v[52:55], a[212:215], v[120:123], v[12:15]\n"
        "ds_read_b64_tr_b16 v[164:165], v242 offset:1024\n"
        "v_mfma_f32_16x16x32_bf16 v[52:55], a[216:219], v[128:131], v[52:55]\n"
        "ds_read_b64_tr_b16 v[166:167], v242 offset:5120\n"
        "v_mfma_f32_16x16x32_bf16 v[52:55], a[220:223], v[136:139], v[52:55]\n"
        "ds_read_b64_tr_b16 v[180:181], v243 offset:1024\n"
        "v_mfma_f32_16x16x32_bf16 v[52:55], a[224:227], v[144:147], v[52:55]\n"
        "ds_read_b64_tr_b16 v[182:183], v243 offset:5120\n"
        "v_mfma_f32_16x16x32_bf16 v[56:59], a[228:231], v[120:123], v[12:15]\n"
        "ds_read_b64_tr_b16 v[196:197], v242 offset:1536\n"
        "v_mfma_f32_16x16x32_bf16 v[56:59], a[232:235], v[128:131], v[56:59]\n"
        "ds_read_b64_tr_b16 v[198:199], v242 offset:5632\n"
        "v_mfma_f32_16x16x32_bf16 v[56:59], a[236:239], v[136:139], v[56:59]\n"
        "ds_read_b64_tr_b16 v[212:213], v243 offset:1536\n"
        "v_mfma_f32_16x16x32_bf16 v[56:59], a[240:243], v[144:147], v[56:59]\n"
        "s_waitcnt lgkmcnt(6)\n"
        "ds_read_b64_tr_b16 v[214:215], v243 offset:5632\n"
        "v_mfma_f32_16x16x32_bf16 a[0:3], v[92:95], v[68:71], a[0:3]\n"
        "v_exp_f32 v44, v44\n"
        "v_mfma_f32_16x16x32_bf16 a[0:3], v[100:103], v[72:75], a[0:3]\n"
        "v_exp_f32 v45, v45\n"
        "v_mfma_f32_16x16x32_bf16 a[4:7], v[108:111], v[68:71], a[4:7]\n"
        "v_exp_f32 v46, v46\n"
        "v_mfma_f32_16x16x32_bf16 a[4:7], v[116:119], v[72:75], a[4:7]\n"
        "v_cvt_pk_bf16_f32 v76, v44, v45\n"
        "v_mfma_f32_16x16x32_bf16 a[8:11], v[124:127], v[68:71], a[8:11]\n"
        "v_exp_f32 v47, v47\n"
        "v_mfma_f32_16x16x32_bf16 a[8:11], v[132:135], v[72:75], a[8:11]\n"
        "v_exp_f32 v48, v48\n"
        "v_mfma_f32_16x16x32_bf16 a[12:15], v[140:143], v[68:71], a[12:15]\n"
        "s_waitcnt lgkmcnt(0)\n"
        "v_cvt_pk_bf16_f32 v77, v46, v47\n"
        "v_mfma_f32_16x16x32_bf16 a[12:15], v[148:151], v[72:75], a[12:15]\n"
        "v_exp_f32 v49, v49\n"
        "v_mfma_f32_16x16x32_bf16 a[16:19], v[156:159], v[68:71], a[16:19]\n"
        "v_exp_f32 v50, v50\n"
        "v_mfma_f32_16x16x32_bf16 a[16:19], v[164:167], v[72:75], a[16:19]\n"
        "v_cvt_pk_bf16_f32 v78, v48, v49\n"
        "s_memrealtime s[60:61]\n"
        "v_mfma_f32_16x16x32_bf16 a[20:23], v[172:175], v[68:71], a[20:23]\n"
        "v_exp_f32 v51, v51\n"
        "v_mfma_f32_16x16x32_bf16 a[20:23], v[180:183], v[72:75], a[20:23]\n"
        "v_exp_f32 v52, v52\n"
        "v_mfma_f32_16x16x32_bf16 a[24:27], v[188:191], v[68:71], a[24:27]\n"
        "v_cvt_pk_bf16_f32 v79, v50, v51\n"
        "v_mfma_f32_16x16x32_bf16 a[24:27], v[196:199], v[72:75], a[24:27]\n"
        "v_exp_f32 v53, v53\n"
        "v_mfma_f32_16x16x32_bf16 a[28:31], v[204:207], v[68:71], a[28:31]\n"
        "v_exp_f32 v54, v54\n"
        "v_mfma_f32_16x16x32_bf16 a[28:31], v[212:215], v[72:75], a[28:31]\n"
        "s_cbranch_vccnz .Lk_SKIPW\n"
        "s_waitcnt vmcnt(8)\n"
        ".Lk_SKIPW:\n"
        "v_cvt_pk_bf16_f32 v80, v52, v53\n"
        "v_smfmac_f32_16x16x64_bf16 a[160:163], v[4:7], v[68:75], v253\n"
        "v_exp_f32 v55, v55\n"
        "v_mfma_f32_16x16x32_bf16 v[28:31], a[180:183], v[152:155], v[16:19]\n"
        "s_barrier\n"
        "v_cvt_pk_bf16_f32 v81, v54, v55\n"
        "v_mfma_f32_16x16x32_bf16 v[28:31], a[184:187], v[160:163], v[28:31]\n"
        "s_add_u32 m0, s65, 81920\n"
        "v_mfma_f32_16x16x32_bf16 v[28:31], a[188:191], v[168:171], v[28:31]\n"
        "buffer_load_dwordx4 v244, s[48:51], s67 offen offset:0 nt lds\n"
        "v_mfma_f32_16x16x32_bf16 v[28:31], a[192:195], v[176:179], v[28:31]\n"
        "v_exp_f32 v56, v56\n"
        "v_mfma_f32_16x16x32_bf16 v[32:35], a[196:199], v[152:155], v[16:19]\n"
        "v_exp_f32 v57, v57\n"
        "v_mfma_f32_16x16x32_bf16 v[32:35], a[200:203], v[160:163], v[32:35]\n"
        "v_exp_f32 v58, v58\n"
        "v_mfma_f32_16x16x32_bf16 v[32:35], a[204:207], v[168:171], v[32:35]\n"
        "v_cvt_pk_bf16_f32 v82, v56, v57\n"
        "s_waitcnt lgkmcnt(0)\n"
        "s_sub_u32 s62, s60, s83\n"
        "v_mfma_f32_16x16x32_bf16 v[32:35], a[208:211], v[176:179], v[32:35]\n"
        "v_exp_f32 v59, v59\n"
        "v_mfma_f32_16x16x32_bf16 v[36:39], a[212:215], v[152:155], v[16:19]\n"
        "v_cvt_pk_bf16_f32 v83, v58, v59\n"
        "s_bitcmp1_b32 s62, 31\n"
        "s_cbranch_scc1 .Lk_PACE1\n"
        ".Lk_PACER1:\n"
        "v_mfma_f32_16x16x32_bf16 v[36:39], a[216:219], v[160:163], v[36:39]\n"
        "v_exp_f32 v28, v28\n"
        "v_mfma_f32_16x16x32_bf16 v[36:39], a[220:223], v[168:171], v[36:39]\n"
        "buffer_load_dwordx4 v245, s[48:51], s67 offen offset:1024 nt lds\n"
        "v_mfma_f32_16x16x32_bf16 v[36:39], a[224:227], v[176:179], v[36:39]\n"
        "v_exp_f32 v29, v29\n"
        "v_mfma_f32_16x16x32_bf16 v[40:43], a[228:231], v[152:155], v[16:19]\n"
        "v_exp_f32 v30, v30\n"
        "v_mfma_f32_16x16x32_bf16 v[40:43], a[232:235], v[160:163], v[40:43]\n"
        "v_cvt_pk_bf16_f32 v68, v28, v29\n"
        "s_sub_u32 s79, s79, 1\n"
        "v_mfma_f32_16x16x32_bf16 v[40:43], a[236:239], v[168:171], v[40:43]\n"
        "v_exp_f32 v31, v31\n"
        "v_mfma_f32_16x16x32_bf16 v[40:43], a[240:243], v[176:179], v[40:43]\n"
        "v_exp_f32 v32, v32\n"
        "v_mfma_f32_16x16x32_bf16 a[32:35], v[92:95], v[76:79], a[32:35]\n"
        "v_cvt_pk_bf16_f32 v69, v30, v31\n"
        "s_max_u32 s79, s79, 130\n"
        "v_mfma_f32_16x16x32_bf16 a[32:35], v[100:103], v[80:83], a[32:35]\n"
        "v_exp_f32 v33, v33\n"
        "v_mfma_f32_16x16x32_bf16 a[36:39], v[108:111], v[76:79], a[36:39]\n"
        "buffer_load_dwordx4 v246, s[48:51], s67 offen offset:2048 nt lds\n"
        "v_mfma_f32_16x16x32_bf16 a[36:39], v[116:119], v[80:83], a[36:39]\n"
        "v_exp_f32 v34, v34\n"
        "v_mfma_f32_16x16x32_bf16 a[40:43], v[124:127], v[76:79], a[40:43]\n"
        "v_cvt_pk_bf16_f32 v70, v32, v33\n"
        "s_cmp_gt_u32 s62, s63\n"
        "v_mfma_f32_16x16x32_bf16 a[40:43], v[132:135], v[80:83], a[40:43]\n"
        "v_exp_f32 v35, v35\n"
        "v_mfma_f32_16x16x32_bf16 a[44:47], v[140:143], v[76:79], a[44:47]\n"
        "v_exp_f32 v36, v36\n"
        "v_mfma_f32_16x16x32_bf16 a[44:47], v[148:151], v[80:83], a[44:47]\n"
        "v_cvt_pk_bf16_f32 v71, v34, v35\n"
        "s_cselect_b32 s79, 152, s79\n"
        "v_mfma_f32_16x16x32_bf16 a[48:51], v[156:159], v[76:79], a[48:51]\n"
        "v_exp_f32 v37, v37\n"
        "v_mfma_f32_16x16x32_bf16 a[48:51], v[164:167], v[80:83], a[48:51]\n"
        "v_exp_f32 v38, v38\n"
        "v_mfma_f32_16x16x32_bf16 a[52:55], v[172:175], v[76:79], a[52:55]\n"
        "buffer_load_dwordx4 v247, s[48:51], s67 offen offset:3072 nt lds\n"
        "v_mfma_f32_16x16x32_bf16 a[52:55], v[180:183], v[80:83], a[52:55]\n"
        "v_cvt_pk_bf16_f32 v72, v36, v37\n"
        "s_min_u32 s62, s62, 8\n"
        "v_mfma_f32_16x16x32_bf16 a[56:59], v[188:191], v[76:79], a[56:59]\n"
        "v_exp_f32 v39, v39\n"
        "v_mfma_f32_16x16x32_bf16 a[56:59], v[196:199], v[80:83], a[56:59]\n"
        "v_exp_f32 v40, v40\n"
        "v_mfma_f32_16x16x32_bf16 a[60:63], v[204:207], v[76:79], a[60:63]\n"
        "v_cvt_pk_bf16_f32 v73, v38, v39\n"
        "s_sub_u32 s60, s60, s62\n"
        "v_mfma_f32_16x16x32_bf16 a[60:63], v[212:215], v[80:83], a[60:63]\n"
        "v_exp_f32 v41, v41\n"
        "v_smfmac_f32_16x16x64_bf16 a[164:167], v[4:7], v[76:83], v253\n"
        "v_exp_f32 v42, v42\n"
        "v_mfma_f32_16x16x32_bf16 v[44:47], a[180:183], v[184:187], v[20:23]\n"
        "s_add_u32 m0, s89, 114688\n"
        "ds_read_b128 a[180:183], v232 offset:32768\n"
        "v_cvt_pk_bf16_f32 v74, v40, v41\n"
        "v_mfma_f32_16x16x32_bf16 v[44:47], a[184:187], v[192:195], v[44:47]\n"
        "buffer_load_dwordx4 v248, s[52:55], s67 offen offset:0 nt lds\n"
        "v_mfma_f32_16x16x32_bf16 v[44:47], a[188:191], v[200:203], v[44:47]\n"
        "v_exp_f32 v43, v43\n"
        "v_mfma_f32_16x16x32_bf16 v[44:47], a[192:195], v[208:211], v[44:47]\n"
        "ds_read_b128 a[184:187], v234 offset:32768\n"
        "v_cvt_pk_bf16_f32 v75, v42, v43\n"
        "v_mfma_f32_16x16x32_bf16 v[48:51], a[196:199], v[184:187], v[20:23]\n"
        "ds_read_b128 a[188:191], v236 offset:32768\n"
        "v_mfma_f32_16x16x32_bf16 v[48:51], a[200:203], v[192:195], v[48:51]\n"
        "ds_read_b128 a[192:195], v238 offset:32768\n"
        "v_mfma_f32_16x16x32_bf16 v[48:51], a[204:207], v[200:203], v[48:51]\n"
        "ds_read_b128 a[196:199], v232 offset:36864\n"
        "v_mfma_f32_16x16x32_bf16 v[48:51], a[208:211], v[208:211], v[48:51]\n"
        "v_exp_f32 v44, v44\n"
        "v_mfma_f32_16x16x32_bf16 v[52:55], a[212:215], v[184:187], v[20:23]\n"
        "v_exp_f32 v45, v45\n"
        "v_mfma_f32_16x16x32_bf16 v[52:55], a[216:219], v[192:195], v[52:55]\n"
        "buffer_load_dwordx4 v249, s[52:55], s67 offen offset:1024 nt lds\n"
        "v_mfma_f32_16x16x32_bf16 v[52:55], a[220:223], v[200:203], v[52:55]\n"
        "v_exp_f32 v46, v46\n"
        "v_mfma_f32_16x16x32_bf16 v[52:55], a[224:227], v[208:211], v[52:55]\n"
        "ds_read_b128 a[200:203], v234 offset:36864\n"
        "v_cvt_pk_bf16_f32 v76, v44, v45\n"
        "v_mfma_f32_16x16x32_bf16 v[56:59], a[228:231], v[184:187], v[20:23]\n"
        "v_exp_f32 v47, v47\n"
        "v_mfma_f32_16x16x32_bf16 v[56:59], a[232:235], v[192:195], v[56:59]\n"
        "v_exp_f32 v48, v48\n"
        "v_mfma_f32_16x16x32_bf16 v[56:59], a[236:239], v[200:203], v[56:59]\n"
        "ds_read_b128 a[204:207], v236 offset:36864\n"
        "v_cvt_pk_bf16_f32 v77, v46, v47\n"
        "v_mfma_f32_16x16x32_bf16 v[56:59], a[240:243], v[208:211], v[56:59]\n"
        "v_exp_f32 v49, v49\n"
        "v_mfma_f32_16x16x32_bf16 a[64:67], v[92:95], v[68:71], a[64:67]\n"
        "v_exp_f32 v50, v50\n"
        "v_mfma_f32_16x16x32_bf16 a[64:67], v[100:103], v[72:75], a[64:67]\n"
        "buffer_load_dwordx4 v248, s[52:55], s67 offen offset:2048 nt lds\n"
        "v_mfma_f32_16x16x32_bf16 a[68:71], v[108:111], v[68:71], a[68:71]\n"
        "ds_read_b128 a[208:211], v238 offset:36864\n"
        "v_cvt_pk_bf16_f32 v78, v48, v49\n"
        "v_mfma_f32_16x16x32_bf16 a[68:71], v[116:119], v[72:75], a[68:71]\n"
        "v_exp_f32 v51, v51\n"
        "v_mfma_f32_16x16x32_bf16 a[72:75], v[124:127], v[68:71], a[72:75]\n"
        "v_exp_f32 v52, v52\n"
        "v_mfma_f32_16x16x32_bf16 a[72:75], v[132:135], v[72:75], a[72:75]\n"
        "v_cvt_pk_bf16_f32 v79, v50, v51\n"
        "s_add_u32 s83, s60, s79\n"
        "v_mfma_f32_16x16x32_bf16 a[76:79], v[140:143], v[68:71], a[76:79]\n"
        "v_exp_f32 v53, v53\n"
        "v_mfma_f32_16x16x32_bf16 a[76:79], v[148:151], v[72:75], a[76:79]\n"
        "v_exp_f32 v54, v54\n"
        "v_mfma_f32_16x16x32_bf16 a[80:83], v[156:159], v[68:71], a[80:83]\n"
        "v_cvt_pk_bf16_f32 v80, v52, v53\n"
        "v_mfma_f32_16x16x32_bf16 a[80:83], v[164:167], v[72:75], a[80:83]\n"
        "buffer_load_dwordx4 v249, s[52:55], s67 offen offset:3072 nt lds\n"
        "v_mfma_f32_16x16x32_bf16 a[84:87], v[172:175], v[68:71], a[84:87]\n"
        "s_add_u32 s67, s67, 16384\n"
        "v_mfma_f32_16x16x32_bf16 a[84:87], v[180:183], v[72:75], a[84:87]\n"
        "v_exp_f32 v55, v55\n"
        "v_mfma_f32_16x16x32_bf16 a[88:91], v[188:191], v[68:71], a[88:91]\n"
        "v_exp_f32 v56, v56\n"
        "v_mfma_f32_16x16x32_bf16 a[88:91], v[196:199], v[72:75], a[88:91]\n"
        "v_cvt_pk_bf16_f32 v81, v54, v55\n"
        "v_mfma_f32_16x16x32_bf16 a[92:95], v[204:207], v[68:71], a[92:95]\n"
        "v_exp_f32 v57, v57\n"
        "v_mfma_f32_16x16x32_bf16 a[92:95], v[212:215], v[72:75], a[92:95]\n"
        "v_exp_f32 v58, v58\n"
        "v_smfmac_f32_16x16x64_bf16 a[168:171], v[4:7], v[68:75], v253\n"
        "s_sub_u32 s91, s91, 1\n"
        "v_cvt_pk_bf16_f32 v82, v56, v57\n"
        "v_mfma_f32_16x16x32_bf16 v[60:63], a[212:215], v[216:219], v[24:27]\n"
        "v_exp_f32 v59, v59\n"
        "v_mfma_f32_16x16x32_bf16 v[60:63], a[216:219], v[220:223], v[60:63]\n"
        "ds_read_b128 a[212:215], v233 offset:32768\n"
        "v_cvt_pk_bf16_f32 v83, v58, v59\n"
        "v_mfma_f32_16x16x32_bf16 v[60:63], a[220:223], v[224:227], v[60:63]\n"
        "ds_read_b128 a[216:219], v235 offset:32768\n"
        "v_mfma_f32_16x16x32_bf16 v[60:63], a[224:227], v[228:231], v[60:63]\n"
        "ds_read_b128 a[220:223], v237 offset:32768\n"
        "v_mfma_f32_16x16x32_bf16 v[64:67], a[228:231], v[216:219], v[24:27]\n"
        "ds_read_b128 a[224:227], v239 offset:32768\n"
        "v_mfma_f32_16x16x32_bf16 v[64:67], a[232:235], v[220:223], v[64:67]\n"
        "ds_read_b128 a[228:231], v233 offset:36864\n"
        "v_mfma_f32_16x16x32_bf16 v[64:67], a[236:239], v[224:227], v[64:67]\n"
        "ds_read_b128 a[232:235], v235 offset:36864\n"
        "v_mfma_f32_16x16x32_bf16 v[64:67], a[240:243], v[228:231], v[64:67]\n"
        "v_exp_f32 v60, v60\n"
        "v_mfma_f32_16x16x32_bf16 a[96:99], v[92:95], v[76:79], a[96:99]\n"
        "v_exp_f32 v61, v61\n"
        "v_mfma_f32_16x16x32_bf16 a[96:99], v[100:103], v[80:83], a[96:99]\n"
        "v_exp_f32 v62, v62\n"
        "v_mfma_f32_16x16x32_bf16 a[100:103], v[108:111], v[76:79], a[100:103]\n"
        "ds_read_b128 a[236:239], v237 offset:36864\n"
        "v_cvt_pk_bf16_f32 v84, v60, v61\n"
        "v_mfma_f32_16x16x32_bf16 a[100:103], v[116:119], v[80:83], a[100:103]\n"
        "v_exp_f32 v63, v63\n"
        "v_mfma_f32_16x16x32_bf16 a[104:107], v[124:127], v[76:79], a[104:107]\n"
        "v_exp_f32 v64, v64\n"
        "v_mfma_f32_16x16x32_bf16 a[104:107], v[132:135], v[80:83], a[104:107]\n"
        "ds_read_b128 a[240:243], v239 offset:36864\n"
        "v_cvt_pk_bf16_f32 v85, v62, v63\n"
        "v_mfma_f32_16x16x32_bf16 a[108:111], v[140:143], v[76:79], a[108:111]\n"
        "v_exp_f32 v65, v65\n"
        "v_mfma_f32_16x16x32_bf16 a[108:111], v[148:151], v[80:83], a[108:111]\n"
        "v_exp_f32 v66, v66\n"
        "v_mfma_f32_16x16x32_bf16 a[112:115], v[156:159], v[76:79], a[112:115]\n"
        "ds_read_b64_tr_b16 v[92:93], v240 offset:16384\n"
        "v_cvt_pk_bf16_f32 v86, v64, v65\n"
        "v_mfma_f32_16x16x32_bf16 a[112:115], v[164:167], v[80:83], a[112:115]\n"
        "v_exp_f32 v67, v67\n"
        "v_mfma_f32_16x16x32_bf16 a[116:119], v[172:175], v[76:79], a[116:119]\n"
        "ds_read_b64_tr_b16 v[94:95], v240 offset:20480\n"
        "v_cvt_pk_bf16_f32 v87, v66, v67\n"
        "v_mfma_f32_16x16x32_bf16 a[116:119], v[180:183], v[80:83], a[116:119]\n"
        "ds_read_b64_tr_b16 v[108:109], v241 offset:16384\n"
        "v_mfma_f32_16x16x32_bf16 a[120:123], v[188:191], v[76:79], a[120:123]\n"
        "ds_read_b64_tr_b16 v[110:111], v241 offset:20480\n"
        "v_mfma_f32_16x16x32_bf16 a[120:123], v[196:199], v[80:83], a[120:123]\n"
        "ds_read_b64_tr_b16 v[124:125], v240 offset:16896\n"
        "v_mfma_f32_16x16x32_bf16 a[124:127], v[204:207], v[76:79], a[124:127]\n"
        "ds_read_b64_tr_b16 v[126:127], v240 offset:20992\n"
        "v_mfma_f32_16x16x32_bf16 a[124:127], v[212:215], v[80:83], a[124:127]\n"
        "s_waitcnt lgkmcnt(5)\n"
        "ds_read_b64_tr_b16 v[140:141], v241 offset:16896\n"
        "v_smfmac_f32_16x16x64_bf16 a[172:175], v[4:7], v[76:83], v253\n"
        "s_cbranch_scc1 .Lk_END1\n"
        ".Lk_T2:\n"
        "v_mfma_f32_16x16x32_bf16 v[28:31], a[180:183], v[88:91], v[8:11]\n"
        "ds_read_b64_tr_b16 v[142:143], v241 offset:20992\n"
        "v_mfma_f32_16x16x32_bf16 v[28:31], a[184:187], v[96:99], v[28:31]\n"
        "ds_read_b64_tr_b16 v[156:157], v240 offset:17408\n"
        "v_mfma_f32_16x16x32_bf16 v[28:31], a[188:191], v[104:107], v[28:31]\n"
        "ds_read_b64_tr_b16 v[158:159], v240 offset:21504\n"
        "v_mfma_f32_16x16x32_bf16 v[28:31], a[192:195], v[112:115], v[28:31]\n"
        "ds_read_b64_tr_b16 v[172:173], v241 offset:17408\n"
        "v_mfma_f32_16x16x32_bf16 v[32:35], a[196:199], v[88:91], v[8:11]\n"
        "ds_read_b64_tr_b16 v[174:175], v241 offset:21504\n"
        "v_mfma_f32_16x16x32_bf16 v[32:35], a[200:203], v[96:99], v[32:35]\n"
        "ds_read_b64_tr_b16 v[188:189], v240 offset:17920\n"
        "v_mfma_f32_16x16x32_bf16 v[32:35], a[204:207], v[104:107], v[32:35]\n"
        "ds_read_b64_tr_b16 v[190:191], v240 offset:22016\n"
        "v_mfma_f32_16x16x32_bf16 v[32:35], a[208:211], v[112:115], v[32:35]\n"
        "v_exp_f32 v28, v28\n"
        "v_mfma_f32_16x16x32_bf16 v[36:39], a[212:215], v[88:91], v[8:11]\n"
        "v_exp_f32 v29, v29\n"
        "v_mfma_f32_16x16x32_bf16 v[36:39], a[216:219], v[96:99], v[36:39]\n"
        "v_exp_f32 v30, v30\n"
        "v_mfma_f32_16x16x32_bf16 v[36:39], a[220:223], v[104:107], v[36:39]\n"
        "ds_read_b64_tr_b16 v[204:205], v241 offset:17920\n"
        "v_cvt_pk_bf16_f32 v68, v28, v29\n"
        "v_mfma_f32_16x16x32_bf16 v[36:39], a[224:227], v[112:115], v[36:39]\n"
        "v_exp_f32 v31, v31\n"
        "v_mfma_f32_16x16x32_bf16 v[40:43], a[228:231], v[88:91], v[8:11]\n"
        "v_exp_f32 v32, v32\n"
        "v_mfma_f32_16x16x32_bf16 v[40:43], a[232:235], v[96:99], v[40:43]\n"
        "ds_read_b64_tr_b16 v[206:207], v241 offset:22016\n"
        "v_cvt_pk_bf16_f32 v69, v30, v31\n"
        "v_mfma_f32_16x16x32_bf16 v[40:43], a[236:239], v[104:107], v[40:43]\n"
        "v_exp_f32 v33, v33\n"
        "v_mfma_f32_16x16x32_bf16 v[40:43], a[240:243], v[112:115], v[40:43]\n"
        "v_exp_f32 v34, v34\n"
        "v_mfma_f32_16x16x32_bf16 a[128:131], v[100:103], v[84:87], a[128:131]\n"
        "ds_read_b64_tr_b16 v[100:101], v242 offset:16384\n"
        "v_cvt_pk_bf16_f32 v70, v32, v33\n"
        "v_mfma_f32_16x16x32_bf16 a[132:135], v[116:119], v[84:87], a[132:135]\n"
        "v_exp_f32 v35, v35\n"
        "v_mfma_f32_16x16x32_bf16 a[136:139], v[132:135], v[84:87], a[136:139]\n"
        "v_exp_f32 v36, v36\n"
        "v_mfma_f32_16x16x32_bf16 a[140:143], v[148:151], v[84:87], a[140:143]\n"
        "ds_read_b64_tr_b16 v[102:103], v242 offset:20480\n"
        "v_cvt_pk_bf16_f32 v71, v34, v35\n"
        "v_mfma_f32_16x16x32_bf16 a[144:147], v[164:167], v[84:87], a[144:147]\n"
        "v_exp_f32 v37, v37\n"
        "v_mfma_f32_16x16x32_bf16 a[148:151], v[180:183], v[84:87], a[148:151]\n"
        "v_exp_f32 v38, v38\n"
        "v_mfma_f32_16x16x32_bf16 a[152:155], v[196:199], v[84:87], a[152:155]\n"
        "ds_read_b64_tr_b16 v[116:117], v243 offset:16384\n"
        "v_cvt_pk_bf16_f32 v72, v36, v37\n"
        "v_mfma_f32_16x16x32_bf16 a[156:159], v[212:215], v[84:87], a[156:159]\n"
        "v_exp_f32 v39, v39\n"
        "v_mfma_f32_16x16x32_bf16 a[176:179], v[4:7], v[84:87], a[176:179]\n"
        "v_exp_f32 v40, v40\n"
        "v_mfma_f32_16x16x32_bf16 v[44:47], a[180:183], v[120:123], v[12:15]\n"
        "ds_read_b64_tr_b16 v[118:119], v243 offset:20480\n"
        "v_cvt_pk_bf16_f32 v73, v38, v39\n"
        "v_mfma_f32_16x16x32_bf16 v[44:47], a[184:187], v[128:131], v[44:47]\n"
        "v_exp_f32 v41, v41\n"
        "v_mfma_f32_16x16x32_bf16 v[44:47], a[188:191], v[136:139], v[44:47]\n"
        "v_exp_f32 v42, v42\n"
        "v_mfma_f32_16x16x32_bf16 v[44:47], a[192:195], v[144:147], v[44:47]\n"
        "ds_read_b64_tr_b16 v[132:133], v242 offset:16896\n"
        "v_cvt_pk_bf16_f32 v74, v40, v41\n"
        "v_mfma_f32_16x16x32_bf16 v[48:51], a[196:199], v[120:123], v[12:15]\n"
        "ds_read_b64_tr_b16 v[134:135], v242 offset:20992\n"
        "v_mfma_f32_16x16x32_bf16 v[48:51], a[200:203], v[128:131], v[48:51]\n"
        "v_exp_f32 v43, v43\n"
        "v_mfma_f32_16x16x32_bf16 v[48:51], a[204:207], v[136:139], v[48:51]\n"
        "ds_read_b64_tr_b16 v[148:149], v243 offset:16896\n"
        "v_cvt_pk_bf16_f32 v75, v42, v43\n"
        "v_mfma_f32_16x16x32_bf16 v[48:51], a[208:211], v[144:147], v[48:51]\n"
        "ds_read_b64_tr_b16 v[150:151], v243 offset:20992\n"
        "v_mfma_f32_16x16x32_bf16 v[52:55], a[212:215], v[120:123], v[12:15]\n"
        "ds_read_b64_tr_b16 v[164:165], v242 offset:17408\n"
        "v_mfma_f32_16x16x32_bf16 v[52:55], a[216:219], v[128:131], v[52:55]\n"
        "ds_read_b64_tr_b16 v[166:167], v242 offset:21504\n"
        "v_mfma_f32_16x16x32_bf16 v[52:55], a[220:223], v[136:139], v[52:55]\n"
        "ds_read_b64_tr_b16 v[180:181], v243 offset:17408\n"
        "v_mfma_f32_16x16x32_bf16 v[52:55], a[224:227], v[144:147], v[52:55]\n"
        "ds_read_b64_tr_b16 v[182:183], v243 offset:21504\n"
        "v_mfma_f32_16x16x32_bf16 v[56:59], a[228:231], v[120:123], v[12:15]\n"
        "ds_read_b64_tr_b16 v[196:197], v242 offset:17920\n"
        "v_mfma_f32_16x16x32_bf16 v[56:59], a[232:235], v[128:131], v[56:59]\n"
        "ds_read_b64_tr_b16 v[198:199], v242 offset:22016\n"
        "v_mfma_f32_16x16x32_bf16 v[56:59], a[236:239], v[136:139], v[56:59]\n"
        "ds_read_b64_tr_b16 v[212:213], v243 offset:17920\n"
        "v_mfma_f32_16x16x32_bf16 v[56:59], a[240:243], v[144:147], v[56:59]\n"
        "s_waitcnt lgkmcnt(6)\n"
        "ds_read_b64_tr_b16 v[214:215], v243 offset:22016\n"
        "v_mfma_f32_16x16x32_bf16 a[0:3], v[92:95], v[68:71], a[0:3]\n"
        "v_exp_f32 v44, v44\n"
        "v_mfma_f32_16x16x32_bf16 a[0:3], v[100:103], v[72:75], a[0:3]\n"
        "v_exp_f32 v45, v45\n"
        "v_mfma_f32_16x16x32_bf16 a[4:7], v[108:111], v[68:71], a[4:7]\n"
        "v_exp_f32 v46, v46\n"
        "v_mfma_f32_16x16x32_bf16 a[4:7], v[116:119], v[72:75], a[4:7]\n"
        "v_cvt_pk_bf16_f32 v76, v44, v45\n"
        "v_mfma_f32_16x16x32_bf16 a[8:11], v[124:127], v[68:71], a[8:11]\n"
        "v_exp_f32 v47, v47\n"
        "v_mfma_f32_16x16x32_bf16 a[8:11], v[132:135], v[72:75], a[8:11]\n"
        "v_exp_f32 v48, v48\n"
        "v_mfma_f32_16x16x32_bf16 a[12:15], v[140:143], v[68:71], a[12:15]\n"
        "s_waitcnt lgkmcnt(0)\n"
        "v_cvt_pk_bf16_f32 v77, v46, v47\n"
        "v_mfma_f32_16x16x32_bf16 a[12:15], v[148:151], v[72:75], a[12:15]\n"
        "v_exp_f32 v49, v49\n"
        "v_mfma_f32_16x16x32_bf16 a[16:19], v[156:159], v[68:71], a[16:19]\n"
        "v_exp_f32 v50, v50\n"
        "v_mfma_f32_16x16x32_bf16 a[16:19], v[164:167], v[72:75], a[16:19]\n"
        "v_cvt_pk_bf16_f32 v78, v48, v49\n"
        "s_memrealtime s[60:61]\n"
        "v_mfma_f32_16x16x32_bf16 a[20:23], v[172:175], v[68:71], a[20:23]\n"
        "v_exp_f32 v51, v51\n"
        "v_mfma_f32_16x16x32_bf16 a[20:23], v[180:183], v[72:75], a[20:23]\n"
        "v_exp_f32 v52, v52\n"
        "v_mfma_f32_16x16x32_bf16 a[24:27], v[188:191], v[68:71], a[24:27]\n"
        "v_cvt_pk_bf16_f32 v79, v50, v51\n"
        "v_mfma_f32_16x16x32_bf16 a[24:27], v[196:199], v[72:75], a[24:27]\n"
        "v_exp_f32 v53, v53\n"
        "v_mfma_f32_16x16x32_bf16 a[28:31], v[204:207], v[68:71], a[28:31]\n"
        "v_exp_f32 v54, v54\n"
        "v_mfma_f32_16x16x32_bf16 a[28:31], v[212:215], v[72:75], a[28:31]\n"
        "s_waitcnt vmcnt(8)\n"
        "v_cvt_pk_bf16_f32 v80, v52, v53\n"
        "v_smfmac_f32_16x16x64_bf16 a[160:163], v[4:7], v[68:75], v253\n"
        "v_exp_f32 v55, v55\n"
        "v_mfma_f32_16x16x32_bf16 v[28:31], a[180:183], v[152:155], v[16:19]\n"
        "s_barrier\n"
        "v_cvt_pk_bf16_f32 v81, v54, v55\n"
        "v_mfma_f32_16x16x32_bf16 v[28:31], a[184:187], v[160:163], v[28:31]\n"
        "s_add_u32 m0, s65, 98304\n"
        "v_mfma_f32_16x16x32_bf16 v[28:31], a[188:191], v[168:171], v[28:31]\n"
        "buffer_load_dwordx4 v244, s[48:51], s67 offen offset:0 nt lds\n"
        "v_mfma_f32_16x16x32_bf16 v[28:31], a[192:195], v[176:179], v[28:31]\n"
        "v_exp_f32 v56, v56\n"
        "v_mfma_f32_16x16x32_bf16 v[32:35], a[196:199], v[152:155], v[16:19]\n"
        "v_exp_f32 v57, v57\n"
        "v_mfma_f32_16x16x32_bf16 v[32:35], a[200:203], v[160:163], v[32:35]\n"
        "v_exp_f32 v58, v58\n"
        "v_mfma_f32_16x16x32_bf16 v[32:35], a[204:207], v[168:171], v[32:35]\n"
        "v_cvt_pk_bf16_f32 v82, v56, v57\n"
        "s_waitcnt lgkmcnt(0)\n"
        "s_sub_u32 s62, s60, s83\n"
        "v_mfma_f32_16x16x32_bf16 v[32:35], a[208:211], v[176:179], v[32:35]\n"
        "v_exp_f32 v59, v59\n"
        "v_mfma_f32_16x16x32_bf16 v[36:39], a[212:215], v[152:155], v[16:19]\n"
        "v_cvt_pk_bf16_f32 v83, v58, v59\n"
        "s_bitcmp1_b32 s62, 31\n"
        "s_cbranch_scc1 .Lk_PACE2\n"
        ".Lk_PACER2:\n"
        "v_mfma_f32_16x16x32_bf16 v[36:39], a[216:219], v[160:163], v[36:39]\n"
        "v_exp_f32 v28, v28\n"
        "v_mfma_f32_16x16x32_bf16 v[36:39], a[220:223], v[168:171], v[36:39]\n"
        "buffer_load_dwordx4 v245, s[48:51], s67 offen offset:1024 nt lds\n"
        "v_mfma_f32_16x16x32_bf16 v[36:39], a[224:227], v[176:179], v[36:39]\n"
        "v_exp_f32 v29, v29\n"
        "v_mfma_f32_16x16x32_bf16 v[40:43], a[228:231], v[152:155], v[16:19]\n"
        "v_exp_f32 v30, v30\n"
        "v_mfma_f32_16x16x32_bf16 v[40:43], a[232:235], v[160:163], v[40:43]\n"
        "v_cvt_pk_bf16_f32 v68, v28, v29\n"
        "s_sub_u32 s79, s79, 1\n"
        "v_mfma_f32_16x16x32_bf16 v[40:43], a[236:239], v[168:171], v[40:43]\n"
        "v_exp_f32 v31, v31\n"
        "v_mfma_f32_16x16x32_bf16 v[40:43], a[240:243], v[176:179], v[40:43]\n"
        "v_exp_f32 v32, v32\n"
        "v_mfma_f32_16x16x32_bf16 a[32:35], v[92:95], v[76:79], a[32:35]\n"
        "v_cvt_pk_bf16_f32 v69, v30, v31\n"
        "s_max_u32 s79, s79, 130\n"
        "v_mfma_f32_16x16x32_bf16 a[32:35], v[100:103], v[80:83], a[32:35]\n"
        "v_exp_f32 v33, v33\n"
        "v_mfma_f32_16x16x32_bf16 a[36:39], v[108:111], v[76:79], a[36:39]\n"
        "buffer_load_dwordx4 v246, s[48:51], s67 offen offset:2048 nt lds\n"
        "v_mfma_f32_16x16x32_bf16 a[36:39], v[116:119], v[80:83], a[36:39]\n"
        "v_exp_f32 v34, v34\n"
        "v_mfma_f32_16x16x32_bf16 a[40:43], v[124:127], v[76:79], a[40:43]\n"
        "v_cvt_pk_bf16_f32 v70, v32, v33\n"
        "s_cmp_gt_u32 s62, s63\n"
        "v_mfma_f32_16x16x32_bf16 a[40:43], v[132:135], v[80:83], a[40:43]\n"
        "v_exp_f32 v35, v35\n"
        "v_mfma_f32_16x16x32_bf16 a[44:47], v[140:143], v[76:79], a[44:47]\n"
        "v_exp_f32 v36, v36\n"
        "v_mfma_f32_16x16x32_bf16 a[44:47], v[148:151], v[80:83], a[44:47]\n"
        "v_cvt_pk_bf16_f32 v71, v34, v35\n"
        "s_cselect_b32 s79, 152, s79\n"
        "v_mfma_f32_16x16x32_bf16 a[48:51], v[156:159], v[76:79], a[48:51]\n"
        "v_exp_f32 v37, v37\n"
        "v_mfma_f32_16x16x32_bf16 a[48:51], v[164:167], v[80:83], a[48:51]\n"
        "v_exp_f32 v38, v38\n"
        "v_mfma_f32_16x16x32_bf16 a[52:55], v[172:175], v[76:79], a[52:55]\n"
        "buffer_load_dwordx4 v247, s[48:51], s67 offen offset:3072 nt lds\n"
        "v_mfma_f32_16x16x32_bf16 a[52:55], v[180:183], v[80:83], a[52:55]\n"
        "v_cvt_pk_bf16_f32 v72, v36, v37\n"
        "s_min_u32 s62, s62, 8\n"
        "v_mfma_f32_16x16x32_bf16 a[56:59], v[188:191], v[76:79], a[56:59]\n"
        "v_exp_f32 v39, v39\n"
        "v_mfma_f32_16x16x32_bf16 a[56:59], v[196:199], v[80:83], a[56:59]\n"
        "v_exp_f32 v40, v40\n"
        "v_mfma_f32_16x16x32_bf16 a[60:63], v[204:207], v[76:79], a[60:63]\n"
        "v_cvt_pk_bf16_f32 v73, v38, v39\n"
        "s_sub_u32 s60, s60, s62\n"
        "v_mfma_f32_16x16x32_bf16 a[60:63], v[212:215], v[80:83], a[60:63]\n"
        "v_exp_f32 v41, v41\n"
        "v_smfmac_f32_16x16x64_bf16 a[164:167], v[4:7], v[76:83], v253\n"
        "v_exp_f32 v42, v42\n"
        "v_mfma_f32_16x16x32_bf16 v[44:47], a[180:183], v[184:187], v[20:23]\n"
        "s_add_u32 m0, s89, 131072\n"
        "ds_read_b128 a[180:183], v232 offset:0\n"
        "v_cvt_pk_bf16_f32 v74, v40, v41\n"
        "v_mfma_f32_16x16x32_bf16 v[44:47], a[184:187], v[192:195], v[44:47]\n"
        "buffer_load_dwordx4 v248, s[52:55], s67 offen offset:0 nt lds\n"
        "v_mfma_f32_16x16x32_bf16 v[44:47], a[188:191], v[200:203], v[44:47]\n"
        "v_exp_f32 v43, v43\n"
        "v_mfma_f32_16x16x32_bf16 v[44:47], a[192:195], v[208:211], v[44:47]\n"
        "ds_read_b128 a[184:187], v234 offset:0\n"
        "v_cvt_pk_bf16_f32 v75, v42, v43\n"
        "v_mfma_f32_16x16x32_bf16 v[48:51], a[196:199], v[184:187], v[20:23]\n"
        "ds_read_b128 a[188:191], v236 offset:0\n"
        "v_mfma_f32_16x16x32_bf16 v[48:51], a[200:203], v[192:195], v[48:51]\n"
        "ds_read_b128 a[192:195], v238 offset:0\n"
        "v_mfma_f32_16x16x32_bf16 v[48:51], a[204:207], v[200:203], v[48:51]\n"
        "ds_read_b128 a[196:199], v232 offset:4096\n"
        "v_mfma_f32_16x16x32_bf16 v[48:51], a[208:211], v[208:211], v[48:51]\n"
        "v_exp_f32 v44, v44\n"
        "v_mfma_f32_16x16x32_bf16 v[52:55], a[212:215], v[184:187], v[20:23]\n"
        "v_exp_f32 v45, v45\n"
        "v_mfma_f32_16x16x32_bf16 v[52:55], a[216:219], v[192:195], v[52:55]\n"
        "buffer_load_dwordx4 v249, s[52:55], s67 offen offset:1024 nt lds\n"
        "v_mfma_f32_16x16x32_bf16 v[52:55], a[220:223], v[200:203], v[52:55]\n"
        "v_exp_f32 v46, v46\n"
        "v_mfma_f32_16x16x32_bf16 v[52:55], a[224:227], v[208:211], v[52:55]\n"
        "ds_read_b128 a[200:203], v234 offset:4096\n"
        "v_cvt_pk_bf16_f32 v76, v44, v45\n"
        "v_mfma_f32_16x16x32_bf16 v[56:59], a[228:231], v[184:187], v[20:23]\n"
        "v_exp_f32 v47, v47\n"
        "v_mfma_f32_16x16x32_bf16 v[56:59], a[232:235], v[192:195], v[56:59]\n"
        "v_exp_f32 v48, v48\n"
        "v_mfma_f32_16x16x32_bf16 v[56:59], a[236:239], v[200:203], v[56:59]\n"
        "ds_read_b128 a[204:207], v236 offset:4096\n"
        "v_cvt_pk_bf16_f32 v77, v46, v47\n"
        "v_mfma_f32_16x16x32_bf16 v[56:59], a[240:243], v[208:211], v[56:59]\n"
        "v_exp_f32 v49, v49\n"
        "v_mfma_f32_16x16x32_bf16 a[64:67], v[92:95], v[68:71], a[64:67]\n"
        "v_exp_f32 v50, v50\n"
        "v_mfma_f32_16x16x32_bf16 a[64:67], v[100:103], v[72:75], a[64:67]\n"
        "buffer_load_dwordx4 v248, s[52:55], s67 offen offset:2048 nt lds\n"
        "v_mfma_f32_16x16x32_bf16 a[68:71], v[108:111], v[68:71], a[68:71]\n"
        "ds_read_b128 a[208:211], v238 offset:4096\n"
        "v_cvt_pk_bf16_f32 v78, v48, v49\n"
        "v_mfma_f32_16x16x32_bf16 a[68:71], v[116:119], v[72:75], a[68:71]\n"
        "v_exp_f32 v51, v51\n"
        "v_mfma_f32_16x16x32_bf16 a[72:75], v[124:127], v[68:71], a[72:75]\n"
        "v_exp_f32 v52, v52\n"
        "v_mfma_f32_16x16x32_bf16 a[72:75], v[132:135], v[72:75], a[72:75]\n"
        "v_cvt_pk_bf16_f32 v79, v50, v51\n"
        "s_add_u32 s83, s60, s79\n"
        "v_mfma_f32_16x16x32_bf16 a[76:79], v[140:143], v[68:71], a[76:79]\n"
        "v_exp_f32 v53, v53\n"
        "v_mfma_f32_16x16x32_bf16 a[76:79], v[148:151], v[72:75], a[76:79]\n"
        "v_exp_f32 v54, v54\n"
        "v_mfma_f32_16x16x32_bf16 a[80:83], v[156:159], v[68:71], a[80:83]\n"
        "v_cvt_pk_bf16_f32 v80, v52, v53\n"
        "v_mfma_f32_16x16x32_bf16 a[80:83], v[164:167], v[72:75], a[80:83]\n"
        "buffer_load_dwordx4 v249, s[52:55], s67 offen offset:3072 nt lds\n"
        "v_mfma_f32_16x16x32_bf16 a[84:87], v[172:175], v[68:71], a[84:87]\n"
        "s_add_u32 s67, s67, 16384\n"
        "v_mfma_f32_16x16x32_bf16 a[84:87], v[180:183], v[72:75], a[84:87]\n"
        "v_exp_f32 v55, v55\n"
        "v_mfma_f32_16x16x32_bf16 a[88:91], v[188:191], v[68:71], a[88:91]\n"
        "v_exp_f32 v56, v56\n"
        "v_mfma_f32_16x16x32_bf16 a[88:91], v[196:199], v[72:75], a[88:91]\n"
        "v_cvt_pk_bf16_f32 v81, v54, v55\n"
        "v_mfma_f32_16x16x32_bf16 a[92:95], v[204:207], v[68:71], a[92:95]\n"
        "v_exp_f32 v57, v57\n"
        "v_mfma_f32_16x16x32_bf16 a[92:95], v[212:215], v[72:75], a[92:95]\n"
        "v_exp_f32 v58, v58\n"
        "v_smfmac_f32_16x16x64_bf16 a[168:171], v[4:7], v[68:75], v253\n"
        "s_sub_u32 s90, s90, 1\n"
        "v_cvt_pk_bf16_f32 v82, v56, v57\n"
        "v_mfma_f32_16x16x32_bf16 v[60:63], a[212:215], v[216:219], v[24:27]\n"
        "v_exp_f32 v59, v59\n"
        "v_mfma_f32_16x16x32_bf16 v[60:63], a[216:219], v[220:223], v[60:63]\n"
        "ds_read_b128 a[212:215], v233 offset:0\n"
        "v_cvt_pk_bf16_f32 v83, v58, v59\n"
        "v_mfma_f32_16x16x32_bf16 v[60:63], a[220:223], v[224:227], v[60:63]\n"
        "ds_read_b128 a[216:219], v235 offset:0\n"
        "v_mfma_f32_16x16x32_bf16 v[60:63], a[224:227], v[228:231], v[60:63]\n"
        "ds_read_b128 a[220:223], v237 offset:0\n"
        "v_mfma_f32_16x16x32_bf16 v[64:67], a[228:231], v[216:219], v[24:27]\n"
        "ds_read_b128 a[224:227], v239 offset:0\n"
        "v_mfma_f32_16x16x32_bf16 v[64:67], a[232:235], v[220:223], v[64:67]\n"
        "ds_read_b128 a[228:231], v233 offset:4096\n"
        "v_mfma_f32_16x16x32_bf16 v[64:67], a[236:239], v[224:227], v[64:67]\n"
        "ds_read_b128 a[232:235], v235 offset:4096\n"
        "v_mfma_f32_16x16x32_bf16 v[64:67], a[240:243], v[228:231], v[64:67]\n"
        "v_exp_f32 v60, v60\n"
        "v_mfma_f32_16x16x32_bf16 a[96:99], v[92:95], v[76:79], a[96:99]\n"
        "v_exp_f32 v61, v61\n"
        "v_mfma_f32_16x16x32_bf16 a[96:99], v[100:103], v[80:83], a[96:99]\n"
        "v_exp_f32 v62, v62\n"
        "v_mfma_f32_16x16x32_bf16 a[100:103], v[108:111], v[76:79], a[100:103]\n"
        "ds_read_b128 a[236:239], v237 offset:4096\n"
        "v_cvt_pk_bf16_f32 v84, v60, v61\n"
        "v_mfma_f32_16x16x32_bf16 a[100:103], v[116:119], v[80:83], a[100:103]\n"
        "v_exp_f32 v63, v63\n"
        "v_mfma_f32_16x16x32_bf16 a[104:107], v[124:127], v[76:79], a[104:107]\n"
        "v_exp_f32 v64, v64\n"
        "v_mfma_f32_16x16x32_bf16 a[104:107], v[132:135], v[80:83], a[104:107]\n"
        "ds_read_b128 a[240:243], v239 offset:4096\n"
        "v_cvt_pk_bf16_f32 v85, v62, v63\n"
        "v_mfma_f32_16x16x32_bf16 a[108:111], v[140:143], v[76:79], a[108:111]\n"
        "v_exp_f32 v65, v65\n"
        "v_mfma_f32_16x16x32_bf16 a[108:111], v[148:151], v[80:83], a[108:111]\n"
        "v_exp_f32 v66, v66\n"
        "v_mfma_f32_16x16x32_bf16 a[112:115], v[156:159], v[76:79], a[112:115]\n"
        "ds_read_b64_tr_b16 v[92:93], v240 offset:32768\n"
        "v_cvt_pk_bf16_f32 v86, v64, v65\n"
        "v_mfma_f32_16x16x32_bf16 a[112:115], v[164:167], v[80:83], a[112:115]\n"
        "v_exp_f32 v67, v67\n"
        "v_mfma_f32_16x16x32_bf16 a[116:119], v[172:175], v[76:79], a[116:119]\n"
        "ds_read_b64_tr_b16 v[94:95], v240 offset:36864\n"
        "v_cvt_pk_bf16_f32 v87, v66, v67\n"
        "v_mfma_f32_16x16x32_bf16 a[116:119], v[180:183], v[80:83], a[116:119]\n"
        "ds_read_b64_tr_b16 v[108:109], v241 offset:32768\n"
        "v_mfma_f32_16x16x32_bf16 a[120:123], v[188:191], v[76:79], a[120:123]\n"
        "ds_read_b64_tr_b16 v[110:111], v241 offset:36864\n"
        "v_mfma_f32_16x16x32_bf16 a[120:123], v[196:199], v[80:83], a[120:123]\n"
        "ds_read_b64_tr_b16 v[124:125], v240 offset:33280\n"
        "v_mfma_f32_16x16x32_bf16 a[124:127], v[204:207], v[76:79], a[124:127]\n"
        "ds_read_b64_tr_b16 v[126:127], v240 offset:37376\n"
        "v_mfma_f32_16x16x32_bf16 a[124:127], v[212:215], v[80:83], a[124:127]\n"
        "s_waitcnt lgkmcnt(5)\n"
        "ds_read_b64_tr_b16 v[140:141], v241 offset:33280\n"
        "v_smfmac_f32_16x16x64_bf16 a[172:175], v[4:7], v[76:83], v253\n"
        "s_cbranch_scc1 .Lk_END2\n"
        "s_cbranch_vccz .Lk_T0\n"
        "s_mov_b64 vcc, 0\n"
        "s_cmp_lg_u32 s77, 0\n"
        "s_cbranch_scc1 .Lk_T0\n"
        "s_lshl_b32 s80, %7, 8\n"
        "s_lshl_b32 s81, %6, 2\n"
        "s_add_u32 s80, s80, s81\n"
        "v_mov_b32 v1, s95\n"
        "buffer_store_dword v1, off, s[96:99], s80 offset:128 sc1\n"
        "s_branch .Lk_T0\n"
        ".Lk_END0:\n"
        "s_cmp_eq_u32 s67, 311296\n"
        "s_cbranch_scc1 .Lk_EPIA\n"
        "s_cmp_lg_u32 s69, 0\n"
        "s_cbranch_scc1 .Lk_WOD\n"
        "s_bitcmp1_b32 %12, 0\n"
        "s_cbranch_scc0 .Lk_RS0\n"
        "s_sleep 8\n"
        "s_add_u32 s83, s83, 18\n"
        ".Lk_RS0:\n"
        "s_cmp_lt_u32 s67, 114688\n"
        "s_cselect_b32 s68, 0, 3\n"
        "s_branch .Lk_T1\n"
        ".Lk_END1:\n"
        "s_cmp_lg_u32 s69, 0\n"
        "s_cbranch_scc1 .Lk_WO1\n"
        "s_bitcmp1_b32 %12, 0\n"
        "s_cbranch_scc0 .Lk_RS1\n"
        "s_sleep 8\n"
        "s_add_u32 s83, s83, 18\n"
        ".Lk_RS1:\n"
        "s_cmp_lt_u32 s67, 114688\n"
        "s_cselect_b32 s91, 0, 99\n"
        "s_branch .Lk_T2\n"
        ".Lk_END2:\n"
        "s_cmp_lg_u32 s69, 0\n"
        "s_cbranch_scc1 .Lk_WOT\n"
        "s_mov_b32 s63, 46\n"
        "s_bitcmp1_b32 %12, 0\n"
        "s_cbranch_scc0 .Lk_RS2\n"
        "s_sleep 8\n"
        "s_add_u32 s83, s83, 18\n"
        ".Lk_RS2:\n"
        "s_cmp_lt_u32 s67, 114688\n"
        "s_cselect_b32 s90, 0, 99\n"
        "s_branch .Lk_T0\n"
        ".Lk_PACE0:\n"
        "s_max_u32 s62, s62, 0xfffffe00\n"
        "s_mov_b32 s60, s83\n"
        ".Lk_PACL0:\n"
        "s_nop 15\n"
        "s_add_u32 s62, s62, 3\n"
        "s_cbranch_scc0 .Lk_PACL0\n"
        "s_mov_b32 s62, 0\n"
        "s_branch .Lk_PACER0\n"
        ".Lk_PACE1:\n"
        "s_max_u32 s62, s62, 0xfffffe00\n"
        "s_mov_b32 s60, s83\n"
        ".Lk_PACL1:\n"
        "s_nop 15\n"
        "s_add_u32 s62, s62, 3\n"
        "s_cbranch_scc0 .Lk_PACL1\n"
        "s_mov_b32 s62, 0\n"
        "s_branch .Lk_PACER1\n"
        ".Lk_PACE2:\n"
        "s_max_u32 s62, s62, 0xfffffe00\n"
        "s_mov_b32 s60, s83\n"
        ".Lk_PACL2:\n"
        "s_nop 15\n"
        "s_add_u32 s62, s62, 3\n"
        "s_cbranch_scc0 .Lk_PACL2\n"
        "s_mov_b32 s62, 0\n"
        "s_branch .Lk_PACER2\n"
        ".Lk_WOT:\n"
        "s_cmp_gt_u32 s67, 491520\n"
        "s_cbranch_scc1 .Lk_WOT2\n"
        "s_mov_b32 s63, 46\n"
        "s_mov_b32 s90, 2\n"
        "s_branch .Lk_T0\n"
        ".Lk_WOT2:\n"
        "s_mov_b32 s90, 99\n"
        "s_mov_b64 s[40:41], %11\n"
        "s_mov_b32 s42, 5242880\n"
        "s_mov_b32 s43, 0x20000\n"
        "s_lshr_b32 s80, %7, 3\n"
        "s_mul_i32 s80, s80, 2621440\n"
        "s_and_b32 s81, %12, 31\n"
        "s_mul_i32 s81, s81, 81920\n"
        "s_add_u32 s80, s80, s81\n"
        "s_mul_i32 s81, s64, 20480\n"
        "s_add_u32 s80, s80, s81\n"
        "s_bfe_u32 s81, %12, 0x10005\n"
        "s_mul_i32 s81, s81, 10240\n"
        "s_add_u32 s80, s80, s81\n"
        "v_lshlrev_b32 v1, 7, v0\n"
        "v_min_u32 v2, 15, v0\n"
        "v_lshlrev_b32 v2, 7, v2\n"
        "buffer_load_dword a244, v1, s[40:43], s80 offen\n"
        "s_add_u32 s80, s80, 8192\n"
        "buffer_load_dword a246, v2, s[40:43], s80 offen\n"
        "s_mov_b64 vcc, -1\n"
        "s_add_u32 s48, s48, 65536\n"
        "s_addc_u32 s49, s49, 0\n"
        "s_mov_b32 s50, 622592\n"
        "s_sub_u32 s65, s92, 45056\n"
        "s_add_u32 m0, s92, 4096\n"
        "s_add_u32 s80, s93, 163840\n"
        "buffer_load_dwordx4 v248, s[44:47], s80 offen offset:0 lds\n"
        "s_add_u32 m0, s92, 24576\n"
        "s_add_u32 s80, s93, 245760\n"
        "buffer_load_dwordx4 v248, s[44:47], s80 offen offset:0 lds\n"
        "s_branch .Lk_T0\n"
        ".Lk_WO1:\n"
        "s_cmp_lg_u32 s67, 524288\n"
        "s_cbranch_scc1 .Lk_LAST\n"
        "s_mov_b32 s91, 0\n"
        "s_mov_b32 s54, 0\n"
        "s_mov_b32 s89, 49152\n"
        "s_mov_b32 s63, -1\n"
        "s_and_b32 s40, %7, 3\n"
        "s_mul_i32 s41, s40, 320\n"
        "s_sub_u32 s46, 1310720, s41\n"
        "s_mul_i32 s42, s85, 1310720\n"
        "s_add_u32 s41, s41, s42\n"
        "s_mov_b64 s[44:45], %11\n"
        "s_add_u32 s44, s44, s41\n"
        "s_addc_u32 s45, s45, 0\n"
        "s_mov_b32 s47, 0x20000\n"
        "s_mul_i32 s41, s64, 320\n"
        "v_add_u32 v1, s41, v0\n"
        "s_add_u32 s41, s41, 64\n"
        "v_mul_u32_u24 v2, 3277, v1\n"
        "v_lshrrev_b32 v2, 16, v2\n"
        "v_mul_u32_u24 v3, 20, v2\n"
        "v_sub_u32 v1, v1, v3\n"
        "v_lshrrev_b32 v3, 1, v2\n"
        "v_and_b32 v3, 2, v3\n"
        "v_xor_b32 v1, v1, v3\n"
        "v_mul_u32_u24 v2, 1280, v2\n"
        "v_lshl_add_u32 v244, v1, 4, v2\n"
        "v_add_u32 v1, s41, v0\n"
        "s_add_u32 s41, s41, 64\n"
        "v_mul_u32_u24 v2, 3277, v1\n"
        "v_lshrrev_b32 v2, 16, v2\n"
        "v_mul_u32_u24 v3, 20, v2\n"
        "v_sub_u32 v1, v1, v3\n"
        "v_lshrrev_b32 v3, 1, v2\n"
        "v_and_b32 v3, 2, v3\n"
        "v_xor_b32 v1, v1, v3\n"
        "v_mul_u32_u24 v2, 1280, v2\n"
        "v_lshl_add_u32 v245, v1, 4, v2\n"
        "v_subrev_u32 v245, 1024, v245\n"
        "v_add_u32 v1, s41, v0\n"
        "s_add_u32 s41, s41, 64\n"
        "v_mul_u32_u24 v2, 3277, v1\n"
        "v_lshrrev_b32 v2, 16, v2\n"
        "v_mul_u32_u24 v3, 20, v2\n"
        "v_sub_u32 v1, v1, v3\n"
        "v_lshrrev_b32 v3, 1, v2\n"
        "v_and_b32 v3, 2, v3\n"
        "v_xor_b32 v1, v1, v3\n"
        "v_mul_u32_u24 v2, 1280, v2\n"
        "v_lshl_add_u32 v246, v1, 4, v2\n"
        "v_subrev_u32 v246, 2048, v246\n"
        "v_add_u32 v1, s41, v0\n"
        "s_add_u32 s41, s41, 64\n"
        "v_mul_u32_u24 v2, 3277, v1\n"
        "v_lshrrev_b32 v2, 16, v2\n"
        "v_mul_u32_u24 v3, 20, v2\n"
        "v_sub_u32 v1, v1, v3\n"
        "v_lshrrev_b32 v3, 1, v2\n"
        "v_and_b32 v3, 2, v3\n"
        "v_xor_b32 v1, v1, v3\n"
        "v_mul_u32_u24 v2, 1280, v2\n"
        "v_lshl_add_u32 v247, v1, 4, v2\n"
        "v_subrev_u32 v247, 3072, v247\n"
        "v_add_u32 v1, s41, v0\n"
        "v_mul_u32_u24 v2, 3277, v1\n"
        "v_lshrrev_b32 v2, 16, v2\n"
        "v_mul_u32_u24 v3, 20, v2\n"
        "v_sub_u32 v1, v1, v3\n"
        "v_lshrrev_b32 v3, 1, v2\n"
        "v_and_b32 v3, 2, v3\n"
        "v_xor_b32 v1, v1, v3\n"
        "v_mul_u32_u24 v2, 1280, v2\n"
        "v_lshl_add_u32 v248, v1, 4, v2\n"
        "s_mul_i32 s93, s40, 327680\n"
        "s_mul_i32 s92, s64, 5120\n"
        "s_add_u32 s48, s44, s93\n"
        "s_addc_u32 s49, s45, 0\n"
        "s_sub_u32 s48, s48, 360448\n"
        "s_subb_u32 s49, s49, 0\n"
        "s_mov_b32 s50, 606208\n"
        "s_sub_u32 s65, s92, 98304\n"
        "s_branch .Lk_T2\n"
        ".Lk_WOD:\n"
        "s_sub_u32 s48, s48, 262144\n"
        "s_subb_u32 s49, s49, 0\n"
        "s_mov_b32 s50, 638976\n"
        "s_sub_u32 s65, s92, 40960\n"
        "s_add_u32 m0, s92, 45056\n"
        "s_add_u32 s80, s93, 0\n"
        "buffer_load_dwordx4 v248, s[44:47], s80 offen offset:0 lds\n"
        "s_branch .Lk_T1\n"
        ".Lk_EPIA:\n"
        "s_cmp_eq_u32 s78, 1\n"
        "s_cbranch_scc1 .Lk_EPIA1\n"
        "ds_read_b128 v[88:91], v251 offset:0\n"
        "v_accvgpr_read_b32 v24, a160\n"
        "v_mfma_f32_16x16x32_bf16 a[144:147], v[164:167], v[84:87], a[144:147]\n"
        "ds_read_b128 v[96:99], v251 offset:1024\n"
        "v_accvgpr_read_b32 v26, a164\n"
        "v_mfma_f32_16x16x32_bf16 a[148:151], v[180:183], v[84:87], a[148:151]\n"
        "ds_read_b128 v[104:107], v251 offset:2048\n"
        "v_accvgpr_read_b32 v2, a161\n"
        "v_mfma_f32_16x16x32_bf16 a[152:155], v[196:199], v[84:87], a[152:155]\n"
        "ds_read_b128 v[112:115], v251 offset:3072\n"
        "v_accvgpr_read_b32 v3, a165\n"
        "v_mfma_f32_16x16x32_bf16 a[156:159], v[212:215], v[84:87], a[156:159]\n"
        "ds_read_b128 v[120:123], v251 offset:4096\n"
        "v_add_f32 v24, v24, v2\n"
        "v_mfma_f32_16x16x32_bf16 a[176:179], v[4:7], v[84:87], a[176:179]\n"
        "ds_read_b128 v[128:131], v251 offset:5120\n"
        "v_add_f32 v26, v26, v3\n"
        "v_mfma_f32_16x16x32_bf16 a[128:131], v[100:103], v[84:87], a[128:131]\n"
        "ds_read_b128 v[136:139], v251 offset:6144\n"
        "v_accvgpr_read_b32 v25, a168\n"
        "v_mfma_f32_16x16x32_bf16 a[132:135], v[116:119], v[84:87], a[132:135]\n"
        "ds_read_b128 v[144:147], v251 offset:7168\n"
        "v_accvgpr_read_b32 v27, a172\n"
        "v_mfma_f32_16x16x32_bf16 a[136:139], v[132:135], v[84:87], a[136:139]\n"
        "ds_read_b128 v[152:155], v251 offset:8192\n"
        "v_accvgpr_read_b32 v2, a169\n"
        "v_mfma_f32_16x16x32_bf16 a[140:143], v[148:151], v[84:87], a[140:143]\n"
        "ds_read_b128 v[160:163], v251 offset:9216\n"
        "v_mov_b32 v84, 0\n"
        "s_waitcnt lgkmcnt(9)\n"
        "v_mfma_f32_16x16x32_bf16 v[28:31], a[180:183], v[88:91], 0\n"
        "ds_read_b128 v[168:171], v251 offset:10240\n"
        "v_mov_b32 v85, 0\n"
        "v_mov_b32 v86, 0\n"
        "s_waitcnt lgkmcnt(9)\n"
        "v_mfma_f32_16x16x32_bf16 v[28:31], a[184:187], v[96:99], v[28:31]\n"
        "ds_read_b128 v[176:179], v251 offset:11264\n"
        "v_mov_b32 v87, 0\n"
        "s_waitcnt lgkmcnt(9)\n"
        "v_mfma_f32_16x16x32_bf16 v[28:31], a[188:191], v[104:107], v[28:31]\n"
        "ds_read_b128 v[184:187], v251 offset:12288\n"
        "v_accvgpr_read_b32 v3, a173\n"
        "s_waitcnt lgkmcnt(9)\n"
        "v_mfma_f32_16x16x32_bf16 v[28:31], a[192:195], v[112:115], v[28:31]\n"
        "ds_read_b128 v[192:195], v251 offset:13312\n"
        "v_add_f32 v25, v25, v2\n"
        "s_waitcnt lgkmcnt(9)\n"
        "v_mfma_f32_16x16x32_bf16 v[32:35], a[180:183], v[120:123], 0\n"
        "ds_read_b128 v[200:203], v251 offset:14336\n"
        "v_add_f32 v27, v27, v3\n"
        "s_waitcnt lgkmcnt(9)\n"
        "v_mfma_f32_16x16x32_bf16 v[32:35], a[184:187], v[128:131], v[32:35]\n"
        "ds_read_b128 v[208:211], v251 offset:15360\n"
        "v_max3_f32 v3, v24, v25, v26\n"
        "s_waitcnt lgkmcnt(9)\n"
        "v_mfma_f32_16x16x32_bf16 v[32:35], a[188:191], v[136:139], v[32:35]\n"
        "ds_write_b128 v251, a[144:147] offset:0\n"
        "v_rcp_f32 v24, v24\n"
        "s_waitcnt lgkmcnt(9)\n"
        "v_mfma_f32_16x16x32_bf16 v[32:35], a[192:195], v[144:147], v[32:35]\n"
        "v_accvgpr_read_b32 v52, a0\n"
        "s_waitcnt lgkmcnt(8)\n"
        "v_mfma_f32_16x16x32_bf16 v[36:39], a[180:183], v[152:155], 0\n"
        "v_accvgpr_read_b32 v53, a1\n"
        "v_accvgpr_read_b32 v54, a2\n"
        "s_waitcnt lgkmcnt(7)\n"
        "v_mfma_f32_16x16x32_bf16 v[36:39], a[184:187], v[160:163], v[36:39]\n"
        "ds_write_b128 v251, a[148:151] offset:1024\n"
        "v_accvgpr_read_b32 v55, a3\n"
        "s_waitcnt lgkmcnt(7)\n"
        "v_mfma_f32_16x16x32_bf16 v[36:39], a[188:191], v[168:171], v[36:39]\n"
        "v_accvgpr_read_b32 v56, a4\n"
        "v_accvgpr_read_b32 v57, a5\n"
        "v_mfma_f32_16x16x32_bf16 a[0:3], v[84:87], v[84:87], 0\n"
        "v_accvgpr_read_b32 v58, a6\n"
        "v_accvgpr_read_b32 v59, a7\n"
        "s_waitcnt lgkmcnt(6)\n"
        "v_mfma_f32_16x16x32_bf16 v[36:39], a[192:195], v[176:179], v[36:39]\n"
        "ds_write_b128 v251, a[152:155] offset:2048\n"
        "v_accvgpr_read_b32 v60, a8\n"
        "s_waitcnt lgkmcnt(6)\n"
        "v_mfma_f32_16x16x32_bf16 v[40:43], a[180:183], v[184:187], 0\n"
        "v_accvgpr_read_b32 v61, a9\n"
        "v_accvgpr_read_b32 v62, a10\n"
        "s_waitcnt lgkmcnt(5)\n"
        "v_mfma_f32_16x16x32_bf16 v[40:43], a[184:187], v[192:195], v[40:43]\n"
        "v_accvgpr_read_b32 v63, a11\n"
        "s_waitcnt lgkmcnt(4)\n"
        "v_mfma_f32_16x16x32_bf16 v[40:43], a[188:191], v[200:203], v[40:43]\n"
        "v_accvgpr_read_b32 v64, a12\n"
        "ds_write_b128 v251, a[156:159] offset:3072\n"
        "s_waitcnt lgkmcnt(4)\n"
        "v_mfma_f32_16x16x32_bf16 v[40:43], a[192:195], v[208:211], v[40:43]\n"
        "v_accvgpr_read_b32 v65, a13\n"
        "v_accvgpr_read_b32 v66, a14\n"
        "v_mfma_f32_16x16x32_bf16 a[4:7], v[84:87], v[84:87], 0\n"
        "v_accvgpr_read_b32 v67, a15\n"
        "v_accvgpr_read_b32 v68, a16\n"
        "v_accvgpr_read_b32 v69, a17\n"
        "v_pk_mul_f32 v[52:53], v[52:53], v[24:25] op_sel_hi:[1,0]\n"
        "v_pk_mul_f32 v[54:55], v[54:55], v[24:25] op_sel_hi:[1,0]\n"
        "v_pk_mul_f32 v[56:57], v[56:57], v[24:25] op_sel_hi:[1,0]\n"
        "v_pk_mul_f32 v[58:59], v[58:59], v[24:25] op_sel_hi:[1,0]\n"
        "v_pk_mul_f32 v[60:61], v[60:61], v[24:25] op_sel_hi:[1,0]\n"
        "v_pk_mul_f32 v[62:63], v[62:63], v[24:25] op_sel_hi:[1,0]\n"
        "v_pk_mul_f32 v[64:65], v[64:65], v[24:25] op_sel_hi:[1,0]\n"
        "v_pk_mul_f32 v[66:67], v[66:67], v[24:25] op_sel_hi:[1,0]\n"
        "ds_write_b128 v251, a[176:179] offset:8192\n"
        "v_mfma_f32_16x16x32_bf16 a[8:11], v[84:87], v[84:87], 0\n"
        "v_cvt_pk_bf16_f32 v52, v52, v53\n"
        "v_mfma_f32_16x16x32_bf16 a[12:15], v[84:87], v[84:87], 0\n"
        "v_cvt_pk_bf16_f32 v53, v54, v55\n"
        "v_cvt_pk_bf16_f32 v54, v56, v57\n"
        "v_cvt_pk_bf16_f32 v55, v58, v59\n"
        "s_waitcnt vmcnt(8)\n"
        "buffer_store_dwordx4 v[52:55], v250, s[56:59], s72 offen offset:0 sc1\n"
        "v_cvt_pk_bf16_f32 v56, v60, v61\n"
        "v_mfma_f32_16x16x32_bf16 a[252:255], v[84:87], v[52:55], 0\n"
        "ds_write_b128 v251, a[244:247] offset:4096\n"
        "v_cvt_pk_bf16_f32 v57, v62, v63\n"
        "v_cvt_pk_bf16_f32 v58, v64, v65\n"
        "v_cvt_pk_bf16_f32 v59, v66, v67\n"
        "v_accvgpr_read_b32 v70, a18\n"
        "v_accvgpr_read_b32 v71, a19\n"
        "v_mfma_f32_16x16x32_bf16 a[252:255], v[84:87], v[56:59], a[252:255]\n"
        "v_accvgpr_read_b32 v72, a20\n"
        "v_mfma_f32_16x16x32_bf16 a[16:19], v[84:87], v[84:87], 0\n"
        "ds_write_b128 v251, a[248:251] offset:5120\n"
        "v_accvgpr_read_b32 v73, a21\n"
        "v_accvgpr_read_b32 v74, a22\n"
        "v_accvgpr_read_b32 v75, a23\n"
        "v_mfma_f32_16x16x32_bf16 a[20:23], v[84:87], v[84:87], 0\n"
        "buffer_store_dwordx4 v[56:59], v250, s[56:59], s72 offen offset:1024 sc1\n"
        "v_accvgpr_read_b32 v76, a24\n"
        "v_accvgpr_read_b32 v77, a25\n"
        "v_accvgpr_read_b32 v78, a26\n"
        "v_accvgpr_read_b32 v79, a27\n"
        "v_mfma_f32_16x16x32_bf16 a[24:27], v[84:87], v[84:87], 0\n"
        "v_accvgpr_read_b32 v80, a28\n"
        "v_accvgpr_read_b32 v81, a29\n"
        "v_accvgpr_read_b32 v82, a30\n"
        "v_accvgpr_read_b32 v83, a31\n"
        "v_pk_mul_f32 v[68:69], v[68:69], v[24:25] op_sel_hi:[1,0]\n"
        "v_pk_mul_f32 v[70:71], v[70:71], v[24:25] op_sel_hi:[1,0]\n"
        "v_pk_mul_f32 v[72:73], v[72:73], v[24:25] op_sel_hi:[1,0]\n"
        "v_pk_mul_f32 v[74:75], v[74:75], v[24:25] op_sel_hi:[1,0]\n"
        "v_pk_mul_f32 v[76:77], v[76:77], v[24:25] op_sel_hi:[1,0]\n"
        "v_pk_mul_f32 v[78:79], v[78:79], v[24:25] op_sel_hi:[1,0]\n"
        "v_pk_mul_f32 v[80:81], v[80:81], v[24:25] op_sel_hi:[1,0]\n"
        "v_pk_mul_f32 v[82:83], v[82:83], v[24:25] op_sel_hi:[1,0]\n"
        "v_cvt_pk_bf16_f32 v68, v68, v69\n"
        "v_mfma_f32_16x16x32_bf16 a[28:31], v[84:87], v[84:87], 0\n"
        "v_cvt_pk_bf16_f32 v69, v70, v71\n"
        "v_cvt_pk_bf16_f32 v70, v72, v73\n"
        "s_waitcnt lgkmcnt(0)\n"
        "s_barrier\n"
        "v_mfma_f32_16x16x32_bf16 a[144:147], v[84:87], v[84:87], 0\n"
        "ds_read_b128 v[224:227], v252 offset:4096\n"
        "v_mfma_f32_16x16x32_bf16 a[148:151], v[84:87], v[84:87], 0\n"
        "v_cvt_pk_bf16_f32 v71, v74, v75\n"
        "ds_read_b128 v[228:231], v252 offset:5120\n"
        "v_mfma_f32_16x16x32_bf16 a[152:155], v[84:87], v[84:87], 0\n"
        "buffer_store_dwordx4 v[68:71], v250, s[56:59], s72 offen offset:2048 sc1\n"
        "ds_read_b32 v1, v252 offset:8192\n"
        "v_mfma_f32_16x16x32_bf16 a[252:255], v[84:87], v[68:71], a[252:255]\n"
        "ds_read_b128 v[8:11], v252 offset:0\n"
        "v_cvt_pk_bf16_f32 v72, v76, v77\n"
        "v_mfma_f32_16x16x32_bf16 a[156:159], v[84:87], v[84:87], 0\n"
        "ds_read_b128 v[12:15], v252 offset:1024\n"
        "v_cvt_pk_bf16_f32 v73, v78, v79\n"
        "v_mfma_f32_16x16x32_bf16 a[160:163], v[84:87], v[84:87], 0\n"
        "ds_read_b128 v[16:19], v252 offset:2048\n"
        "v_cvt_pk_bf16_f32 v74, v80, v81\n"
        "v_mfma_f32_16x16x32_bf16 a[164:167], v[84:87], v[84:87], 0\n"
        "ds_read_b128 v[20:23], v252 offset:3072\n"
        "v_cvt_pk_bf16_f32 v75, v82, v83\n"
        "v_mfma_f32_16x16x32_bf16 a[168:171], v[84:87], v[84:87], 0\n"
        "v_rcp_f32 v26, v26\n"
        "v_mfma_f32_16x16x32_bf16 a[252:255], v[84:87], v[72:75], a[252:255]\n"
        "buffer_store_dwordx4 v[72:75], v250, s[56:59], s72 offen offset:3072 sc1\n"
        "v_accvgpr_read_b32 v52, a32\n"
        "v_mfma_f32_16x16x32_bf16 a[172:175], v[84:87], v[84:87], 0\n"
        "v_accvgpr_read_b32 v53, a33\n"
        "v_accvgpr_read_b32 v54, a34\n"
        "v_accvgpr_read_b32 v55, a35\n"
        "v_mfma_f32_16x16x32_bf16 a[32:35], v[84:87], v[84:87], 0\n"
        "v_accvgpr_read_b32 v56, a36\n"
        "v_accvgpr_read_b32 v57, a37\n"
        "v_accvgpr_read_b32 v58, a38\n"
        "v_accvgpr_read_b32 v59, a39\n"
        "v_mfma_f32_16x16x32_bf16 a[36:39], v[84:87], v[84:87], 0\n"
        "v_accvgpr_read_b32 v60, a40\n"
        "v_accvgpr_read_b32 v61, a41\n"
        "v_accvgpr_read_b32 v62, a42\n"
        "v_accvgpr_read_b32 v63, a43\n"
        "v_mfma_f32_16x16x32_bf16 a[40:43], v[84:87], v[84:87], 0\n"
        "v_accvgpr_read_b32 v64, a44\n"
        "v_accvgpr_read_b32 v65, a45\n"
        "v_accvgpr_read_b32 v66, a46\n"
        "v_accvgpr_read_b32 v67, a47\n"
        "v_pk_mul_f32 v[52:53], v[52:53], v[26:27] op_sel_hi:[1,0]\n"
        "v_pk_mul_f32 v[54:55], v[54:55], v[26:27] op_sel_hi:[1,0]\n"
        "v_pk_mul_f32 v[56:57], v[56:57], v[26:27] op_sel_hi:[1,0]\n"
        "v_pk_mul_f32 v[58:59], v[58:59], v[26:27] op_sel_hi:[1,0]\n"
        "v_pk_mul_f32 v[60:61], v[60:61], v[26:27] op_sel_hi:[1,0]\n"
        "v_pk_mul_f32 v[62:63], v[62:63], v[26:27] op_sel_hi:[1,0]\n"
        "v_pk_mul_f32 v[64:65], v[64:65], v[26:27] op_sel_hi:[1,0]\n"
        "v_pk_mul_f32 v[66:67], v[66:67], v[26:27] op_sel_hi:[1,0]\n"
        "v_cvt_pk_bf16_f32 v52, v52, v53\n"
        "v_mfma_f32_16x16x32_bf16 a[44:47], v[84:87], v[84:87], 0\n"
        "v_cvt_pk_bf16_f32 v53, v54, v55\n"
        "v_cvt_pk_bf16_f32 v54, v56, v57\n"
        "v_cvt_pk_bf16_f32 v55, v58, v59\n"
        "buffer_store_dwordx4 v[52:55], v250, s[56:59], s73 offen offset:0 sc1\n"
        "v_cvt_pk_bf16_f32 v56, v60, v61\n"
        "v_mfma_f32_16x16x32_bf16 a[252:255], v[84:87], v[52:55], a[252:255]\n"
        "v_cvt_pk_bf16_f32 v57, v62, v63\n"
        "v_cvt_pk_bf16_f32 v58, v64, v65\n"
        "v_cvt_pk_bf16_f32 v59, v66, v67\n"
        "v_accvgpr_read_b32 v68, a48\n"
        "v_accvgpr_read_b32 v69, a49\n"
        "v_mfma_f32_16x16x32_bf16 a[252:255], v[84:87], v[56:59], a[252:255]\n"
        "v_accvgpr_read_b32 v70, a50\n"
        "v_accvgpr_read_b32 v71, a51\n"
        "v_mfma_f32_16x16x32_bf16 a[48:51], v[84:87], v[84:87], 0\n"
        "v_accvgpr_read_b32 v72, a52\n"
        "v_accvgpr_read_b32 v73, a53\n"
        "v_accvgpr_read_b32 v74, a54\n"
        "v_accvgpr_read_b32 v75, a55\n"
        "v_mfma_f32_16x16x32_bf16 a[52:55], v[84:87], v[84:87], 0\n"
        "buffer_store_dwordx4 v[56:59], v250, s[56:59], s73 offen offset:1024 sc1\n"
        "v_accvgpr_read_b32 v76, a56\n"
        "v_accvgpr_read_b32 v77, a57\n"
        "v_accvgpr_read_b32 v78, a58\n"
        "v_accvgpr_read_b32 v79, a59\n"
        "v_mfma_f32_16x16x32_bf16 a[56:59], v[84:87], v[84:87], 0\n"
        "v_accvgpr_read_b32 v80, a60\n"
        "v_accvgpr_read_b32 v81, a61\n"
        "v_accvgpr_read_b32 v82, a62\n"
        "v_accvgpr_read_b32 v83, a63\n"
        "v_pk_mul_f32 v[68:69], v[68:69], v[26:27] op_sel_hi:[1,0]\n"
        "v_pk_mul_f32 v[70:71], v[70:71], v[26:27] op_sel_hi:[1,0]\n"
        "v_pk_mul_f32 v[72:73], v[72:73], v[26:27] op_sel_hi:[1,0]\n"
        "v_pk_mul_f32 v[74:75], v[74:75], v[26:27] op_sel_hi:[1,0]\n"
        "v_pk_mul_f32 v[76:77], v[76:77], v[26:27] op_sel_hi:[1,0]\n"
        "v_pk_mul_f32 v[78:79], v[78:79], v[26:27] op_sel_hi:[1,0]\n"
        "v_pk_mul_f32 v[80:81], v[80:81], v[26:27] op_sel_hi:[1,0]\n"
        "v_pk_mul_f32 v[82:83], v[82:83], v[26:27] op_sel_hi:[1,0]\n"
        "v_cvt_pk_bf16_f32 v68, v68, v69\n"
        "v_mfma_f32_16x16x32_bf16 a[60:63], v[84:87], v[84:87], 0\n"
        "v_cvt_pk_bf16_f32 v69, v70, v71\n"
        "v_cvt_pk_bf16_f32 v70, v72, v73\n"
        "v_cvt_pk_bf16_f32 v71, v74, v75\n"
        "buffer_store_dwordx4 v[68:71], v250, s[56:59], s73 offen offset:2048 sc1\n"
        "v_cvt_pk_bf16_f32 v72, v76, v77\n"
        "v_mfma_f32_16x16x32_bf16 a[252:255], v[84:87], v[68:71], a[252:255]\n"
        "v_cvt_pk_bf16_f32 v73, v78, v79\n"
        "v_cvt_pk_bf16_f32 v74, v80, v81\n"
        "v_cvt_pk_bf16_f32 v75, v82, v83\n"
        "v_rcp_f32 v24, v25\n"
        "v_accvgpr_read_b32 v52, a64\n"
        "v_mfma_f32_16x16x32_bf16 a[252:255], v[84:87], v[72:75], a[252:255]\n"
        "v_accvgpr_read_b32 v53, a65\n"
        "v_accvgpr_read_b32 v54, a66\n"
        "v_accvgpr_read_b32 v55, a67\n"
        "v_mfma_f32_16x16x32_bf16 a[64:67], v[84:87], v[84:87], 0\n"
        "v_accvgpr_read_b32 v56, a68\n"
        "v_accvgpr_read_b32 v57, a69\n"
        "buffer_store_dwordx4 v[72:75], v250, s[56:59], s73 offen offset:3072 sc1\n"
        "v_accvgpr_read_b32 v58, a70\n"
        "v_accvgpr_read_b32 v59, a71\n"
        "v_mfma_f32_16x16x32_bf16 a[68:71], v[84:87], v[84:87], 0\n"
        "v_accvgpr_read_b32 v60, a72\n"
        "v_accvgpr_read_b32 v61, a73\n"
        "v_accvgpr_read_b32 v62, a74\n"
        "v_accvgpr_read_b32 v63, a75\n"
        "v_mfma_f32_16x16x32_bf16 a[72:75], v[84:87], v[84:87], 0\n"
        "v_accvgpr_read_b32 v64, a76\n"
        "v_accvgpr_read_b32 v65, a77\n"
        "v_accvgpr_read_b32 v66, a78\n"
        "v_accvgpr_read_b32 v67, a79\n"
        "v_pk_mul_f32 v[52:53], v[52:53], v[24:25] op_sel_hi:[1,0]\n"
        "v_pk_mul_f32 v[54:55], v[54:55], v[24:25] op_sel_hi:[1,0]\n"
        "v_pk_mul_f32 v[56:57], v[56:57], v[24:25] op_sel_hi:[1,0]\n"
        "v_pk_mul_f32 v[58:59], v[58:59], v[24:25] op_sel_hi:[1,0]\n"
        "v_pk_mul_f32 v[60:61], v[60:61], v[24:25] op_sel_hi:[1,0]\n"
        "v_pk_mul_f32 v[62:63], v[62:63], v[24:25] op_sel_hi:[1,0]\n"
        "v_pk_mul_f32 v[64:65], v[64:65], v[24:25] op_sel_hi:[1,0]\n"
        "v_pk_mul_f32 v[66:67], v[66:67], v[24:25] op_sel_hi:[1,0]\n"
        "v_cvt_pk_bf16_f32 v52, v52, v53\n"
        "v_mfma_f32_16x16x32_bf16 a[76:79], v[84:87], v[84:87], 0\n"
        "v_cvt_pk_bf16_f32 v53, v54, v55\n"
        "v_cvt_pk_bf16_f32 v54, v56, v57\n"
        "v_cvt_pk_bf16_f32 v55, v58, v59\n"
        "buffer_store_dwordx4 v[52:55], v250, s[56:59], s74 offen offset:0 sc1\n"
        "v_cvt_pk_bf16_f32 v56, v60, v61\n"
        "v_mfma_f32_16x16x32_bf16 a[252:255], v[84:87], v[52:55], a[252:255]\n"
        "v_cvt_pk_bf16_f32 v57, v62, v63\n"
        "v_cvt_pk_bf16_f32 v58, v64, v65\n"
        "v_cvt_pk_bf16_f32 v59, v66, v67\n"
        "v_accvgpr_read_b32 v68, a80\n"
        "v_accvgpr_read_b32 v69, a81\n"
        "v_mfma_f32_16x16x32_bf16 a[252:255], v[84:87], v[56:59], a[252:255]\n"
        "v_accvgpr_read_b32 v70, a82\n"
        "v_accvgpr_read_b32 v71, a83\n"
        "v_mfma_f32_16x16x32_bf16 a[80:83], v[84:87], v[84:87], 0\n"
        "v_accvgpr_read_b32 v72, a84\n"
        "v_accvgpr_read_b32 v73, a85\n"
        "v_accvgpr_read_b32 v74, a86\n"
        "v_accvgpr_read_b32 v75, a87\n"
        "v_mfma_f32_16x16x32_bf16 a[84:87], v[84:87], v[84:87], 0\n"
        "buffer_store_dwordx4 v[56:59], v250, s[56:59], s74 offen offset:1024 sc1\n"
        "v_accvgpr_read_b32 v76, a88\n"
        "v_accvgpr_read_b32 v77, a89\n"
        "v_accvgpr_read_b32 v78, a90\n"
        "v_accvgpr_read_b32 v79, a91\n"
        "v_mfma_f32_16x16x32_bf16 a[88:91], v[84:87], v[84:87], 0\n"
        "v_accvgpr_read_b32 v80, a92\n"
        "v_accvgpr_read_b32 v81, a93\n"
        "v_accvgpr_read_b32 v82, a94\n"
        "v_accvgpr_read_b32 v83, a95\n"
        "v_pk_mul_f32 v[68:69], v[68:69], v[24:25] op_sel_hi:[1,0]\n"
        "v_pk_mul_f32 v[70:71], v[70:71], v[24:25] op_sel_hi:[1,0]\n"
        "v_pk_mul_f32 v[72:73], v[72:73], v[24:25] op_sel_hi:[1,0]\n"
        "v_pk_mul_f32 v[74:75], v[74:75], v[24:25] op_sel_hi:[1,0]\n"
        "v_pk_mul_f32 v[76:77], v[76:77], v[24:25] op_sel_hi:[1,0]\n"
        "v_pk_mul_f32 v[78:79], v[78:79], v[24:25] op_sel_hi:[1,0]\n"
        "v_pk_mul_f32 v[80:81], v[80:81], v[24:25] op_sel_hi:[1,0]\n"
        "v_pk_mul_f32 v[82:83], v[82:83], v[24:25] op_sel_hi:[1,0]\n"
        "v_cvt_pk_bf16_f32 v68, v68, v69\n"
        "v_mfma_f32_16x16x32_bf16 a[92:95], v[84:87], v[84:87], 0\n"
        "v_cvt_pk_bf16_f32 v69, v70, v71\n"
        "v_cvt_pk_bf16_f32 v70, v72, v73\n"
        "v_cvt_pk_bf16_f32 v71, v74, v75\n"
        "buffer_store_dwordx4 v[68:71], v250, s[56:59], s74 offen offset:2048 sc1\n"
        "v_cvt_pk_bf16_f32 v72, v76, v77\n"
        "v_mfma_f32_16x16x32_bf16 a[252:255], v[84:87], v[68:71], a[252:255]\n"
        "v_cvt_pk_bf16_f32 v73, v78, v79\n"
        "v_cvt_pk_bf16_f32 v74, v80, v81\n"
        "v_cvt_pk_bf16_f32 v75, v82, v83\n"
        "v_rcp_f32 v26, v27\n"
        "v_accvgpr_read_b32 v52, a96\n"
        "v_mfma_f32_16x16x32_bf16 a[252:255], v[84:87], v[72:75], a[252:255]\n"
        "v_accvgpr_read_b32 v53, a97\n"
        "v_accvgpr_read_b32 v54, a98\n"
        "v_accvgpr_read_b32 v55, a99\n"
        "v_mfma_f32_16x16x32_bf16 a[96:99], v[84:87], v[84:87], 0\n"
        "v_accvgpr_read_b32 v56, a100\n"
        "v_accvgpr_read_b32 v57, a101\n"
        "buffer_store_dwordx4 v[72:75], v250, s[56:59], s74 offen offset:3072 sc1\n"
        "v_accvgpr_read_b32 v58, a102\n"
        "v_accvgpr_read_b32 v59, a103\n"
        "v_mfma_f32_16x16x32_bf16 a[100:103], v[84:87], v[84:87], 0\n"
        "v_accvgpr_read_b32 v60, a104\n"
        "v_accvgpr_read_b32 v61, a105\n"
        "v_accvgpr_read_b32 v62, a106\n"
        "v_accvgpr_read_b32 v63, a107\n"
        "v_mfma_f32_16x16x32_bf16 a[104:107], v[84:87], v[84:87], 0\n"
        "v_accvgpr_read_b32 v64, a108\n"
        "v_accvgpr_read_b32 v65, a109\n"
        "v_accvgpr_read_b32 v66, a110\n"
        "v_accvgpr_read_b32 v67, a111\n"
        "v_pk_mul_f32 v[52:53], v[52:53], v[26:27] op_sel_hi:[1,0]\n"
        "v_pk_mul_f32 v[54:55], v[54:55], v[26:27] op_sel_hi:[1,0]\n"
        "v_pk_mul_f32 v[56:57], v[56:57], v[26:27] op_sel_hi:[1,0]\n"
        "v_pk_mul_f32 v[58:59], v[58:59], v[26:27] op_sel_hi:[1,0]\n"
        "v_pk_mul_f32 v[60:61], v[60:61], v[26:27] op_sel_hi:[1,0]\n"
        "v_pk_mul_f32 v[62:63], v[62:63], v[26:27] op_sel_hi:[1,0]\n"
        "v_pk_mul_f32 v[64:65], v[64:65], v[26:27] op_sel_hi:[1,0]\n"
        "v_pk_mul_f32 v[66:67], v[66:67], v[26:27] op_sel_hi:[1,0]\n"
        "v_cvt_pk_bf16_f32 v52, v52, v53\n"
        "v_mfma_f32_16x16x32_bf16 a[108:111], v[84:87], v[84:87], 0\n"
        "v_cvt_pk_bf16_f32 v53, v54, v55\n"
        "v_cvt_pk_bf16_f32 v54, v56, v57\n"
        "v_cvt_pk_bf16_f32 v55, v58, v59\n"
        "buffer_store_dwordx4 v[52:55], v250, s[56:59], s75 offen offset:0 sc1\n"
        "v_cvt_pk_bf16_f32 v56, v60, v61\n"
        "v_mfma_f32_16x16x32_bf16 a[252:255], v[84:87], v[52:55], a[252:255]\n"
        "v_cvt_pk_bf16_f32 v57, v62, v63\n"
        "v_cvt_pk_bf16_f32 v58, v64, v65\n"
        "v_cvt_pk_bf16_f32 v59, v66, v67\n"
        "v_accvgpr_read_b32 v68, a112\n"
        "v_accvgpr_read_b32 v69, a113\n"
        "v_mfma_f32_16x16x32_bf16 a[252:255], v[84:87], v[56:59], a[252:255]\n"
        "v_accvgpr_read_b32 v70, a114\n"
        "v_accvgpr_read_b32 v71, a115\n"
        "v_mfma_f32_16x16x32_bf16 a[112:115], v[84:87], v[84:87], 0\n"
        "v_accvgpr_read_b32 v72, a116\n"
        "v_accvgpr_read_b32 v73, a117\n"
        "v_accvgpr_read_b32 v74, a118\n"
        "v_accvgpr_read_b32 v75, a119\n"
        "v_mfma_f32_16x16x32_bf16 a[116:119], v[84:87], v[84:87], 0\n"
        "buffer_store_dwordx4 v[56:59], v250, s[56:59], s75 offen offset:1024 sc1\n"
        "v_accvgpr_read_b32 v76, a120\n"
        "v_accvgpr_read_b32 v77, a121\n"
        "v_accvgpr_read_b32 v78, a122\n"
        "v_accvgpr_read_b32 v79, a123\n"
        "v_mfma_f32_16x16x32_bf16 a[120:123], v[84:87], v[84:87], 0\n"
        "v_accvgpr_read_b32 v80, a124\n"
        "v_accvgpr_read_b32 v81, a125\n"
        "v_accvgpr_read_b32 v82, a126\n"
        "v_accvgpr_read_b32 v83, a127\n"
        "v_pk_mul_f32 v[68:69], v[68:69], v[26:27] op_sel_hi:[1,0]\n"
        "v_pk_mul_f32 v[70:71], v[70:71], v[26:27] op_sel_hi:[1,0]\n"
        "v_pk_mul_f32 v[72:73], v[72:73], v[26:27] op_sel_hi:[1,0]\n"
        "v_pk_mul_f32 v[74:75], v[74:75], v[26:27] op_sel_hi:[1,0]\n"
        "v_pk_mul_f32 v[76:77], v[76:77], v[26:27] op_sel_hi:[1,0]\n"
        "v_pk_mul_f32 v[78:79], v[78:79], v[26:27] op_sel_hi:[1,0]\n"
        "v_pk_mul_f32 v[80:81], v[80:81], v[26:27] op_sel_hi:[1,0]\n"
        "v_pk_mul_f32 v[82:83], v[82:83], v[26:27] op_sel_hi:[1,0]\n"
        "v_cvt_pk_bf16_f32 v68, v68, v69\n"
        "v_mfma_f32_16x16x32_bf16 a[124:127], v[84:87], v[84:87], 0\n"
        "v_cvt_pk_bf16_f32 v69, v70, v71\n"
        "v_cvt_pk_bf16_f32 v70, v72, v73\n"
        "v_cvt_pk_bf16_f32 v71, v74, v75\n"
        "buffer_store_dwordx4 v[68:71], v250, s[56:59], s75 offen offset:2048 sc1\n"
        "v_accvgpr_read_b32 v52, a128\n"
        "v_mfma_f32_16x16x32_bf16 a[252:255], v[84:87], v[68:71], a[252:255]\n"
        "v_accvgpr_read_b32 v53, a129\n"
        "v_accvgpr_read_b32 v54, a130\n"
        "v_accvgpr_read_b32 v55, a131\n"
        "v_mfma_f32_16x16x32_bf16 a[128:131], v[84:87], v[84:87], 0\n"
        "v_accvgpr_read_b32 v56, a132\n"
        "v_accvgpr_read_b32 v57, a133\n"
        "v_accvgpr_read_b32 v58, a134\n"
        "v_accvgpr_read_b32 v59, a135\n"
        "v_mfma_f32_16x16x32_bf16 a[132:135], v[84:87], v[84:87], 0\n"
        "v_accvgpr_read_b32 v60, a136\n"
        "v_accvgpr_read_b32 v61, a137\n"
        "v_accvgpr_read_b32 v62, a138\n"
        "v_accvgpr_read_b32 v63, a139\n"
        "v_mfma_f32_16x16x32_bf16 a[136:139], v[84:87], v[84:87], 0\n"
        "v_accvgpr_read_b32 v64, a140\n"
        "v_accvgpr_read_b32 v65, a141\n"
        "v_accvgpr_read_b32 v66, a142\n"
        "v_accvgpr_read_b32 v67, a143\n"
        "s_waitcnt lgkmcnt(0)\n"
        "v_pk_add_f32 v[52:53], v[52:53], v[8:9]\n"
        "v_pk_add_f32 v[54:55], v[54:55], v[10:11]\n"
        "v_pk_add_f32 v[56:57], v[56:57], v[12:13]\n"
        "v_pk_add_f32 v[58:59], v[58:59], v[14:15]\n"
        "v_pk_add_f32 v[60:61], v[60:61], v[16:17]\n"
        "v_pk_add_f32 v[62:63], v[62:63], v[18:19]\n"
        "v_pk_add_f32 v[64:65], v[64:65], v[20:21]\n"
        "v_pk_add_f32 v[66:67], v[66:67], v[22:23]\n"
        "v_cvt_pk_bf16_f32 v72, v76, v77\n"
        "v_mfma_f32_16x16x32_bf16 a[140:143], v[84:87], v[84:87], 0\n"
        "v_cvt_pk_bf16_f32 v73, v78, v79\n"
        "v_cvt_pk_bf16_f32 v74, v80, v81\n"
        "v_cvt_pk_bf16_f32 v75, v82, v83\n"
        "buffer_store_dwordx4 v[72:75], v250, s[56:59], s75 offen offset:3072 sc1\n"
        "v_accvgpr_read_b32 v2, a176\n"
        "v_mfma_f32_16x16x32_bf16 a[252:255], v[84:87], v[72:75], a[252:255]\n"
        "v_max_f32 v3, v3, v27\n"
        "v_add_f32 v2, v2, v1\n"
        "v_mfma_f32_16x16x32_bf16 a[176:179], v[84:87], v[84:87], 0\n"
        "v_max_f32 v3, v3, v2\n"
        "v_rcp_f32 v2, v2\n"
        "v_accvgpr_read_b32 v216, a244\n"
        "v_pk_mul_f32 v[52:53], v[52:53], v[2:3] op_sel_hi:[1,0]\n"
        "v_pk_mul_f32 v[54:55], v[54:55], v[2:3] op_sel_hi:[1,0]\n"
        "v_pk_mul_f32 v[56:57], v[56:57], v[2:3] op_sel_hi:[1,0]\n"
        "v_pk_mul_f32 v[58:59], v[58:59], v[2:3] op_sel_hi:[1,0]\n"
        "v_pk_mul_f32 v[60:61], v[60:61], v[2:3] op_sel_hi:[1,0]\n"
        "v_pk_mul_f32 v[62:63], v[62:63], v[2:3] op_sel_hi:[1,0]\n"
        "v_pk_mul_f32 v[64:65], v[64:65], v[2:3] op_sel_hi:[1,0]\n"
        "v_pk_mul_f32 v[66:67], v[66:67], v[2:3] op_sel_hi:[1,0]\n"
        "v_cvt_pk_bf16_f32 v52, v52, v53\n"
        "v_cvt_pk_bf16_f32 v53, v54, v55\n"
        "v_cvt_pk_bf16_f32 v54, v56, v57\n"
        "v_cvt_pk_bf16_f32 v55, v58, v59\n"
        "v_accvgpr_read_b32 v217, a245\n"
        "v_accvgpr_read_b32 v218, a246\n"
        "v_mfma_f32_16x16x32_bf16 a[252:255], v[84:87], v[52:55], a[252:255]\n"
        "v_accvgpr_read_b32 v219, a247\n"
        "buffer_store_dwordx4 v[52:55], v250, s[56:59], s76 offen offset:0 sc1\n"
        "v_accvgpr_read_b32 v220, a248\n"
        "v_mfma_f32_16x16x32_bf16 v[44:47], a[180:183], v[216:219], 0\n"
        "v_accvgpr_read_b32 v221, a249\n"
        "v_mfma_f32_16x16x32_bf16 v[48:51], a[212:215], v[216:219], 0\n"
        "v_accvgpr_read_b32 v222, a250\n"
        "v_accvgpr_read_b32 v223, a251\n"
        "v_cvt_pk_bf16_f32 v56, v60, v61\n"
        "v_cvt_pk_bf16_f32 v57, v62, v63\n"
        "v_mfma_f32_16x16x32_bf16 v[44:47], a[184:187], v[220:223], v[44:47]\n"
        "v_cvt_pk_bf16_f32 v58, v64, v65\n"
        "v_mfma_f32_16x16x32_bf16 v[48:51], a[216:219], v[220:223], v[48:51]\n"
        "v_cvt_pk_bf16_f32 v59, v66, v67\n"
        "v_max3_f32 v28, v28, v29, v30\n"
        "v_mfma_f32_16x16x32_bf16 v[44:47], a[188:191], v[224:227], v[44:47]\n"
        "v_max3_f32 v32, v32, v33, v34\n"
        "v_max3_f32 v36, v36, v37, v38\n"
        "v_mfma_f32_16x16x32_bf16 v[48:51], a[220:223], v[224:227], v[48:51]\n"
        "v_max3_f32 v40, v40, v41, v42\n"
        "v_max_f32 v28, v28, v31\n"
        "v_mfma_f32_16x16x32_bf16 a[252:255], v[84:87], v[56:59], a[252:255]\n"
        "v_max_f32 v32, v32, v35\n"
        "v_max_f32 v36, v36, v39\n"
        "v_mfma_f32_16x16x32_bf16 v[44:47], a[192:195], v[228:231], v[44:47]\n"
        "v_max_f32 v40, v40, v43\n"
        "v_mov_b32 v29, v28\n"
        "v_mov_b32 v33, v32\n"
        "v_mfma_f32_16x16x32_bf16 v[48:51], a[224:227], v[228:231], v[48:51]\n"
        "v_mov_b32 v37, v36\n"
        "v_accvgpr_read_b32 v1, a252\n"
        "v_add_f32 v3, v3, v1\n"
        "v_cmp_nlt_f32 vcc, v3, v255\n"
        "v_mov_b32 v41, v40\n"
        "v_permlane32_swap_b32 v28, v29\n"
        "s_cmp_lg_u64 vcc, 0\n"
        "s_cselect_b32 s80, 3, 0\n"
        "v_max3_f32 v44, v44, v48, v49\n"
        "v_max3_f32 v45, v45, v50, v51\n"
        "v_max3_f32 v44, v44, v45, v46\n"
        "v_max_f32 v44, v44, v47\n"
        "v_mov_b32 v45, v44\n"
        "v_permlane32_swap_b32 v32, v33\n"
        "v_permlane32_swap_b32 v36, v37\n"
        "v_permlane32_swap_b32 v40, v41\n"
        "v_permlane32_swap_b32 v44, v45\n"
        "s_or_b32 s77, s77, s80\n"
        "v_max_f32 v28, v28, v29\n"
        "v_max_f32 v32, v32, v33\n"
        "v_max_f32 v36, v36, v37\n"
        "v_max_f32 v40, v40, v41\n"
        "v_max_f32 v44, v44, v45\n"
        "s_add_u32 s72, s72, 4096\n"
        "v_mov_b32 v29, v28\n"
        "v_mov_b32 v33, v32\n"
        "v_mov_b32 v37, v36\n"
        "v_mov_b32 v41, v40\n"
        "v_mov_b32 v45, v44\n"
        "s_add_u32 s73, s73, 4096\n"
        "v_permlane16_swap_b32 v28, v29\n"
        "v_permlane16_swap_b32 v32, v33\n"
        "v_permlane16_swap_b32 v36, v37\n"
        "v_permlane16_swap_b32 v40, v41\n"
        "v_permlane16_swap_b32 v44, v45\n"
        "s_add_u32 s74, s74, 4096\n"
        "v_max_f32 v28, v28, v29\n"
        "v_max_f32 v32, v32, v33\n"
        "v_max_f32 v36, v36, v37\n"
        "v_max_f32 v40, v40, v41\n"
        "v_max_f32 v44, v44, v45\n"
        "buffer_store_dwordx4 v[56:59], v250, s[56:59], s76 offen offset:1024 sc1\n"
        "s_add_u32 s75, s75, 4096\n"
        "v_xor_b32 v8, 0x80000000, v28\n"
        "v_xor_b32 v9, 0x80000000, v28\n"
        "v_xor_b32 v10, 0x80000000, v28\n"
        "s_memrealtime s[60:61]\n"
        "v_xor_b32 v11, 0x80000000, v28\n"
        "v_xor_b32 v12, 0x80000000, v32\n"
        "v_xor_b32 v13, 0x80000000, v32\n"
        "v_xor_b32 v14, 0x80000000, v32\n"
        "v_xor_b32 v15, 0x80000000, v32\n"
        "v_xor_b32 v16, 0x80000000, v36\n"
        "v_xor_b32 v17, 0x80000000, v36\n"
        "v_xor_b32 v18, 0x80000000, v36\n"
        "v_xor_b32 v19, 0x80000000, v36\n"
        "v_xor_b32 v20, 0x80000000, v40\n"
        "v_xor_b32 v21, 0x80000000, v40\n"
        "v_xor_b32 v22, 0x80000000, v40\n"
        "v_xor_b32 v23, 0x80000000, v40\n"
        "v_xor_b32 v24, 0x80000000, v44\n"
        "v_xor_b32 v25, 0x80000000, v44\n"
        "v_xor_b32 v26, 0x80000000, v44\n"
        "v_xor_b32 v27, 0x80000000, v44\n"
        "s_add_u32 s76, s76, 4096\n"
        "s_mov_b64 vcc, -1\n"
        "s_mov_b32 s69, 1\n"
        "s_mov_b32 s68, 4\n"
        "s_mov_b32 s91, 4\n"
        "s_mov_b32 s90, 1\n"
        "s_mov_b32 s63, -1\n"
        "s_waitcnt lgkmcnt(0)\n"
        "s_add_u32 s83, s60, 32\n"
        "s_max_u32 s79, s79, 146\n"
        "s_nop 0\n"
        "s_branch .Lk_T1\n"
        ".Lk_EPIA1:\n"
        "ds_read_b128 v[88:91], v251 offset:0\n"
        "v_accvgpr_read_b32 v24, a160\n"
        "v_mfma_f32_16x16x32_bf16 a[128:131], v[100:103], v[84:87], a[128:131]\n"
        "ds_read_b128 v[96:99], v251 offset:1024\n"
        "v_accvgpr_read_b32 v26, a164\n"
        "v_mfma_f32_16x16x32_bf16 a[132:135], v[116:119], v[84:87], a[132:135]\n"
        "ds_read_b128 v[104:107], v251 offset:2048\n"
        "v_accvgpr_read_b32 v2, a161\n"
        "v_mfma_f32_16x16x32_bf16 a[136:139], v[132:135], v[84:87], a[136:139]\n"
        "ds_read_b128 v[112:115], v251 offset:3072\n"
        "v_accvgpr_read_b32 v3, a165\n"
        "v_mfma_f32_16x16x32_bf16 a[140:143], v[148:151], v[84:87], a[140:143]\n"
        "ds_read_b128 v[120:123], v251 offset:4096\n"
        "v_add_f32 v24, v24, v2\n"
        "v_mfma_f32_16x16x32_bf16 a[176:179], v[4:7], v[84:87], a[176:179]\n"
        "ds_read_b128 v[128:131], v251 offset:5120\n"
        "v_add_f32 v26, v26, v3\n"
        "v_mfma_f32_16x16x32_bf16 a[144:147], v[164:167], v[84:87], a[144:147]\n"
        "ds_read_b128 v[136:139], v251 offset:6144\n"
        "v_accvgpr_read_b32 v25, a168\n"
        "v_mfma_f32_16x16x32_bf16 a[148:151], v[180:183], v[84:87], a[148:151]\n"
        "ds_read_b128 v[144:147], v251 offset:7168\n"
        "v_accvgpr_read_b32 v27, a172\n"
        "v_mfma_f32_16x16x32_bf16 a[152:155], v[196:199], v[84:87], a[152:155]\n"
        "ds_read_b128 v[152:155], v251 offset:8192\n"
        "v_accvgpr_read_b32 v2, a169\n"
        "v_mfma_f32_16x16x32_bf16 a[156:159], v[212:215], v[84:87], a[156:159]\n"
        "ds_read_b128 v[160:163], v251 offset:9216\n"
        "v_mov_b32 v84, 0\n"
        "s_waitcnt lgkmcnt(9)\n"
        "v_mfma_f32_16x16x32_bf16 v[28:31], a[180:183], v[88:91], 0\n"
        "ds_read_b128 v[168:171], v251 offset:10240\n"
        "v_mov_b32 v85, 0\n"
        "v_mov_b32 v86, 0\n"
        "s_waitcnt lgkmcnt(9)\n"
        "v_mfma_f32_16x16x32_bf16 v[28:31], a[184:187], v[96:99], v[28:31]\n"
        "ds_read_b128 v[176:179], v251 offset:11264\n"
        "v_mov_b32 v87, 0\n"
        "s_waitcnt lgkmcnt(9)\n"
        "v_mfma_f32_16x16x32_bf16 v[28:31], a[188:191], v[104:107], v[28:31]\n"
        "ds_read_b128 v[184:187], v251 offset:12288\n"
        "v_accvgpr_read_b32 v3, a173\n"
        "s_waitcnt lgkmcnt(9)\n"
        "v_mfma_f32_16x16x32_bf16 v[28:31], a[192:195], v[112:115], v[28:31]\n"
        "ds_read_b128 v[192:195], v251 offset:13312\n"
        "v_add_f32 v25, v25, v2\n"
        "s_waitcnt lgkmcnt(9)\n"
        "v_mfma_f32_16x16x32_bf16 v[32:35], a[180:183], v[120:123], 0\n"
        "ds_read_b128 v[200:203], v251 offset:14336\n"
        "v_add_f32 v27, v27, v3\n"
        "s_waitcnt lgkmcnt(9)\n"
        "v_mfma_f32_16x16x32_bf16 v[32:35], a[184:187], v[128:131], v[32:35]\n"
        "ds_read_b128 v[208:211], v251 offset:15360\n"
        "v_max3_f32 v3, v24, v25, v26\n"
        "s_waitcnt lgkmcnt(9)\n"
        "v_mfma_f32_16x16x32_bf16 v[32:35], a[188:191], v[136:139], v[32:35]\n"
        "ds_write_b128 v251, a[128:131] offset:0\n"
        "v_rcp_f32 v24, v24\n"
        "s_waitcnt lgkmcnt(9)\n"
        "v_mfma_f32_16x16x32_bf16 v[32:35], a[192:195], v[144:147], v[32:35]\n"
        "v_accvgpr_read_b32 v52, a0\n"
        "s_waitcnt lgkmcnt(8)\n"
        "v_mfma_f32_16x16x32_bf16 v[36:39], a[180:183], v[152:155], 0\n"
        "v_accvgpr_read_b32 v53, a1\n"
        "v_accvgpr_read_b32 v54, a2\n"
        "s_waitcnt lgkmcnt(7)\n"
        "v_mfma_f32_16x16x32_bf16 v[36:39], a[184:187], v[160:163], v[36:39]\n"
        "ds_write_b128 v251, a[132:135] offset:1024\n"
        "v_accvgpr_read_b32 v55, a3\n"
        "s_waitcnt lgkmcnt(7)\n"
        "v_mfma_f32_16x16x32_bf16 v[36:39], a[188:191], v[168:171], v[36:39]\n"
        "v_accvgpr_read_b32 v56, a4\n"
        "v_accvgpr_read_b32 v57, a5\n"
        "v_mfma_f32_16x16x32_bf16 a[0:3], v[84:87], v[84:87], 0\n"
        "v_accvgpr_read_b32 v58, a6\n"
        "v_accvgpr_read_b32 v59, a7\n"
        "s_waitcnt lgkmcnt(6)\n"
        "v_mfma_f32_16x16x32_bf16 v[36:39], a[192:195], v[176:179], v[36:39]\n"
        "ds_write_b128 v251, a[136:139] offset:2048\n"
        "v_accvgpr_read_b32 v60, a8\n"
        "s_waitcnt lgkmcnt(6)\n"
        "v_mfma_f32_16x16x32_bf16 v[40:43], a[180:183], v[184:187], 0\n"
        "v_accvgpr_read_b32 v61, a9\n"
        "v_accvgpr_read_b32 v62, a10\n"
        "s_waitcnt lgkmcnt(5)\n"
        "v_mfma_f32_16x16x32_bf16 v[40:43], a[184:187], v[192:195], v[40:43]\n"
        "v_accvgpr_read_b32 v63, a11\n"
        "s_waitcnt lgkmcnt(4)\n"
        "v_mfma_f32_16x16x32_bf16 v[40:43], a[188:191], v[200:203], v[40:43]\n"
        "v_accvgpr_read_b32 v64, a12\n"
        "ds_write_b128 v251, a[140:143] offset:3072\n"
        "s_waitcnt lgkmcnt(4)\n"
        "v_mfma_f32_16x16x32_bf16 v[40:43], a[192:195], v[208:211], v[40:43]\n"
        "v_accvgpr_read_b32 v65, a13\n"
        "v_accvgpr_read_b32 v66, a14\n"
        "v_mfma_f32_16x16x32_bf16 a[4:7], v[84:87], v[84:87], 0\n"
        "v_accvgpr_read_b32 v67, a15\n"
        "v_accvgpr_read_b32 v68, a16\n"
        "v_accvgpr_read_b32 v69, a17\n"
        "v_pk_mul_f32 v[52:53], v[52:53], v[24:25] op_sel_hi:[1,0]\n"
        "v_pk_mul_f32 v[54:55], v[54:55], v[24:25] op_sel_hi:[1,0]\n"
        "v_pk_mul_f32 v[56:57], v[56:57], v[24:25] op_sel_hi:[1,0]\n"
        "v_pk_mul_f32 v[58:59], v[58:59], v[24:25] op_sel_hi:[1,0]\n"
        "v_pk_mul_f32 v[60:61], v[60:61], v[24:25] op_sel_hi:[1,0]\n"
        "v_pk_mul_f32 v[62:63], v[62:63], v[24:25] op_sel_hi:[1,0]\n"
        "v_pk_mul_f32 v[64:65], v[64:65], v[24:25] op_sel_hi:[1,0]\n"
        "v_pk_mul_f32 v[66:67], v[66:67], v[24:25] op_sel_hi:[1,0]\n"
        "ds_write_b128 v251, a[176:179] offset:8192\n"
        "v_mfma_f32_16x16x32_bf16 a[8:11], v[84:87], v[84:87], 0\n"
        "v_cvt_pk_bf16_f32 v52, v52, v53\n"
        "v_mfma_f32_16x16x32_bf16 a[12:15], v[84:87], v[84:87], 0\n"
        "v_cvt_pk_bf16_f32 v53, v54, v55\n"
        "v_cvt_pk_bf16_f32 v54, v56, v57\n"
        "v_cvt_pk_bf16_f32 v55, v58, v59\n"
        "s_waitcnt vmcnt(8)\n"
        "buffer_store_dwordx4 v[52:55], v250, s[56:59], s72 offen offset:0 sc1\n"
        "v_cvt_pk_bf16_f32 v56, v60, v61\n"
        "v_mfma_f32_16x16x32_bf16 a[252:255], v[84:87], v[52:55], 0\n"
        "ds_write_b128 v251, a[244:247] offset:4096\n"
        "v_cvt_pk_bf16_f32 v57, v62, v63\n"
        "v_cvt_pk_bf16_f32 v58, v64, v65\n"
        "v_cvt_pk_bf16_f32 v59, v66, v67\n"
        "v_accvgpr_read_b32 v70, a18\n"
        "v_accvgpr_read_b32 v71, a19\n"
        "v_mfma_f32_16x16x32_bf16 a[252:255], v[84:87], v[56:59], a[252:255]\n"
        "v_accvgpr_read_b32 v72, a20\n"
        "v_mfma_f32_16x16x32_bf16 a[16:19], v[84:87], v[84:87], 0\n"
        "ds_write_b128 v251, a[248:251] offset:5120\n"
        "v_accvgpr_read_b32 v73, a21\n"
        "v_accvgpr_read_b32 v74, a22\n"
        "v_accvgpr_read_b32 v75, a23\n"
        "v_mfma_f32_16x16x32_bf16 a[20:23], v[84:87], v[84:87], 0\n"
        "buffer_store_dwordx4 v[56:59], v250, s[56:59], s72 offen offset:1024 sc1\n"
        "v_accvgpr_read_b32 v76, a24\n"
        "v_accvgpr_read_b32 v77, a25\n"
        "v_accvgpr_read_b32 v78, a26\n"
        "v_accvgpr_read_b32 v79, a27\n"
        "v_mfma_f32_16x16x32_bf16 a[24:27], v[84:87], v[84:87], 0\n"
        "v_accvgpr_read_b32 v80, a28\n"
        "v_accvgpr_read_b32 v81, a29\n"
        "v_accvgpr_read_b32 v82, a30\n"
        "v_accvgpr_read_b32 v83, a31\n"
        "v_pk_mul_f32 v[68:69], v[68:69], v[24:25] op_sel_hi:[1,0]\n"
        "v_pk_mul_f32 v[70:71], v[70:71], v[24:25] op_sel_hi:[1,0]\n"
        "v_pk_mul_f32 v[72:73], v[72:73], v[24:25] op_sel_hi:[1,0]\n"
        "v_pk_mul_f32 v[74:75], v[74:75], v[24:25] op_sel_hi:[1,0]\n"
        "v_pk_mul_f32 v[76:77], v[76:77], v[24:25] op_sel_hi:[1,0]\n"
        "v_pk_mul_f32 v[78:79], v[78:79], v[24:25] op_sel_hi:[1,0]\n"
        "v_pk_mul_f32 v[80:81], v[80:81], v[24:25] op_sel_hi:[1,0]\n"
        "v_pk_mul_f32 v[82:83], v[82:83], v[24:25] op_sel_hi:[1,0]\n"
        "v_cvt_pk_bf16_f32 v68, v68, v69\n"
        "v_mfma_f32_16x16x32_bf16 a[28:31], v[84:87], v[84:87], 0\n"
        "v_cvt_pk_bf16_f32 v69, v70, v71\n"
        "v_cvt_pk_bf16_f32 v70, v72, v73\n"
        "s_waitcnt lgkmcnt(0)\n"
        "s_barrier\n"
        "v_mfma_f32_16x16x32_bf16 a[128:131], v[84:87], v[84:87], 0\n"
        "ds_read_b128 v[224:227], v252 offset:4096\n"
        "v_mfma_f32_16x16x32_bf16 a[132:135], v[84:87], v[84:87], 0\n"
        "v_cvt_pk_bf16_f32 v71, v74, v75\n"
        "ds_read_b128 v[228:231], v252 offset:5120\n"
        "v_mfma_f32_16x16x32_bf16 a[136:139], v[84:87], v[84:87], 0\n"
        "buffer_store_dwordx4 v[68:71], v250, s[56:59], s72 offen offset:2048 sc1\n"
        "ds_read_b32 v1, v252 offset:8192\n"
        "v_mfma_f32_16x16x32_bf16 a[252:255], v[84:87], v[68:71], a[252:255]\n"
        "ds_read_b128 v[8:11], v252 offset:0\n"
        "v_cvt_pk_bf16_f32 v72, v76, v77\n"
        "v_mfma_f32_16x16x32_bf16 a[140:143], v[84:87], v[84:87], 0\n"
        "ds_read_b128 v[12:15], v252 offset:1024\n"
        "v_cvt_pk_bf16_f32 v73, v78, v79\n"
        "v_mfma_f32_16x16x32_bf16 a[160:163], v[84:87], v[84:87], 0\n"
        "ds_read_b128 v[16:19], v252 offset:2048\n"
        "v_cvt_pk_bf16_f32 v74, v80, v81\n"
        "v_mfma_f32_16x16x32_bf16 a[164:167], v[84:87], v[84:87], 0\n"
        "ds_read_b128 v[20:23], v252 offset:3072\n"
        "v_cvt_pk_bf16_f32 v75, v82, v83\n"
        "v_mfma_f32_16x16x32_bf16 a[168:171], v[84:87], v[84:87], 0\n"
        "v_rcp_f32 v26, v26\n"
        "v_mfma_f32_16x16x32_bf16 a[252:255], v[84:87], v[72:75], a[252:255]\n"
        "buffer_store_dwordx4 v[72:75], v250, s[56:59], s72 offen offset:3072 sc1\n"
        "v_accvgpr_read_b32 v52, a32\n"
        "v_mfma_f32_16x16x32_bf16 a[172:175], v[84:87], v[84:87], 0\n"
        "v_accvgpr_read_b32 v53, a33\n"
        "v_accvgpr_read_b32 v54, a34\n"
        "v_accvgpr_read_b32 v55, a35\n"
        "v_mfma_f32_16x16x32_bf16 a[32:35], v[84:87], v[84:87], 0\n"
        "v_accvgpr_read_b32 v56, a36\n"
        "v_accvgpr_read_b32 v57, a37\n"
        "v_accvgpr_read_b32 v58, a38\n"
        "v_accvgpr_read_b32 v59, a39\n"
        "v_mfma_f32_16x16x32_bf16 a[36:39], v[84:87], v[84:87], 0\n"
        "v_accvgpr_read_b32 v60, a40\n"
        "v_accvgpr_read_b32 v61, a41\n"
        "v_accvgpr_read_b32 v62, a42\n"
        "v_accvgpr_read_b32 v63, a43\n"
        "v_mfma_f32_16x16x32_bf16 a[40:43], v[84:87], v[84:87], 0\n"
        "v_accvgpr_read_b32 v64, a44\n"
        "v_accvgpr_read_b32 v65, a45\n"
        "v_accvgpr_read_b32 v66, a46\n"
        "v_accvgpr_read_b32 v67, a47\n"
        "v_pk_mul_f32 v[52:53], v[52:53], v[26:27] op_sel_hi:[1,0]\n"
        "v_pk_mul_f32 v[54:55], v[54:55], v[26:27] op_sel_hi:[1,0]\n"
        "v_pk_mul_f32 v[56:57], v[56:57], v[26:27] op_sel_hi:[1,0]\n"
        "v_pk_mul_f32 v[58:59], v[58:59], v[26:27] op_sel_hi:[1,0]\n"
        "v_pk_mul_f32 v[60:61], v[60:61], v[26:27] op_sel_hi:[1,0]\n"
        "v_pk_mul_f32 v[62:63], v[62:63], v[26:27] op_sel_hi:[1,0]\n"
        "v_pk_mul_f32 v[64:65], v[64:65], v[26:27] op_sel_hi:[1,0]\n"
        "v_pk_mul_f32 v[66:67], v[66:67], v[26:27] op_sel_hi:[1,0]\n"
        "v_cvt_pk_bf16_f32 v52, v52, v53\n"
        "v_mfma_f32_16x16x32_bf16 a[44:47], v[84:87], v[84:87], 0\n"
        "v_cvt_pk_bf16_f32 v53, v54, v55\n"
        "v_cvt_pk_bf16_f32 v54, v56, v57\n"
        "v_cvt_pk_bf16_f32 v55, v58, v59\n"
        "buffer_store_dwordx4 v[52:55], v250, s[56:59], s73 offen offset:0 sc1\n"
        "v_cvt_pk_bf16_f32 v56, v60, v61\n"
        "v_mfma_f32_16x16x32_bf16 a[252:255], v[84:87], v[52:55], a[252:255]\n"
        "v_cvt_pk_bf16_f32 v57, v62, v63\n"
        "v_cvt_pk_bf16_f32 v58, v64, v65\n"
        "v_cvt_pk_bf16_f32 v59, v66, v67\n"
        "v_accvgpr_read_b32 v68, a48\n"
        "v_accvgpr_read_b32 v69, a49\n"
        "v_mfma_f32_16x16x32_bf16 a[252:255], v[84:87], v[56:59], a[252:255]\n"
        "v_accvgpr_read_b32 v70, a50\n"
        "v_accvgpr_read_b32 v71, a51\n"
        "v_mfma_f32_16x16x32_bf16 a[48:51], v[84:87], v[84:87], 0\n"
        "v_accvgpr_read_b32 v72, a52\n"
        "v_accvgpr_read_b32 v73, a53\n"
        "v_accvgpr_read_b32 v74, a54\n"
        "v_accvgpr_read_b32 v75, a55\n"
        "v_mfma_f32_16x16x32_bf16 a[52:55], v[84:87], v[84:87], 0\n"
        "buffer_store_dwordx4 v[56:59], v250, s[56:59], s73 offen offset:1024 sc1\n"
        "v_accvgpr_read_b32 v76, a56\n"
        "v_accvgpr_read_b32 v77, a57\n"
        "v_accvgpr_read_b32 v78, a58\n"
        "v_accvgpr_read_b32 v79, a59\n"
        "v_mfma_f32_16x16x32_bf16 a[56:59], v[84:87], v[84:87], 0\n"
        "v_accvgpr_read_b32 v80, a60\n"
        "v_accvgpr_read_b32 v81, a61\n"
        "v_accvgpr_read_b32 v82, a62\n"
        "v_accvgpr_read_b32 v83, a63\n"
        "v_pk_mul_f32 v[68:69], v[68:69], v[26:27] op_sel_hi:[1,0]\n"
        "v_pk_mul_f32 v[70:71], v[70:71], v[26:27] op_sel_hi:[1,0]\n"
        "v_pk_mul_f32 v[72:73], v[72:73], v[26:27] op_sel_hi:[1,0]\n"
        "v_pk_mul_f32 v[74:75], v[74:75], v[26:27] op_sel_hi:[1,0]\n"
        "v_pk_mul_f32 v[76:77], v[76:77], v[26:27] op_sel_hi:[1,0]\n"
        "v_pk_mul_f32 v[78:79], v[78:79], v[26:27] op_sel_hi:[1,0]\n"
        "v_pk_mul_f32 v[80:81], v[80:81], v[26:27] op_sel_hi:[1,0]\n"
        "v_pk_mul_f32 v[82:83], v[82:83], v[26:27] op_sel_hi:[1,0]\n"
        "v_cvt_pk_bf16_f32 v68, v68, v69\n"
        "v_mfma_f32_16x16x32_bf16 a[60:63], v[84:87], v[84:87], 0\n"
        "v_cvt_pk_bf16_f32 v69, v70, v71\n"
        "v_cvt_pk_bf16_f32 v70, v72, v73\n"
        "v_cvt_pk_bf16_f32 v71, v74, v75\n"
        "buffer_store_dwordx4 v[68:71], v250, s[56:59], s73 offen offset:2048 sc1\n"
        "v_cvt_pk_bf16_f32 v72, v76, v77\n"
        "v_mfma_f32_16x16x32_bf16 a[252:255], v[84:87], v[68:71], a[252:255]\n"
        "v_cvt_pk_bf16_f32 v73, v78, v79\n"
        "v_cvt_pk_bf16_f32 v74, v80, v81\n"
        "v_cvt_pk_bf16_f32 v75, v82, v83\n"
        "v_rcp_f32 v24, v25\n"
        "v_accvgpr_read_b32 v52, a64\n"
        "v_mfma_f32_16x16x32_bf16 a[252:255], v[84:87], v[72:75], a[252:255]\n"
        "v_accvgpr_read_b32 v53, a65\n"
        "v_accvgpr_read_b32 v54, a66\n"
        "v_accvgpr_read_b32 v55, a67\n"
        "v_mfma_f32_16x16x32_bf16 a[64:67], v[84:87], v[84:87], 0\n"
        "v_accvgpr_read_b32 v56, a68\n"
        "v_accvgpr_read_b32 v57, a69\n"
        "buffer_store_dwordx4 v[72:75], v250, s[56:59], s73 offen offset:3072 sc1\n"
        "v_accvgpr_read_b32 v58, a70\n"
        "v_accvgpr_read_b32 v59, a71\n"
        "v_mfma_f32_16x16x32_bf16 a[68:71], v[84:87], v[84:87], 0\n"
        "v_accvgpr_read_b32 v60, a72\n"
        "v_accvgpr_read_b32 v61, a73\n"
        "v_accvgpr_read_b32 v62, a74\n"
        "v_accvgpr_read_b32 v63, a75\n"
        "v_mfma_f32_16x16x32_bf16 a[72:75], v[84:87], v[84:87], 0\n"
        "v_accvgpr_read_b32 v64, a76\n"
        "v_accvgpr_read_b32 v65, a77\n"
        "v_accvgpr_read_b32 v66, a78\n"
        "v_accvgpr_read_b32 v67, a79\n"
        "v_pk_mul_f32 v[52:53], v[52:53], v[24:25] op_sel_hi:[1,0]\n"
        "v_pk_mul_f32 v[54:55], v[54:55], v[24:25] op_sel_hi:[1,0]\n"
        "v_pk_mul_f32 v[56:57], v[56:57], v[24:25] op_sel_hi:[1,0]\n"
        "v_pk_mul_f32 v[58:59], v[58:59], v[24:25] op_sel_hi:[1,0]\n"
        "v_pk_mul_f32 v[60:61], v[60:61], v[24:25] op_sel_hi:[1,0]\n"
        "v_pk_mul_f32 v[62:63], v[62:63], v[24:25] op_sel_hi:[1,0]\n"
        "v_pk_mul_f32 v[64:65], v[64:65], v[24:25] op_sel_hi:[1,0]\n"
        "v_pk_mul_f32 v[66:67], v[66:67], v[24:25] op_sel_hi:[1,0]\n"
        "v_cvt_pk_bf16_f32 v52, v52, v53\n"
        "v_mfma_f32_16x16x32_bf16 a[76:79], v[84:87], v[84:87], 0\n"
        "v_cvt_pk_bf16_f32 v53, v54, v55\n"
        "v_cvt_pk_bf16_f32 v54, v56, v57\n"
        "v_cvt_pk_bf16_f32 v55, v58, v59\n"
        "buffer_store_dwordx4 v[52:55], v250, s[56:59], s74 offen offset:0 sc1\n"
        "v_cvt_pk_bf16_f32 v56, v60, v61\n"
        "v_mfma_f32_16x16x32_bf16 a[252:255], v[84:87], v[52:55], a[252:255]\n"
        "v_cvt_pk_bf16_f32 v57, v62, v63\n"
        "v_cvt_pk_bf16_f32 v58, v64, v65\n"
        "v_cvt_pk_bf16_f32 v59, v66, v67\n"
        "v_accvgpr_read_b32 v68, a80\n"
        "v_accvgpr_read_b32 v69, a81\n"
        "v_mfma_f32_16x16x32_bf16 a[252:255], v[84:87], v[56:59], a[252:255]\n"
        "v_accvgpr_read_b32 v70, a82\n"
        "v_accvgpr_read_b32 v71, a83\n"
        "v_mfma_f32_16x16x32_bf16 a[80:83], v[84:87], v[84:87], 0\n"
        "v_accvgpr_read_b32 v72, a84\n"
        "v_accvgpr_read_b32 v73, a85\n"
        "v_accvgpr_read_b32 v74, a86\n"
        "v_accvgpr_read_b32 v75, a87\n"
        "v_mfma_f32_16x16x32_bf16 a[84:87], v[84:87], v[84:87], 0\n"
        "buffer_store_dwordx4 v[56:59], v250, s[56:59], s74 offen offset:1024 sc1\n"
        "v_accvgpr_read_b32 v76, a88\n"
        "v_accvgpr_read_b32 v77, a89\n"
        "v_accvgpr_read_b32 v78, a90\n"
        "v_accvgpr_read_b32 v79, a91\n"
        "v_mfma_f32_16x16x32_bf16 a[88:91], v[84:87], v[84:87], 0\n"
        "v_accvgpr_read_b32 v80, a92\n"
        "v_accvgpr_read_b32 v81, a93\n"
        "v_accvgpr_read_b32 v82, a94\n"
        "v_accvgpr_read_b32 v83, a95\n"
        "v_pk_mul_f32 v[68:69], v[68:69], v[24:25] op_sel_hi:[1,0]\n"
        "v_pk_mul_f32 v[70:71], v[70:71], v[24:25] op_sel_hi:[1,0]\n"
        "v_pk_mul_f32 v[72:73], v[72:73], v[24:25] op_sel_hi:[1,0]\n"
        "v_pk_mul_f32 v[74:75], v[74:75], v[24:25] op_sel_hi:[1,0]\n"
        "v_pk_mul_f32 v[76:77], v[76:77], v[24:25] op_sel_hi:[1,0]\n"
        "v_pk_mul_f32 v[78:79], v[78:79], v[24:25] op_sel_hi:[1,0]\n"
        "v_pk_mul_f32 v[80:81], v[80:81], v[24:25] op_sel_hi:[1,0]\n"
        "v_pk_mul_f32 v[82:83], v[82:83], v[24:25] op_sel_hi:[1,0]\n"
        "v_cvt_pk_bf16_f32 v68, v68, v69\n"
        "v_mfma_f32_16x16x32_bf16 a[92:95], v[84:87], v[84:87], 0\n"
        "v_cvt_pk_bf16_f32 v69, v70, v71\n"
        "v_cvt_pk_bf16_f32 v70, v72, v73\n"
        "v_cvt_pk_bf16_f32 v71, v74, v75\n"
        "buffer_store_dwordx4 v[68:71], v250, s[56:59], s74 offen offset:2048 sc1\n"
        "v_cvt_pk_bf16_f32 v72, v76, v77\n"
        "v_mfma_f32_16x16x32_bf16 a[252:255], v[84:87], v[68:71], a[252:255]\n"
        "v_cvt_pk_bf16_f32 v73, v78, v79\n"
        "v_cvt_pk_bf16_f32 v74, v80, v81\n"
        "v_cvt_pk_bf16_f32 v75, v82, v83\n"
        "v_rcp_f32 v26, v27\n"
        "v_accvgpr_read_b32 v52, a96\n"
        "v_mfma_f32_16x16x32_bf16 a[252:255], v[84:87], v[72:75], a[252:255]\n"
        "v_accvgpr_read_b32 v53, a97\n"
        "v_accvgpr_read_b32 v54, a98\n"
        "v_accvgpr_read_b32 v55, a99\n"
        "v_mfma_f32_16x16x32_bf16 a[96:99], v[84:87], v[84:87], 0\n"
        "v_accvgpr_read_b32 v56, a100\n"
        "v_accvgpr_read_b32 v57, a101\n"
        "buffer_store_dwordx4 v[72:75], v250, s[56:59], s74 offen offset:3072 sc1\n"
        "v_accvgpr_read_b32 v58, a102\n"
        "v_accvgpr_read_b32 v59, a103\n"
        "v_mfma_f32_16x16x32_bf16 a[100:103], v[84:87], v[84:87], 0\n"
        "v_accvgpr_read_b32 v60, a104\n"
        "v_accvgpr_read_b32 v61, a105\n"
        "v_accvgpr_read_b32 v62, a106\n"
        "v_accvgpr_read_b32 v63, a107\n"
        "v_mfma_f32_16x16x32_bf16 a[104:107], v[84:87], v[84:87], 0\n"
        "v_accvgpr_read_b32 v64, a108\n"
        "v_accvgpr_read_b32 v65, a109\n"
        "v_accvgpr_read_b32 v66, a110\n"
        "v_accvgpr_read_b32 v67, a111\n"
        "v_pk_mul_f32 v[52:53], v[52:53], v[26:27] op_sel_hi:[1,0]\n"
        "v_pk_mul_f32 v[54:55], v[54:55], v[26:27] op_sel_hi:[1,0]\n"
        "v_pk_mul_f32 v[56:57], v[56:57], v[26:27] op_sel_hi:[1,0]\n"
        "v_pk_mul_f32 v[58:59], v[58:59], v[26:27] op_sel_hi:[1,0]\n"
        "v_pk_mul_f32 v[60:61], v[60:61], v[26:27] op_sel_hi:[1,0]\n"
        "v_pk_mul_f32 v[62:63], v[62:63], v[26:27] op_sel_hi:[1,0]\n"
        "v_pk_mul_f32 v[64:65], v[64:65], v[26:27] op_sel_hi:[1,0]\n"
        "v_pk_mul_f32 v[66:67], v[66:67], v[26:27] op_sel_hi:[1,0]\n"
        "v_cvt_pk_bf16_f32 v52, v52, v53\n"
        "v_mfma_f32_16x16x32_bf16 a[108:111], v[84:87], v[84:87], 0\n"
        "v_cvt_pk_bf16_f32 v53, v54, v55\n"
        "v_cvt_pk_bf16_f32 v54, v56, v57\n"
        "v_cvt_pk_bf16_f32 v55, v58, v59\n"
        "buffer_store_dwordx4 v[52:55], v250, s[56:59], s75 offen offset:0 sc1\n"
        "v_cvt_pk_bf16_f32 v56, v60, v61\n"
        "v_mfma_f32_16x16x32_bf16 a[252:255], v[84:87], v[52:55], a[252:255]\n"
        "v_cvt_pk_bf16_f32 v57, v62, v63\n"
        "v_cvt_pk_bf16_f32 v58, v64, v65\n"
        "v_cvt_pk_bf16_f32 v59, v66, v67\n"
        "v_accvgpr_read_b32 v68, a112\n"
        "v_accvgpr_read_b32 v69, a113\n"
        "v_mfma_f32_16x16x32_bf16 a[252:255], v[84:87], v[56:59], a[252:255]\n"
        "v_accvgpr_read_b32 v70, a114\n"
        "v_accvgpr_read_b32 v71, a115\n"
        "v_mfma_f32_16x16x32_bf16 a[112:115], v[84:87], v[84:87], 0\n"
        "v_accvgpr_read_b32 v72, a116\n"
        "v_accvgpr_read_b32 v73, a117\n"
        "v_accvgpr_read_b32 v74, a118\n"
        "v_accvgpr_read_b32 v75, a119\n"
        "v_mfma_f32_16x16x32_bf16 a[116:119], v[84:87], v[84:87], 0\n"
        "buffer_store_dwordx4 v[56:59], v250, s[56:59], s75 offen offset:1024 sc1\n"
        "v_accvgpr_read_b32 v76, a120\n"
        "v_accvgpr_read_b32 v77, a121\n"
        "v_accvgpr_read_b32 v78, a122\n"
        "v_accvgpr_read_b32 v79, a123\n"
        "v_mfma_f32_16x16x32_bf16 a[120:123], v[84:87], v[84:87], 0\n"
        "v_accvgpr_read_b32 v80, a124\n"
        "v_accvgpr_read_b32 v81, a125\n"
        "v_accvgpr_read_b32 v82, a126\n"
        "v_accvgpr_read_b32 v83, a127\n"
        "v_pk_mul_f32 v[68:69], v[68:69], v[26:27] op_sel_hi:[1,0]\n"
        "v_pk_mul_f32 v[70:71], v[70:71], v[26:27] op_sel_hi:[1,0]\n"
        "v_pk_mul_f32 v[72:73], v[72:73], v[26:27] op_sel_hi:[1,0]\n"
        "v_pk_mul_f32 v[74:75], v[74:75], v[26:27] op_sel_hi:[1,0]\n"
        "v_pk_mul_f32 v[76:77], v[76:77], v[26:27] op_sel_hi:[1,0]\n"
        "v_pk_mul_f32 v[78:79], v[78:79], v[26:27] op_sel_hi:[1,0]\n"
        "v_pk_mul_f32 v[80:81], v[80:81], v[26:27] op_sel_hi:[1,0]\n"
        "v_pk_mul_f32 v[82:83], v[82:83], v[26:27] op_sel_hi:[1,0]\n"
        "v_cvt_pk_bf16_f32 v68, v68, v69\n"
        "v_mfma_f32_16x16x32_bf16 a[124:127], v[84:87], v[84:87], 0\n"
        "v_cvt_pk_bf16_f32 v69, v70, v71\n"
        "v_cvt_pk_bf16_f32 v70, v72, v73\n"
        "v_cvt_pk_bf16_f32 v71, v74, v75\n"
        "buffer_store_dwordx4 v[68:71], v250, s[56:59], s75 offen offset:2048 sc1\n"
        "v_accvgpr_read_b32 v52, a144\n"
        "v_mfma_f32_16x16x32_bf16 a[252:255], v[84:87], v[68:71], a[252:255]\n"
        "v_accvgpr_read_b32 v53, a145\n"
        "v_accvgpr_read_b32 v54, a146\n"
        "v_accvgpr_read_b32 v55, a147\n"
        "v_mfma_f32_16x16x32_bf16 a[144:147], v[84:87], v[84:87], 0\n"
        "v_accvgpr_read_b32 v56, a148\n"
        "v_accvgpr_read_b32 v57, a149\n"
        "v_accvgpr_read_b32 v58, a150\n"
        "v_accvgpr_read_b32 v59, a151\n"
        "v_mfma_f32_16x16x32_bf16 a[148:151], v[84:87], v[84:87], 0\n"
        "v_accvgpr_read_b32 v60, a152\n"
        "v_accvgpr_read_b32 v61, a153\n"
        "v_accvgpr_read_b32 v62, a154\n"
        "v_accvgpr_read_b32 v63, a155\n"
        "v_mfma_f32_16x16x32_bf16 a[152:155], v[84:87], v[84:87], 0\n"
        "v_accvgpr_read_b32 v64, a156\n"
        "v_accvgpr_read_b32 v65, a157\n"
        "v_accvgpr_read_b32 v66, a158\n"
        "v_accvgpr_read_b32 v67, a159\n"
        "s_waitcnt lgkmcnt(0)\n"
        "v_pk_add_f32 v[52:53], v[52:53], v[8:9]\n"
        "v_pk_add_f32 v[54:55], v[54:55], v[10:11]\n"
        "v_pk_add_f32 v[56:57], v[56:57], v[12:13]\n"
        "v_pk_add_f32 v[58:59], v[58:59], v[14:15]\n"
        "v_pk_add_f32 v[60:61], v[60:61], v[16:17]\n"
        "v_pk_add_f32 v[62:63], v[62:63], v[18:19]\n"
        "v_pk_add_f32 v[64:65], v[64:65], v[20:21]\n"
        "v_pk_add_f32 v[66:67], v[66:67], v[22:23]\n"
        "v_cvt_pk_bf16_f32 v72, v76, v77\n"
        "v_mfma_f32_16x16x32_bf16 a[156:159], v[84:87], v[84:87], 0\n"
        "v_cvt_pk_bf16_f32 v73, v78, v79\n"
        "v_cvt_pk_bf16_f32 v74, v80, v81\n"
        "v_cvt_pk_bf16_f32 v75, v82, v83\n"
        "buffer_store_dwordx4 v[72:75], v250, s[56:59], s75 offen offset:3072 sc1\n"
        "v_accvgpr_read_b32 v2, a176\n"
        "v_mfma_f32_16x16x32_bf16 a[252:255], v[84:87], v[72:75], a[252:255]\n"
        "v_max_f32 v3, v3, v27\n"
        "v_add_f32 v2, v2, v1\n"
        "v_mfma_f32_16x16x32_bf16 a[176:179], v[84:87], v[84:87], 0\n"
        "v_max_f32 v3, v3, v2\n"
        "v_rcp_f32 v2, v2\n"
        "v_accvgpr_read_b32 v216, a244\n"
        "v_pk_mul_f32 v[52:53], v[52:53], v[2:3] op_sel_hi:[1,0]\n"
        "v_pk_mul_f32 v[54:55], v[54:55], v[2:3] op_sel_hi:[1,0]\n"
        "v_pk_mul_f32 v[56:57], v[56:57], v[2:3] op_sel_hi:[1,0]\n"
        "v_pk_mul_f32 v[58:59], v[58:59], v[2:3] op_sel_hi:[1,0]\n"
        "v_pk_mul_f32 v[60:61], v[60:61], v[2:3] op_sel_hi:[1,0]\n"
        "v_pk_mul_f32 v[62:63], v[62:63], v[2:3] op_sel_hi:[1,0]\n"
        "v_pk_mul_f32 v[64:65], v[64:65], v[2:3] op_sel_hi:[1,0]\n"
        "v_pk_mul_f32 v[66:67], v[66:67], v[2:3] op_sel_hi:[1,0]\n"
        "v_cvt_pk_bf16_f32 v52, v52, v53\n"
        "v_cvt_pk_bf16_f32 v53, v54, v55\n"
        "v_cvt_pk_bf16_f32 v54, v56, v57\n"
        "v_cvt_pk_bf16_f32 v55, v58, v59\n"
        "v_accvgpr_read_b32 v217, a245\n"
        "v_accvgpr_read_b32 v218, a246\n"
        "v_mfma_f32_16x16x32_bf16 a[252:255], v[84:87], v[52:55], a[252:255]\n"
        "v_accvgpr_read_b32 v219, a247\n"
        "buffer_store_dwordx4 v[52:55], v250, s[56:59], s76 offen offset:0 sc1\n"
        "v_accvgpr_read_b32 v220, a248\n"
        "v_mfma_f32_16x16x32_bf16 v[44:47], a[180:183], v[216:219], 0\n"
        "v_accvgpr_read_b32 v221, a249\n"
        "v_mfma_f32_16x16x32_bf16 v[48:51], a[212:215], v[216:219], 0\n"
        "v_accvgpr_read_b32 v222, a250\n"
        "v_accvgpr_read_b32 v223, a251\n"
        "v_cvt_pk_bf16_f32 v56, v60, v61\n"
        "v_cvt_pk_bf16_f32 v57, v62, v63\n"
        "v_mfma_f32_16x16x32_bf16 v[44:47], a[184:187], v[220:223], v[44:47]\n"
        "v_cvt_pk_bf16_f32 v58, v64, v65\n"
        "v_mfma_f32_16x16x32_bf16 v[48:51], a[216:219], v[220:223], v[48:51]\n"
        "v_cvt_pk_bf16_f32 v59, v66, v67\n"
        "v_max3_f32 v28, v28, v29, v30\n"
        "v_mfma_f32_16x16x32_bf16 v[44:47], a[188:191], v[224:227], v[44:47]\n"
        "v_max3_f32 v32, v32, v33, v34\n"
        "v_max3_f32 v36, v36, v37, v38\n"
        "v_mfma_f32_16x16x32_bf16 v[48:51], a[220:223], v[224:227], v[48:51]\n"
        "v_max3_f32 v40, v40, v41, v42\n"
        "v_max_f32 v28, v28, v31\n"
        "v_mfma_f32_16x16x32_bf16 a[252:255], v[84:87], v[56:59], a[252:255]\n"
        "v_max_f32 v32, v32, v35\n"
        "v_max_f32 v36, v36, v39\n"
        "v_mfma_f32_16x16x32_bf16 v[44:47], a[192:195], v[228:231], v[44:47]\n"
        "v_max_f32 v40, v40, v43\n"
        "v_mov_b32 v29, v28\n"
        "v_mov_b32 v33, v32\n"
        "v_mfma_f32_16x16x32_bf16 v[48:51], a[224:227], v[228:231], v[48:51]\n"
        "v_mov_b32 v37, v36\n"
        "v_accvgpr_read_b32 v1, a252\n"
        "v_add_f32 v3, v3, v1\n"
        "v_cmp_nlt_f32 vcc, v3, v255\n"
        "v_mov_b32 v41, v40\n"
        "v_permlane32_swap_b32 v28, v29\n"
        "s_cmp_lg_u64 vcc, 0\n"
        "s_cselect_b32 s80, 3, 0\n"
        "v_max3_f32 v44, v44, v48, v49\n"
        "v_max3_f32 v45, v45, v50, v51\n"
        "v_max3_f32 v44, v44, v45, v46\n"
        "v_max_f32 v44, v44, v47\n"
        "v_mov_b32 v45, v44\n"
        "v_permlane32_swap_b32 v32, v33\n"
        "v_permlane32_swap_b32 v36, v37\n"
        "v_permlane32_swap_b32 v40, v41\n"
        "v_permlane32_swap_b32 v44, v45\n"
        "s_or_b32 s77, s77, s80\n"
        "v_max_f32 v28, v28, v29\n"
        "v_max_f32 v32, v32, v33\n"
        "v_max_f32 v36, v36, v37\n"
        "v_max_f32 v40, v40, v41\n"
        "v_max_f32 v44, v44, v45\n"
        "s_add_u32 s72, s72, 4096\n"
        "v_mov_b32 v29, v28\n"
        "v_mov_b32 v33, v32\n"
        "v_mov_b32 v37, v36\n"
        "v_mov_b32 v41, v40\n"
        "v_mov_b32 v45, v44\n"
        "s_add_u32 s73, s73, 4096\n"
        "v_permlane16_swap_b32 v28, v29\n"
        "v_permlane16_swap_b32 v32, v33\n"
        "v_permlane16_swap_b32 v36, v37\n"
        "v_permlane16_swap_b32 v40, v41\n"
        "v_permlane16_swap_b32 v44, v45\n"
        "s_add_u32 s74, s74, 4096\n"
        "v_max_f32 v28, v28, v29\n"
        "v_max_f32 v32, v32, v33\n"
        "v_max_f32 v36, v36, v37\n"
        "v_max_f32 v40, v40, v41\n"
        "v_max_f32 v44, v44, v45\n"
        "buffer_store_dwordx4 v[56:59], v250, s[56:59], s76 offen offset:1024 sc1\n"
        "s_add_u32 s75, s75, 4096\n"
        "v_xor_b32 v8, 0x80000000, v28\n"
        "v_xor_b32 v9, 0x80000000, v28\n"
        "v_xor_b32 v10, 0x80000000, v28\n"
        "s_memrealtime s[60:61]\n"
        "v_xor_b32 v11, 0x80000000, v28\n"
        "v_xor_b32 v12, 0x80000000, v32\n"
        "v_xor_b32 v13, 0x80000000, v32\n"
        "v_xor_b32 v14, 0x80000000, v32\n"
        "v_xor_b32 v15, 0x80000000, v32\n"
        "v_xor_b32 v16, 0x80000000, v36\n"
        "v_xor_b32 v17, 0x80000000, v36\n"
        "v_xor_b32 v18, 0x80000000, v36\n"
        "v_xor_b32 v19, 0x80000000, v36\n"
        "v_xor_b32 v20, 0x80000000, v40\n"
        "v_xor_b32 v21, 0x80000000, v40\n"
        "v_xor_b32 v22, 0x80000000, v40\n"
        "v_xor_b32 v23, 0x80000000, v40\n"
        "v_xor_b32 v24, 0x80000000, v44\n"
        "v_xor_b32 v25, 0x80000000, v44\n"
        "v_xor_b32 v26, 0x80000000, v44\n"
        "v_xor_b32 v27, 0x80000000, v44\n"
        "s_add_u32 s76, s76, 4096\n"
        "s_mov_b64 vcc, -1\n"
        "s_mov_b32 s69, 1\n"
        "s_mov_b32 s68, 4\n"
        "s_mov_b32 s91, 4\n"
        "s_mov_b32 s90, 1\n"
        "s_mov_b32 s63, -1\n"
        "s_waitcnt lgkmcnt(0)\n"
        "s_add_u32 s83, s60, 32\n"
        "s_max_u32 s79, s79, 146\n"
        "s_nop 0\n"
        "s_branch .Lk_T1\n"
        ".Lk_LAST:\n"
        "s_cmp_eq_u32 s78, 1\n"
        "s_cbranch_scc1 .Lk_EPIB1\n"
        "v_accvgpr_read_b32 v24, a160\n"
        "v_accvgpr_read_b32 v2, a161\n"
        "v_mfma_f32_16x16x32_bf16 a[128:131], v[100:103], v[84:87], a[128:131]\n"
        "v_accvgpr_read_b32 v25, a164\n"
        "v_accvgpr_read_b32 v3, a165\n"
        "v_mfma_f32_16x16x32_bf16 a[132:135], v[116:119], v[84:87], a[132:135]\n"
        "v_add_f32 v24, v24, v2\n"
        "v_add_f32 v25, v25, v3\n"
        "v_mfma_f32_16x16x32_bf16 a[136:139], v[132:135], v[84:87], a[136:139]\n"
        "v_accvgpr_read_b32 v26, a168\n"
        "v_accvgpr_read_b32 v2, a169\n"
        "v_mfma_f32_16x16x32_bf16 a[140:143], v[148:151], v[84:87], a[140:143]\n"
        "v_accvgpr_read_b32 v27, a172\n"
        "v_accvgpr_read_b32 v3, a173\n"
        "v_mfma_f32_16x16x32_bf16 a[144:147], v[164:167], v[84:87], a[144:147]\n"
        "v_add_f32 v26, v26, v2\n"
        "v_add_f32 v27, v27, v3\n"
        "v_mfma_f32_16x16x32_bf16 a[148:151], v[180:183], v[84:87], a[148:151]\n"
        "v_max3_f32 v3, v24, v25, v26\n"
        "v_accvgpr_read_b32 v168, a0\n"
        "v_mfma_f32_16x16x32_bf16 a[152:155], v[196:199], v[84:87], a[152:155]\n"
        "v_accvgpr_read_b32 v169, a1\n"
        "v_accvgpr_read_b32 v170, a2\n"
        "v_mfma_f32_16x16x32_bf16 a[156:159], v[212:215], v[84:87], a[156:159]\n"
        "v_accvgpr_read_b32 v171, a3\n"
        "v_accvgpr_read_b32 v172, a4\n"
        "v_mfma_f32_16x16x32_bf16 a[176:179], v[4:7], v[84:87], a[176:179]\n"
        "v_rcp_f32 v84, v24\n"
        "v_rcp_f32 v85, v25\n"
        "v_accvgpr_read_b32 v173, a5\n"
        "v_accvgpr_read_b32 v174, a6\n"
        "v_accvgpr_read_b32 v175, a7\n"
        "v_accvgpr_read_b32 v176, a32\n"
        "v_accvgpr_read_b32 v177, a33\n"
        "v_accvgpr_read_b32 v178, a34\n"
        "v_accvgpr_read_b32 v179, a35\n"
        "v_accvgpr_read_b32 v180, a36\n"
        "v_accvgpr_read_b32 v181, a37\n"
        "v_accvgpr_read_b32 v182, a38\n"
        "v_accvgpr_read_b32 v183, a39\n"
        "v_mov_b32 v24, v85\n"
        "v_pk_mul_f32 v[168:169], v[168:169], v[84:85] op_sel_hi:[1,0]\n"
        "v_pk_mul_f32 v[170:171], v[170:171], v[84:85] op_sel_hi:[1,0]\n"
        "v_pk_mul_f32 v[172:173], v[172:173], v[84:85] op_sel_hi:[1,0]\n"
        "v_pk_mul_f32 v[174:175], v[174:175], v[84:85] op_sel_hi:[1,0]\n"
        "v_pk_mul_f32 v[176:177], v[176:177], v[24:25] op_sel_hi:[1,0]\n"
        "v_pk_mul_f32 v[178:179], v[178:179], v[24:25] op_sel_hi:[1,0]\n"
        "v_pk_mul_f32 v[180:181], v[180:181], v[24:25] op_sel_hi:[1,0]\n"
        "v_pk_mul_f32 v[182:183], v[182:183], v[24:25] op_sel_hi:[1,0]\n"
        "v_cvt_pk_bf16_f32 v88, v168, v169\n"
        "v_cvt_pk_bf16_f32 v89, v170, v171\n"
        "v_cvt_pk_bf16_f32 v90, v172, v173\n"
        "v_cvt_pk_bf16_f32 v91, v174, v175\n"
        "buffer_store_dwordx4 v[88:91], v250, s[56:59], s72 offen offset:0 sc1\n"
        "s_waitcnt lgkmcnt(6)\n"
        "v_cvt_pk_bf16_f32 v92, v176, v177\n"
        "v_cvt_pk_bf16_f32 v93, v178, v179\n"
        "s_waitcnt lgkmcnt(5)\n"
        "v_cvt_pk_bf16_f32 v94, v180, v181\n"
        "v_cvt_pk_bf16_f32 v95, v182, v183\n"
        "v_rcp_f32 v86, v26\n"
        "v_rcp_f32 v87, v27\n"
        "v_accvgpr_read_b32 v184, a64\n"
        "v_accvgpr_read_b32 v185, a65\n"
        "v_accvgpr_read_b32 v186, a66\n"
        "v_accvgpr_read_b32 v187, a67\n"
        "v_accvgpr_read_b32 v188, a68\n"
        "v_accvgpr_read_b32 v189, a69\n"
        "v_accvgpr_read_b32 v190, a70\n"
        "v_accvgpr_read_b32 v191, a71\n"
        "buffer_store_dwordx4 v[92:95], v250, s[56:59], s73 offen offset:0 sc1\n"
        "v_accvgpr_read_b32 v192, a96\n"
        "v_accvgpr_read_b32 v193, a97\n"
        "v_accvgpr_read_b32 v194, a98\n"
        "v_accvgpr_read_b32 v195, a99\n"
        "v_accvgpr_read_b32 v196, a100\n"
        "v_accvgpr_read_b32 v197, a101\n"
        "v_accvgpr_read_b32 v198, a102\n"
        "v_accvgpr_read_b32 v199, a103\n"
        "v_mov_b32 v26, v87\n"
        "v_pk_mul_f32 v[184:185], v[184:185], v[86:87] op_sel_hi:[1,0]\n"
        "v_pk_mul_f32 v[186:187], v[186:187], v[86:87] op_sel_hi:[1,0]\n"
        "v_pk_mul_f32 v[188:189], v[188:189], v[86:87] op_sel_hi:[1,0]\n"
        "v_pk_mul_f32 v[190:191], v[190:191], v[86:87] op_sel_hi:[1,0]\n"
        "v_pk_mul_f32 v[192:193], v[192:193], v[26:27] op_sel_hi:[1,0]\n"
        "v_pk_mul_f32 v[194:195], v[194:195], v[26:27] op_sel_hi:[1,0]\n"
        "v_pk_mul_f32 v[196:197], v[196:197], v[26:27] op_sel_hi:[1,0]\n"
        "v_pk_mul_f32 v[198:199], v[198:199], v[26:27] op_sel_hi:[1,0]\n"
        "v_cvt_pk_bf16_f32 v96, v184, v185\n"
        "v_cvt_pk_bf16_f32 v97, v186, v187\n"
        "v_cvt_pk_bf16_f32 v98, v188, v189\n"
        "v_cvt_pk_bf16_f32 v99, v190, v191\n"
        "buffer_store_dwordx4 v[96:99], v250, s[56:59], s74 offen offset:0 sc1\n"
        "s_and_b32 s80, s64, 2\n"
        "s_lshl_b32 s80, s80, 14\n"
        "s_add_u32 s80, s80, 81920\n"
        "v_add_u32 v251, s80, v250\n"
        "v_cvt_pk_bf16_f32 v100, v192, v193\n"
        "v_cvt_pk_bf16_f32 v101, v194, v195\n"
        "v_cvt_pk_bf16_f32 v102, v196, v197\n"
        "v_cvt_pk_bf16_f32 v103, v198, v199\n"
        "ds_write_b128 v251, a[144:147] offset:0\n"
        "v_xor_b32 v252, 8192, v251\n"
        "v_accvgpr_read_b32 v200, a8\n"
        "v_accvgpr_read_b32 v201, a9\n"
        "v_accvgpr_read_b32 v202, a10\n"
        "v_accvgpr_read_b32 v203, a11\n"
        "v_accvgpr_read_b32 v204, a12\n"
        "v_accvgpr_read_b32 v205, a13\n"
        "buffer_store_dwordx4 v[100:103], v250, s[56:59], s75 offen offset:0 sc1\n"
        "ds_write_b128 v251, a[148:151] offset:1024\n"
        "v_accvgpr_read_b32 v206, a14\n"
        "v_accvgpr_read_b32 v207, a15\n"
        "v_accvgpr_read_b32 v208, a16\n"
        "v_accvgpr_read_b32 v209, a17\n"
        "v_accvgpr_read_b32 v210, a18\n"
        "v_accvgpr_read_b32 v211, a19\n"
        "v_accvgpr_read_b32 v212, a20\n"
        "v_accvgpr_read_b32 v213, a21\n"
        "ds_write_b128 v251, a[152:155] offset:2048\n"
        "v_accvgpr_read_b32 v214, a22\n"
        "v_accvgpr_read_b32 v215, a23\n"
        "v_pk_mul_f32 v[200:201], v[200:201], v[84:85] op_sel_hi:[1,0]\n"
        "v_pk_mul_f32 v[202:203], v[202:203], v[84:85] op_sel_hi:[1,0]\n"
        "v_pk_mul_f32 v[204:205], v[204:205], v[84:85] op_sel_hi:[1,0]\n"
        "v_pk_mul_f32 v[206:207], v[206:207], v[84:85] op_sel_hi:[1,0]\n"
        "v_pk_mul_f32 v[208:209], v[208:209], v[84:85] op_sel_hi:[1,0]\n"
        "v_pk_mul_f32 v[210:211], v[210:211], v[84:85] op_sel_hi:[1,0]\n"
        "v_pk_mul_f32 v[212:213], v[212:213], v[84:85] op_sel_hi:[1,0]\n"
        "v_pk_mul_f32 v[214:215], v[214:215], v[84:85] op_sel_hi:[1,0]\n"
        "ds_write_b128 v251, a[156:159] offset:3072\n"
        "s_waitcnt lgkmcnt(8)\n"
        "v_cvt_pk_bf16_f32 v108, v200, v201\n"
        "v_cvt_pk_bf16_f32 v109, v202, v203\n"
        "s_waitcnt lgkmcnt(7)\n"
        "v_cvt_pk_bf16_f32 v110, v204, v205\n"
        "v_cvt_pk_bf16_f32 v111, v206, v207\n"
        "buffer_store_dwordx4 v[108:111], v250, s[56:59], s72 offen offset:1024 sc1\n"
        "v_cvt_pk_bf16_f32 v128, v208, v209\n"
        "v_cvt_pk_bf16_f32 v129, v210, v211\n"
        "v_cvt_pk_bf16_f32 v130, v212, v213\n"
        "ds_write_b128 v251, a[176:179] offset:4096\n"
        "v_cvt_pk_bf16_f32 v131, v214, v215\n"
        "v_accvgpr_read_b32 v52, a128\n"
        "v_accvgpr_read_b32 v53, a129\n"
        "v_accvgpr_read_b32 v54, a130\n"
        "v_accvgpr_read_b32 v55, a131\n"
        "v_accvgpr_read_b32 v56, a132\n"
        "v_accvgpr_read_b32 v57, a133\n"
        "v_accvgpr_read_b32 v58, a134\n"
        "v_accvgpr_read_b32 v59, a135\n"
        "v_accvgpr_read_b32 v60, a136\n"
        "v_accvgpr_read_b32 v61, a137\n"
        "buffer_store_dwordx4 v[128:131], v250, s[56:59], s72 offen offset:2048 sc1\n"
        "v_accvgpr_read_b32 v62, a138\n"
        "v_accvgpr_read_b32 v63, a139\n"
        "v_accvgpr_read_b32 v64, a140\n"
        "v_accvgpr_read_b32 v65, a141\n"
        "v_accvgpr_read_b32 v66, a142\n"
        "v_accvgpr_read_b32 v67, a143\n"
        "v_accvgpr_read_b32 v168, a24\n"
        "v_accvgpr_read_b32 v169, a25\n"
        "v_accvgpr_read_b32 v170, a26\n"
        "v_accvgpr_read_b32 v171, a27\n"
        "v_accvgpr_read_b32 v172, a28\n"
        "v_accvgpr_read_b32 v173, a29\n"
        "v_accvgpr_read_b32 v174, a30\n"
        "v_accvgpr_read_b32 v175, a31\n"
        "s_waitcnt vmcnt(10) lgkmcnt(0)\n"
        "s_barrier\n"
        "ds_read_b32 v1, v252 offset:4096\n"
        "v_accvgpr_read_b32 v176, a40\n"
        "ds_read_b128 v[8:11], v252 offset:0\n"
        "v_accvgpr_read_b32 v177, a41\n"
        "v_accvgpr_read_b32 v178, a42\n"
        "ds_read_b128 v[12:15], v252 offset:1024\n"
        "v_accvgpr_read_b32 v179, a43\n"
        "v_accvgpr_read_b32 v180, a44\n"
        "ds_read_b128 v[16:19], v252 offset:2048\n"
        "v_accvgpr_read_b32 v181, a45\n"
        "v_accvgpr_read_b32 v182, a46\n"
        "ds_read_b128 v[20:23], v252 offset:3072\n"
        "v_accvgpr_read_b32 v183, a47\n"
        "v_pk_mul_f32 v[168:169], v[168:169], v[84:85] op_sel_hi:[1,0]\n"
        "v_pk_mul_f32 v[170:171], v[170:171], v[84:85] op_sel_hi:[1,0]\n"
        "v_pk_mul_f32 v[172:173], v[172:173], v[84:85] op_sel_hi:[1,0]\n"
        "v_pk_mul_f32 v[174:175], v[174:175], v[84:85] op_sel_hi:[1,0]\n"
        "v_pk_mul_f32 v[176:177], v[176:177], v[24:25] op_sel_hi:[1,0]\n"
        "v_pk_mul_f32 v[178:179], v[178:179], v[24:25] op_sel_hi:[1,0]\n"
        "v_pk_mul_f32 v[180:181], v[180:181], v[24:25] op_sel_hi:[1,0]\n"
        "v_pk_mul_f32 v[182:183], v[182:183], v[24:25] op_sel_hi:[1,0]\n"
        "v_cvt_pk_bf16_f32 v148, v168, v169\n"
        "v_cvt_pk_bf16_f32 v149, v170, v171\n"
        "v_cvt_pk_bf16_f32 v150, v172, v173\n"
        "v_cvt_pk_bf16_f32 v151, v174, v175\n"
        "buffer_store_dwordx4 v[148:151], v250, s[56:59], s72 offen offset:3072 sc1\n"
        "v_accvgpr_read_b32 v2, a176\n"
        "v_max_f32 v3, v3, v27\n"
        "v_cvt_pk_bf16_f32 v112, v176, v177\n"
        "s_waitcnt lgkmcnt(4)\n"
        "v_add_f32 v2, v2, v1\n"
        "v_max_f32 v3, v3, v2\n"
        "v_rcp_f32 v2, v2\n"
        "v_cvt_pk_bf16_f32 v113, v178, v179\n"
        "v_cvt_pk_bf16_f32 v114, v180, v181\n"
        "v_cvt_pk_bf16_f32 v115, v182, v183\n"
        "v_accvgpr_read_b32 v184, a72\n"
        "v_accvgpr_read_b32 v185, a73\n"
        "v_accvgpr_read_b32 v186, a74\n"
        "v_accvgpr_read_b32 v187, a75\n"
        "v_accvgpr_read_b32 v188, a76\n"
        "s_waitcnt lgkmcnt(0)\n"
        "v_pk_add_f32 v[52:53], v[52:53], v[8:9]\n"
        "v_pk_add_f32 v[54:55], v[54:55], v[10:11]\n"
        "v_pk_add_f32 v[56:57], v[56:57], v[12:13]\n"
        "v_pk_add_f32 v[58:59], v[58:59], v[14:15]\n"
        "v_pk_add_f32 v[60:61], v[60:61], v[16:17]\n"
        "v_pk_add_f32 v[62:63], v[62:63], v[18:19]\n"
        "v_pk_add_f32 v[64:65], v[64:65], v[20:21]\n"
        "v_pk_add_f32 v[66:67], v[66:67], v[22:23]\n"
        "v_pk_mul_f32 v[52:53], v[52:53], v[2:3] op_sel_hi:[1,0]\n"
        "v_pk_mul_f32 v[54:55], v[54:55], v[2:3] op_sel_hi:[1,0]\n"
        "v_pk_mul_f32 v[56:57], v[56:57], v[2:3] op_sel_hi:[1,0]\n"
        "v_pk_mul_f32 v[58:59], v[58:59], v[2:3] op_sel_hi:[1,0]\n"
        "v_pk_mul_f32 v[60:61], v[60:61], v[2:3] op_sel_hi:[1,0]\n"
        "v_pk_mul_f32 v[62:63], v[62:63], v[2:3] op_sel_hi:[1,0]\n"
        "v_pk_mul_f32 v[64:65], v[64:65], v[2:3] op_sel_hi:[1,0]\n"
        "v_pk_mul_f32 v[66:67], v[66:67], v[2:3] op_sel_hi:[1,0]\n"
        "buffer_store_dwordx4 v[112:115], v250, s[56:59], s73 offen offset:1024 sc1\n"
        "v_cvt_pk_bf16_f32 v52, v52, v53\n"
        "v_cvt_pk_bf16_f32 v53, v54, v55\n"
        "v_accvgpr_read_b32 v189, a77\n"
        "v_accvgpr_read_b32 v190, a78\n"
        "v_accvgpr_read_b32 v191, a79\n"
        "v_accvgpr_read_b32 v192, a104\n"
        "v_accvgpr_read_b32 v193, a105\n"
        "v_accvgpr_read_b32 v194, a106\n"
        "v_accvgpr_read_b32 v195, a107\n"
        "v_accvgpr_read_b32 v196, a108\n"
        "v_accvgpr_read_b32 v197, a109\n"
        "v_accvgpr_read_b32 v198, a110\n"
        "v_accvgpr_read_b32 v199, a111\n"
        "v_pk_mul_f32 v[184:185], v[184:185], v[86:87] op_sel_hi:[1,0]\n"
        "v_pk_mul_f32 v[186:187], v[186:187], v[86:87] op_sel_hi:[1,0]\n"
        "v_pk_mul_f32 v[188:189], v[188:189], v[86:87] op_sel_hi:[1,0]\n"
        "v_pk_mul_f32 v[190:191], v[190:191], v[86:87] op_sel_hi:[1,0]\n"
        "v_pk_mul_f32 v[192:193], v[192:193], v[26:27] op_sel_hi:[1,0]\n"
        "v_pk_mul_f32 v[194:195], v[194:195], v[26:27] op_sel_hi:[1,0]\n"
        "v_pk_mul_f32 v[196:197], v[196:197], v[26:27] op_sel_hi:[1,0]\n"
        "v_pk_mul_f32 v[198:199], v[198:199], v[26:27] op_sel_hi:[1,0]\n"
        "v_cvt_pk_bf16_f32 v116, v184, v185\n"
        "v_cvt_pk_bf16_f32 v117, v186, v187\n"
        "v_cvt_pk_bf16_f32 v118, v188, v189\n"
        "v_cvt_pk_bf16_f32 v119, v190, v191\n"
        "buffer_store_dwordx4 v[116:119], v250, s[56:59], s74 offen offset:1024 sc1\n"
        "v_cvt_pk_bf16_f32 v54, v56, v57\n"
        "v_cvt_pk_bf16_f32 v55, v58, v59\n"
        "v_accvgpr_write_b32 a252, 0\n"
        "v_accvgpr_write_b32 a253, 0\n"
        "v_accvgpr_write_b32 a254, 0\n"
        "v_accvgpr_write_b32 a255, 0\n"
        "v_mov_b32 v253, 0xdddd\n"
        "v_mov_b32 v28, 0\n"
        "v_mov_b32 v29, 0\n"
        "v_mov_b32 v30, 0\n"
        "v_mov_b32 v31, 0\n"
        "s_and_b32 s90, %7, 3\n"
        "s_lshl_b32 s91, s90, 2\n"
        "v_smfmac_f32_16x16x64_bf16 a[252:255], v[28:31], a[0:7], v253\n"
        "s_lshr_b32 s52, 0x64206420, s91\n"
        "s_and_b32 s52, s52, 0xffff\n"
        "v_smfmac_f32_16x16x64_bf16 a[252:255], v[28:31], a[8:15], v253\n"
        "v_lshrrev_b32 v228, 2, v0\n"
        "s_lshr_b32 s91, s52, 4\n"
        "v_smfmac_f32_16x16x64_bf16 a[252:255], v[28:31], a[16:23], v253\n"
        "v_mul_u32_u24 v228, 320, v228\n"
        "v_and_b32 v229, 3, v0\n"
        "v_smfmac_f32_16x16x64_bf16 a[252:255], v[28:31], a[24:31], v253\n"
        "s_lshl_b32 s91, s91, 16\n"
        "v_lshl_add_u32 v228, v229, 3, v228\n"
        "v_smfmac_f32_16x16x64_bf16 a[252:255], v[28:31], a[32:39], v253\n"
        "s_or_b32 s52, s52, s91\n"
        "v_bfe_u32 v229, v0, 4, 1\n"
        "v_smfmac_f32_16x16x64_bf16 a[252:255], v[28:31], a[40:47], v253\n"
        "s_or_b32 s52, s52, 0xf1110000\n"
        "v_xor_b32 v229, s78, v229\n"
        "v_smfmac_f32_16x16x64_bf16 a[252:255], v[28:31], a[48:55], v253\n"
        "s_mul_i32 s54, s90, 327680\n"
        "s_lshl_b32 s89, s90, 8\n"
        "s_mul_i32 s91, s64, 5120\n"
        "v_smfmac_f32_16x16x64_bf16 a[252:255], v[28:31], a[56:63], v253\n"
        "v_lshlrev_b32 v229, 5, v229\n"
        "s_add_u32 s90, s91, 61440\n"
        "v_smfmac_f32_16x16x64_bf16 a[252:255], v[28:31], a[64:71], v253\n"
        "s_mul_i32 s80, s78, 128\n"
        "v_add_u32 v230, v228, v229\n"
        "v_smfmac_f32_16x16x64_bf16 a[252:255], v[28:31], a[72:79], v253\n"
        "s_add_u32 s80, s80, 0\n"
        "v_add_u32 v232, s80, v230\n"
        "s_mul_i32 s80, s78, 192\n"
        "v_smfmac_f32_16x16x64_bf16 a[252:255], v[28:31], a[80:87], v253\n"
        "v_sub_u32 v228, v228, v229\n"
        "s_add_u32 s80, s80, 0\n"
        "v_smfmac_f32_16x16x64_bf16 a[252:255], v[28:31], a[88:95], v253\n"
        "v_add_u32 v233, s80, v228\n"
        "s_mul_i32 s80, s78, -192\n"
        "v_smfmac_f32_16x16x64_bf16 a[252:255], v[28:31], a[96:103], v253\n"
        "s_add_u32 s80, s80, 0\n"
        "v_add_u32 v234, s80, v230\n"
        "v_smfmac_f32_16x16x64_bf16 a[252:255], v[28:31], a[104:111], v253\n"
        "s_mul_i32 s80, s78, -128\n"
        "s_add_u32 s80, s80, 0\n"
        "v_add_u32 v235, s80, v228\n"
        "v_smfmac_f32_16x16x64_bf16 a[252:255], v[28:31], a[112:119], v253\n"
        "s_lshl_b32 s80, s89, 5\n"
        "s_add_u32 s80, s80, 4096\n"
        "v_smfmac_f32_16x16x64_bf16 a[252:255], v[28:31], a[120:127], v253\n"
        "v_subrev_u32 v228, s80, v250\n"
        "s_lshl_b32 s80, s78, 11\n"
        "v_mfma_f32_16x16x32_bf16 a[252:255], v[28:31], v[52:55], a[252:255]\n"
        "s_and_b32 s81, s64, 2\n"
        "s_lshl_b32 s93, s81, 11\n"
        "s_add_u32 s93, s93, 147456\n"
        "v_add_u32 v241, s93, v250\n"
        "v_add_u32 v249, s80, v241\n"
        "ds_write_b128 v249, v[52:55] offset:0\n"
        "v_cvt_pk_bf16_f32 v56, v60, v61\n"
        "v_cvt_pk_bf16_f32 v57, v62, v63\n"
        "v_cvt_pk_bf16_f32 v58, v64, v65\n"
        "v_cvt_pk_bf16_f32 v59, v66, v67\n"
        "v_cvt_pk_bf16_f32 v120, v192, v193\n"
        "v_cvt_pk_bf16_f32 v121, v194, v195\n"
        "v_mfma_f32_16x16x32_bf16 a[252:255], v[28:31], v[56:59], a[252:255]\n"
        "v_cvt_pk_bf16_f32 v122, v196, v197\n"
        "v_cvt_pk_bf16_f32 v123, v198, v199\n"
        "buffer_store_dwordx4 v[120:123], v250, s[56:59], s75 offen offset:1024 sc1\n"
        "ds_write_b128 v249, v[56:59] offset:1024\n"
        "v_add_u32 v240, s76, v228\n"
        "v_subrev_u32 v240, s80, v240\n"
        "s_lshl_b32 s81, s81, 2\n"
        "s_lshl_b32 s80, s85, 10\n"
        "v_accvgpr_read_b32 v1, a252\n"
        "v_add_f32 v3, v3, v1\n"
        "v_add_u32 v236, s72, v228\n"
        "v_add_u32 v237, s73, v228\n"
        "v_add_u32 v238, s74, v228\n"
        "v_add_u32 v239, s75, v228\n"
        "v_cmp_nlt_f32 vcc, v3, v255\n"
        "s_add_u32 s80, s80, s81\n"
        "v_and_b32 v228, 1, v0\n"
        "s_or_b32 vcc_lo, vcc_lo, s77\n"
        "v_lshl_add_u32 v243, v228, 2, s80\n"
        "s_cmp_lg_u64 vcc, 0\n"
        "buffer_store_dwordx4 v[52:55], v250, s[56:59], s76 offen offset:0 sc1\n"
        "s_cselect_b32 s80, 12, 0\n"
        "v_mov_b32 v242, s95\n"
        "s_mov_b32 s92, 40960\n"
        "s_mov_b32 s93, 5\n"
        "s_or_b32 s77, s77, s80\n"
        "buffer_store_dwordx4 v[56:59], v250, s[56:59], s76 offen offset:1024 sc1\n"
        "s_branch .Lk_EPIBX\n"
        ".Lk_EPIB1:\n"
        "v_accvgpr_read_b32 v24, a160\n"
        "v_accvgpr_read_b32 v2, a161\n"
        "v_mfma_f32_16x16x32_bf16 a[128:131], v[100:103], v[84:87], a[128:131]\n"
        "v_accvgpr_read_b32 v25, a164\n"
        "v_accvgpr_read_b32 v3, a165\n"
        "v_mfma_f32_16x16x32_bf16 a[132:135], v[116:119], v[84:87], a[132:135]\n"
        "v_add_f32 v24, v24, v2\n"
        "v_add_f32 v25, v25, v3\n"
        "v_mfma_f32_16x16x32_bf16 a[136:139], v[132:135], v[84:87], a[136:139]\n"
        "v_accvgpr_read_b32 v26, a168\n"
        "v_accvgpr_read_b32 v2, a169\n"
        "v_mfma_f32_16x16x32_bf16 a[140:143], v[148:151], v[84:87], a[140:143]\n"
        "v_accvgpr_read_b32 v27, a172\n"
        "v_accvgpr_read_b32 v3, a173\n"
        "v_mfma_f32_16x16x32_bf16 a[144:147], v[164:167], v[84:87], a[144:147]\n"
        "v_add_f32 v26, v26, v2\n"
        "v_add_f32 v27, v27, v3\n"
        "v_mfma_f32_16x16x32_bf16 a[148:151], v[180:183], v[84:87], a[148:151]\n"
        "v_max3_f32 v3, v24, v25, v26\n"
        "v_accvgpr_read_b32 v168, a0\n"
        "v_mfma_f32_16x16x32_bf16 a[152:155], v[196:199], v[84:87], a[152:155]\n"
        "v_accvgpr_read_b32 v169, a1\n"
        "v_accvgpr_read_b32 v170, a2\n"
        "v_mfma_f32_16x16x32_bf16 a[156:159], v[212:215], v[84:87], a[156:159]\n"
        "v_accvgpr_read_b32 v171, a3\n"
        "v_accvgpr_read_b32 v172, a4\n"
        "v_mfma_f32_16x16x32_bf16 a[176:179], v[4:7], v[84:87], a[176:179]\n"
        "v_rcp_f32 v84, v24\n"
        "v_rcp_f32 v85, v25\n"
        "v_accvgpr_read_b32 v173, a5\n"
        "v_accvgpr_read_b32 v174, a6\n"
        "v_accvgpr_read_b32 v175, a7\n"
        "v_accvgpr_read_b32 v176, a32\n"
        "v_accvgpr_read_b32 v177, a33\n"
        "v_accvgpr_read_b32 v178, a34\n"
        "v_accvgpr_read_b32 v179, a35\n"
        "v_accvgpr_read_b32 v180, a36\n"
        "v_accvgpr_read_b32 v181, a37\n"
        "v_accvgpr_read_b32 v182, a38\n"
        "v_accvgpr_read_b32 v183, a39\n"
        "v_mov_b32 v24, v85\n"
        "v_pk_mul_f32 v[168:169], v[168:169], v[84:85] op_sel_hi:[1,0]\n"
        "v_pk_mul_f32 v[170:171], v[170:171], v[84:85] op_sel_hi:[1,0]\n"
        "v_pk_mul_f32 v[172:173], v[172:173], v[84:85] op_sel_hi:[1,0]\n"
        "v_pk_mul_f32 v[174:175], v[174:175], v[84:85] op_sel_hi:[1,0]\n"
        "v_pk_mul_f32 v[176:177], v[176:177], v[24:25] op_sel_hi:[1,0]\n"
        "v_pk_mul_f32 v[178:179], v[178:179], v[24:25] op_sel_hi:[1,0]\n"
        "v_pk_mul_f32 v[180:181], v[180:181], v[24:25] op_sel_hi:[1,0]\n"
        "v_pk_mul_f32 v[182:183], v[182:183], v[24:25] op_sel_hi:[1,0]\n"
        "v_cvt_pk_bf16_f32 v88, v168, v169\n"
        "v_cvt_pk_bf16_f32 v89, v170, v171\n"
        "v_cvt_pk_bf16_f32 v90, v172, v173\n"
        "v_cvt_pk_bf16_f32 v91, v174, v175\n"
        "buffer_store_dwordx4 v[88:91], v250, s[56:59], s72 offen offset:0 sc1\n"
        "s_waitcnt lgkmcnt(6)\n"
        "v_cvt_pk_bf16_f32 v92, v176, v177\n"
        "v_cvt_pk_bf16_f32 v93, v178, v179\n"
        "s_waitcnt lgkmcnt(5)\n"
        "v_cvt_pk_bf16_f32 v94, v180, v181\n"
        "v_cvt_pk_bf16_f32 v95, v182, v183\n"
        "v_rcp_f32 v86, v26\n"
        "v_rcp_f32 v87, v27\n"
        "v_accvgpr_read_b32 v184, a64\n"
        "v_accvgpr_read_b32 v185, a65\n"
        "v_accvgpr_read_b32 v186, a66\n"
        "v_accvgpr_read_b32 v187, a67\n"
        "v_accvgpr_read_b32 v188, a68\n"
        "v_accvgpr_read_b32 v189, a69\n"
        "v_accvgpr_read_b32 v190, a70\n"
        "v_accvgpr_read_b32 v191, a71\n"
        "buffer_store_dwordx4 v[92:95], v250, s[56:59], s73 offen offset:0 sc1\n"
        "v_accvgpr_read_b32 v192, a96\n"
        "v_accvgpr_read_b32 v193, a97\n"
        "v_accvgpr_read_b32 v194, a98\n"
        "v_accvgpr_read_b32 v195, a99\n"
        "v_accvgpr_read_b32 v196, a100\n"
        "v_accvgpr_read_b32 v197, a101\n"
        "v_accvgpr_read_b32 v198, a102\n"
        "v_accvgpr_read_b32 v199, a103\n"
        "v_mov_b32 v26, v87\n"
        "v_pk_mul_f32 v[184:185], v[184:185], v[86:87] op_sel_hi:[1,0]\n"
        "v_pk_mul_f32 v[186:187], v[186:187], v[86:87] op_sel_hi:[1,0]\n"
        "v_pk_mul_f32 v[188:189], v[188:189], v[86:87] op_sel_hi:[1,0]\n"
        "v_pk_mul_f32 v[190:191], v[190:191], v[86:87] op_sel_hi:[1,0]\n"
        "v_pk_mul_f32 v[192:193], v[192:193], v[26:27] op_sel_hi:[1,0]\n"
        "v_pk_mul_f32 v[194:195], v[194:195], v[26:27] op_sel_hi:[1,0]\n"
        "v_pk_mul_f32 v[196:197], v[196:197], v[26:27] op_sel_hi:[1,0]\n"
        "v_pk_mul_f32 v[198:199], v[198:199], v[26:27] op_sel_hi:[1,0]\n"
        "v_cvt_pk_bf16_f32 v96, v184, v185\n"
        "v_cvt_pk_bf16_f32 v97, v186, v187\n"
        "v_cvt_pk_bf16_f32 v98, v188, v189\n"
        "v_cvt_pk_bf16_f32 v99, v190, v191\n"
        "buffer_store_dwordx4 v[96:99], v250, s[56:59], s74 offen offset:0 sc1\n"
        "s_and_b32 s80, s64, 2\n"
        "s_lshl_b32 s80, s80, 14\n"
        "s_add_u32 s80, s80, 90112\n"
        "v_add_u32 v251, s80, v250\n"
        "v_cvt_pk_bf16_f32 v100, v192, v193\n"
        "v_cvt_pk_bf16_f32 v101, v194, v195\n"
        "v_cvt_pk_bf16_f32 v102, v196, v197\n"
        "v_cvt_pk_bf16_f32 v103, v198, v199\n"
        "ds_write_b128 v251, a[128:131] offset:0\n"
        "v_xor_b32 v252, 8192, v251\n"
        "v_accvgpr_read_b32 v200, a8\n"
        "v_accvgpr_read_b32 v201, a9\n"
        "v_accvgpr_read_b32 v202, a10\n"
        "v_accvgpr_read_b32 v203, a11\n"
        "v_accvgpr_read_b32 v204, a12\n"
        "v_accvgpr_read_b32 v205, a13\n"
        "buffer_store_dwordx4 v[100:103], v250, s[56:59], s75 offen offset:0 sc1\n"
        "ds_write_b128 v251, a[132:135] offset:1024\n"
        "v_accvgpr_read_b32 v206, a14\n"
        "v_accvgpr_read_b32 v207, a15\n"
        "v_accvgpr_read_b32 v208, a16\n"
        "v_accvgpr_read_b32 v209, a17\n"
        "v_accvgpr_read_b32 v210, a18\n"
        "v_accvgpr_read_b32 v211, a19\n"
        "v_accvgpr_read_b32 v212, a20\n"
        "v_accvgpr_read_b32 v213, a21\n"
        "ds_write_b128 v251, a[136:139] offset:2048\n"
        "v_accvgpr_read_b32 v214, a22\n"
        "v_accvgpr_read_b32 v215, a23\n"
        "v_pk_mul_f32 v[200:201], v[200:201], v[84:85] op_sel_hi:[1,0]\n"
        "v_pk_mul_f32 v[202:203], v[202:203], v[84:85] op_sel_hi:[1,0]\n"
        "v_pk_mul_f32 v[204:205], v[204:205], v[84:85] op_sel_hi:[1,0]\n"
        "v_pk_mul_f32 v[206:207], v[206:207], v[84:85] op_sel_hi:[1,0]\n"
        "v_pk_mul_f32 v[208:209], v[208:209], v[84:85] op_sel_hi:[1,0]\n"
        "v_pk_mul_f32 v[210:211], v[210:211], v[84:85] op_sel_hi:[1,0]\n"
        "v_pk_mul_f32 v[212:213], v[212:213], v[84:85] op_sel_hi:[1,0]\n"
        "v_pk_mul_f32 v[214:215], v[214:215], v[84:85] op_sel_hi:[1,0]\n"
        "ds_write_b128 v251, a[140:143] offset:3072\n"
        "s_waitcnt lgkmcnt(8)\n"
        "v_cvt_pk_bf16_f32 v108, v200, v201\n"
        "v_cvt_pk_bf16_f32 v109, v202, v203\n"
        "s_waitcnt lgkmcnt(7)\n"
        "v_cvt_pk_bf16_f32 v110, v204, v205\n"
        "v_cvt_pk_bf16_f32 v111, v206, v207\n"
        "buffer_store_dwordx4 v[108:111], v250, s[56:59], s72 offen offset:1024 sc1\n"
        "v_cvt_pk_bf16_f32 v128, v208, v209\n"
        "v_cvt_pk_bf16_f32 v129, v210, v211\n"
        "v_cvt_pk_bf16_f32 v130, v212, v213\n"
        "ds_write_b128 v251, a[176:179] offset:4096\n"
        "v_cvt_pk_bf16_f32 v131, v214, v215\n"
        "v_accvgpr_read_b32 v52, a144\n"
        "v_accvgpr_read_b32 v53, a145\n"
        "v_accvgpr_read_b32 v54, a146\n"
        "v_accvgpr_read_b32 v55, a147\n"
        "v_accvgpr_read_b32 v56, a148\n"
        "v_accvgpr_read_b32 v57, a149\n"
        "v_accvgpr_read_b32 v58, a150\n"
        "v_accvgpr_read_b32 v59, a151\n"
        "v_accvgpr_read_b32 v60, a152\n"
        "v_accvgpr_read_b32 v61, a153\n"
        "buffer_store_dwordx4 v[128:131], v250, s[56:59], s72 offen offset:2048 sc1\n"
        "v_accvgpr_read_b32 v62, a154\n"
        "v_accvgpr_read_b32 v63, a155\n"
        "v_accvgpr_read_b32 v64, a156\n"
        "v_accvgpr_read_b32 v65, a157\n"
        "v_accvgpr_read_b32 v66, a158\n"
        "v_accvgpr_read_b32 v67, a159\n"
        "v_accvgpr_read_b32 v168, a24\n"
        "v_accvgpr_read_b32 v169, a25\n"
        "v_accvgpr_read_b32 v170, a26\n"
        "v_accvgpr_read_b32 v171, a27\n"
        "v_accvgpr_read_b32 v172, a28\n"
        "v_accvgpr_read_b32 v173, a29\n"
        "v_accvgpr_read_b32 v174, a30\n"
        "v_accvgpr_read_b32 v175, a31\n"
        "s_waitcnt vmcnt(10) lgkmcnt(0)\n"
        "s_barrier\n"
        "ds_read_b32 v1, v252 offset:4096\n"
        "v_accvgpr_read_b32 v176, a40\n"
        "ds_read_b128 v[8:11], v252 offset:0\n"
        "v_accvgpr_read_b32 v177, a41\n"
        "v_accvgpr_read_b32 v178, a42\n"
        "ds_read_b128 v[12:15], v252 offset:1024\n"
        "v_accvgpr_read_b32 v179, a43\n"
        "v_accvgpr_read_b32 v180, a44\n"
        "ds_read_b128 v[16:19], v252 offset:2048\n"
        "v_accvgpr_read_b32 v181, a45\n"
        "v_accvgpr_read_b32 v182, a46\n"
        "ds_read_b128 v[20:23], v252 offset:3072\n"
        "v_accvgpr_read_b32 v183, a47\n"
        "v_pk_mul_f32 v[168:169], v[168:169], v[84:85] op_sel_hi:[1,0]\n"
        "v_pk_mul_f32 v[170:171], v[170:171], v[84:85] op_sel_hi:[1,0]\n"
        "v_pk_mul_f32 v[172:173], v[172:173], v[84:85] op_sel_hi:[1,0]\n"
        "v_pk_mul_f32 v[174:175], v[174:175], v[84:85] op_sel_hi:[1,0]\n"
        "v_pk_mul_f32 v[176:177], v[176:177], v[24:25] op_sel_hi:[1,0]\n"
        "v_pk_mul_f32 v[178:179], v[178:179], v[24:25] op_sel_hi:[1,0]\n"
        "v_pk_mul_f32 v[180:181], v[180:181], v[24:25] op_sel_hi:[1,0]\n"
        "v_pk_mul_f32 v[182:183], v[182:183], v[24:25] op_sel_hi:[1,0]\n"
        "v_cvt_pk_bf16_f32 v148, v168, v169\n"
        "v_cvt_pk_bf16_f32 v149, v170, v171\n"
        "v_cvt_pk_bf16_f32 v150, v172, v173\n"
        "v_cvt_pk_bf16_f32 v151, v174, v175\n"
        "buffer_store_dwordx4 v[148:151], v250, s[56:59], s72 offen offset:3072 sc1\n"
        "v_accvgpr_read_b32 v2, a176\n"
        "v_max_f32 v3, v3, v27\n"
        "v_cvt_pk_bf16_f32 v112, v176, v177\n"
        "s_waitcnt lgkmcnt(4)\n"
        "v_add_f32 v2, v2, v1\n"
        "v_max_f32 v3, v3, v2\n"
        "v_rcp_f32 v2, v2\n"
        "v_cvt_pk_bf16_f32 v113, v178, v179\n"
        "v_cvt_pk_bf16_f32 v114, v180, v181\n"
        "v_cvt_pk_bf16_f32 v115, v182, v183\n"
        "v_accvgpr_read_b32 v184, a72\n"
        "v_accvgpr_read_b32 v185, a73\n"
        "v_accvgpr_read_b32 v186, a74\n"
        "v_accvgpr_read_b32 v187, a75\n"
        "v_accvgpr_read_b32 v188, a76\n"
        "s_waitcnt lgkmcnt(0)\n"
        "v_pk_add_f32 v[52:53], v[52:53], v[8:9]\n"
        "v_pk_add_f32 v[54:55], v[54:55], v[10:11]\n"
        "v_pk_add_f32 v[56:57], v[56:57], v[12:13]\n"
        "v_pk_add_f32 v[58:59], v[58:59], v[14:15]\n"
        "v_pk_add_f32 v[60:61], v[60:61], v[16:17]\n"
        "v_pk_add_f32 v[62:63], v[62:63], v[18:19]\n"
        "v_pk_add_f32 v[64:65], v[64:65], v[20:21]\n"
        "v_pk_add_f32 v[66:67], v[66:67], v[22:23]\n"
        "v_pk_mul_f32 v[52:53], v[52:53], v[2:3] op_sel_hi:[1,0]\n"
        "v_pk_mul_f32 v[54:55], v[54:55], v[2:3] op_sel_hi:[1,0]\n"
        "v_pk_mul_f32 v[56:57], v[56:57], v[2:3] op_sel_hi:[1,0]\n"
        "v_pk_mul_f32 v[58:59], v[58:59], v[2:3] op_sel_hi:[1,0]\n"
        "v_pk_mul_f32 v[60:61], v[60:61], v[2:3] op_sel_hi:[1,0]\n"
        "v_pk_mul_f32 v[62:63], v[62:63], v[2:3] op_sel_hi:[1,0]\n"
        "v_pk_mul_f32 v[64:65], v[64:65], v[2:3] op_sel_hi:[1,0]\n"
        "v_pk_mul_f32 v[66:67], v[66:67], v[2:3] op_sel_hi:[1,0]\n"
        "buffer_store_dwordx4 v[112:115], v250, s[56:59], s73 offen offset:1024 sc1\n"
        "v_cvt_pk_bf16_f32 v52, v52, v53\n"
        "v_cvt_pk_bf16_f32 v53, v54, v55\n"
        "v_accvgpr_read_b32 v189, a77\n"
        "v_accvgpr_read_b32 v190, a78\n"
        "v_accvgpr_read_b32 v191, a79\n"
        "v_accvgpr_read_b32 v192, a104\n"
        "v_accvgpr_read_b32 v193, a105\n"
        "v_accvgpr_read_b32 v194, a106\n"
        "v_accvgpr_read_b32 v195, a107\n"
        "v_accvgpr_read_b32 v196, a108\n"
        "v_accvgpr_read_b32 v197, a109\n"
        "v_accvgpr_read_b32 v198, a110\n"
        "v_accvgpr_read_b32 v199, a111\n"
        "v_pk_mul_f32 v[184:185], v[184:185], v[86:87] op_sel_hi:[1,0]\n"
        "v_pk_mul_f32 v[186:187], v[186:187], v[86:87] op_sel_hi:[1,0]\n"
        "v_pk_mul_f32 v[188:189], v[188:189], v[86:87] op_sel_hi:[1,0]\n"
        "v_pk_mul_f32 v[190:191], v[190:191], v[86:87] op_sel_hi:[1,0]\n"
        "v_pk_mul_f32 v[192:193], v[192:193], v[26:27] op_sel_hi:[1,0]\n"
        "v_pk_mul_f32 v[194:195], v[194:195], v[26:27] op_sel_hi:[1,0]\n"
        "v_pk_mul_f32 v[196:197], v[196:197], v[26:27] op_sel_hi:[1,0]\n"
        "v_pk_mul_f32 v[198:199], v[198:199], v[26:27] op_sel_hi:[1,0]\n"
        "v_cvt_pk_bf16_f32 v116, v184, v185\n"
        "v_cvt_pk_bf16_f32 v117, v186, v187\n"
        "v_cvt_pk_bf16_f32 v118, v188, v189\n"
        "v_cvt_pk_bf16_f32 v119, v190, v191\n"
        "buffer_store_dwordx4 v[116:119], v250, s[56:59], s74 offen offset:1024 sc1\n"
        "v_cvt_pk_bf16_f32 v54, v56, v57\n"
        "v_cvt_pk_bf16_f32 v55, v58, v59\n"
        "v_accvgpr_write_b32 a252, 0\n"
        "v_accvgpr_write_b32 a253, 0\n"
        "v_accvgpr_write_b32 a254, 0\n"
        "v_accvgpr_write_b32 a255, 0\n"
        "v_mov_b32 v253, 0xdddd\n"
        "v_mov_b32 v28, 0\n"
        "v_mov_b32 v29, 0\n"
        "v_mov_b32 v30, 0\n"
        "v_mov_b32 v31, 0\n"
        "s_and_b32 s90, %7, 3\n"
        "s_lshl_b32 s91, s90, 2\n"
        "v_smfmac_f32_16x16x64_bf16 a[252:255], v[28:31], a[0:7], v253\n"
        "s_lshr_b32 s52, 0x64206420, s91\n"
        "s_and_b32 s52, s52, 0xffff\n"
        "v_smfmac_f32_16x16x64_bf16 a[252:255], v[28:31], a[8:15], v253\n"
        "v_lshrrev_b32 v228, 2, v0\n"
        "s_lshr_b32 s91, s52, 4\n"
        "v_smfmac_f32_16x16x64_bf16 a[252:255], v[28:31], a[16:23], v253\n"
        "v_mul_u32_u24 v228, 320, v228\n"
        "v_and_b32 v229, 3, v0\n"
        "v_smfmac_f32_16x16x64_bf16 a[252:255], v[28:31], a[24:31], v253\n"
        "s_lshl_b32 s91, s91, 16\n"
        "v_lshl_add_u32 v228, v229, 3, v228\n"
        "v_smfmac_f32_16x16x64_bf16 a[252:255], v[28:31], a[32:39], v253\n"
        "s_or_b32 s52, s52, s91\n"
        "v_bfe_u32 v229, v0, 4, 1\n"
        "v_smfmac_f32_16x16x64_bf16 a[252:255], v[28:31], a[40:47], v253\n"
        "s_or_b32 s52, s52, 0xf1110000\n"
        "v_xor_b32 v229, s78, v229\n"
        "v_smfmac_f32_16x16x64_bf16 a[252:255], v[28:31], a[48:55], v253\n"
        "s_mul_i32 s54, s90, 327680\n"
        "s_lshl_b32 s89, s90, 8\n"
        "s_mul_i32 s91, s64, 5120\n"
        "v_smfmac_f32_16x16x64_bf16 a[252:255], v[28:31], a[56:63], v253\n"
        "v_lshlrev_b32 v229, 5, v229\n"
        "s_add_u32 s90, s91, 61440\n"
        "v_smfmac_f32_16x16x64_bf16 a[252:255], v[28:31], a[64:71], v253\n"
        "s_mul_i32 s80, s78, 128\n"
        "v_add_u32 v230, v228, v229\n"
        "v_smfmac_f32_16x16x64_bf16 a[252:255], v[28:31], a[72:79], v253\n"
        "s_add_u32 s80, s80, 0\n"
        "v_add_u32 v232, s80, v230\n"
        "s_mul_i32 s80, s78, 192\n"
        "v_smfmac_f32_16x16x64_bf16 a[252:255], v[28:31], a[80:87], v253\n"
        "v_sub_u32 v228, v228, v229\n"
        "s_add_u32 s80, s80, 0\n"
        "v_smfmac_f32_16x16x64_bf16 a[252:255], v[28:31], a[88:95], v253\n"
        "v_add_u32 v233, s80, v228\n"
        "s_mul_i32 s80, s78, -192\n"
        "v_smfmac_f32_16x16x64_bf16 a[252:255], v[28:31], a[96:103], v253\n"
        "s_add_u32 s80, s80, 0\n"
        "v_add_u32 v234, s80, v230\n"
        "v_smfmac_f32_16x16x64_bf16 a[252:255], v[28:31], a[104:111], v253\n"
        "s_mul_i32 s80, s78, -128\n"
        "s_add_u32 s80, s80, 0\n"
        "v_add_u32 v235, s80, v228\n"
        "v_smfmac_f32_16x16x64_bf16 a[252:255], v[28:31], a[112:119], v253\n"
        "s_lshl_b32 s80, s89, 5\n"
        "s_add_u32 s80, s80, 4096\n"
        "v_smfmac_f32_16x16x64_bf16 a[252:255], v[28:31], a[120:127], v253\n"
        "v_subrev_u32 v228, s80, v250\n"
        "s_lshl_b32 s80, s78, 11\n"
        "v_mfma_f32_16x16x32_bf16 a[252:255], v[28:31], v[52:55], a[252:255]\n"
        "s_and_b32 s81, s64, 2\n"
        "s_lshl_b32 s93, s81, 11\n"
        "s_add_u32 s93, s93, 147456\n"
        "v_add_u32 v241, s93, v250\n"
        "v_add_u32 v249, s80, v241\n"
        "ds_write_b128 v249, v[52:55] offset:0\n"
        "v_cvt_pk_bf16_f32 v56, v60, v61\n"
        "v_cvt_pk_bf16_f32 v57, v62, v63\n"
        "v_cvt_pk_bf16_f32 v58, v64, v65\n"
        "v_cvt_pk_bf16_f32 v59, v66, v67\n"
        "v_cvt_pk_bf16_f32 v120, v192, v193\n"
        "v_cvt_pk_bf16_f32 v121, v194, v195\n"
        "v_mfma_f32_16x16x32_bf16 a[252:255], v[28:31], v[56:59], a[252:255]\n"
        "v_cvt_pk_bf16_f32 v122, v196, v197\n"
        "v_cvt_pk_bf16_f32 v123, v198, v199\n"
        "buffer_store_dwordx4 v[120:123], v250, s[56:59], s75 offen offset:1024 sc1\n"
        "ds_write_b128 v249, v[56:59] offset:1024\n"
        "v_add_u32 v240, s76, v228\n"
        "v_subrev_u32 v240, s80, v240\n"
        "s_lshl_b32 s81, s81, 2\n"
        "s_lshl_b32 s80, s85, 10\n"
        "v_accvgpr_read_b32 v1, a252\n"
        "v_add_f32 v3, v3, v1\n"
        "v_add_u32 v236, s72, v228\n"
        "v_add_u32 v237, s73, v228\n"
        "v_add_u32 v238, s74, v228\n"
        "v_add_u32 v239, s75, v228\n"
        "v_cmp_nlt_f32 vcc, v3, v255\n"
        "s_add_u32 s80, s80, s81\n"
        "v_and_b32 v228, 1, v0\n"
        "s_or_b32 vcc_lo, vcc_lo, s77\n"
        "v_lshl_add_u32 v243, v228, 2, s80\n"
        "s_cmp_lg_u64 vcc, 0\n"
        "buffer_store_dwordx4 v[52:55], v250, s[56:59], s76 offen offset:0 sc1\n"
        "s_cselect_b32 s80, 12, 0\n"
        "v_mov_b32 v242, s95\n"
        "s_mov_b32 s92, 40960\n"
        "s_mov_b32 s93, 5\n"
        "s_or_b32 s77, s77, s80\n"
        "buffer_store_dwordx4 v[56:59], v250, s[56:59], s76 offen offset:1024 sc1\n"
        ".Lk_EPIBX:\n"
        "s_cmp_lg_u32 s77, 0\n"
        "s_cbranch_scc1 .Lk_EXIT\n"
        ".Lk_P2ENTRY:\n"
        "s_waitcnt vmcnt(63) lgkmcnt(0)\n"
        "s_barrier\n"
        "ds_read_b64_tr_b16 a[192:193], v232 offset:0\n"
        "ds_read_b64_tr_b16 a[194:195], v232 offset:5120\n"
        "ds_read_b64_tr_b16 a[196:197], v233 offset:32\n"
        "ds_read_b64_tr_b16 a[198:199], v233 offset:5152\n"
        "ds_read_b128 v[104:107], v241\n"
        "ds_read_b64_tr_b16 a[200:201], v232 offset:64\n"
        "ds_read_b64_tr_b16 a[202:203], v232 offset:5184\n"
        "ds_read_b64_tr_b16 a[204:205], v233 offset:96\n"
        "ds_read_b64_tr_b16 a[206:207], v233 offset:5216\n"
        "ds_read_b64_tr_b16 a[208:209], v232 offset:128\n"
        "ds_read_b64_tr_b16 a[210:211], v232 offset:5248\n"
        "ds_read_b64_tr_b16 a[212:213], v235 offset:160\n"
        "ds_read_b64_tr_b16 a[214:215], v235 offset:5280\n"
        "ds_read_b64_tr_b16 a[216:217], v234 offset:192\n"
        "ds_read_b64_tr_b16 a[218:219], v234 offset:5312\n"
        "ds_read_b64_tr_b16 a[220:221], v235 offset:224\n"
        "ds_read_b64_tr_b16 a[222:223], v235 offset:5344\n"
        "ds_read_b64_tr_b16 a[224:225], v234 offset:256\n"
        "ds_read_b64_tr_b16 a[226:227], v234 offset:5376\n"
        "ds_read_b64_tr_b16 a[228:229], v235 offset:288\n"
        "ds_read_b64_tr_b16 a[230:231], v235 offset:5408\n"
        "ds_read_b128 v[124:127], v241 offset:1024\n"
        "ds_read_b128 v[144:147], v241 offset:2048\n"
        "ds_read_b128 v[164:167], v241 offset:3072\n"
        "s_waitcnt lgkmcnt(15)\n"
        "v_mfma_f32_16x16x32_bf16 a[0:3], a[192:195], v[88:91], 0\n"
        "v_accvgpr_read_b32 v132, a48\n"
        "v_accvgpr_read_b32 v133, a49\n"
        "v_mfma_f32_16x16x32_bf16 v[4:7], a[192:195], v[92:95], 0\n"
        "v_accvgpr_read_b32 v134, a50\n"
        "v_accvgpr_read_b32 v135, a51\n"
        "v_mfma_f32_16x16x32_bf16 v[44:47], a[192:195], v[96:99], 0\n"
        "v_mul_f32 v132, v85, v132\n"
        "v_mul_f32 v133, v85, v133\n"
        "v_mfma_f32_16x16x32_bf16 v[168:171], a[192:195], v[100:103], 0\n"
        "v_mul_f32 v134, v85, v134\n"
        "v_mul_f32 v135, v85, v135\n"
        "v_mfma_f32_16x16x32_bf16 v[208:211], a[192:195], v[104:107], 0\n"
        "ds_read_b64_tr_b16 a[192:193], v232 offset:10240\n"
        "v_cvt_pk_bf16_f32 v132, v132, v133\n"
        "v_mfma_f32_16x16x32_bf16 a[4:7], a[196:199], v[88:91], 0\n"
        "v_cvt_pk_bf16_f32 v133, v134, v135\n"
        "v_accvgpr_read_b32 v1, a52\n"
        "v_mfma_f32_16x16x32_bf16 v[8:11], a[196:199], v[92:95], 0\n"
        "ds_read_b64_tr_b16 a[194:195], v232 offset:15360\n"
        "v_accvgpr_read_b32 v2, a53\n"
        "v_mfma_f32_16x16x32_bf16 v[48:51], a[196:199], v[96:99], 0\n"
        "v_accvgpr_read_b32 v251, a54\n"
        "v_accvgpr_read_b32 v252, a55\n"
        "v_mfma_f32_16x16x32_bf16 v[172:175], a[196:199], v[100:103], 0\n"
        "v_cmp_ne_u32 vcc, s95, v242\n"
        "v_mul_f32 v1, v85, v1\n"
        "v_mfma_f32_16x16x32_bf16 v[212:215], a[196:199], v[104:107], 0\n"
        "ds_read_b64_tr_b16 a[196:197], v233 offset:10272\n"
        "v_mul_f32 v2, v85, v2\n"
        "v_mfma_f32_16x16x32_bf16 a[8:11], a[200:203], v[88:91], 0\n"
        "s_lshl_b32 s55, s89, 5\n"
        "ds_read_b64_tr_b16 a[198:199], v233 offset:15392\n"
        "v_mul_f32 v251, v85, v251\n"
        "v_mfma_f32_16x16x32_bf16 v[12:15], a[200:203], v[92:95], 0\n"
        "s_add_u32 s53, s54, 81920\n"
        "v_mul_f32 v252, v85, v252\n"
        "v_cvt_pk_bf16_f32 v134, v1, v2\n"
        "v_mfma_f32_16x16x32_bf16 v[52:55], a[200:203], v[96:99], 0\n"
        "s_ashr_i32 s52, s52, 4\n"
        "v_cvt_pk_bf16_f32 v135, v251, v252\n"
        "v_mfma_f32_16x16x32_bf16 v[176:179], a[200:203], v[100:103], 0\n"
        "s_and_b32 s54, s52, 15\n"
        "v_accvgpr_read_b32 v136, a80\n"
        "v_accvgpr_read_b32 v137, a81\n"
        "v_mfma_f32_16x16x32_bf16 v[216:219], a[200:203], v[104:107], 0\n"
        "s_and_b32 s89, s52, 7\n"
        "ds_read_b64_tr_b16 a[200:201], v232 offset:10304\n"
        "v_accvgpr_read_b32 v138, a82\n"
        "v_mfma_f32_16x16x32_bf16 a[12:15], a[204:207], v[88:91], 0\n"
        "s_mul_i32 s54, s54, 163840\n"
        "ds_read_b64_tr_b16 a[202:203], v232 offset:15424\n"
        "v_accvgpr_read_b32 v139, a83\n"
        "v_mfma_f32_16x16x32_bf16 v[16:19], a[204:207], v[92:95], 0\n"
        "s_lshl_b32 s89, s89, 7\n"
        "v_mul_f32 v136, v86, v136\n"
        "v_mul_f32 v137, v86, v137\n"
        "v_mfma_f32_16x16x32_bf16 v[56:59], a[204:207], v[96:99], 0\n"
        "buffer_store_dwordx4 v[132:135], v250, s[56:59], s73 offen offset:2048 sc1\n"
        "v_mfma_f32_16x16x32_bf16 v[180:183], a[204:207], v[100:103], 0\n"
        "v_mul_f32 v138, v86, v138\n"
        "v_mul_f32 v139, v86, v139\n"
        "v_mfma_f32_16x16x32_bf16 v[220:223], a[204:207], v[104:107], 0\n"
        "s_waitcnt lgkmcnt(2)\n"
        "ds_read_b64_tr_b16 a[204:205], v233 offset:10336\n"
        "v_cvt_pk_bf16_f32 v136, v136, v137\n"
        "v_mfma_f32_16x16x32_bf16 a[16:19], a[208:211], v[88:91], 0\n"
        "ds_read_b64_tr_b16 a[206:207], v233 offset:15456\n"
        "v_cvt_pk_bf16_f32 v137, v138, v139\n"
        "v_mfma_f32_16x16x32_bf16 v[20:23], a[208:211], v[92:95], 0\n"
        "v_accvgpr_read_b32 v1, a84\n"
        "v_accvgpr_read_b32 v2, a85\n"
        "v_mfma_f32_16x16x32_bf16 v[60:63], a[208:211], v[96:99], 0\n"
        "v_accvgpr_read_b32 v251, a86\n"
        "v_accvgpr_read_b32 v252, a87\n"
        "v_mfma_f32_16x16x32_bf16 v[184:187], a[208:211], v[100:103], 0\n"
        "v_mul_f32 v1, v86, v1\n"
        "v_mul_f32 v2, v86, v2\n"
        "v_mfma_f32_16x16x32_bf16 v[224:227], a[208:211], v[104:107], 0\n"
        "buffer_load_dwordx4 v[104:107], v240, s[56:59], s55 offen offset:0 sc1\n"
        "v_mfma_f32_16x16x32_bf16 a[20:23], a[212:215], v[88:91], 0\n"
        "ds_read_b64_tr_b16 a[208:209], v232 offset:10368\n"
        "v_mul_f32 v251, v86, v251\n"
        "v_mfma_f32_16x16x32_bf16 v[24:27], a[212:215], v[92:95], 0\n"
        "ds_read_b64_tr_b16 a[210:211], v232 offset:15488\n"
        "v_mul_f32 v252, v86, v252\n"
        "v_mfma_f32_16x16x32_bf16 v[64:67], a[212:215], v[96:99], 0\n"
        "v_cvt_pk_bf16_f32 v138, v1, v2\n"
        "v_cvt_pk_bf16_f32 v139, v251, v252\n"
        "v_mfma_f32_16x16x32_bf16 v[188:191], a[212:215], v[100:103], 0\n"
        "ds_read_b64_tr_b16 a[212:213], v235 offset:10400\n"
        "v_accvgpr_read_b32 v140, a112\n"
        "v_mfma_f32_16x16x32_bf16 a[24:27], a[216:219], v[88:91], 0\n"
        "ds_read_b64_tr_b16 a[214:215], v235 offset:15520\n"
        "v_accvgpr_read_b32 v141, a113\n"
        "v_mfma_f32_16x16x32_bf16 v[28:31], a[216:219], v[92:95], 0\n"
        "buffer_store_dwordx4 v[136:139], v250, s[56:59], s74 offen offset:2048 sc1\n"
        "v_mfma_f32_16x16x32_bf16 v[68:71], a[216:219], v[96:99], 0\n"
        "v_accvgpr_read_b32 v142, a114\n"
        "v_accvgpr_read_b32 v143, a115\n"
        "v_mfma_f32_16x16x32_bf16 v[192:195], a[216:219], v[100:103], 0\n"
        "ds_read_b64_tr_b16 a[216:217], v234 offset:10432\n"
        "v_mul_f32 v140, v87, v140\n"
        "v_mfma_f32_16x16x32_bf16 a[28:31], a[220:223], v[88:91], 0\n"
        "ds_read_b64_tr_b16 a[218:219], v234 offset:15552\n"
        "v_mul_f32 v141, v87, v141\n"
        "v_mfma_f32_16x16x32_bf16 v[32:35], a[220:223], v[92:95], 0\n"
        "v_mul_f32 v142, v87, v142\n"
        "v_mul_f32 v143, v87, v143\n"
        "v_mfma_f32_16x16x32_bf16 v[72:75], a[220:223], v[96:99], 0\n"
        "v_cvt_pk_bf16_f32 v140, v140, v141\n"
        "v_cvt_pk_bf16_f32 v141, v142, v143\n"
        "v_mfma_f32_16x16x32_bf16 v[196:199], a[220:223], v[100:103], 0\n"
        "ds_read_b64_tr_b16 a[220:221], v235 offset:10464\n"
        "v_accvgpr_read_b32 v1, a116\n"
        "v_mfma_f32_16x16x32_bf16 a[32:35], a[224:227], v[88:91], 0\n"
        "ds_read_b64_tr_b16 a[222:223], v235 offset:15584\n"
        "v_accvgpr_read_b32 v2, a117\n"
        "v_mfma_f32_16x16x32_bf16 v[36:39], a[224:227], v[92:95], 0\n"
        "v_accvgpr_read_b32 v251, a118\n"
        "v_accvgpr_read_b32 v252, a119\n"
        "v_mfma_f32_16x16x32_bf16 v[76:79], a[224:227], v[96:99], 0\n"
        "v_mul_f32 v1, v87, v1\n"
        "v_mul_f32 v2, v87, v2\n"
        "v_mfma_f32_16x16x32_bf16 v[200:203], a[224:227], v[100:103], 0\n"
        "ds_read_b64_tr_b16 a[224:225], v234 offset:10496\n"
        "v_mul_f32 v251, v87, v251\n"
        "v_mfma_f32_16x16x32_bf16 a[36:39], a[228:231], v[88:91], 0\n"
        "buffer_load_dwordx4 v[88:91], v236, s[56:59], s55 offen offset:0 sc1\n"
        "v_mfma_f32_16x16x32_bf16 v[40:43], a[228:231], v[92:95], 0\n"
        "ds_read_b64_tr_b16 a[226:227], v234 offset:15616\n"
        "v_mul_f32 v252, v87, v252\n"
        "v_mfma_f32_16x16x32_bf16 v[80:83], a[228:231], v[96:99], 0\n"
        "v_cvt_pk_bf16_f32 v142, v1, v2\n"
        "v_cvt_pk_bf16_f32 v143, v251, v252\n"
        "v_mfma_f32_16x16x32_bf16 v[204:207], a[228:231], v[100:103], 0\n"
        "buffer_load_dwordx4 v[92:95], v237, s[56:59], s55 offen offset:0 sc1\n"
        "v_mfma_f32_16x16x32_bf16 a[0:3], a[192:195], v[108:111], a[0:3]\n"
        "ds_read_b64_tr_b16 a[228:229], v235 offset:10528\n"
        "v_accvgpr_read_b32 v152, a56\n"
        "v_mfma_f32_16x16x32_bf16 v[4:7], a[192:195], v[112:115], v[4:7]\n"
        "s_barrier\n"
        "ds_read_b64_tr_b16 a[230:231], v235 offset:15648\n"
        "v_accvgpr_read_b32 v153, a57\n"
        "v_mfma_f32_16x16x32_bf16 v[44:47], a[192:195], v[116:119], v[44:47]\n"
        "buffer_load_dwordx4 v[96:99], v238, s[56:59], s55 offen offset:0 sc1\n"
        "v_mfma_f32_16x16x32_bf16 v[168:171], a[192:195], v[120:123], v[168:171]\n"
        "s_add_u32 m0, s90, 0\n"
        "v_accvgpr_read_b32 v154, a58\n"
        "v_accvgpr_read_b32 v155, a59\n"
        "v_mfma_f32_16x16x32_bf16 v[208:211], a[192:195], v[124:127], v[208:211]\n"
        "ds_read_b64_tr_b16 a[192:193], v232 offset:20480\n"
        "v_mul_f32 v152, v85, v152\n"
        "v_mfma_f32_16x16x32_bf16 a[4:7], a[196:199], v[108:111], a[4:7]\n"
        "buffer_load_dwordx4 v244, s[44:47], s53 offen offset:0 lds\n"
        "v_mfma_f32_16x16x32_bf16 v[8:11], a[196:199], v[112:115], v[8:11]\n"
        "ds_read_b64_tr_b16 a[194:195], v232 offset:25600\n"
        "v_mul_f32 v153, v85, v153\n"
        "v_mfma_f32_16x16x32_bf16 v[48:51], a[196:199], v[116:119], v[48:51]\n"
        "v_mul_f32 v154, v85, v154\n"
        "v_mul_f32 v155, v85, v155\n"
        "v_mfma_f32_16x16x32_bf16 v[172:175], a[196:199], v[120:123], v[172:175]\n"
        "buffer_load_dwordx4 v245, s[44:47], s53 offen offset:1024 lds\n"
        "v_mfma_f32_16x16x32_bf16 v[212:215], a[196:199], v[124:127], v[212:215]\n"
        "s_waitcnt lgkmcnt(2)\n"
        "ds_read_b64_tr_b16 a[196:197], v233 offset:20512\n"
        "v_cvt_pk_bf16_f32 v152, v152, v153\n"
        "v_mfma_f32_16x16x32_bf16 a[8:11], a[200:203], v[108:111], a[8:11]\n"
        "ds_read_b64_tr_b16 a[198:199], v233 offset:25632\n"
        "v_cvt_pk_bf16_f32 v153, v154, v155\n"
        "v_mfma_f32_16x16x32_bf16 v[12:15], a[200:203], v[112:115], v[12:15]\n"
        "buffer_load_dwordx4 v246, s[44:47], s53 offen offset:2048 lds\n"
        "v_mfma_f32_16x16x32_bf16 v[52:55], a[200:203], v[116:119], v[52:55]\n"
        "v_accvgpr_read_b32 v1, a60\n"
        "v_accvgpr_read_b32 v2, a61\n"
        "v_mfma_f32_16x16x32_bf16 v[176:179], a[200:203], v[120:123], v[176:179]\n"
        "v_accvgpr_read_b32 v251, a62\n"
        "v_accvgpr_read_b32 v252, a63\n"
        "v_mfma_f32_16x16x32_bf16 v[216:219], a[200:203], v[124:127], v[216:219]\n"
        "buffer_load_dwordx4 v[100:103], v239, s[56:59], s55 offen offset:0 sc1\n"
        "v_mfma_f32_16x16x32_bf16 a[12:15], a[204:207], v[108:111], a[12:15]\n"
        "ds_read_b64_tr_b16 a[200:201], v232 offset:20544\n"
        "v_mul_f32 v1, v85, v1\n"
        "v_mfma_f32_16x16x32_bf16 v[16:19], a[204:207], v[112:115], v[16:19]\n"
        "ds_read_b64_tr_b16 a[202:203], v232 offset:25664\n"
        "v_mul_f32 v2, v85, v2\n"
        "v_mfma_f32_16x16x32_bf16 v[56:59], a[204:207], v[116:119], v[56:59]\n"
        "buffer_load_dwordx4 v247, s[44:47], s53 offen offset:3072 lds\n"
        "v_mfma_f32_16x16x32_bf16 v[180:183], a[204:207], v[120:123], v[180:183]\n"
        "s_add_u32 m0, s90, 4096\n"
        "v_mul_f32 v251, v85, v251\n"
        "v_mul_f32 v252, v85, v252\n"
        "v_mfma_f32_16x16x32_bf16 v[220:223], a[204:207], v[124:127], v[220:223]\n"
        "ds_read_b64_tr_b16 a[204:205], v233 offset:20576\n"
        "v_cvt_pk_bf16_f32 v154, v1, v2\n"
        "v_mfma_f32_16x16x32_bf16 a[16:19], a[208:211], v[108:111], a[16:19]\n"
        "buffer_load_dwordx4 v248, s[44:47], s53 offen offset:0 lds\n"
        "v_mfma_f32_16x16x32_bf16 v[20:23], a[208:211], v[112:115], v[20:23]\n"
        "s_sub_u32 s90, s90, s92\n"
        "ds_read_b64_tr_b16 a[206:207], v233 offset:25696\n"
        "v_cvt_pk_bf16_f32 v155, v251, v252\n"
        "v_mfma_f32_16x16x32_bf16 v[60:63], a[208:211], v[116:119], v[60:63]\n"
        "v_accvgpr_read_b32 v156, a88\n"
        "v_accvgpr_read_b32 v157, a89\n"
        "v_mfma_f32_16x16x32_bf16 v[184:187], a[208:211], v[120:123], v[184:187]\n"
        "buffer_load_dword v242, v243, s[96:99], s89 offen offset:128 sc1\n"
        "v_mfma_f32_16x16x32_bf16 v[224:227], a[208:211], v[124:127], v[224:227]\n"
        "ds_read_b64_tr_b16 a[208:209], v232 offset:20608\n"
        "v_accvgpr_read_b32 v158, a90\n"
        "v_mfma_f32_16x16x32_bf16 a[20:23], a[212:215], v[108:111], a[20:23]\n"
        "ds_read_b64_tr_b16 a[210:211], v232 offset:25728\n"
        "v_accvgpr_read_b32 v159, a91\n"
        "v_mfma_f32_16x16x32_bf16 v[24:27], a[212:215], v[112:115], v[24:27]\n"
        "buffer_load_dwordx4 v[124:127], v240, s[56:59], s55 offen offset:1024 sc1\n"
        "v_mfma_f32_16x16x32_bf16 v[64:67], a[212:215], v[116:119], v[64:67]\n"
        "v_mul_f32 v156, v86, v156\n"
        "v_mul_f32 v157, v86, v157\n"
        "v_mfma_f32_16x16x32_bf16 v[188:191], a[212:215], v[120:123], v[188:191]\n"
        "ds_read_b64_tr_b16 a[212:213], v235 offset:20640\n"
        "v_mul_f32 v158, v86, v158\n"
        "v_mfma_f32_16x16x32_bf16 a[24:27], a[216:219], v[108:111], a[24:27]\n"
        "ds_read_b64_tr_b16 a[214:215], v235 offset:25760\n"
        "v_mul_f32 v159, v86, v159\n"
        "v_mfma_f32_16x16x32_bf16 v[28:31], a[216:219], v[112:115], v[28:31]\n"
        "buffer_store_dwordx4 v[140:143], v250, s[56:59], s75 offen offset:2048 sc1\n"
        "v_mfma_f32_16x16x32_bf16 v[68:71], a[216:219], v[116:119], v[68:71]\n"
        "v_cvt_pk_bf16_f32 v156, v156, v157\n"
        "v_cvt_pk_bf16_f32 v157, v158, v159\n"
        "v_mfma_f32_16x16x32_bf16 v[192:195], a[216:219], v[120:123], v[192:195]\n"
        "ds_read_b64_tr_b16 a[216:217], v234 offset:20672\n"
        "v_accvgpr_read_b32 v1, a92\n"
        "v_mfma_f32_16x16x32_bf16 a[28:31], a[220:223], v[108:111], a[28:31]\n"
        "ds_read_b64_tr_b16 a[218:219], v234 offset:25792\n"
        "v_accvgpr_read_b32 v2, a93\n"
        "v_mfma_f32_16x16x32_bf16 v[32:35], a[220:223], v[112:115], v[32:35]\n"
        "buffer_store_dwordx4 v[152:155], v250, s[56:59], s73 offen offset:3072 sc1\n"
        "v_mfma_f32_16x16x32_bf16 v[72:75], a[220:223], v[116:119], v[72:75]\n"
        "v_accvgpr_read_b32 v251, a94\n"
        "v_accvgpr_read_b32 v252, a95\n"
        "v_mfma_f32_16x16x32_bf16 v[196:199], a[220:223], v[120:123], v[196:199]\n"
        "ds_read_b64_tr_b16 a[220:221], v235 offset:20704\n"
        "v_mul_f32 v1, v86, v1\n"
        "v_mfma_f32_16x16x32_bf16 a[32:35], a[224:227], v[108:111], a[32:35]\n"
        "ds_read_b64_tr_b16 a[222:223], v235 offset:25824\n"
        "v_mul_f32 v2, v86, v2\n"
        "v_mfma_f32_16x16x32_bf16 v[36:39], a[224:227], v[112:115], v[36:39]\n"
        "v_mul_f32 v251, v86, v251\n"
        "v_mul_f32 v252, v86, v252\n"
        "v_mfma_f32_16x16x32_bf16 v[76:79], a[224:227], v[116:119], v[76:79]\n"
        "v_cvt_pk_bf16_f32 v158, v1, v2\n"
        "v_cvt_pk_bf16_f32 v159, v251, v252\n"
        "v_mfma_f32_16x16x32_bf16 v[200:203], a[224:227], v[120:123], v[200:203]\n"
        "ds_read_b64_tr_b16 a[224:225], v234 offset:20736\n"
        "v_accvgpr_read_b32 v160, a120\n"
        "v_mfma_f32_16x16x32_bf16 a[36:39], a[228:231], v[108:111], a[36:39]\n"
        "buffer_load_dwordx4 v[108:111], v236, s[56:59], s55 offen offset:1024 sc1\n"
        "v_mfma_f32_16x16x32_bf16 v[40:43], a[228:231], v[112:115], v[40:43]\n"
        "ds_read_b64_tr_b16 a[226:227], v234 offset:25856\n"
        "v_accvgpr_read_b32 v161, a121\n"
        "v_mfma_f32_16x16x32_bf16 v[80:83], a[228:231], v[116:119], v[80:83]\n"
        "s_waitcnt lgkmcnt(3)\n"
        "v_accvgpr_read_b32 v162, a122\n"
        "v_accvgpr_read_b32 v163, a123\n"
        "v_mfma_f32_16x16x32_bf16 v[204:207], a[228:231], v[120:123], v[204:207]\n"
        "buffer_load_dwordx4 v[112:115], v237, s[56:59], s55 offen offset:1024 sc1\n"
        "v_mfma_f32_16x16x32_bf16 a[0:3], a[192:195], v[128:131], a[0:3]\n"
        "ds_read_b64_tr_b16 a[228:229], v235 offset:20768\n"
        "v_mul_f32 v160, v87, v160\n"
        "v_mfma_f32_16x16x32_bf16 v[4:7], a[192:195], v[132:135], v[4:7]\n"
        "ds_read_b64_tr_b16 a[230:231], v235 offset:25888\n"
        "v_mul_f32 v161, v87, v161\n"
        "v_mfma_f32_16x16x32_bf16 v[44:47], a[192:195], v[136:139], v[44:47]\n"
        "buffer_load_dwordx4 v[116:119], v238, s[56:59], s55 offen offset:1024 sc1\n"
        "v_mfma_f32_16x16x32_bf16 v[168:171], a[192:195], v[140:143], v[168:171]\n"
        "v_mul_f32 v162, v87, v162\n"
        "v_mul_f32 v163, v87, v163\n"
        "v_mfma_f32_16x16x32_bf16 v[208:211], a[192:195], v[144:147], v[208:211]\n"
        "ds_read_b64_tr_b16 a[192:193], v232 offset:30720\n"
        "v_cvt_pk_bf16_f32 v160, v160, v161\n"
        "v_mfma_f32_16x16x32_bf16 a[4:7], a[196:199], v[128:131], a[4:7]\n"
        "buffer_load_dwordx4 v[120:123], v239, s[56:59], s55 offen offset:1024 sc1\n"
        "v_mfma_f32_16x16x32_bf16 v[8:11], a[196:199], v[132:135], v[8:11]\n"
        "ds_read_b64_tr_b16 a[194:195], v232 offset:35840\n"
        "v_cvt_pk_bf16_f32 v161, v162, v163\n"
        "v_mfma_f32_16x16x32_bf16 v[48:51], a[196:199], v[136:139], v[48:51]\n"
        "v_accvgpr_read_b32 v1, a124\n"
        "v_accvgpr_read_b32 v2, a125\n"
        "v_mfma_f32_16x16x32_bf16 v[172:175], a[196:199], v[140:143], v[172:175]\n"
        "buffer_store_dwordx4 v[156:159], v250, s[56:59], s74 offen offset:3072 sc1\n"
        "v_mfma_f32_16x16x32_bf16 v[212:215], a[196:199], v[144:147], v[212:215]\n"
        "ds_read_b64_tr_b16 a[196:197], v233 offset:30752\n"
        "v_accvgpr_read_b32 v251, a126\n"
        "v_mfma_f32_16x16x32_bf16 a[8:11], a[200:203], v[128:131], a[8:11]\n"
        "ds_read_b64_tr_b16 a[198:199], v233 offset:35872\n"
        "v_accvgpr_read_b32 v252, a127\n"
        "v_mfma_f32_16x16x32_bf16 v[12:15], a[200:203], v[132:135], v[12:15]\n"
        "v_mul_f32 v1, v87, v1\n"
        "v_mul_f32 v2, v87, v2\n"
        "v_mfma_f32_16x16x32_bf16 v[52:55], a[200:203], v[136:139], v[52:55]\n"
        "v_mul_f32 v251, v87, v251\n"
        "v_mul_f32 v252, v87, v252\n"
        "v_mfma_f32_16x16x32_bf16 v[176:179], a[200:203], v[140:143], v[176:179]\n"
        "v_cvt_pk_bf16_f32 v162, v1, v2\n"
        "v_cvt_pk_bf16_f32 v163, v251, v252\n"
        "v_mfma_f32_16x16x32_bf16 v[216:219], a[200:203], v[144:147], v[216:219]\n"
        "ds_read_b64_tr_b16 a[200:201], v232 offset:30784\n"
        "v_mfma_f32_16x16x32_bf16 a[12:15], a[204:207], v[128:131], a[12:15]\n"
        "ds_read_b64_tr_b16 a[202:203], v232 offset:35904\n"
        "v_mfma_f32_16x16x32_bf16 v[16:19], a[204:207], v[132:135], v[16:19]\n"
        "buffer_store_dwordx4 v[160:163], v250, s[56:59], s75 offen offset:3072 sc1\n"
        "v_mfma_f32_16x16x32_bf16 v[56:59], a[204:207], v[136:139], v[56:59]\n"
        "v_mfma_f32_16x16x32_bf16 v[180:183], a[204:207], v[140:143], v[180:183]\n"
        "v_mfma_f32_16x16x32_bf16 v[220:223], a[204:207], v[144:147], v[220:223]\n"
        "ds_read_b64_tr_b16 a[204:205], v233 offset:30816\n"
        "v_mfma_f32_16x16x32_bf16 a[16:19], a[208:211], v[128:131], a[16:19]\n"
        "ds_read_b64_tr_b16 a[206:207], v233 offset:35936\n"
        "v_mfma_f32_16x16x32_bf16 v[20:23], a[208:211], v[132:135], v[20:23]\n"
        "v_add_u32 v233, s92, v233\n"
        "v_mfma_f32_16x16x32_bf16 v[60:63], a[208:211], v[136:139], v[60:63]\n"
        "v_mfma_f32_16x16x32_bf16 v[184:187], a[208:211], v[140:143], v[184:187]\n"
        "v_mfma_f32_16x16x32_bf16 v[224:227], a[208:211], v[144:147], v[224:227]\n"
        "buffer_load_dwordx4 v[144:147], v240, s[56:59], s55 offen offset:2048 sc1\n"
        "v_mfma_f32_16x16x32_bf16 a[20:23], a[212:215], v[128:131], a[20:23]\n"
        "ds_read_b64_tr_b16 a[208:209], v232 offset:30848\n"
        "v_mfma_f32_16x16x32_bf16 v[24:27], a[212:215], v[132:135], v[24:27]\n"
        "ds_read_b64_tr_b16 a[210:211], v232 offset:35968\n"
        "v_mfma_f32_16x16x32_bf16 v[64:67], a[212:215], v[136:139], v[64:67]\n"
        "v_add_u32 v232, s92, v232\n"
        "v_mfma_f32_16x16x32_bf16 v[188:191], a[212:215], v[140:143], v[188:191]\n"
        "ds_read_b64_tr_b16 a[212:213], v235 offset:30880\n"
        "v_mfma_f32_16x16x32_bf16 a[24:27], a[216:219], v[128:131], a[24:27]\n"
        "ds_read_b64_tr_b16 a[214:215], v235 offset:36000\n"
        "v_mfma_f32_16x16x32_bf16 v[28:31], a[216:219], v[132:135], v[28:31]\n"
        "v_mfma_f32_16x16x32_bf16 v[68:71], a[216:219], v[136:139], v[68:71]\n"
        "v_mfma_f32_16x16x32_bf16 v[192:195], a[216:219], v[140:143], v[192:195]\n"
        "s_waitcnt lgkmcnt(3)\n"
        "ds_read_b64_tr_b16 a[216:217], v234 offset:30912\n"
        "v_mfma_f32_16x16x32_bf16 a[28:31], a[220:223], v[128:131], a[28:31]\n"
        "ds_read_b64_tr_b16 a[218:219], v234 offset:36032\n"
        "v_mfma_f32_16x16x32_bf16 v[32:35], a[220:223], v[132:135], v[32:35]\n"
        "s_mov_b64 s[40:41], %14\n"
        "s_mov_b32 s42, 786432\n"
        "s_mov_b32 s43, 0x20000\n"
        "v_mfma_f32_16x16x32_bf16 v[72:75], a[220:223], v[136:139], v[72:75]\n"
        "s_lshl_b32 s81, s64, 14\n"
        "s_add_u32 s80, s85, 1\n"
        "s_and_b32 s80, s80, 3\n"
        "v_mfma_f32_16x16x32_bf16 v[196:199], a[220:223], v[140:143], v[196:199]\n"
        "s_mul_i32 s80, s80, 196608\n"
        "ds_read_b64_tr_b16 a[220:221], v235 offset:30944\n"
        "v_mfma_f32_16x16x32_bf16 a[32:35], a[224:227], v[128:131], a[32:35]\n"
        "s_add_u32 s80, s80, s81\n"
        "ds_read_b64_tr_b16 a[222:223], v235 offset:36064\n"
        "v_mfma_f32_16x16x32_bf16 v[36:39], a[224:227], v[132:135], v[36:39]\n"
        "s_add_u32 s60, s80, 0\n"
        "s_add_u32 s80, s85, 2\n"
        "s_and_b32 s80, s80, 3\n"
        "v_mfma_f32_16x16x32_bf16 v[76:79], a[224:227], v[136:139], v[76:79]\n"
        "s_mul_i32 s80, s80, 196608\n"
        "s_add_u32 s80, s80, s81\n"
        "s_add_u32 s61, s80, 65536\n"
        "v_mfma_f32_16x16x32_bf16 v[200:203], a[224:227], v[140:143], v[200:203]\n"
        "s_add_u32 s80, s85, 3\n"
        "ds_read_b64_tr_b16 a[224:225], v234 offset:30976\n"
        "v_mfma_f32_16x16x32_bf16 a[36:39], a[228:231], v[128:131], a[36:39]\n"
        "buffer_load_dwordx4 v[128:131], v236, s[56:59], s55 offen offset:2048 sc1\n"
        "v_mfma_f32_16x16x32_bf16 v[40:43], a[228:231], v[132:135], v[40:43]\n"
        "s_and_b32 s80, s80, 3\n"
        "ds_read_b64_tr_b16 a[226:227], v234 offset:36096\n"
        "v_mfma_f32_16x16x32_bf16 v[80:83], a[228:231], v[136:139], v[80:83]\n"
        "s_mul_i32 s80, s80, 196608\n"
        "v_add_u32 v234, s92, v234\n"
        "v_mfma_f32_16x16x32_bf16 v[204:207], a[228:231], v[140:143], v[204:207]\n"
        "buffer_load_dwordx4 v[132:135], v237, s[56:59], s55 offen offset:2048 sc1\n"
        "v_mfma_f32_16x16x32_bf16 a[0:3], a[192:195], v[148:151], a[0:3]\n"
        "s_add_u32 s80, s80, s81\n"
        "ds_read_b64_tr_b16 a[228:229], v235 offset:31008\n"
        "v_mfma_f32_16x16x32_bf16 v[4:7], a[192:195], v[152:155], v[4:7]\n"
        "s_barrier\n"
        "ds_read_b64_tr_b16 a[230:231], v235 offset:36128\n"
        "v_mfma_f32_16x16x32_bf16 v[44:47], a[192:195], v[156:159], v[44:47]\n"
        "buffer_load_dwordx4 v[136:139], v238, s[56:59], s55 offen offset:2048 sc1\n"
        "v_mfma_f32_16x16x32_bf16 v[168:171], a[192:195], v[160:163], v[168:171]\n"
        "s_add_u32 m0, s91, 0\n"
        "v_add_u32 v235, s92, v235\n"
        "v_mfma_f32_16x16x32_bf16 v[208:211], a[192:195], v[164:167], v[208:211]\n"
        "s_add_u32 s62, s80, 131072\n"
        "ds_read_b64_tr_b16 a[192:193], v232 offset:0\n"
        "v_mfma_f32_16x16x32_bf16 a[4:7], a[196:199], v[148:151], a[4:7]\n"
        "buffer_load_dwordx4 v244, s[44:47], s54 offen offset:0 lds\n"
        "v_mfma_f32_16x16x32_bf16 v[8:11], a[196:199], v[152:155], v[8:11]\n"
        "s_sub_u32 s80, s64, s85\n"
        "ds_read_b64_tr_b16 a[194:195], v232 offset:5120\n"
        "v_mfma_f32_16x16x32_bf16 v[48:51], a[196:199], v[156:159], v[48:51]\n"
        "s_and_b32 s80, s80, 3\n"
        "s_sub_u32 s80, s80, 1\n"
        "s_lshl_b32 s80, s80, 16\n"
        "v_mfma_f32_16x16x32_bf16 v[172:175], a[196:199], v[160:163], v[172:175]\n"
        "buffer_load_dwordx4 v245, s[44:47], s54 offen offset:1024 lds\n"
        "v_mfma_f32_16x16x32_bf16 v[212:215], a[196:199], v[164:167], v[212:215]\n"
        "s_mul_i32 s63, s64, 212992\n"
        "ds_read_b64_tr_b16 a[196:197], v233 offset:32\n"
        "v_mfma_f32_16x16x32_bf16 a[8:11], a[200:203], v[148:151], a[8:11]\n"
        "s_add_u32 s63, s63, s80\n"
        "ds_read_b64_tr_b16 a[198:199], v233 offset:5152\n"
        "v_mfma_f32_16x16x32_bf16 v[12:15], a[200:203], v[152:155], v[12:15]\n"
        "buffer_load_dwordx4 v246, s[44:47], s54 offen offset:2048 lds\n"
        "v_mfma_f32_16x16x32_bf16 v[52:55], a[200:203], v[156:159], v[52:55]\n"
        "s_cmp_eq_u32 s64, s85\n"
        "s_cselect_b32 s50, 0, s42\n"
        "s_cselect_b32 s63, 0, s63\n"
        "v_mfma_f32_16x16x32_bf16 v[176:179], a[200:203], v[160:163], v[176:179]\n"
        "s_mov_b64 s[48:49], %14\n"
        "s_mov_b32 s51, 0x20000\n"
        "s_mul_i32 s68, s85, 196608\n"
        "v_mfma_f32_16x16x32_bf16 v[216:219], a[200:203], v[164:167], v[216:219]\n"
        "buffer_load_dwordx4 v[140:143], v239, s[56:59], s55 offen offset:2048 sc1\n"
        "v_mfma_f32_16x16x32_bf16 a[12:15], a[204:207], v[148:151], a[12:15]\n"
        "s_add_u32 s68, s68, s81\n"
        "ds_read_b64_tr_b16 a[200:201], v232 offset:64\n"
        "v_mov_b32 v228, v250\n"
        "v_mfma_f32_16x16x32_bf16 v[16:19], a[204:207], v[152:155], v[16:19]\n"
        "s_mov_b32 s82, 0x100000\n"
        "ds_read_b64_tr_b16 a[202:203], v232 offset:5184\n"
        "v_add_u32 v229, 4096, v250\n"
        "v_mfma_f32_16x16x32_bf16 v[56:59], a[204:207], v[156:159], v[56:59]\n"
        "buffer_load_dwordx4 v247, s[44:47], s54 offen offset:3072 lds\n"
        "v_mfma_f32_16x16x32_bf16 v[180:183], a[204:207], v[160:163], v[180:183]\n"
        "s_add_u32 m0, s91, 4096\n"
        "v_mfma_f32_16x16x32_bf16 v[220:223], a[204:207], v[164:167], v[220:223]\n"
        "s_waitcnt lgkmcnt(2)\n"
        "ds_read_b64_tr_b16 a[204:205], v233 offset:96\n"
        "v_mfma_f32_16x16x32_bf16 a[16:19], a[208:211], v[148:151], a[16:19]\n"
        "buffer_load_dwordx4 v248, s[44:47], s54 offen offset:0 lds\n"
        "v_mfma_f32_16x16x32_bf16 v[20:23], a[208:211], v[152:155], v[20:23]\n"
        "s_add_u32 s91, s91, s92\n"
        "ds_read_b64_tr_b16 a[206:207], v233 offset:5216\n"
        "v_mfma_f32_16x16x32_bf16 v[60:63], a[208:211], v[156:159], v[60:63]\n"
        "s_sub_u32 s92, 0, s92\n"
        "v_mfma_f32_16x16x32_bf16 v[184:187], a[208:211], v[160:163], v[184:187]\n"
        "v_mfma_f32_16x16x32_bf16 v[224:227], a[208:211], v[164:167], v[224:227]\n"
        "buffer_load_dwordx4 v[164:167], v240, s[56:59], s55 offen offset:3072 sc1\n"
        "v_mfma_f32_16x16x32_bf16 a[20:23], a[212:215], v[148:151], a[20:23]\n"
        "ds_read_b64_tr_b16 a[208:209], v232 offset:128\n"
        "v_mfma_f32_16x16x32_bf16 v[24:27], a[212:215], v[152:155], v[24:27]\n"
        "ds_read_b64_tr_b16 a[210:211], v232 offset:5248\n"
        "v_mfma_f32_16x16x32_bf16 v[64:67], a[212:215], v[156:159], v[64:67]\n"
        "v_mfma_f32_16x16x32_bf16 v[188:191], a[212:215], v[160:163], v[188:191]\n"
        "ds_read_b64_tr_b16 a[212:213], v235 offset:160\n"
        "v_mfma_f32_16x16x32_bf16 a[24:27], a[216:219], v[148:151], a[24:27]\n"
        "ds_read_b64_tr_b16 a[214:215], v235 offset:5280\n"
        "v_mfma_f32_16x16x32_bf16 v[28:31], a[216:219], v[152:155], v[28:31]\n"
        "v_mfma_f32_16x16x32_bf16 v[68:71], a[216:219], v[156:159], v[68:71]\n"
        "v_mfma_f32_16x16x32_bf16 v[192:195], a[216:219], v[160:163], v[192:195]\n"
        "ds_read_b64_tr_b16 a[216:217], v234 offset:192\n"
        "v_mfma_f32_16x16x32_bf16 a[28:31], a[220:223], v[148:151], a[28:31]\n"
        "ds_read_b64_tr_b16 a[218:219], v234 offset:5312\n"
        "v_mfma_f32_16x16x32_bf16 v[32:35], a[220:223], v[152:155], v[32:35]\n"
        "v_mfma_f32_16x16x32_bf16 v[72:75], a[220:223], v[156:159], v[72:75]\n"
        "v_mfma_f32_16x16x32_bf16 v[196:199], a[220:223], v[160:163], v[196:199]\n"
        "ds_read_b64_tr_b16 a[220:221], v235 offset:224\n"
        "v_mfma_f32_16x16x32_bf16 a[32:35], a[224:227], v[148:151], a[32:35]\n"
        "ds_read_b64_tr_b16 a[222:223], v235 offset:5344\n"
        "v_mfma_f32_16x16x32_bf16 v[36:39], a[224:227], v[152:155], v[36:39]\n"
        "v_mfma_f32_16x16x32_bf16 v[76:79], a[224:227], v[156:159], v[76:79]\n"
        "v_mfma_f32_16x16x32_bf16 v[200:203], a[224:227], v[160:163], v[200:203]\n"
        "ds_read_b64_tr_b16 a[224:225], v234 offset:256\n"
        "v_mfma_f32_16x16x32_bf16 a[36:39], a[228:231], v[148:151], a[36:39]\n"
        "buffer_load_dwordx4 v[148:151], v236, s[56:59], s55 offen offset:3072 sc1\n"
        "v_mfma_f32_16x16x32_bf16 v[40:43], a[228:231], v[152:155], v[40:43]\n"
        "ds_read_b64_tr_b16 a[226:227], v234 offset:5376\n"
        "v_mfma_f32_16x16x32_bf16 v[80:83], a[228:231], v[156:159], v[80:83]\n"
        "s_waitcnt vmcnt(30)\n"
        "v_mfma_f32_16x16x32_bf16 v[204:207], a[228:231], v[160:163], v[204:207]\n"
        "buffer_load_dwordx4 v[152:155], v237, s[56:59], s55 offen offset:3072 sc1\n"
        "s_branch .Lk_BODY\n"
        ".p2align 5\n"
        "s_nop 0\n"
        "s_nop 0\n"
        "s_nop 0\n"
        ".Lk_BODY:\n"
        "v_mfma_f32_16x16x32_bf16 a[0:3], a[192:195], v[88:91], a[0:3]\n"
        "s_waitcnt vmcnt(26)\n"
        "ds_read_b64_tr_b16 a[228:229], v235 offset:288\n"
        "v_mfma_f32_16x16x32_bf16 v[4:7], a[192:195], v[92:95], v[4:7]\n"
        "s_waitcnt vmcnt(21)\n"
        "ds_read_b64_tr_b16 a[230:231], v235 offset:5408\n"
        "v_mfma_f32_16x16x32_bf16 v[44:47], a[192:195], v[96:99], v[44:47]\n"
        "buffer_load_dwordx4 v[156:159], v238, s[56:59], s55 offen offset:3072 sc1\n"
        "v_mfma_f32_16x16x32_bf16 v[168:171], a[192:195], v[100:103], v[168:171]\n"
        "v_mfma_f32_16x16x32_bf16 v[208:211], a[192:195], v[104:107], v[208:211]\n"
        "s_waitcnt lgkmcnt(2)\n"
        "ds_read_b64_tr_b16 a[192:193], v232 offset:10240\n"
        "v_mfma_f32_16x16x32_bf16 a[4:7], a[196:199], v[88:91], a[4:7]\n"
        "buffer_load_dwordx4 v[160:163], v239, s[56:59], s55 offen offset:3072 sc1\n"
        "v_mfma_f32_16x16x32_bf16 v[8:11], a[196:199], v[92:95], v[8:11]\n"
        "ds_read_b64_tr_b16 a[194:195], v232 offset:15360\n"
        "v_mfma_f32_16x16x32_bf16 v[48:51], a[196:199], v[96:99], v[48:51]\n"
        "s_waitcnt vmcnt(20)\n"
        "v_mfma_f32_16x16x32_bf16 v[172:175], a[196:199], v[100:103], v[172:175]\n"
        "v_cmp_ne_u32 vcc, s95, v242\n"
        "v_mfma_f32_16x16x32_bf16 v[212:215], a[196:199], v[104:107], v[212:215]\n"
        "s_cbranch_vccnz .Lk_SPIN\n"
        ".Lk_SPINRET:\n"
        "ds_read_b64_tr_b16 a[196:197], v233 offset:10272\n"
        "v_mfma_f32_16x16x32_bf16 a[8:11], a[200:203], v[88:91], a[8:11]\n"
        "s_lshl_b32 s55, s89, 5\n"
        "ds_read_b64_tr_b16 a[198:199], v233 offset:15392\n"
        "v_mfma_f32_16x16x32_bf16 v[12:15], a[200:203], v[92:95], v[12:15]\n"
        "s_add_u32 s53, s54, 81920\n"
        "v_mfma_f32_16x16x32_bf16 v[52:55], a[200:203], v[96:99], v[52:55]\n"
        "s_ashr_i32 s52, s52, 4\n"
        "v_mfma_f32_16x16x32_bf16 v[176:179], a[200:203], v[100:103], v[176:179]\n"
        "s_and_b32 s54, s52, 15\n"
        "v_mfma_f32_16x16x32_bf16 v[216:219], a[200:203], v[104:107], v[216:219]\n"
        "s_and_b32 s89, s52, 7\n"
        "ds_read_b64_tr_b16 a[200:201], v232 offset:10304\n"
        "v_mfma_f32_16x16x32_bf16 a[12:15], a[204:207], v[88:91], a[12:15]\n"
        "s_mul_i32 s54, s54, 163840\n"
        "ds_read_b64_tr_b16 a[202:203], v232 offset:15424\n"
        "v_mfma_f32_16x16x32_bf16 v[16:19], a[204:207], v[92:95], v[16:19]\n"
        "s_lshl_b32 s89, s89, 7\n"
        "v_mfma_f32_16x16x32_bf16 v[56:59], a[204:207], v[96:99], v[56:59]\n"
        "v_mfma_f32_16x16x32_bf16 v[180:183], a[204:207], v[100:103], v[180:183]\n"
        "v_mfma_f32_16x16x32_bf16 v[220:223], a[204:207], v[104:107], v[220:223]\n"
        "ds_read_b64_tr_b16 a[204:205], v233 offset:10336\n"
        "v_mfma_f32_16x16x32_bf16 a[16:19], a[208:211], v[88:91], a[16:19]\n"
        "ds_read_b64_tr_b16 a[206:207], v233 offset:15456\n"
        "v_mfma_f32_16x16x32_bf16 v[20:23], a[208:211], v[92:95], v[20:23]\n"
        "v_mfma_f32_16x16x32_bf16 v[60:63], a[208:211], v[96:99], v[60:63]\n"
        "v_mfma_f32_16x16x32_bf16 v[184:187], a[208:211], v[100:103], v[184:187]\n"
        "v_mfma_f32_16x16x32_bf16 v[224:227], a[208:211], v[104:107], v[224:227]\n"
        "buffer_load_dwordx4 v[104:107], v240, s[56:59], s55 offen offset:0 sc1\n"
        "v_mfma_f32_16x16x32_bf16 a[20:23], a[212:215], v[88:91], a[20:23]\n"
        "ds_read_b64_tr_b16 a[208:209], v232 offset:10368\n"
        "v_mfma_f32_16x16x32_bf16 v[24:27], a[212:215], v[92:95], v[24:27]\n"
        "ds_read_b64_tr_b16 a[210:211], v232 offset:15488\n"
        "v_mfma_f32_16x16x32_bf16 v[64:67], a[212:215], v[96:99], v[64:67]\n"
        "v_mfma_f32_16x16x32_bf16 v[188:191], a[212:215], v[100:103], v[188:191]\n"
        "ds_read_b64_tr_b16 a[212:213], v235 offset:10400\n"
        "v_mfma_f32_16x16x32_bf16 a[24:27], a[216:219], v[88:91], a[24:27]\n"
        "ds_read_b64_tr_b16 a[214:215], v235 offset:15520\n"
        "v_mfma_f32_16x16x32_bf16 v[28:31], a[216:219], v[92:95], v[28:31]\n"
        "s_cmp_eq_u32 s93, 5\n"
        "v_mfma_f32_16x16x32_bf16 v[68:71], a[216:219], v[96:99], v[68:71]\n"
        "s_cbranch_scc1 .Lk_PUB2\n"
        ".Lk_PUB2RET:\n"
        "v_mfma_f32_16x16x32_bf16 v[192:195], a[216:219], v[100:103], v[192:195]\n"
        "ds_read_b64_tr_b16 a[216:217], v234 offset:10432\n"
        "v_mfma_f32_16x16x32_bf16 a[28:31], a[220:223], v[88:91], a[28:31]\n"
        "ds_read_b64_tr_b16 a[218:219], v234 offset:15552\n"
        "v_mfma_f32_16x16x32_bf16 v[32:35], a[220:223], v[92:95], v[32:35]\n"
        "v_mfma_f32_16x16x32_bf16 v[72:75], a[220:223], v[96:99], v[72:75]\n"
        "v_mfma_f32_16x16x32_bf16 v[196:199], a[220:223], v[100:103], v[196:199]\n"
        "ds_read_b64_tr_b16 a[220:221], v235 offset:10464\n"
        "v_mfma_f32_16x16x32_bf16 a[32:35], a[224:227], v[88:91], a[32:35]\n"
        "ds_read_b64_tr_b16 a[222:223], v235 offset:15584\n"
        "v_mfma_f32_16x16x32_bf16 v[36:39], a[224:227], v[92:95], v[36:39]\n"
        "v_mfma_f32_16x16x32_bf16 v[76:79], a[224:227], v[96:99], v[76:79]\n"
        "v_mfma_f32_16x16x32_bf16 v[200:203], a[224:227], v[100:103], v[200:203]\n"
        "s_waitcnt lgkmcnt(2)\n"
        "ds_read_b64_tr_b16 a[224:225], v234 offset:10496\n"
        "v_mfma_f32_16x16x32_bf16 a[36:39], a[228:231], v[88:91], a[36:39]\n"
        "buffer_load_dwordx4 v[88:91], v236, s[56:59], s55 offen offset:0 sc1\n"
        "v_mfma_f32_16x16x32_bf16 v[40:43], a[228:231], v[92:95], v[40:43]\n"
        "ds_read_b64_tr_b16 a[226:227], v234 offset:15616\n"
        "v_mfma_f32_16x16x32_bf16 v[80:83], a[228:231], v[96:99], v[80:83]\n"
        "s_waitcnt vmcnt(20)\n"
        "v_mfma_f32_16x16x32_bf16 v[204:207], a[228:231], v[100:103], v[204:207]\n"
        "buffer_load_dwordx4 v[92:95], v237, s[56:59], s55 offen offset:0 sc1\n"
        "v_mfma_f32_16x16x32_bf16 a[0:3], a[192:195], v[108:111], a[0:3]\n"
        "s_waitcnt vmcnt(18)\n"
        "ds_read_b64_tr_b16 a[228:229], v235 offset:10528\n"
        "v_mfma_f32_16x16x32_bf16 v[4:7], a[192:195], v[112:115], v[4:7]\n"
        "s_barrier\n"
        "ds_read_b64_tr_b16 a[230:231], v235 offset:15648\n"
        "v_mfma_f32_16x16x32_bf16 v[44:47], a[192:195], v[116:119], v[44:47]\n"
        "buffer_load_dwordx4 v[96:99], v238, s[56:59], s55 offen offset:0 sc1\n"
        "v_mfma_f32_16x16x32_bf16 v[168:171], a[192:195], v[120:123], v[168:171]\n"
        "s_add_u32 m0, s90, 0\n"
        "v_mfma_f32_16x16x32_bf16 v[208:211], a[192:195], v[124:127], v[208:211]\n"
        "ds_read_b64_tr_b16 a[192:193], v232 offset:20480\n"
        "v_mfma_f32_16x16x32_bf16 a[4:7], a[196:199], v[108:111], a[4:7]\n"
        "buffer_load_dwordx4 v244, s[44:47], s53 offen offset:0 lds\n"
        "v_mfma_f32_16x16x32_bf16 v[8:11], a[196:199], v[112:115], v[8:11]\n"
        "ds_read_b64_tr_b16 a[194:195], v232 offset:25600\n"
        "v_mfma_f32_16x16x32_bf16 v[48:51], a[196:199], v[116:119], v[48:51]\n"
        "v_mfma_f32_16x16x32_bf16 v[172:175], a[196:199], v[120:123], v[172:175]\n"
        "buffer_load_dwordx4 v245, s[44:47], s53 offen offset:1024 lds\n"
        "v_mfma_f32_16x16x32_bf16 v[212:215], a[196:199], v[124:127], v[212:215]\n"
        "ds_read_b64_tr_b16 a[196:197], v233 offset:20512\n"
        "v_mfma_f32_16x16x32_bf16 a[8:11], a[200:203], v[108:111], a[8:11]\n"
        "ds_read_b64_tr_b16 a[198:199], v233 offset:25632\n"
        "v_mfma_f32_16x16x32_bf16 v[12:15], a[200:203], v[112:115], v[12:15]\n"
        "buffer_load_dwordx4 v246, s[44:47], s53 offen offset:2048 lds\n"
        "v_mfma_f32_16x16x32_bf16 v[52:55], a[200:203], v[116:119], v[52:55]\n"
        "v_mfma_f32_16x16x32_bf16 v[176:179], a[200:203], v[120:123], v[176:179]\n"
        "v_mfma_f32_16x16x32_bf16 v[216:219], a[200:203], v[124:127], v[216:219]\n"
        "buffer_load_dwordx4 v[100:103], v239, s[56:59], s55 offen offset:0 sc1\n"
        "v_mfma_f32_16x16x32_bf16 a[12:15], a[204:207], v[108:111], a[12:15]\n"
        "ds_read_b64_tr_b16 a[200:201], v232 offset:20544\n"
        "v_mfma_f32_16x16x32_bf16 v[16:19], a[204:207], v[112:115], v[16:19]\n"
        "ds_read_b64_tr_b16 a[202:203], v232 offset:25664\n"
        "v_mfma_f32_16x16x32_bf16 v[56:59], a[204:207], v[116:119], v[56:59]\n"
        "buffer_load_dwordx4 v247, s[44:47], s53 offen offset:3072 lds\n"
        "v_mfma_f32_16x16x32_bf16 v[180:183], a[204:207], v[120:123], v[180:183]\n"
        "s_add_u32 m0, s90, 4096\n"
        "v_mfma_f32_16x16x32_bf16 v[220:223], a[204:207], v[124:127], v[220:223]\n"
        "ds_read_b64_tr_b16 a[204:205], v233 offset:20576\n"
        "v_mfma_f32_16x16x32_bf16 a[16:19], a[208:211], v[108:111], a[16:19]\n"
        "buffer_load_dwordx4 v248, s[44:47], s53 offen offset:0 lds\n"
        "v_mfma_f32_16x16x32_bf16 v[20:23], a[208:211], v[112:115], v[20:23]\n"
        "s_sub_u32 s90, s90, s92\n"
        "ds_read_b64_tr_b16 a[206:207], v233 offset:25696\n"
        "v_mfma_f32_16x16x32_bf16 v[60:63], a[208:211], v[116:119], v[60:63]\n"
        "v_mfma_f32_16x16x32_bf16 v[184:187], a[208:211], v[120:123], v[184:187]\n"
        "buffer_load_dword v242, v243, s[96:99], s89 offen offset:128 sc1\n"
        "v_mfma_f32_16x16x32_bf16 v[224:227], a[208:211], v[124:127], v[224:227]\n"
        "ds_read_b64_tr_b16 a[208:209], v232 offset:20608\n"
        "v_mfma_f32_16x16x32_bf16 a[20:23], a[212:215], v[108:111], a[20:23]\n"
        "ds_read_b64_tr_b16 a[210:211], v232 offset:25728\n"
        "v_mfma_f32_16x16x32_bf16 v[24:27], a[212:215], v[112:115], v[24:27]\n"
        "buffer_load_dwordx4 v[124:127], v240, s[56:59], s55 offen offset:1024 sc1\n"
        "v_mfma_f32_16x16x32_bf16 v[64:67], a[212:215], v[116:119], v[64:67]\n"
        "v_mfma_f32_16x16x32_bf16 v[188:191], a[212:215], v[120:123], v[188:191]\n"
        "ds_read_b64_tr_b16 a[212:213], v235 offset:20640\n"
        "v_mfma_f32_16x16x32_bf16 a[24:27], a[216:219], v[108:111], a[24:27]\n"
        "ds_read_b64_tr_b16 a[214:215], v235 offset:25760\n"
        "v_mfma_f32_16x16x32_bf16 v[28:31], a[216:219], v[112:115], v[28:31]\n"
        "v_mfma_f32_16x16x32_bf16 v[68:71], a[216:219], v[116:119], v[68:71]\n"
        "v_mfma_f32_16x16x32_bf16 v[192:195], a[216:219], v[120:123], v[192:195]\n"
        "s_waitcnt lgkmcnt(2)\n"
        "ds_read_b64_tr_b16 a[216:217], v234 offset:20672\n"
        "v_mfma_f32_16x16x32_bf16 a[28:31], a[220:223], v[108:111], a[28:31]\n"
        "ds_read_b64_tr_b16 a[218:219], v234 offset:25792\n"
        "v_mfma_f32_16x16x32_bf16 v[32:35], a[220:223], v[112:115], v[32:35]\n"
        "v_mfma_f32_16x16x32_bf16 v[72:75], a[220:223], v[116:119], v[72:75]\n"
        "v_mfma_f32_16x16x32_bf16 v[196:199], a[220:223], v[120:123], v[196:199]\n"
        "ds_read_b64_tr_b16 a[220:221], v235 offset:20704\n"
        "v_mfma_f32_16x16x32_bf16 a[32:35], a[224:227], v[108:111], a[32:35]\n"
        "ds_read_b64_tr_b16 a[222:223], v235 offset:25824\n"
        "v_mfma_f32_16x16x32_bf16 v[36:39], a[224:227], v[112:115], v[36:39]\n"
        "v_mfma_f32_16x16x32_bf16 v[76:79], a[224:227], v[116:119], v[76:79]\n"
        "v_mfma_f32_16x16x32_bf16 v[200:203], a[224:227], v[120:123], v[200:203]\n"
        "ds_read_b64_tr_b16 a[224:225], v234 offset:20736\n"
        "v_mfma_f32_16x16x32_bf16 a[36:39], a[228:231], v[108:111], a[36:39]\n"
        "buffer_load_dwordx4 v[108:111], v236, s[56:59], s55 offen offset:1024 sc1\n"
        "v_mfma_f32_16x16x32_bf16 v[40:43], a[228:231], v[112:115], v[40:43]\n"
        "ds_read_b64_tr_b16 a[226:227], v234 offset:25856\n"
        "v_mfma_f32_16x16x32_bf16 v[80:83], a[228:231], v[116:119], v[80:83]\n"
        "s_waitcnt vmcnt(26)\n"
        "v_mfma_f32_16x16x32_bf16 v[204:207], a[228:231], v[120:123], v[204:207]\n"
        "buffer_load_dwordx4 v[112:115], v237, s[56:59], s55 offen offset:1024 sc1\n"
        "v_mfma_f32_16x16x32_bf16 a[0:3], a[192:195], v[128:131], a[0:3]\n"
        "s_waitcnt vmcnt(26)\n"
        "ds_read_b64_tr_b16 a[228:229], v235 offset:20768\n"
        "v_mfma_f32_16x16x32_bf16 v[4:7], a[192:195], v[132:135], v[4:7]\n"
        "s_waitcnt vmcnt(21)\n"
        "ds_read_b64_tr_b16 a[230:231], v235 offset:25888\n"
        "v_mfma_f32_16x16x32_bf16 v[44:47], a[192:195], v[136:139], v[44:47]\n"
        "buffer_load_dwordx4 v[116:119], v238, s[56:59], s55 offen offset:1024 sc1\n"
        "v_mfma_f32_16x16x32_bf16 v[168:171], a[192:195], v[140:143], v[168:171]\n"
        "v_mfma_f32_16x16x32_bf16 v[208:211], a[192:195], v[144:147], v[208:211]\n"
        "ds_read_b64_tr_b16 a[192:193], v232 offset:30720\n"
        "v_mfma_f32_16x16x32_bf16 a[4:7], a[196:199], v[128:131], a[4:7]\n"
        "buffer_load_dwordx4 v[120:123], v239, s[56:59], s55 offen offset:1024 sc1\n"
        "v_mfma_f32_16x16x32_bf16 v[8:11], a[196:199], v[132:135], v[8:11]\n"
        "ds_read_b64_tr_b16 a[194:195], v232 offset:35840\n"
        "v_mfma_f32_16x16x32_bf16 v[48:51], a[196:199], v[136:139], v[48:51]\n"
        "v_mfma_f32_16x16x32_bf16 v[172:175], a[196:199], v[140:143], v[172:175]\n"
        "v_mfma_f32_16x16x32_bf16 v[212:215], a[196:199], v[144:147], v[212:215]\n"
        "ds_read_b64_tr_b16 a[196:197], v233 offset:30752\n"
        "v_mfma_f32_16x16x32_bf16 a[8:11], a[200:203], v[128:131], a[8:11]\n"
        "ds_read_b64_tr_b16 a[198:199], v233 offset:35872\n"
        "v_mfma_f32_16x16x32_bf16 v[12:15], a[200:203], v[132:135], v[12:15]\n"
        "v_mfma_f32_16x16x32_bf16 v[52:55], a[200:203], v[136:139], v[52:55]\n"
        "v_mfma_f32_16x16x32_bf16 v[176:179], a[200:203], v[140:143], v[176:179]\n"
        "v_mfma_f32_16x16x32_bf16 v[216:219], a[200:203], v[144:147], v[216:219]\n"
        "ds_read_b64_tr_b16 a[200:201], v232 offset:30784\n"
        "v_mfma_f32_16x16x32_bf16 a[12:15], a[204:207], v[128:131], a[12:15]\n"
        "ds_read_b64_tr_b16 a[202:203], v232 offset:35904\n"
        "v_mfma_f32_16x16x32_bf16 v[16:19], a[204:207], v[132:135], v[16:19]\n"
        "v_mfma_f32_16x16x32_bf16 v[56:59], a[204:207], v[136:139], v[56:59]\n"
        "v_mfma_f32_16x16x32_bf16 v[180:183], a[204:207], v[140:143], v[180:183]\n"
        "v_mfma_f32_16x16x32_bf16 v[220:223], a[204:207], v[144:147], v[220:223]\n"
        "ds_read_b64_tr_b16 a[204:205], v233 offset:30816\n"
        "v_mfma_f32_16x16x32_bf16 a[16:19], a[208:211], v[128:131], a[16:19]\n"
        "ds_read_b64_tr_b16 a[206:207], v233 offset:35936\n"
        "v_mfma_f32_16x16x32_bf16 v[20:23], a[208:211], v[132:135], v[20:23]\n"
        "v_add_u32 v233, s92, v233\n"
        "v_mfma_f32_16x16x32_bf16 v[60:63], a[208:211], v[136:139], v[60:63]\n"
        "v_mfma_f32_16x16x32_bf16 v[184:187], a[208:211], v[140:143], v[184:187]\n"
        "s_waitcnt lgkmcnt(2)\n"
        "v_mfma_f32_16x16x32_bf16 v[224:227], a[208:211], v[144:147], v[224:227]\n"
        "buffer_load_dwordx4 v[144:147], v240, s[56:59], s55 offen offset:2048 sc1\n"
        "v_mfma_f32_16x16x32_bf16 a[20:23], a[212:215], v[128:131], a[20:23]\n"
        "ds_read_b64_tr_b16 a[208:209], v232 offset:30848\n"
        "v_mfma_f32_16x16x32_bf16 v[24:27], a[212:215], v[132:135], v[24:27]\n"
        "ds_read_b64_tr_b16 a[210:211], v232 offset:35968\n"
        "v_mfma_f32_16x16x32_bf16 v[64:67], a[212:215], v[136:139], v[64:67]\n"
        "v_add_u32 v232, s92, v232\n"
        "v_mfma_f32_16x16x32_bf16 v[188:191], a[212:215], v[140:143], v[188:191]\n"
        "ds_read_b64_tr_b16 a[212:213], v235 offset:30880\n"
        "v_mfma_f32_16x16x32_bf16 a[24:27], a[216:219], v[128:131], a[24:27]\n"
        "ds_read_b64_tr_b16 a[214:215], v235 offset:36000\n"
        "v_mfma_f32_16x16x32_bf16 v[28:31], a[216:219], v[132:135], v[28:31]\n"
        "v_mfma_f32_16x16x32_bf16 v[68:71], a[216:219], v[136:139], v[68:71]\n"
        "v_mfma_f32_16x16x32_bf16 v[192:195], a[216:219], v[140:143], v[192:195]\n"
        "ds_read_b64_tr_b16 a[216:217], v234 offset:30912\n"
        "v_mfma_f32_16x16x32_bf16 a[28:31], a[220:223], v[128:131], a[28:31]\n"
        "ds_read_b64_tr_b16 a[218:219], v234 offset:36032\n"
        "v_mfma_f32_16x16x32_bf16 v[32:35], a[220:223], v[132:135], v[32:35]\n"
        "v_mfma_f32_16x16x32_bf16 v[72:75], a[220:223], v[136:139], v[72:75]\n"
        "v_mfma_f32_16x16x32_bf16 v[196:199], a[220:223], v[140:143], v[196:199]\n"
        "ds_read_b64_tr_b16 a[220:221], v235 offset:30944\n"
        "v_mfma_f32_16x16x32_bf16 a[32:35], a[224:227], v[128:131], a[32:35]\n"
        "ds_read_b64_tr_b16 a[222:223], v235 offset:36064\n"
        "v_mfma_f32_16x16x32_bf16 v[36:39], a[224:227], v[132:135], v[36:39]\n"
        "v_mfma_f32_16x16x32_bf16 v[76:79], a[224:227], v[136:139], v[76:79]\n"
        "v_mfma_f32_16x16x32_bf16 v[200:203], a[224:227], v[140:143], v[200:203]\n"
        "ds_read_b64_tr_b16 a[224:225], v234 offset:30976\n"
        "v_mfma_f32_16x16x32_bf16 a[36:39], a[228:231], v[128:131], a[36:39]\n"
        "buffer_load_dwordx4 v[128:131], v236, s[56:59], s55 offen offset:2048 sc1\n"
        "v_mfma_f32_16x16x32_bf16 v[40:43], a[228:231], v[132:135], v[40:43]\n"
        "ds_read_b64_tr_b16 a[226:227], v234 offset:36096\n"
        "v_mfma_f32_16x16x32_bf16 v[80:83], a[228:231], v[136:139], v[80:83]\n"
        "s_waitcnt vmcnt(21)\n"
        "v_add_u32 v234, s92, v234\n"
        "v_mfma_f32_16x16x32_bf16 v[204:207], a[228:231], v[140:143], v[204:207]\n"
        "buffer_load_dwordx4 v[132:135], v237, s[56:59], s55 offen offset:2048 sc1\n"
        "v_mfma_f32_16x16x32_bf16 a[0:3], a[192:195], v[148:151], a[0:3]\n"
        "s_waitcnt vmcnt(19)\n"
        "ds_read_b64_tr_b16 a[228:229], v235 offset:31008\n"
        "v_mfma_f32_16x16x32_bf16 v[4:7], a[192:195], v[152:155], v[4:7]\n"
        "s_barrier\n"
        "ds_read_b64_tr_b16 a[230:231], v235 offset:36128\n"
        "v_mfma_f32_16x16x32_bf16 v[44:47], a[192:195], v[156:159], v[44:47]\n"
        "buffer_load_dwordx4 v[136:139], v238, s[56:59], s55 offen offset:2048 sc1\n"
        "v_mfma_f32_16x16x32_bf16 v[168:171], a[192:195], v[160:163], v[168:171]\n"
        "s_add_u32 m0, s91, 0\n"
        "v_add_u32 v235, s92, v235\n"
        "v_mfma_f32_16x16x32_bf16 v[208:211], a[192:195], v[164:167], v[208:211]\n"
        "ds_read_b64_tr_b16 a[192:193], v232 offset:0\n"
        "v_mfma_f32_16x16x32_bf16 a[4:7], a[196:199], v[148:151], a[4:7]\n"
        "buffer_load_dwordx4 v244, s[44:47], s54 offen offset:0 lds\n"
        "v_mfma_f32_16x16x32_bf16 v[8:11], a[196:199], v[152:155], v[8:11]\n"
        "ds_read_b64_tr_b16 a[194:195], v232 offset:5120\n"
        "v_mfma_f32_16x16x32_bf16 v[48:51], a[196:199], v[156:159], v[48:51]\n"
        "v_mfma_f32_16x16x32_bf16 v[172:175], a[196:199], v[160:163], v[172:175]\n"
        "buffer_load_dwordx4 v245, s[44:47], s54 offen offset:1024 lds\n"
        "v_mfma_f32_16x16x32_bf16 v[212:215], a[196:199], v[164:167], v[212:215]\n"
        "ds_read_b64_tr_b16 a[196:197], v233 offset:32\n"
        "v_mfma_f32_16x16x32_bf16 a[8:11], a[200:203], v[148:151], a[8:11]\n"
        "ds_read_b64_tr_b16 a[198:199], v233 offset:5152\n"
        "v_mfma_f32_16x16x32_bf16 v[12:15], a[200:203], v[152:155], v[12:15]\n"
        "buffer_load_dwordx4 v246, s[44:47], s54 offen offset:2048 lds\n"
        "v_mfma_f32_16x16x32_bf16 v[52:55], a[200:203], v[156:159], v[52:55]\n"
        "v_mfma_f32_16x16x32_bf16 v[176:179], a[200:203], v[160:163], v[176:179]\n"
        "s_waitcnt lgkmcnt(2)\n"
        "v_mfma_f32_16x16x32_bf16 v[216:219], a[200:203], v[164:167], v[216:219]\n"
        "buffer_load_dwordx4 v[140:143], v239, s[56:59], s55 offen offset:2048 sc1\n"
        "v_mfma_f32_16x16x32_bf16 a[12:15], a[204:207], v[148:151], a[12:15]\n"
        "ds_read_b64_tr_b16 a[200:201], v232 offset:64\n"
        "v_mfma_f32_16x16x32_bf16 v[16:19], a[204:207], v[152:155], v[16:19]\n"
        "ds_read_b64_tr_b16 a[202:203], v232 offset:5184\n"
        "v_mfma_f32_16x16x32_bf16 v[56:59], a[204:207], v[156:159], v[56:59]\n"
        "buffer_load_dwordx4 v247, s[44:47], s54 offen offset:3072 lds\n"
        "v_mfma_f32_16x16x32_bf16 v[180:183], a[204:207], v[160:163], v[180:183]\n"
        "s_add_u32 m0, s91, 4096\n"
        "v_mfma_f32_16x16x32_bf16 v[220:223], a[204:207], v[164:167], v[220:223]\n"
        "ds_read_b64_tr_b16 a[204:205], v233 offset:96\n"
        "v_mfma_f32_16x16x32_bf16 a[16:19], a[208:211], v[148:151], a[16:19]\n"
        "buffer_load_dwordx4 v248, s[44:47], s54 offen offset:0 lds\n"
        "v_mfma_f32_16x16x32_bf16 v[20:23], a[208:211], v[152:155], v[20:23]\n"
        "s_add_u32 s91, s91, s92\n"
        "ds_read_b64_tr_b16 a[206:207], v233 offset:5216\n"
        "v_mfma_f32_16x16x32_bf16 v[60:63], a[208:211], v[156:159], v[60:63]\n"
        "s_sub_u32 s92, 0, s92\n"
        "v_mfma_f32_16x16x32_bf16 v[184:187], a[208:211], v[160:163], v[184:187]\n"
        "v_mfma_f32_16x16x32_bf16 v[224:227], a[208:211], v[164:167], v[224:227]\n"
        "buffer_load_dwordx4 v[164:167], v240, s[56:59], s55 offen offset:3072 sc1\n"
        "v_mfma_f32_16x16x32_bf16 a[20:23], a[212:215], v[148:151], a[20:23]\n"
        "ds_read_b64_tr_b16 a[208:209], v232 offset:128\n"
        "v_mfma_f32_16x16x32_bf16 v[24:27], a[212:215], v[152:155], v[24:27]\n"
        "ds_read_b64_tr_b16 a[210:211], v232 offset:5248\n"
        "v_mfma_f32_16x16x32_bf16 v[64:67], a[212:215], v[156:159], v[64:67]\n"
        "v_mfma_f32_16x16x32_bf16 v[188:191], a[212:215], v[160:163], v[188:191]\n"
        "ds_read_b64_tr_b16 a[212:213], v235 offset:160\n"
        "v_mfma_f32_16x16x32_bf16 a[24:27], a[216:219], v[148:151], a[24:27]\n"
        "ds_read_b64_tr_b16 a[214:215], v235 offset:5280\n"
        "v_mfma_f32_16x16x32_bf16 v[28:31], a[216:219], v[152:155], v[28:31]\n"
        "v_mfma_f32_16x16x32_bf16 v[68:71], a[216:219], v[156:159], v[68:71]\n"
        "v_mfma_f32_16x16x32_bf16 v[192:195], a[216:219], v[160:163], v[192:195]\n"
        "ds_read_b64_tr_b16 a[216:217], v234 offset:192\n"
        "v_mfma_f32_16x16x32_bf16 a[28:31], a[220:223], v[148:151], a[28:31]\n"
        "ds_read_b64_tr_b16 a[218:219], v234 offset:5312\n"
        "v_mfma_f32_16x16x32_bf16 v[32:35], a[220:223], v[152:155], v[32:35]\n"
        "v_mfma_f32_16x16x32_bf16 v[72:75], a[220:223], v[156:159], v[72:75]\n"
        "v_mfma_f32_16x16x32_bf16 v[196:199], a[220:223], v[160:163], v[196:199]\n"
        "ds_read_b64_tr_b16 a[220:221], v235 offset:224\n"
        "v_mfma_f32_16x16x32_bf16 a[32:35], a[224:227], v[148:151], a[32:35]\n"
        "ds_read_b64_tr_b16 a[222:223], v235 offset:5344\n"
        "v_mfma_f32_16x16x32_bf16 v[36:39], a[224:227], v[152:155], v[36:39]\n"
        "v_mfma_f32_16x16x32_bf16 v[76:79], a[224:227], v[156:159], v[76:79]\n"
        "v_mfma_f32_16x16x32_bf16 v[200:203], a[224:227], v[160:163], v[200:203]\n"
        "ds_read_b64_tr_b16 a[224:225], v234 offset:256\n"
        "v_mfma_f32_16x16x32_bf16 a[36:39], a[228:231], v[148:151], a[36:39]\n"
        "buffer_load_dwordx4 v[148:151], v236, s[56:59], s55 offen offset:3072 sc1\n"
        "v_mfma_f32_16x16x32_bf16 v[40:43], a[228:231], v[152:155], v[40:43]\n"
        "ds_read_b64_tr_b16 a[226:227], v234 offset:5376\n"
        "v_mfma_f32_16x16x32_bf16 v[80:83], a[228:231], v[156:159], v[80:83]\n"
        "s_waitcnt vmcnt(26)\n"
        "v_mfma_f32_16x16x32_bf16 v[204:207], a[228:231], v[160:163], v[204:207]\n"
        "buffer_load_dwordx4 v[152:155], v237, s[56:59], s55 offen offset:3072 sc1\n"
        "s_sub_u32 s93, s93, 1\n"
        "s_cbranch_scc0 .Lk_BODY\n"
        "v_mfma_f32_16x16x32_bf16 a[0:3], a[192:195], v[88:91], a[0:3]\n"
        "s_waitcnt vmcnt(26)\n"
        "ds_read_b64_tr_b16 a[228:229], v235 offset:288\n"
        "v_mfma_f32_16x16x32_bf16 v[4:7], a[192:195], v[92:95], v[4:7]\n"
        "s_waitcnt vmcnt(21)\n"
        "ds_read_b64_tr_b16 a[230:231], v235 offset:5408\n"
        "v_mfma_f32_16x16x32_bf16 v[44:47], a[192:195], v[96:99], v[44:47]\n"
        "buffer_load_dwordx4 v[156:159], v238, s[56:59], s55 offen offset:3072 sc1\n"
        "v_mfma_f32_16x16x32_bf16 v[168:171], a[192:195], v[100:103], v[168:171]\n"
        "v_mfma_f32_16x16x32_bf16 v[208:211], a[192:195], v[104:107], v[208:211]\n"
        "s_waitcnt lgkmcnt(15)\n"
        "ds_read_b64_tr_b16 a[192:193], v232 offset:10240\n"
        "v_mfma_f32_16x16x32_bf16 a[4:7], a[196:199], v[88:91], a[4:7]\n"
        "buffer_load_dwordx4 v[160:163], v239, s[56:59], s55 offen offset:3072 sc1\n"
        "v_mfma_f32_16x16x32_bf16 v[8:11], a[196:199], v[92:95], v[8:11]\n"
        "ds_read_b64_tr_b16 a[194:195], v232 offset:15360\n"
        "v_mfma_f32_16x16x32_bf16 v[48:51], a[196:199], v[96:99], v[48:51]\n"
        "v_mfma_f32_16x16x32_bf16 v[172:175], a[196:199], v[100:103], v[172:175]\n"
        "v_mfma_f32_16x16x32_bf16 v[212:215], a[196:199], v[104:107], v[212:215]\n"
        "s_waitcnt lgkmcnt(2)\n"
        "ds_read_b64_tr_b16 a[196:197], v233 offset:10272\n"
        "v_mfma_f32_16x16x32_bf16 a[8:11], a[200:203], v[88:91], a[8:11]\n"
        "ds_read_b64_tr_b16 a[198:199], v233 offset:15392\n"
        "v_mfma_f32_16x16x32_bf16 v[12:15], a[200:203], v[92:95], v[12:15]\n"
        "v_mfma_f32_16x16x32_bf16 v[52:55], a[200:203], v[96:99], v[52:55]\n"
        "v_mfma_f32_16x16x32_bf16 v[176:179], a[200:203], v[100:103], v[176:179]\n"
        "v_mfma_f32_16x16x32_bf16 v[216:219], a[200:203], v[104:107], v[216:219]\n"
        "ds_read_b64_tr_b16 a[200:201], v232 offset:10304\n"
        "v_mfma_f32_16x16x32_bf16 a[12:15], a[204:207], v[88:91], a[12:15]\n"
        "ds_read_b64_tr_b16 a[202:203], v232 offset:15424\n"
        "v_mfma_f32_16x16x32_bf16 v[16:19], a[204:207], v[92:95], v[16:19]\n"
        "v_mfma_f32_16x16x32_bf16 v[56:59], a[204:207], v[96:99], v[56:59]\n"
        "v_mfma_f32_16x16x32_bf16 v[180:183], a[204:207], v[100:103], v[180:183]\n"
        "v_mfma_f32_16x16x32_bf16 v[220:223], a[204:207], v[104:107], v[220:223]\n"
        "ds_read_b64_tr_b16 a[204:205], v233 offset:10336\n"
        "v_mfma_f32_16x16x32_bf16 a[16:19], a[208:211], v[88:91], a[16:19]\n"
        "ds_read_b64_tr_b16 a[206:207], v233 offset:15456\n"
        "v_mfma_f32_16x16x32_bf16 v[20:23], a[208:211], v[92:95], v[20:23]\n"
        "v_mfma_f32_16x16x32_bf16 v[60:63], a[208:211], v[96:99], v[60:63]\n"
        "v_mfma_f32_16x16x32_bf16 v[184:187], a[208:211], v[100:103], v[184:187]\n"
        "v_mfma_f32_16x16x32_bf16 v[224:227], a[208:211], v[104:107], v[224:227]\n"
        "ds_read_b64_tr_b16 a[208:209], v232 offset:10368\n"
        "v_mfma_f32_16x16x32_bf16 a[20:23], a[212:215], v[88:91], a[20:23]\n"
        "ds_read_b64_tr_b16 a[210:211], v232 offset:15488\n"
        "v_mfma_f32_16x16x32_bf16 v[24:27], a[212:215], v[92:95], v[24:27]\n"
        "v_mfma_f32_16x16x32_bf16 v[64:67], a[212:215], v[96:99], v[64:67]\n"
        "v_mfma_f32_16x16x32_bf16 v[188:191], a[212:215], v[100:103], v[188:191]\n"
        "ds_read_b64_tr_b16 a[212:213], v235 offset:10400\n"
        "v_mfma_f32_16x16x32_bf16 a[24:27], a[216:219], v[88:91], a[24:27]\n"
        "ds_read_b64_tr_b16 a[214:215], v235 offset:15520\n"
        "v_mfma_f32_16x16x32_bf16 v[28:31], a[216:219], v[92:95], v[28:31]\n"
        "v_mfma_f32_16x16x32_bf16 v[68:71], a[216:219], v[96:99], v[68:71]\n"
        "v_mfma_f32_16x16x32_bf16 v[192:195], a[216:219], v[100:103], v[192:195]\n"
        "ds_read_b64_tr_b16 a[216:217], v234 offset:10432\n"
        "v_mfma_f32_16x16x32_bf16 a[28:31], a[220:223], v[88:91], a[28:31]\n"
        "ds_read_b64_tr_b16 a[218:219], v234 offset:15552\n"
        "v_mfma_f32_16x16x32_bf16 v[32:35], a[220:223], v[92:95], v[32:35]\n"
        "v_mfma_f32_16x16x32_bf16 v[72:75], a[220:223], v[96:99], v[72:75]\n"
        "v_mfma_f32_16x16x32_bf16 v[196:199], a[220:223], v[100:103], v[196:199]\n"
        "ds_read_b64_tr_b16 a[220:221], v235 offset:10464\n"
        "v_mfma_f32_16x16x32_bf16 a[32:35], a[224:227], v[88:91], a[32:35]\n"
        "ds_read_b64_tr_b16 a[222:223], v235 offset:15584\n"
        "v_mfma_f32_16x16x32_bf16 v[36:39], a[224:227], v[92:95], v[36:39]\n"
        "v_mfma_f32_16x16x32_bf16 v[76:79], a[224:227], v[96:99], v[76:79]\n"
        "v_mfma_f32_16x16x32_bf16 v[200:203], a[224:227], v[100:103], v[200:203]\n"
        "ds_read_b64_tr_b16 a[224:225], v234 offset:10496\n"
        "v_mfma_f32_16x16x32_bf16 a[36:39], a[228:231], v[88:91], a[36:39]\n"
        "ds_read_b64_tr_b16 a[226:227], v234 offset:15616\n"
        "v_mfma_f32_16x16x32_bf16 v[40:43], a[228:231], v[92:95], v[40:43]\n"
        "v_mfma_f32_16x16x32_bf16 v[80:83], a[228:231], v[96:99], v[80:83]\n"
        "v_mfma_f32_16x16x32_bf16 v[204:207], a[228:231], v[100:103], v[204:207]\n"
        "s_waitcnt vmcnt(18) lgkmcnt(2)\n"
        "ds_read_b64_tr_b16 a[228:229], v235 offset:10528\n"
        "v_mfma_f32_16x16x32_bf16 a[0:3], a[192:195], v[108:111], a[0:3]\n"
        "s_waitcnt vmcnt(17)\n"
        "ds_read_b64_tr_b16 a[230:231], v235 offset:15648\n"
        "v_mfma_f32_16x16x32_bf16 v[4:7], a[192:195], v[112:115], v[4:7]\n"
        "s_waitcnt vmcnt(16)\n"
        "s_barrier\n"
        "v_mfma_f32_16x16x32_bf16 v[44:47], a[192:195], v[116:119], v[44:47]\n"
        "s_waitcnt vmcnt(15)\n"
        "ds_read_b64_tr_b16 a[120:121], v232 offset:30720\n"
        "v_mfma_f32_16x16x32_bf16 v[168:171], a[192:195], v[120:123], v[168:171]\n"
        "ds_read_b64_tr_b16 a[122:123], v232 offset:35840\n"
        "v_mfma_f32_16x16x32_bf16 v[208:211], a[192:195], v[124:127], v[208:211]\n"
        "ds_read_b64_tr_b16 a[192:193], v232 offset:20480\n"
        "v_mfma_f32_16x16x32_bf16 a[4:7], a[196:199], v[108:111], a[4:7]\n"
        "ds_read_b64_tr_b16 a[194:195], v232 offset:25600\n"
        "v_mfma_f32_16x16x32_bf16 v[8:11], a[196:199], v[112:115], v[8:11]\n"
        "ds_read_b64_tr_b16 a[124:125], v233 offset:30752\n"
        "v_mfma_f32_16x16x32_bf16 v[48:51], a[196:199], v[116:119], v[48:51]\n"
        "ds_read_b64_tr_b16 a[126:127], v233 offset:35872\n"
        "v_mfma_f32_16x16x32_bf16 v[172:175], a[196:199], v[120:123], v[172:175]\n"
        "ds_read_b64_tr_b16 a[128:129], v232 offset:30784\n"
        "v_mfma_f32_16x16x32_bf16 v[212:215], a[196:199], v[124:127], v[212:215]\n"
        "ds_read_b64_tr_b16 a[196:197], v233 offset:20512\n"
        "v_mfma_f32_16x16x32_bf16 a[8:11], a[200:203], v[108:111], a[8:11]\n"
        "ds_read_b64_tr_b16 a[198:199], v233 offset:25632\n"
        "v_mfma_f32_16x16x32_bf16 v[12:15], a[200:203], v[112:115], v[12:15]\n"
        "ds_read_b64_tr_b16 a[130:131], v232 offset:35904\n"
        "v_mfma_f32_16x16x32_bf16 v[52:55], a[200:203], v[116:119], v[52:55]\n"
        "ds_read_b64_tr_b16 a[132:133], v233 offset:30816\n"
        "v_mfma_f32_16x16x32_bf16 v[176:179], a[200:203], v[120:123], v[176:179]\n"
        "ds_read_b64_tr_b16 a[134:135], v233 offset:35936\n"
        "v_mfma_f32_16x16x32_bf16 v[216:219], a[200:203], v[124:127], v[216:219]\n"
        "ds_read_b64_tr_b16 a[200:201], v232 offset:20544\n"
        "v_mfma_f32_16x16x32_bf16 a[12:15], a[204:207], v[108:111], a[12:15]\n"
        "ds_read_b64_tr_b16 a[202:203], v232 offset:25664\n"
        "v_mfma_f32_16x16x32_bf16 v[16:19], a[204:207], v[112:115], v[16:19]\n"
        "ds_read_b64_tr_b16 a[136:137], v232 offset:30848\n"
        "v_mfma_f32_16x16x32_bf16 v[56:59], a[204:207], v[116:119], v[56:59]\n"
        "ds_read_b64_tr_b16 a[138:139], v232 offset:35968\n"
        "v_mfma_f32_16x16x32_bf16 v[180:183], a[204:207], v[120:123], v[180:183]\n"
        "ds_read_b64_tr_b16 a[140:141], v235 offset:30880\n"
        "v_mfma_f32_16x16x32_bf16 v[220:223], a[204:207], v[124:127], v[220:223]\n"
        "ds_read_b64_tr_b16 a[204:205], v233 offset:20576\n"
        "v_mfma_f32_16x16x32_bf16 a[16:19], a[208:211], v[108:111], a[16:19]\n"
        "ds_read_b64_tr_b16 a[206:207], v233 offset:25696\n"
        "v_mfma_f32_16x16x32_bf16 v[20:23], a[208:211], v[112:115], v[20:23]\n"
        "ds_read_b64_tr_b16 a[142:143], v235 offset:36000\n"
        "v_mfma_f32_16x16x32_bf16 v[60:63], a[208:211], v[116:119], v[60:63]\n"
        "ds_read_b64_tr_b16 a[144:145], v234 offset:30912\n"
        "v_mfma_f32_16x16x32_bf16 v[184:187], a[208:211], v[120:123], v[184:187]\n"
        "ds_read_b64_tr_b16 a[146:147], v234 offset:36032\n"
        "v_mfma_f32_16x16x32_bf16 v[224:227], a[208:211], v[124:127], v[224:227]\n"
        "ds_read_b64_tr_b16 a[208:209], v232 offset:20608\n"
        "v_mfma_f32_16x16x32_bf16 a[20:23], a[212:215], v[108:111], a[20:23]\n"
        "ds_read_b64_tr_b16 a[210:211], v232 offset:25728\n"
        "v_mfma_f32_16x16x32_bf16 v[24:27], a[212:215], v[112:115], v[24:27]\n"
        "ds_read_b64_tr_b16 a[148:149], v235 offset:30944\n"
        "v_mfma_f32_16x16x32_bf16 v[64:67], a[212:215], v[116:119], v[64:67]\n"
        "ds_read_b64_tr_b16 a[150:151], v235 offset:36064\n"
        "v_mfma_f32_16x16x32_bf16 v[188:191], a[212:215], v[120:123], v[188:191]\n"
        "ds_read_b64_tr_b16 a[212:213], v235 offset:20640\n"
        "v_mfma_f32_16x16x32_bf16 a[24:27], a[216:219], v[108:111], a[24:27]\n"
        "ds_read_b64_tr_b16 a[214:215], v235 offset:25760\n"
        "v_mfma_f32_16x16x32_bf16 v[28:31], a[216:219], v[112:115], v[28:31]\n"
        "ds_read_b64_tr_b16 a[152:153], v234 offset:30976\n"
        "v_mfma_f32_16x16x32_bf16 v[68:71], a[216:219], v[116:119], v[68:71]\n"
        "ds_read_b64_tr_b16 a[154:155], v234 offset:36096\n"
        "v_mfma_f32_16x16x32_bf16 v[192:195], a[216:219], v[120:123], v[192:195]\n"
        "ds_read_b64_tr_b16 a[216:217], v234 offset:20672\n"
        "v_mfma_f32_16x16x32_bf16 a[28:31], a[220:223], v[108:111], a[28:31]\n"
        "ds_read_b64_tr_b16 a[218:219], v234 offset:25792\n"
        "v_mfma_f32_16x16x32_bf16 v[32:35], a[220:223], v[112:115], v[32:35]\n"
        "ds_read_b64_tr_b16 a[156:157], v235 offset:31008\n"
        "v_mfma_f32_16x16x32_bf16 v[72:75], a[220:223], v[116:119], v[72:75]\n"
        "ds_read_b64_tr_b16 a[158:159], v235 offset:36128\n"
        "v_mfma_f32_16x16x32_bf16 v[196:199], a[220:223], v[120:123], v[196:199]\n"
        "s_waitcnt lgkmcnt(6)\n"
        "ds_read_b64_tr_b16 a[220:221], v235 offset:20704\n"
        "v_mfma_f32_16x16x32_bf16 a[32:35], a[224:227], v[108:111], a[32:35]\n"
        "ds_read_b64_tr_b16 a[222:223], v235 offset:25824\n"
        "v_mfma_f32_16x16x32_bf16 v[36:39], a[224:227], v[112:115], v[36:39]\n"
        "v_mfma_f32_16x16x32_bf16 v[76:79], a[224:227], v[116:119], v[76:79]\n"
        "v_mfma_f32_16x16x32_bf16 v[200:203], a[224:227], v[120:123], v[200:203]\n"
        "ds_read_b64_tr_b16 a[224:225], v234 offset:20736\n"
        "v_mfma_f32_16x16x32_bf16 a[36:39], a[228:231], v[108:111], a[36:39]\n"
        "ds_read_b64_tr_b16 a[226:227], v234 offset:25856\n"
        "v_mfma_f32_16x16x32_bf16 v[40:43], a[228:231], v[112:115], v[40:43]\n"
        "v_mfma_f32_16x16x32_bf16 v[80:83], a[228:231], v[116:119], v[80:83]\n"
        "v_mfma_f32_16x16x32_bf16 v[204:207], a[228:231], v[120:123], v[204:207]\n"
        "s_waitcnt vmcnt(12)\n"
        "ds_read_b64_tr_b16 a[228:229], v235 offset:20768\n"
        "v_mfma_f32_16x16x32_bf16 v[4:7], a[192:195], v[132:135], v[4:7]\n"
        "s_waitcnt vmcnt(2)\n"
        "ds_read_b64_tr_b16 a[230:231], v235 offset:25888\n"
        "v_mfma_f32_16x16x32_bf16 v[4:7], a[120:123], v[152:155], v[4:7]\n"
        "v_mfma_f32_16x16x32_bf16 v[44:47], a[192:195], v[136:139], v[44:47]\n"
        "s_waitcnt vmcnt(1)\n"
        "v_mfma_f32_16x16x32_bf16 v[44:47], a[120:123], v[156:159], v[44:47]\n"
        "v_mfma_f32_16x16x32_bf16 v[168:171], a[192:195], v[140:143], v[168:171]\n"
        "s_waitcnt vmcnt(0)\n"
        "v_mfma_f32_16x16x32_bf16 v[168:171], a[120:123], v[160:163], v[168:171]\n"
        "v_mfma_f32_16x16x32_bf16 v[208:211], a[192:195], v[144:147], v[208:211]\n"
        "v_mfma_f32_16x16x32_bf16 v[208:211], a[120:123], v[164:167], v[208:211]\n"
        "v_mfma_f32_16x16x32_bf16 a[0:3], a[192:195], v[128:131], a[0:3]\n"
        "v_mfma_f32_16x16x32_bf16 a[0:3], a[120:123], v[148:151], a[0:3]\n"
        "v_cvt_pk_bf16_f32 v4, v4, v5\n"
        "v_cvt_pk_bf16_f32 v5, v6, v7\n"
        "v_mfma_f32_16x16x32_bf16 v[8:11], a[196:199], v[132:135], v[8:11]\n"
        "v_cvt_pk_bf16_f32 v44, v44, v45\n"
        "v_cvt_pk_bf16_f32 v45, v46, v47\n"
        "v_mfma_f32_16x16x32_bf16 v[8:11], a[124:127], v[152:155], v[8:11]\n"
        "v_cvt_pk_bf16_f32 v168, v168, v169\n"
        "v_cvt_pk_bf16_f32 v169, v170, v171\n"
        "v_mfma_f32_16x16x32_bf16 v[48:51], a[196:199], v[136:139], v[48:51]\n"
        "v_cvt_pk_bf16_f32 v84, v208, v209\n"
        "v_cvt_pk_bf16_f32 v85, v210, v211\n"
        "v_mfma_f32_16x16x32_bf16 v[48:51], a[124:127], v[156:159], v[48:51]\n"
        "v_mfma_f32_16x16x32_bf16 v[172:175], a[196:199], v[140:143], v[172:175]\n"
        "v_mfma_f32_16x16x32_bf16 v[172:175], a[124:127], v[160:163], v[172:175]\n"
        "v_cvt_pk_bf16_f32 v6, v8, v9\n"
        "v_cvt_pk_bf16_f32 v7, v10, v11\n"
        "v_mfma_f32_16x16x32_bf16 v[212:215], a[196:199], v[144:147], v[212:215]\n"
        "buffer_store_dwordx4 v[4:7], v228, s[40:43], s60 offen offset:0 sc1\n"
        "v_mfma_f32_16x16x32_bf16 v[212:215], a[124:127], v[164:167], v[212:215]\n"
        "v_mfma_f32_16x16x32_bf16 a[4:7], a[196:199], v[128:131], a[4:7]\n"
        "v_cvt_pk_bf16_f32 v46, v48, v49\n"
        "v_cvt_pk_bf16_f32 v47, v50, v51\n"
        "v_mfma_f32_16x16x32_bf16 a[4:7], a[124:127], v[148:151], a[4:7]\n"
        "v_cvt_pk_bf16_f32 v170, v172, v173\n"
        "v_cvt_pk_bf16_f32 v171, v174, v175\n"
        "v_mfma_f32_16x16x32_bf16 v[12:15], a[200:203], v[132:135], v[12:15]\n"
        "buffer_store_dwordx4 v[44:47], v228, s[40:43], s61 offen offset:0 sc1\n"
        "v_mfma_f32_16x16x32_bf16 v[12:15], a[128:131], v[152:155], v[12:15]\n"
        "v_cvt_pk_bf16_f32 v86, v212, v213\n"
        "v_cvt_pk_bf16_f32 v87, v214, v215\n"
        "v_mfma_f32_16x16x32_bf16 v[52:55], a[200:203], v[136:139], v[52:55]\n"
        "v_mfma_f32_16x16x32_bf16 v[52:55], a[128:131], v[156:159], v[52:55]\n"
        "buffer_store_dwordx4 v[168:171], v228, s[40:43], s62 offen offset:0 sc1\n"
        "v_mfma_f32_16x16x32_bf16 v[176:179], a[200:203], v[140:143], v[176:179]\n"
        "v_mfma_f32_16x16x32_bf16 v[176:179], a[128:131], v[160:163], v[176:179]\n"
        "v_mfma_f32_16x16x32_bf16 v[216:219], a[200:203], v[144:147], v[216:219]\n"
        "v_cvt_pk_bf16_f32 v12, v12, v13\n"
        "v_cvt_pk_bf16_f32 v13, v14, v15\n"
        "v_mfma_f32_16x16x32_bf16 v[216:219], a[128:131], v[164:167], v[216:219]\n"
        "buffer_store_dwordx4 v[84:87], v229, s[48:51], s63 offen offset:1024 sc1\n"
        "v_mfma_f32_16x16x32_bf16 a[8:11], a[200:203], v[128:131], a[8:11]\n"
        "v_cvt_pk_bf16_f32 v52, v52, v53\n"
        "v_cvt_pk_bf16_f32 v53, v54, v55\n"
        "v_mfma_f32_16x16x32_bf16 a[8:11], a[128:131], v[148:151], a[8:11]\n"
        "v_cvt_pk_bf16_f32 v176, v176, v177\n"
        "v_cvt_pk_bf16_f32 v177, v178, v179\n"
        "v_mfma_f32_16x16x32_bf16 v[16:19], a[204:207], v[132:135], v[16:19]\n"
        "v_cvt_pk_bf16_f32 v84, v216, v217\n"
        "v_cvt_pk_bf16_f32 v85, v218, v219\n"
        "v_mfma_f32_16x16x32_bf16 v[16:19], a[132:135], v[152:155], v[16:19]\n"
        "v_mfma_f32_16x16x32_bf16 v[56:59], a[204:207], v[136:139], v[56:59]\n"
        "v_mfma_f32_16x16x32_bf16 v[56:59], a[132:135], v[156:159], v[56:59]\n"
        "v_mfma_f32_16x16x32_bf16 v[180:183], a[204:207], v[140:143], v[180:183]\n"
        "v_mfma_f32_16x16x32_bf16 v[180:183], a[132:135], v[160:163], v[180:183]\n"
        "v_mfma_f32_16x16x32_bf16 v[220:223], a[204:207], v[144:147], v[220:223]\n"
        "v_mfma_f32_16x16x32_bf16 v[220:223], a[132:135], v[164:167], v[220:223]\n"
        "v_mfma_f32_16x16x32_bf16 a[12:15], a[204:207], v[128:131], a[12:15]\n"
        "v_mfma_f32_16x16x32_bf16 a[12:15], a[132:135], v[148:151], a[12:15]\n"
        "v_cvt_pk_bf16_f32 v14, v16, v17\n"
        "v_cvt_pk_bf16_f32 v15, v18, v19\n"
        "v_mfma_f32_16x16x32_bf16 v[20:23], a[208:211], v[132:135], v[20:23]\n"
        "v_cvt_pk_bf16_f32 v54, v56, v57\n"
        "v_cvt_pk_bf16_f32 v55, v58, v59\n"
        "v_mfma_f32_16x16x32_bf16 v[20:23], a[136:139], v[152:155], v[20:23]\n"
        "v_cvt_pk_bf16_f32 v178, v180, v181\n"
        "v_cvt_pk_bf16_f32 v179, v182, v183\n"
        "v_mfma_f32_16x16x32_bf16 v[60:63], a[208:211], v[136:139], v[60:63]\n"
        "buffer_store_dwordx4 v[12:15], v228, s[40:43], s60 offen offset:1024 sc1\n"
        "v_mfma_f32_16x16x32_bf16 v[60:63], a[136:139], v[156:159], v[60:63]\n"
        "v_cvt_pk_bf16_f32 v86, v220, v221\n"
        "v_cvt_pk_bf16_f32 v87, v222, v223\n"
        "v_mfma_f32_16x16x32_bf16 v[184:187], a[208:211], v[140:143], v[184:187]\n"
        "v_cvt_pk_bf16_f32 v20, v20, v21\n"
        "v_cvt_pk_bf16_f32 v21, v22, v23\n"
        "v_mfma_f32_16x16x32_bf16 v[184:187], a[136:139], v[160:163], v[184:187]\n"
        "buffer_store_dwordx4 v[52:55], v228, s[40:43], s61 offen offset:1024 sc1\n"
        "v_mfma_f32_16x16x32_bf16 v[224:227], a[208:211], v[144:147], v[224:227]\n"
        "v_cvt_pk_bf16_f32 v60, v60, v61\n"
        "v_cvt_pk_bf16_f32 v61, v62, v63\n"
        "v_mfma_f32_16x16x32_bf16 v[224:227], a[136:139], v[164:167], v[224:227]\n"
        "v_mfma_f32_16x16x32_bf16 a[16:19], a[208:211], v[128:131], a[16:19]\n"
        "buffer_store_dwordx4 v[176:179], v228, s[40:43], s62 offen offset:1024 sc1\n"
        "v_mfma_f32_16x16x32_bf16 a[16:19], a[136:139], v[148:151], a[16:19]\n"
        "v_cvt_pk_bf16_f32 v184, v184, v185\n"
        "v_cvt_pk_bf16_f32 v185, v186, v187\n"
        "v_mfma_f32_16x16x32_bf16 v[24:27], a[212:215], v[132:135], v[24:27]\n"
        "v_mfma_f32_16x16x32_bf16 v[24:27], a[140:143], v[152:155], v[24:27]\n"
        "buffer_store_dwordx4 v[84:87], v229, s[48:51], s63 offen offset:2048 sc1\n"
        "v_mfma_f32_16x16x32_bf16 v[64:67], a[212:215], v[136:139], v[64:67]\n"
        "v_mfma_f32_16x16x32_bf16 v[64:67], a[140:143], v[156:159], v[64:67]\n"
        "v_cvt_pk_bf16_f32 v84, v224, v225\n"
        "v_cvt_pk_bf16_f32 v85, v226, v227\n"
        "v_mfma_f32_16x16x32_bf16 v[188:191], a[212:215], v[140:143], v[188:191]\n"
        "buffer_store_dwordx4 v[84:87], v229, s[48:51], s63 offen offset:3072 sc1\n"
        "v_mfma_f32_16x16x32_bf16 v[188:191], a[140:143], v[160:163], v[188:191]\n"
        "v_cvt_pk_bf16_f32 v22, v24, v25\n"
        "v_cvt_pk_bf16_f32 v23, v26, v27\n"
        "v_mfma_f32_16x16x32_bf16 a[20:23], a[212:215], v[128:131], a[20:23]\n"
        "s_waitcnt lgkmcnt(0)\n"
        "v_cvt_pk_bf16_f32 v62, v64, v65\n"
        "v_cvt_pk_bf16_f32 v63, v66, v67\n"
        "v_mfma_f32_16x16x32_bf16 a[20:23], a[140:143], v[148:151], a[20:23]\n"
        "buffer_store_dwordx4 v[20:23], v228, s[40:43], s60 offen offset:2048 sc1\n"
        "v_mfma_f32_16x16x32_bf16 v[28:31], a[216:219], v[132:135], v[28:31]\n"
        "v_cvt_pk_bf16_f32 v186, v188, v189\n"
        "v_cvt_pk_bf16_f32 v187, v190, v191\n"
        "v_mfma_f32_16x16x32_bf16 v[28:31], a[144:147], v[152:155], v[28:31]\n"
        "v_mfma_f32_16x16x32_bf16 v[68:71], a[216:219], v[136:139], v[68:71]\n"
        "buffer_store_dwordx4 v[60:63], v228, s[40:43], s61 offen offset:2048 sc1\n"
        "v_mfma_f32_16x16x32_bf16 v[68:71], a[144:147], v[156:159], v[68:71]\n"
        "v_mfma_f32_16x16x32_bf16 v[192:195], a[216:219], v[140:143], v[192:195]\n"
        "v_mfma_f32_16x16x32_bf16 v[192:195], a[144:147], v[160:163], v[192:195]\n"
        "buffer_store_dwordx4 v[184:187], v228, s[40:43], s62 offen offset:2048 sc1\n"
        "v_mfma_f32_16x16x32_bf16 a[24:27], a[216:219], v[128:131], a[24:27]\n"
        "v_mfma_f32_16x16x32_bf16 a[24:27], a[144:147], v[148:151], a[24:27]\n"
        "v_cvt_pk_bf16_f32 v28, v28, v29\n"
        "v_cvt_pk_bf16_f32 v29, v30, v31\n"
        "v_mfma_f32_16x16x32_bf16 v[32:35], a[220:223], v[132:135], v[32:35]\n"
        "v_cvt_pk_bf16_f32 v68, v68, v69\n"
        "v_cvt_pk_bf16_f32 v69, v70, v71\n"
        "v_mfma_f32_16x16x32_bf16 v[32:35], a[148:151], v[152:155], v[32:35]\n"
        "v_cvt_pk_bf16_f32 v192, v192, v193\n"
        "v_cvt_pk_bf16_f32 v193, v194, v195\n"
        "v_mfma_f32_16x16x32_bf16 v[72:75], a[220:223], v[136:139], v[72:75]\n"
        "v_mfma_f32_16x16x32_bf16 v[72:75], a[148:151], v[156:159], v[72:75]\n"
        "v_mfma_f32_16x16x32_bf16 v[196:199], a[220:223], v[140:143], v[196:199]\n"
        "v_mfma_f32_16x16x32_bf16 v[196:199], a[148:151], v[160:163], v[196:199]\n"
        "v_mfma_f32_16x16x32_bf16 a[28:31], a[220:223], v[128:131], a[28:31]\n"
        "v_mfma_f32_16x16x32_bf16 a[28:31], a[148:151], v[148:151], a[28:31]\n"
        "v_cvt_pk_bf16_f32 v30, v32, v33\n"
        "v_cvt_pk_bf16_f32 v31, v34, v35\n"
        "v_mfma_f32_16x16x32_bf16 v[36:39], a[224:227], v[132:135], v[36:39]\n"
        "buffer_store_dwordx4 v[28:31], v228, s[40:43], s60 offen offset:3072 sc1\n"
        "v_mfma_f32_16x16x32_bf16 v[36:39], a[152:155], v[152:155], v[36:39]\n"
        "v_cvt_pk_bf16_f32 v70, v72, v73\n"
        "v_cvt_pk_bf16_f32 v71, v74, v75\n"
        "v_mfma_f32_16x16x32_bf16 v[76:79], a[224:227], v[136:139], v[76:79]\n"
        "v_cvt_pk_bf16_f32 v194, v196, v197\n"
        "v_cvt_pk_bf16_f32 v195, v198, v199\n"
        "v_mfma_f32_16x16x32_bf16 v[76:79], a[152:155], v[156:159], v[76:79]\n"
        "buffer_store_dwordx4 v[68:71], v228, s[40:43], s61 offen offset:3072 sc1\n"
        "v_mfma_f32_16x16x32_bf16 v[200:203], a[224:227], v[140:143], v[200:203]\n"
        "v_cvt_pk_bf16_f32 v36, v36, v37\n"
        "v_cvt_pk_bf16_f32 v37, v38, v39\n"
        "v_mfma_f32_16x16x32_bf16 v[200:203], a[152:155], v[160:163], v[200:203]\n"
        "v_mfma_f32_16x16x32_bf16 v[40:43], a[228:231], v[132:135], v[40:43]\n"
        "buffer_store_dwordx4 v[192:195], v228, s[40:43], s62 offen offset:3072 sc1\n"
        "v_mfma_f32_16x16x32_bf16 v[40:43], a[156:159], v[152:155], v[40:43]\n"
        "v_cvt_pk_bf16_f32 v76, v76, v77\n"
        "v_cvt_pk_bf16_f32 v77, v78, v79\n"
        "v_mfma_f32_16x16x32_bf16 v[80:83], a[228:231], v[136:139], v[80:83]\n"
        "v_mfma_f32_16x16x32_bf16 v[80:83], a[156:159], v[156:159], v[80:83]\n"
        "v_mfma_f32_16x16x32_bf16 v[204:207], a[228:231], v[140:143], v[204:207]\n"
        "v_cvt_pk_bf16_f32 v200, v200, v201\n"
        "v_cvt_pk_bf16_f32 v201, v202, v203\n"
        "v_mfma_f32_16x16x32_bf16 v[204:207], a[156:159], v[160:163], v[204:207]\n"
        "v_cvt_pk_bf16_f32 v38, v40, v41\n"
        "v_cvt_pk_bf16_f32 v39, v42, v43\n"
        "v_mfma_f32_16x16x32_bf16 a[32:35], a[224:227], v[128:131], a[32:35]\n"
        "buffer_store_dwordx4 v[36:39], v229, s[40:43], s60 offen offset:0 sc1\n"
        "v_mfma_f32_16x16x32_bf16 a[32:35], a[152:155], v[148:151], a[32:35]\n"
        "v_cvt_pk_bf16_f32 v78, v80, v81\n"
        "v_cvt_pk_bf16_f32 v79, v82, v83\n"
        "v_mfma_f32_16x16x32_bf16 a[36:39], a[228:231], v[128:131], a[36:39]\n"
        "v_cvt_pk_bf16_f32 v202, v204, v205\n"
        "v_cvt_pk_bf16_f32 v203, v206, v207\n"
        "v_mfma_f32_16x16x32_bf16 a[36:39], a[156:159], v[148:151], a[36:39]\n"
        "buffer_store_dwordx4 v[76:79], v229, s[40:43], s61 offen offset:0 sc1\n"
        "s_nop 0\n"
        "s_nop 0\n"
        "s_nop 0\n"
        "buffer_store_dwordx4 v[200:203], v229, s[40:43], s62 offen offset:0 sc1\n"
        "s_lshl_b32 s80, %6, 2\n"
        "s_add_u32 s80, s80, 4352\n"
        "s_lshl_b32 s81, %7, 7\n"
        "s_add_u32 s67, s80, s81\n"
        "s_and_b32 s81, s81, 384\n"
        "s_add_u32 s80, s80, s81\n"
        "v_min_u32 v1, 2, v0\n"
        "v_add3_u32 v1, v1, s85, 1\n"
        "v_and_b32 v1, 3, v1\n"
        "v_lshl_add_u32 v243, v1, 9, s80\n"
        "buffer_load_dword v242, v243, s[96:99], 0 offen sc1\n"
        "s_mov_b64 s[48:49], %15\n"
        "s_mov_b32 s50, 735040\n"
        "s_mov_b32 s51, 0x20000\n"
        "v_and_b32 v1, 15, v0\n"
        "v_mul_u32_u24 v1, 2560, v1\n"
        "v_lshrrev_b32 v2, 4, v0\n"
        "v_lshl_add_u32 v232, v2, 3, v1\n"
        "s_mul_i32 s81, s78, 160\n"
        "s_lshl_b32 s80, %6, 2\n"
        "s_add_u32 s80, s80, s85\n"
        "s_mul_i32 s80, s80, 40960\n"
        "s_add_u32 s64, s80, s81\n"
        "s_sub_u32 s65, s80, s81\n"
        "s_lshr_b32 s80, %6, 1\n"
        "s_mul_i32 s80, s80, 40960\n"
        "s_add_u32 s80, s80, 655360\n"
        "s_add_u32 s66, s80, s81\n"
        "s_add_u32 s60, s68, 65536\n"
        "s_add_u32 s61, s68, 131072\n"
        "s_cmp_eq_u32 %6, s85\n"
        "s_cselect_b32 s46, s42, 0\n"
        "s_cselect_b32 s58, s50, 0\n"
        "s_mov_b64 s[44:45], %14\n"
        "s_mov_b32 s47, 0x20000\n"
        "s_mov_b64 s[56:57], %15\n"
        "v_mov_b32 v236, 0\n"
        "v_mov_b32 v237, 0\n"
        "v_mov_b32 v240, 0\n"
        "v_mov_b32 v241, 0\n"
        "v_bfe_u32 v3, v0, 2, 2\n"
        "v_cmp_eq_u32 vcc, v2, v3\n"
        "v_and_b32 v3, 1, v0\n"
        "v_lshlrev_b32 v3, 4, v3\n"
        "v_mov_b32 v2, 0x3f80\n"
        "v_lshlrev_b32 v2, v3, v2\n"
        "v_cndmask_b32 v2, 0, v2, vcc\n"
        "v_and_b32 v3, 2, v0\n"
        "v_cmp_eq_u32 vcc, 0, v3\n"
        "v_accvgpr_read_b32 v88, a0\n"
        "v_accvgpr_read_b32 v89, a1\n"
        "v_accvgpr_read_b32 v90, a2\n"
        "v_accvgpr_read_b32 v91, a3\n"
        "v_accvgpr_read_b32 v92, a4\n"
        "v_accvgpr_read_b32 v93, a5\n"
        "v_accvgpr_read_b32 v94, a6\n"
        "v_accvgpr_read_b32 v95, a7\n"
        "v_accvgpr_read_b32 v96, a8\n"
        "v_accvgpr_read_b32 v97, a9\n"
        "v_accvgpr_read_b32 v98, a10\n"
        "v_accvgpr_read_b32 v99, a11\n"
        "v_accvgpr_read_b32 v100, a12\n"
        "v_accvgpr_read_b32 v101, a13\n"
        "v_accvgpr_read_b32 v102, a14\n"
        "v_accvgpr_read_b32 v103, a15\n"
        "v_accvgpr_read_b32 v104, a16\n"
        "v_accvgpr_read_b32 v105, a17\n"
        "v_accvgpr_read_b32 v106, a18\n"
        "v_accvgpr_read_b32 v107, a19\n"
        "v_accvgpr_read_b32 v108, a20\n"
        "v_accvgpr_read_b32 v109, a21\n"
        "v_accvgpr_read_b32 v110, a22\n"
        "v_accvgpr_read_b32 v111, a23\n"
        "v_accvgpr_read_b32 v112, a24\n"
        "v_accvgpr_read_b32 v113, a25\n"
        "v_accvgpr_read_b32 v114, a26\n"
        "v_accvgpr_read_b32 v115, a27\n"
        "v_accvgpr_read_b32 v116, a28\n"
        "v_accvgpr_read_b32 v117, a29\n"
        "v_accvgpr_read_b32 v118, a30\n"
        "v_accvgpr_read_b32 v119, a31\n"
        "v_accvgpr_read_b32 v120, a32\n"
        "v_accvgpr_read_b32 v121, a33\n"
        "v_accvgpr_read_b32 v122, a34\n"
        "v_accvgpr_read_b32 v123, a35\n"
        "v_accvgpr_read_b32 v124, a36\n"
        "v_accvgpr_read_b32 v125, a37\n"
        "v_accvgpr_read_b32 v126, a38\n"
        "v_accvgpr_read_b32 v127, a39\n"
        "v_cndmask_b32 v238, 0, v2, vcc\n"
        "v_cndmask_b32 v239, v2, v236, vcc\n"
        "v_mov_b32 v1, s95\n"
        "s_mov_b32 s53, 0\n"
        "s_waitcnt vmcnt(1) lgkmcnt(0)\n"
        "buffer_store_dword v1, off, s[96:99], s67 sc1\n"
        "s_waitcnt vmcnt(1)\n"
        ".Lk_POLL:\n"
        "v_cmp_eq_u32 vcc, s95, v242\n"
        "s_andn2_b32 s54, vcc_lo, s53\n"
        "s_or_b32 s53, s53, vcc_lo\n"
        "s_bitcmp1_b32 s54, 0\n"
        "s_cbranch_scc0 .Lk_NOTYET0\n"
        "buffer_load_dwordx4 v[12:15], v228, s[40:43], s61 offen offset:0 sc1\n"
        "buffer_load_dwordx4 v[24:27], v228, s[40:43], s61 offen offset:1024 sc1\n"
        "buffer_load_dwordx4 v[36:39], v228, s[40:43], s61 offen offset:2048 sc1\n"
        "buffer_load_dwordx4 v[48:51], v228, s[40:43], s61 offen offset:3072 sc1\n"
        "buffer_load_dwordx4 v[60:63], v229, s[40:43], s61 offen offset:0 sc1\n"
        "buffer_load_dwordx4 v[72:75], v229, s[44:47], s61 offen offset:1024 sc1\n"
        "buffer_load_dwordx4 v[128:131], v229, s[44:47], s61 offen offset:2048 sc1\n"
        "buffer_load_dwordx4 v[140:143], v229, s[44:47], s61 offen offset:3072 sc1\n"
        ".Lk_NOTYET0:\n"
        "s_bitcmp1_b32 s54, 1\n"
        "s_cbranch_scc0 .Lk_NOTYET1\n"
        "buffer_load_dwordx4 v[8:11], v228, s[40:43], s60 offen offset:0 sc1\n"
        "buffer_load_dwordx4 v[20:23], v228, s[40:43], s60 offen offset:1024 sc1\n"
        "buffer_load_dwordx4 v[32:35], v228, s[40:43], s60 offen offset:2048 sc1\n"
        "buffer_load_dwordx4 v[44:47], v228, s[40:43], s60 offen offset:3072 sc1\n"
        "buffer_load_dwordx4 v[56:59], v229, s[40:43], s60 offen offset:0 sc1\n"
        "buffer_load_dwordx4 v[68:71], v229, s[44:47], s60 offen offset:1024 sc1\n"
        "buffer_load_dwordx4 v[80:83], v229, s[44:47], s60 offen offset:2048 sc1\n"
        "buffer_load_dwordx4 v[136:139], v229, s[44:47], s60 offen offset:3072 sc1\n"
        ".Lk_NOTYET1:\n"
        "s_bitcmp1_b32 s54, 2\n"
        "s_cbranch_scc0 .Lk_NOTYET2\n"
        "buffer_load_dwordx4 v[4:7], v228, s[40:43], s68 offen offset:0 sc1\n"
        "buffer_load_dwordx4 v[16:19], v228, s[40:43], s68 offen offset:1024 sc1\n"
        "buffer_load_dwordx4 v[28:31], v228, s[40:43], s68 offen offset:2048 sc1\n"
        "buffer_load_dwordx4 v[40:43], v228, s[40:43], s68 offen offset:3072 sc1\n"
        "buffer_load_dwordx4 v[52:55], v229, s[40:43], s68 offen offset:0 sc1\n"
        "buffer_load_dwordx4 v[64:67], v229, s[44:47], s68 offen offset:1024 sc1\n"
        "buffer_load_dwordx4 v[76:79], v229, s[44:47], s68 offen offset:2048 sc1\n"
        "buffer_load_dwordx4 v[132:135], v229, s[44:47], s68 offen offset:3072 sc1\n"
        ".Lk_NOTYET2:\n"
        "s_cmp_eq_u32 s53, -1\n"
        "s_cbranch_scc1 .Lk_POLLED\n"
        "buffer_load_dword v242, v243, s[96:99], 0 offen sc1\n"
        "s_sub_u32 s82, s82, 1\n"
        "s_waitcnt vmcnt(0)\n"
        "s_cbranch_scc0 .Lk_POLL\n"
        ".Lk_POLLED:\n"
        "s_waitcnt vmcnt(7)\n"
        "v_mfma_f32_16x16x32_bf16 v[88:91], v[238:241], v[4:7], v[88:91]\n"
        "v_mfma_f32_16x16x32_bf16 v[92:95], v[236:239], v[4:7], v[92:95]\n"
        "v_mfma_f32_16x16x32_bf16 v[88:91], v[238:241], v[8:11], v[88:91]\n"
        "v_mfma_f32_16x16x32_bf16 v[92:95], v[236:239], v[8:11], v[92:95]\n"
        "v_mfma_f32_16x16x32_bf16 v[88:91], v[238:241], v[12:15], v[88:91]\n"
        "v_mfma_f32_16x16x32_bf16 v[92:95], v[236:239], v[12:15], v[92:95]\n"
        "s_waitcnt vmcnt(6)\n"
        "v_mfma_f32_16x16x32_bf16 v[96:99], v[238:241], v[16:19], v[96:99]\n"
        "v_mfma_f32_16x16x32_bf16 v[100:103], v[236:239], v[16:19], v[100:103]\n"
        "v_mfma_f32_16x16x32_bf16 v[96:99], v[238:241], v[20:23], v[96:99]\n"
        "v_mfma_f32_16x16x32_bf16 v[100:103], v[236:239], v[20:23], v[100:103]\n"
        "v_mfma_f32_16x16x32_bf16 v[96:99], v[238:241], v[24:27], v[96:99]\n"
        "v_mfma_f32_16x16x32_bf16 v[100:103], v[236:239], v[24:27], v[100:103]\n"
        "v_cvt_pk_bf16_f32 v88, v88, v89\n"
        "v_cvt_pk_bf16_f32 v89, v90, v91\n"
        "buffer_store_dwordx2 v[88:89], v232, s[48:51], s64 offen offset:0\n"
        "v_cvt_pk_bf16_f32 v92, v92, v93\n"
        "v_cvt_pk_bf16_f32 v93, v94, v95\n"
        "buffer_store_dwordx2 v[92:93], v232, s[48:51], s64 offen offset:32\n"
        "s_waitcnt vmcnt(7)\n"
        "v_mfma_f32_16x16x32_bf16 v[104:107], v[238:241], v[28:31], v[104:107]\n"
        "v_mfma_f32_16x16x32_bf16 v[108:111], v[236:239], v[28:31], v[108:111]\n"
        "v_mfma_f32_16x16x32_bf16 v[104:107], v[238:241], v[32:35], v[104:107]\n"
        "v_mfma_f32_16x16x32_bf16 v[108:111], v[236:239], v[32:35], v[108:111]\n"
        "v_mfma_f32_16x16x32_bf16 v[104:107], v[238:241], v[36:39], v[104:107]\n"
        "v_mfma_f32_16x16x32_bf16 v[108:111], v[236:239], v[36:39], v[108:111]\n"
        "v_cvt_pk_bf16_f32 v96, v96, v97\n"
        "v_cvt_pk_bf16_f32 v97, v98, v99\n"
        "buffer_store_dwordx2 v[96:97], v232, s[48:51], s64 offen offset:64\n"
        "v_cvt_pk_bf16_f32 v100, v100, v101\n"
        "v_cvt_pk_bf16_f32 v101, v102, v103\n"
        "buffer_store_dwordx2 v[100:101], v232, s[48:51], s64 offen offset:96\n"
        "s_waitcnt vmcnt(8)\n"
        "v_mfma_f32_16x16x32_bf16 v[112:115], v[238:241], v[40:43], v[112:115]\n"
        "v_mfma_f32_16x16x32_bf16 v[116:119], v[236:239], v[40:43], v[116:119]\n"
        "v_mfma_f32_16x16x32_bf16 v[112:115], v[238:241], v[44:47], v[112:115]\n"
        "v_mfma_f32_16x16x32_bf16 v[116:119], v[236:239], v[44:47], v[116:119]\n"
        "v_mfma_f32_16x16x32_bf16 v[112:115], v[238:241], v[48:51], v[112:115]\n"
        "v_mfma_f32_16x16x32_bf16 v[116:119], v[236:239], v[48:51], v[116:119]\n"
        "v_cvt_pk_bf16_f32 v104, v104, v105\n"
        "v_cvt_pk_bf16_f32 v105, v106, v107\n"
        "buffer_store_dwordx2 v[104:105], v232, s[48:51], s64 offen offset:128\n"
        "v_cvt_pk_bf16_f32 v108, v108, v109\n"
        "v_cvt_pk_bf16_f32 v109, v110, v111\n"
        "buffer_store_dwordx2 v[108:109], v232, s[48:51], s65 offen offset:160\n"
        "s_waitcnt vmcnt(9)\n"
        "v_mfma_f32_16x16x32_bf16 v[120:123], v[238:241], v[52:55], v[120:123]\n"
        "v_mfma_f32_16x16x32_bf16 v[124:127], v[236:239], v[52:55], v[124:127]\n"
        "v_mfma_f32_16x16x32_bf16 v[120:123], v[238:241], v[56:59], v[120:123]\n"
        "v_mfma_f32_16x16x32_bf16 v[124:127], v[236:239], v[56:59], v[124:127]\n"
        "v_mfma_f32_16x16x32_bf16 v[120:123], v[238:241], v[60:63], v[120:123]\n"
        "v_mfma_f32_16x16x32_bf16 v[124:127], v[236:239], v[60:63], v[124:127]\n"
        "v_cvt_pk_bf16_f32 v112, v112, v113\n"
        "v_cvt_pk_bf16_f32 v113, v114, v115\n"
        "buffer_store_dwordx2 v[112:113], v232, s[48:51], s65 offen offset:192\n"
        "v_cvt_pk_bf16_f32 v116, v116, v117\n"
        "v_cvt_pk_bf16_f32 v117, v118, v119\n"
        "buffer_store_dwordx2 v[116:117], v232, s[48:51], s65 offen offset:224\n"
        "s_waitcnt vmcnt(10)\n"
        "v_mfma_f32_16x16x32_bf16 v[208:211], v[238:241], v[64:67], v[208:211]\n"
        "v_mfma_f32_16x16x32_bf16 v[212:215], v[236:239], v[64:67], v[212:215]\n"
        "v_mfma_f32_16x16x32_bf16 v[208:211], v[238:241], v[68:71], v[208:211]\n"
        "v_mfma_f32_16x16x32_bf16 v[212:215], v[236:239], v[68:71], v[212:215]\n"
        "v_mfma_f32_16x16x32_bf16 v[208:211], v[238:241], v[72:75], v[208:211]\n"
        "v_mfma_f32_16x16x32_bf16 v[212:215], v[236:239], v[72:75], v[212:215]\n"
        "v_cvt_pk_bf16_f32 v120, v120, v121\n"
        "v_cvt_pk_bf16_f32 v121, v122, v123\n"
        "buffer_store_dwordx2 v[120:121], v232, s[48:51], s65 offen offset:256\n"
        "v_cvt_pk_bf16_f32 v124, v124, v125\n"
        "v_cvt_pk_bf16_f32 v125, v126, v127\n"
        "buffer_store_dwordx2 v[124:125], v232, s[48:51], s65 offen offset:288\n"
        "s_waitcnt vmcnt(11)\n"
        "v_mfma_f32_16x16x32_bf16 v[216:219], v[238:241], v[76:79], v[216:219]\n"
        "v_mfma_f32_16x16x32_bf16 v[220:223], v[236:239], v[76:79], v[220:223]\n"
        "v_mfma_f32_16x16x32_bf16 v[216:219], v[238:241], v[80:83], v[216:219]\n"
        "v_mfma_f32_16x16x32_bf16 v[220:223], v[236:239], v[80:83], v[220:223]\n"
        "v_mfma_f32_16x16x32_bf16 v[216:219], v[238:241], v[128:131], v[216:219]\n"
        "v_mfma_f32_16x16x32_bf16 v[220:223], v[236:239], v[128:131], v[220:223]\n"
        "v_cvt_pk_bf16_f32 v208, v208, v209\n"
        "v_cvt_pk_bf16_f32 v209, v210, v211\n"
        "buffer_store_dwordx2 v[208:209], v232, s[56:59], s66 offen offset:0\n"
        "v_cvt_pk_bf16_f32 v212, v212, v213\n"
        "v_cvt_pk_bf16_f32 v213, v214, v215\n"
        "buffer_store_dwordx2 v[212:213], v232, s[56:59], s66 offen offset:32\n"
        "s_waitcnt vmcnt(12)\n"
        "v_mfma_f32_16x16x32_bf16 v[224:227], v[238:241], v[132:135], v[224:227]\n"
        "v_mfma_f32_16x16x32_bf16 v[224:227], v[238:241], v[136:139], v[224:227]\n"
        "v_mfma_f32_16x16x32_bf16 v[224:227], v[238:241], v[140:143], v[224:227]\n"
        "v_cvt_pk_bf16_f32 v216, v216, v217\n"
        "v_cvt_pk_bf16_f32 v217, v218, v219\n"
        "buffer_store_dwordx2 v[216:217], v232, s[56:59], s66 offen offset:64\n"
        "v_cvt_pk_bf16_f32 v220, v220, v221\n"
        "v_cvt_pk_bf16_f32 v221, v222, v223\n"
        "buffer_store_dwordx2 v[220:221], v232, s[56:59], s66 offen offset:96\n"
        "s_nop 4\n"
        "v_cvt_pk_bf16_f32 v224, v224, v225\n"
        "v_cvt_pk_bf16_f32 v225, v226, v227\n"
        "buffer_store_dwordx2 v[224:225], v232, s[56:59], s66 offen offset:128\n"
        "s_or_b32 s80, %6, %7\n"
        "s_cbranch_scc1 .Lk_NOBUMP\n"
        "s_add_u32 s80, s94, 1\n"
        "v_mov_b32 v1, s80\n"
        "buffer_store_dword v1, off, s[96:99], 0 sc1\n"
        ".Lk_NOBUMP:\n"
        "s_waitcnt vmcnt(0) lgkmcnt(0)\n"
        "s_branch .Lk_FIN\n"
        ".Lk_SPIN:\n"
        "s_sub_u32 s82, s82, 1\n"
        "s_cbranch_scc1 .Lk_SPINRET\n"
        "s_sleep 2\n"
        "buffer_load_dword v242, v243, s[96:99], s89 offen offset:128 sc1\n"
        "s_waitcnt vmcnt(0)\n"
        "v_cmp_ne_u32 vcc, s95, v242\n"
        "s_cbranch_vccnz .Lk_SPIN\n"
        "s_branch .Lk_SPINRET\n"
        ".Lk_PUB2:\n"
        "s_waitcnt vmcnt(16)\n"
        "s_lshl_b32 s80, %7, 8\n"
        "s_lshl_b32 s81, %6, 2\n"
        "s_add_u32 s80, s80, s81\n"
        "v_mov_b32 v1, s95\n"
        "buffer_store_dword v1, off, s[96:99], s80 offset:256 sc1\n"
        "s_branch .Lk_PUB2RET\n"
        ".Lk_EXIT:\n"
        "s_waitcnt vmcnt(0) lgkmcnt(0)\n"
        ".Lk_FIN:\n"
        "s_mov_b32 %0, s77\n"
        ".Lk_CODE_END:\n"
            OPERANDS);
    }
    if (flag) {
        asm volatile("" : "+s"(ka), "+s"(wg), "+s"(wave));
        int lane;
        asm volatile("v_mbcnt_lo_u32_b32 %0, -1, 0\n\tv_mbcnt_hi_u32_b32 %0, -1, %0" : "=v"(lane));
        {
            SETUP(ka)
            (void)wo; (void)flags; (void)part; (void)dst;
#pragma unroll 1
            for (int i = 0; i < 10; ++i) {
                const int h = i >= 5, j = i - 5 * h;
                if (!((flag >> (2 * h + (j == 4))) & 1)) continue;
                slow_tile(x, kc + (size_t)h * SK * DH, vc + (size_t)h * SK * DH, wq + h * DH, ows, 2 * pair + h,
                          j == 4 ? 16 + (wave >> 1) : 4 * wave + j, lane, j == 4 ? 3 << (2 * (wave & 1)) : 15);
            }
        }
        asm volatile("s_waitcnt vmcnt(0)" : "+s"(ka), "+s"(wg), "+s"(wave) : : "memory");
        SETUP(ka)
        asm volatile(
        ".Lr_CODE_START:\n"
        "v_mbcnt_lo_u32_b32 v0, -1, 0\n"
        "v_mbcnt_hi_u32_b32 v0, -1, v0\n"
        "s_mov_b64 s[96:97], %13\n"
        "s_mov_b32 s98, 8192\n"
        "s_mov_b32 s99, 0x20000\n"
        "buffer_load_dword v37, off, s[96:99], 0 sc1\n"
        "s_mov_b32 s64, %6\n"
        "s_and_b32 s78, %6, 1\n"
        "s_lshr_b32 s85, %7, 2\n"
        "v_lshlrev_b32 v250, 4, v0\n"
        "s_mov_b64 s[56:57], %5\n"
        "s_mov_b32 s58, 2433024\n"
        "s_mov_b32 s59, 0x20000\n"
        "s_lshl_b32 s80, %7, 13\n"
        "s_add_u32 s80, s80, 4096\n"
        "s_mul_i32 s81, s64, 540672\n"
        "s_add_u32 s81, s80, s81\n"
        "s_add_u32 s72, s85, 0\n"
        "s_and_b32 s72, s72, 3\n"
        "s_mul_i32 s72, s72, 135168\n"
        "s_add_u32 s72, s72, s81\n"
        "s_add_u32 s73, s85, 1\n"
        "s_and_b32 s73, s73, 3\n"
        "s_mul_i32 s73, s73, 135168\n"
        "s_add_u32 s73, s73, s81\n"
        "s_add_u32 s74, s85, 2\n"
        "s_and_b32 s74, s74, 3\n"
        "s_mul_i32 s74, s74, 135168\n"
        "s_add_u32 s74, s74, s81\n"
        "s_add_u32 s75, s85, 3\n"
        "s_and_b32 s75, s75, 3\n"
        "s_mul_i32 s75, s75, 135168\n"
        "s_add_u32 s75, s75, s81\n"
        "s_lshr_b32 s81, s64, 1\n"
        "s_mul_i32 s81, s81, 135168\n"
        "s_add_u32 s76, s80, s81\n"
        "s_lshl_b32 s81, s78, 11\n"
        "s_add_u32 s76, s76, s81\n"
        "s_add_u32 s76, s76, 2162688\n"
        "buffer_load_dwordx4 v[88:91], v250, s[56:59], s72 offen offset:0 sc1\n"
        "buffer_load_dwordx4 v[108:111], v250, s[56:59], s72 offen offset:1024 sc1\n"
        "buffer_load_dwordx4 v[128:131], v250, s[56:59], s72 offen offset:2048 sc1\n"
        "buffer_load_dwordx4 v[148:151], v250, s[56:59], s72 offen offset:3072 sc1\n"
        "buffer_load_dwordx4 v[92:95], v250, s[56:59], s73 offen offset:0 sc1\n"
        "buffer_load_dwordx4 v[112:115], v250, s[56:59], s73 offen offset:1024 sc1\n"
        "buffer_load_dwordx4 v[132:135], v250, s[56:59], s73 offen offset:2048 sc1\n"
        "buffer_load_dwordx4 v[152:155], v250, s[56:59], s73 offen offset:3072 sc1\n"
        "buffer_load_dwordx4 v[96:99], v250, s[56:59], s74 offen offset:0 sc1\n"
        "buffer_load_dwordx4 v[116:119], v250, s[56:59], s74 offen offset:1024 sc1\n"
        "buffer_load_dwordx4 v[136:139], v250, s[56:59], s74 offen offset:2048 sc1\n"
        "buffer_load_dwordx4 v[156:159], v250, s[56:59], s74 offen offset:3072 sc1\n"
        "buffer_load_dwordx4 v[100:103], v250, s[56:59], s75 offen offset:0 sc1\n"
        "buffer_load_dwordx4 v[120:123], v250, s[56:59], s75 offen offset:1024 sc1\n"
        "buffer_load_dwordx4 v[140:143], v250, s[56:59], s75 offen offset:2048 sc1\n"
        "buffer_load_dwordx4 v[160:163], v250, s[56:59], s75 offen offset:3072 sc1\n"
        "buffer_load_dwordx4 v[28:31], v250, s[56:59], s76 offen offset:0 sc1\n"
        "buffer_load_dwordx4 v[32:35], v250, s[56:59], s76 offen offset:1024 sc1\n"
        "s_waitcnt vmcnt(0)\n"
        "v_readfirstlane_b32 s94, v37\n"
        "s_mul_i32 s95, s94, 0x9e3779b1\n"
        "s_add_u32 s95, s95, 0x7f4a7c15\n"
        "s_lshl_b32 s80, %7, 8\n"
        "s_lshl_b32 s81, %6, 2\n"
        "s_add_u32 s80, s80, s81\n"
        "v_mov_b32 v1, s95\n"
        "buffer_store_dword v1, off, s[96:99], s80 offset:128 sc1\n"
        "s_and_b32 s90, %7, 3\n"
        "s_mul_i32 s91, s90, 320\n"
        "s_sub_u32 s46, 1310720, s91\n"
        "s_mul_i32 s53, s85, 1310720\n"
        "s_add_u32 s91, s91, s53\n"
        "s_mov_b64 s[44:45], %11\n"
        "s_add_u32 s44, s44, s91\n"
        "s_addc_u32 s45, s45, 0\n"
        "s_mov_b32 s47, 0x20000\n"
        "s_mul_i32 s91, s64, 320\n"
        "v_add_u32 v228, s91, v0\n"
        "s_add_u32 s91, s91, 64\n"
        "v_mul_u32_u24 v229, 3277, v228\n"
        "v_lshrrev_b32 v229, 16, v229\n"
        "v_mul_u32_u24 v230, 20, v229\n"
        "v_sub_u32 v228, v228, v230\n"
        "v_lshrrev_b32 v230, 1, v229\n"
        "v_and_b32 v230, 2, v230\n"
        "v_xor_b32 v228, v228, v230\n"
        "v_mul_u32_u24 v229, 1280, v229\n"
        "v_lshl_add_u32 v244, v228, 4, v229\n"
        "v_add_u32 v228, s91, v0\n"
        "s_add_u32 s91, s91, 64\n"
        "v_mul_u32_u24 v229, 3277, v228\n"
        "v_lshrrev_b32 v229, 16, v229\n"
        "v_mul_u32_u24 v230, 20, v229\n"
        "v_sub_u32 v228, v228, v230\n"
        "v_lshrrev_b32 v230, 1, v229\n"
        "v_and_b32 v230, 2, v230\n"
        "v_xor_b32 v228, v228, v230\n"
        "v_mul_u32_u24 v229, 1280, v229\n"
        "v_lshl_add_u32 v245, v228, 4, v229\n"
        "v_subrev_u32 v245, 1024, v245\n"
        "v_add_u32 v228, s91, v0\n"
        "s_add_u32 s91, s91, 64\n"
        "v_mul_u32_u24 v229, 3277, v228\n"
        "v_lshrrev_b32 v229, 16, v229\n"
        "v_mul_u32_u24 v230, 20, v229\n"
        "v_sub_u32 v228, v228, v230\n"
        "v_lshrrev_b32 v230, 1, v229\n"
        "v_and_b32 v230, 2, v230\n"
        "v_xor_b32 v228, v228, v230\n"
        "v_mul_u32_u24 v229, 1280, v229\n"
        "v_lshl_add_u32 v246, v228, 4, v229\n"
        "v_subrev_u32 v246, 2048, v246\n"
        "v_add_u32 v228, s91, v0\n"
        "s_add_u32 s91, s91, 64\n"
        "v_mul_u32_u24 v229, 3277, v228\n"
        "v_lshrrev_b32 v229, 16, v229\n"
        "v_mul_u32_u24 v230, 20, v229\n"
        "v_sub_u32 v228, v228, v230\n"
        "v_lshrrev_b32 v230, 1, v229\n"
        "v_and_b32 v230, 2, v230\n"
        "v_xor_b32 v228, v228, v230\n"
        "v_mul_u32_u24 v229, 1280, v229\n"
        "v_lshl_add_u32 v247, v228, 4, v229\n"
        "v_subrev_u32 v247, 3072, v247\n"
        "v_add_u32 v228, s91, v0\n"
        "v_mul_u32_u24 v229, 3277, v228\n"
        "v_lshrrev_b32 v229, 16, v229\n"
        "v_mul_u32_u24 v230, 20, v229\n"
        "v_sub_u32 v228, v228, v230\n"
        "v_lshrrev_b32 v230, 1, v229\n"
        "v_and_b32 v230, 2, v230\n"
        "v_xor_b32 v228, v228, v230\n"
        "v_mul_u32_u24 v229, 1280, v229\n"
        "v_lshl_add_u32 v248, v228, 4, v229\n"
        "s_lshl_b32 s91, s90, 2\n"
        "s_lshr_b32 s52, 0x64206420, s91\n"
        "s_and_b32 s52, s52, 0xffff\n"
        "s_lshr_b32 s91, s52, 4\n"
        "s_lshl_b32 s91, s91, 16\n"
        "s_or_b32 s52, s52, s91\n"
        "s_or_b32 s52, s52, 0xf1110000\n"
        "s_mul_i32 s54, s90, 327680\n"
        "s_lshl_b32 s89, s90, 8\n"
        "s_mul_i32 s91, s64, 5120\n"
        "s_add_u32 s90, s91, 61440\n"
        "v_lshrrev_b32 v228, 2, v0\n"
        "v_mul_u32_u24 v228, 320, v228\n"
        "v_and_b32 v229, 3, v0\n"
        "v_lshl_add_u32 v228, v229, 3, v228\n"
        "v_bfe_u32 v229, v0, 4, 1\n"
        "v_xor_b32 v229, s78, v229\n"
        "v_lshlrev_b32 v229, 5, v229\n"
        "v_add_u32 v230, v228, v229\n"
        "v_sub_u32 v228, v228, v229\n"
        "s_mul_i32 s80, s78, 128\n"
        "s_add_u32 s80, s80, 0\n"
        "v_add_u32 v232, s80, v230\n"
        "s_mul_i32 s80, s78, 192\n"
        "s_add_u32 s80, s80, 0\n"
        "v_add_u32 v233, s80, v228\n"
        "s_mul_i32 s80, s78, -192\n"
        "s_add_u32 s80, s80, 0\n"
        "v_add_u32 v234, s80, v230\n"
        "s_mul_i32 s80, s78, -128\n"
        "s_add_u32 s80, s80, 0\n"
        "v_add_u32 v235, s80, v228\n"
        "s_lshl_b32 s80, s89, 5\n"
        "s_add_u32 s80, s80, 4096\n"
        "v_subrev_u32 v228, s80, v250\n"
        "v_add_u32 v236, s72, v228\n"
        "v_add_u32 v237, s73, v228\n"
        "v_add_u32 v238, s74, v228\n"
        "v_add_u32 v239, s75, v228\n"
        "v_add_u32 v240, s76, v228\n"
        "s_lshl_b32 s80, s78, 11\n"
        "v_subrev_u32 v240, s80, v240\n"
        "s_and_b32 s81, s64, 2\n"
        "s_lshl_b32 s93, s81, 11\n"
        "s_add_u32 s93, s93, 147456\n"
        "v_add_u32 v241, s93, v250\n"
        "v_add_u32 v249, s80, v241\n"
        "s_lshl_b32 s81, s81, 2\n"
        "s_lshl_b32 s80, s85, 10\n"
        "s_add_u32 s80, s80, s81\n"
        "v_and_b32 v228, 1, v0\n"
        "v_lshl_add_u32 v243, v228, 2, s80\n"
        "v_mov_b32 v242, s95\n"
        "s_mov_b32 s92, 40960\n"
        "s_mov_b32 s93, 5\n"
        "ds_write_b128 v249, v[28:31] offset:0\n"
        "ds_write_b128 v249, v[32:35] offset:1024\n"
        "s_mov_b32 s77, 0\n"
        ".Lr_P2ENTRY:\n"
        "s_waitcnt vmcnt(63) lgkmcnt(0)\n"
        "s_barrier\n"
        "ds_read_b64_tr_b16 a[192:193], v232 offset:0\n"
        "ds_read_b64_tr_b16 a[194:195], v232 offset:5120\n"
        "ds_read_b64_tr_b16 a[196:197], v233 offset:32\n"
        "ds_read_b64_tr_b16 a[198:199], v233 offset:5152\n"
        "ds_read_b128 v[104:107], v241\n"
        "ds_read_b64_tr_b16 a[200:201], v232 offset:64\n"
        "ds_read_b64_tr_b16 a[202:203], v232 offset:5184\n"
        "ds_read_b64_tr_b16 a[204:205], v233 offset:96\n"
        "ds_read_b64_tr_b16 a[206:207], v233 offset:5216\n"
        "ds_read_b64_tr_b16 a[208:209], v232 offset:128\n"
        "ds_read_b64_tr_b16 a[210:211], v232 offset:5248\n"
        "ds_read_b64_tr_b16 a[212:213], v235 offset:160\n"
        "ds_read_b64_tr_b16 a[214:215], v235 offset:5280\n"
        "ds_read_b64_tr_b16 a[216:217], v234 offset:192\n"
        "ds_read_b64_tr_b16 a[218:219], v234 offset:5312\n"
        "ds_read_b64_tr_b16 a[220:221], v235 offset:224\n"
        "ds_read_b64_tr_b16 a[222:223], v235 offset:5344\n"
        "ds_read_b64_tr_b16 a[224:225], v234 offset:256\n"
        "ds_read_b64_tr_b16 a[226:227], v234 offset:5376\n"
        "ds_read_b64_tr_b16 a[228:229], v235 offset:288\n"
        "ds_read_b64_tr_b16 a[230:231], v235 offset:5408\n"
        "ds_read_b128 v[124:127], v241 offset:1024\n"
        "ds_read_b128 v[144:147], v241 offset:2048\n"
        "ds_read_b128 v[164:167], v241 offset:3072\n"
        "s_waitcnt lgkmcnt(15)\n"
        "v_mfma_f32_16x16x32_bf16 a[0:3], a[192:195], v[88:91], 0\n"
        "v_mfma_f32_16x16x32_bf16 v[4:7], a[192:195], v[92:95], 0\n"
        "v_mfma_f32_16x16x32_bf16 v[44:47], a[192:195], v[96:99], 0\n"
        "v_mfma_f32_16x16x32_bf16 v[168:171], a[192:195], v[100:103], 0\n"
        "v_mfma_f32_16x16x32_bf16 v[208:211], a[192:195], v[104:107], 0\n"
        "ds_read_b64_tr_b16 a[192:193], v232 offset:10240\n"
        "v_mfma_f32_16x16x32_bf16 a[4:7], a[196:199], v[88:91], 0\n"
        "v_mfma_f32_16x16x32_bf16 v[8:11], a[196:199], v[92:95], 0\n"
        "ds_read_b64_tr_b16 a[194:195], v232 offset:15360\n"
        "v_mfma_f32_16x16x32_bf16 v[48:51], a[196:199], v[96:99], 0\n"
        "v_mfma_f32_16x16x32_bf16 v[172:175], a[196:199], v[100:103], 0\n"
        "v_cmp_ne_u32 vcc, s95, v242\n"
        "v_mfma_f32_16x16x32_bf16 v[212:215], a[196:199], v[104:107], 0\n"
        "ds_read_b64_tr_b16 a[196:197], v233 offset:10272\n"
        "v_mfma_f32_16x16x32_bf16 a[8:11], a[200:203], v[88:91], 0\n"
        "s_lshl_b32 s55, s89, 5\n"
        "ds_read_b64_tr_b16 a[198:199], v233 offset:15392\n"
        "v_mfma_f32_16x16x32_bf16 v[12:15], a[200:203], v[92:95], 0\n"
        "s_add_u32 s53, s54, 81920\n"
        "v_mfma_f32_16x16x32_bf16 v[52:55], a[200:203], v[96:99], 0\n"
        "s_ashr_i32 s52, s52, 4\n"
        "v_mfma_f32_16x16x32_bf16 v[176:179], a[200:203], v[100:103], 0\n"
        "s_and_b32 s54, s52, 15\n"
        "v_mfma_f32_16x16x32_bf16 v[216:219], a[200:203], v[104:107], 0\n"
        "s_and_b32 s89, s52, 7\n"
        "ds_read_b64_tr_b16 a[200:201], v232 offset:10304\n"
        "v_mfma_f32_16x16x32_bf16 a[12:15], a[204:207], v[88:91], 0\n"
        "s_mul_i32 s54, s54, 163840\n"
        "ds_read_b64_tr_b16 a[202:203], v232 offset:15424\n"
        "v_mfma_f32_16x16x32_bf16 v[16:19], a[204:207], v[92:95], 0\n"
        "s_lshl_b32 s89, s89, 7\n"
        "v_mfma_f32_16x16x32_bf16 v[56:59], a[204:207], v[96:99], 0\n"
        "v_mfma_f32_16x16x32_bf16 v[180:183], a[204:207], v[100:103], 0\n"
        "v_mfma_f32_16x16x32_bf16 v[220:223], a[204:207], v[104:107], 0\n"
        "s_waitcnt lgkmcnt(2)\n"
        "ds_read_b64_tr_b16 a[204:205], v233 offset:10336\n"
        "v_mfma_f32_16x16x32_bf16 a[16:19], a[208:211], v[88:91], 0\n"
        "ds_read_b64_tr_b16 a[206:207], v233 offset:15456\n"
        "v_mfma_f32_16x16x32_bf16 v[20:23], a[208:211], v[92:95], 0\n"
        "v_mfma_f32_16x16x32_bf16 v[60:63], a[208:211], v[96:99], 0\n"
        "v_mfma_f32_16x16x32_bf16 v[184:187], a[208:211], v[100:103], 0\n"
        "v_mfma_f32_16x16x32_bf16 v[224:227], a[208:211], v[104:107], 0\n"
        "buffer_load_dwordx4 v[104:107], v240, s[56:59], s55 offen offset:0 sc1\n"
        "v_mfma_f32_16x16x32_bf16 a[20:23], a[212:215], v[88:91], 0\n"
        "ds_read_b64_tr_b16 a[208:209], v232 offset:10368\n"
        "v_mfma_f32_16x16x32_bf16 v[24:27], a[212:215], v[92:95], 0\n"
        "ds_read_b64_tr_b16 a[210:211], v232 offset:15488\n"
        "v_mfma_f32_16x16x32_bf16 v[64:67], a[212:215], v[96:99], 0\n"
        "v_mfma_f32_16x16x32_bf16 v[188:191], a[212:215], v[100:103], 0\n"
        "ds_read_b64_tr_b16 a[212:213], v235 offset:10400\n"
        "v_mfma_f32_16x16x32_bf16 a[24:27], a[216:219], v[88:91], 0\n"
        "ds_read_b64_tr_b16 a[214:215], v235 offset:15520\n"
        "v_mfma_f32_16x16x32_bf16 v[28:31], a[216:219], v[92:95], 0\n"
        "v_mfma_f32_16x16x32_bf16 v[68:71], a[216:219], v[96:99], 0\n"
        "v_mfma_f32_16x16x32_bf16 v[192:195], a[216:219], v[100:103], 0\n"
        "ds_read_b64_tr_b16 a[216:217], v234 offset:10432\n"
        "v_mfma_f32_16x16x32_bf16 a[28:31], a[220:223], v[88:91], 0\n"
        "ds_read_b64_tr_b16 a[218:219], v234 offset:15552\n"
        "v_mfma_f32_16x16x32_bf16 v[32:35], a[220:223], v[92:95], 0\n"
        "v_mfma_f32_16x16x32_bf16 v[72:75], a[220:223], v[96:99], 0\n"
        "v_mfma_f32_16x16x32_bf16 v[196:199], a[220:223], v[100:103], 0\n"
        "ds_read_b64_tr_b16 a[220:221], v235 offset:10464\n"
        "v_mfma_f32_16x16x32_bf16 a[32:35], a[224:227], v[88:91], 0\n"
        "ds_read_b64_tr_b16 a[222:223], v235 offset:15584\n"
        "v_mfma_f32_16x16x32_bf16 v[36:39], a[224:227], v[92:95], 0\n"
        "v_mfma_f32_16x16x32_bf16 v[76:79], a[224:227], v[96:99], 0\n"
        "v_mfma_f32_16x16x32_bf16 v[200:203], a[224:227], v[100:103], 0\n"
        "ds_read_b64_tr_b16 a[224:225], v234 offset:10496\n"
        "v_mfma_f32_16x16x32_bf16 a[36:39], a[228:231], v[88:91], 0\n"
        "buffer_load_dwordx4 v[88:91], v236, s[56:59], s55 offen offset:0 sc1\n"
        "v_mfma_f32_16x16x32_bf16 v[40:43], a[228:231], v[92:95], 0\n"
        "ds_read_b64_tr_b16 a[226:227], v234 offset:15616\n"
        "v_mfma_f32_16x16x32_bf16 v[80:83], a[228:231], v[96:99], 0\n"
        "v_mfma_f32_16x16x32_bf16 v[204:207], a[228:231], v[100:103], 0\n"
        "buffer_load_dwordx4 v[92:95], v237, s[56:59], s55 offen offset:0 sc1\n"
        "v_mfma_f32_16x16x32_bf16 a[0:3], a[192:195], v[108:111], a[0:3]\n"
        "ds_read_b64_tr_b16 a[228:229], v235 offset:10528\n"
        "v_mfma_f32_16x16x32_bf16 v[4:7], a[192:195], v[112:115], v[4:7]\n"
        "s_barrier\n"
        "ds_read_b64_tr_b16 a[230:231], v235 offset:15648\n"
        "v_mfma_f32_16x16x32_bf16 v[44:47], a[192:195], v[116:119], v[44:47]\n"
        "buffer_load_dwordx4 v[96:99], v238, s[56:59], s55 offen offset:0 sc1\n"
        "v_mfma_f32_16x16x32_bf16 v[168:171], a[192:195], v[120:123], v[168:171]\n"
        "s_add_u32 m0, s90, 0\n"
        "v_mfma_f32_16x16x32_bf16 v[208:211], a[192:195], v[124:127], v[208:211]\n"
        "ds_read_b64_tr_b16 a[192:193], v232 offset:20480\n"
        "v_mfma_f32_16x16x32_bf16 a[4:7], a[196:199], v[108:111], a[4:7]\n"
        "buffer_load_dwordx4 v244, s[44:47], s53 offen offset:0 lds\n"
        "v_mfma_f32_16x16x32_bf16 v[8:11], a[196:199], v[112:115], v[8:11]\n"
        "ds_read_b64_tr_b16 a[194:195], v232 offset:25600\n"
        "v_mfma_f32_16x16x32_bf16 v[48:51], a[196:199], v[116:119], v[48:51]\n"
        "v_mfma_f32_16x16x32_bf16 v[172:175], a[196:199], v[120:123], v[172:175]\n"
        "buffer_load_dwordx4 v245, s[44:47], s53 offen offset:1024 lds\n"
        "v_mfma_f32_16x16x32_bf16 v[212:215], a[196:199], v[124:127], v[212:215]\n"
        "s_waitcnt lgkmcnt(2)\n"
        "ds_read_b64_tr_b16 a[196:197], v233 offset:20512\n"
        "v_mfma_f32_16x16x32_bf16 a[8:11], a[200:203], v[108:111], a[8:11]\n"
        "ds_read_b64_tr_b16 a[198:199], v233 offset:25632\n"
        "v_mfma_f32_16x16x32_bf16 v[12:15], a[200:203], v[112:115], v[12:15]\n"
        "buffer_load_dwordx4 v246, s[44:47], s53 offen offset:2048 lds\n"
        "v_mfma_f32_16x16x32_bf16 v[52:55], a[200:203], v[116:119], v[52:55]\n"
        "v_mfma_f32_16x16x32_bf16 v[176:179], a[200:203], v[120:123], v[176:179]\n"
        "v_mfma_f32_16x16x32_bf16 v[216:219], a[200:203], v[124:127], v[216:219]\n"
        "buffer_load_dwordx4 v[100:103], v239, s[56:59], s55 offen offset:0 sc1\n"
        "v_mfma_f32_16x16x32_bf16 a[12:15], a[204:207], v[108:111], a[12:15]\n"
        "ds_read_b64_tr_b16 a[200:201], v232 offset:20544\n"
        "v_mfma_f32_16x16x32_bf16 v[16:19], a[204:207], v[112:115], v[16:19]\n"
        "ds_read_b64_tr_b16 a[202:203], v232 offset:25664\n"
        "v_mfma_f32_16x16x32_bf16 v[56:59], a[204:207], v[116:119], v[56:59]\n"
        "buffer_load_dwordx4 v247, s[44:47], s53 offen offset:3072 lds\n"
        "v_mfma_f32_16x16x32_bf16 v[180:183], a[204:207], v[120:123], v[180:183]\n"
        "s_add_u32 m0, s90, 4096\n"
        "v_mfma_f32_16x16x32_bf16 v[220:223], a[204:207], v[124:127], v[220:223]\n"
        "ds_read_b64_tr_b16 a[204:205], v233 offset:20576\n"
        "v_mfma_f32_16x16x32_bf16 a[16:19], a[208:211], v[108:111], a[16:19]\n"
        "buffer_load_dwordx4 v248, s[44:47], s53 offen offset:0 lds\n"
        "v_mfma_f32_16x16x32_bf16 v[20:23], a[208:211], v[112:115], v[20:23]\n"
        "s_sub_u32 s90, s90, s92\n"
        "ds_read_b64_tr_b16 a[206:207], v233 offset:25696\n"
        "v_mfma_f32_16x16x32_bf16 v[60:63], a[208:211], v[116:119], v[60:63]\n"
        "v_mfma_f32_16x16x32_bf16 v[184:187], a[208:211], v[120:123], v[184:187]\n"
        "buffer_load_dword v242, v243, s[96:99], s89 offen offset:128 sc1\n"
        "v_mfma_f32_16x16x32_bf16 v[224:227], a[208:211], v[124:127], v[224:227]\n"
        "ds_read_b64_tr_b16 a[208:209], v232 offset:20608\n"
        "v_mfma_f32_16x16x32_bf16 a[20:23], a[212:215], v[108:111], a[20:23]\n"
        "ds_read_b64_tr_b16 a[210:211], v232 offset:25728\n"
        "v_mfma_f32_16x16x32_bf16 v[24:27], a[212:215], v[112:115], v[24:27]\n"
        "buffer_load_dwordx4 v[124:127], v240, s[56:59], s55 offen offset:1024 sc1\n"
        "v_mfma_f32_16x16x32_bf16 v[64:67], a[212:215], v[116:119], v[64:67]\n"
        "v_mfma_f32_16x16x32_bf16 v[188:191], a[212:215], v[120:123], v[188:191]\n"
        "ds_read_b64_tr_b16 a[212:213], v235 offset:20640\n"
        "v_mfma_f32_16x16x32_bf16 a[24:27], a[216:219], v[108:111], a[24:27]\n"
        "ds_read_b64_tr_b16 a[214:215], v235 offset:25760\n"
        "v_mfma_f32_16x16x32_bf16 v[28:31], a[216:219], v[112:115], v[28:31]\n"
        "v_mfma_f32_16x16x32_bf16 v[68:71], a[216:219], v[116:119], v[68:71]\n"
        "v_mfma_f32_16x16x32_bf16 v[192:195], a[216:219], v[120:123], v[192:195]\n"
        "ds_read_b64_tr_b16 a[216:217], v234 offset:20672\n"
        "v_mfma_f32_16x16x32_bf16 a[28:31], a[220:223], v[108:111], a[28:31]\n"
        "ds_read_b64_tr_b16 a[218:219], v234 offset:25792\n"
        "v_mfma_f32_16x16x32_bf16 v[32:35], a[220:223], v[112:115], v[32:35]\n"
        "v_mfma_f32_16x16x32_bf16 v[72:75], a[220:223], v[116:119], v[72:75]\n"
        "v_mfma_f32_16x16x32_bf16 v[196:199], a[220:223], v[120:123], v[196:199]\n"
        "ds_read_b64_tr_b16 a[220:221], v235 offset:20704\n"
        "v_mfma_f32_16x16x32_bf16 a[32:35], a[224:227], v[108:111], a[32:35]\n"
        "ds_read_b64_tr_b16 a[222:223], v235 offset:25824\n"
        "v_mfma_f32_16x16x32_bf16 v[36:39], a[224:227], v[112:115], v[36:39]\n"
        "v_mfma_f32_16x16x32_bf16 v[76:79], a[224:227], v[116:119], v[76:79]\n"
        "v_mfma_f32_16x16x32_bf16 v[200:203], a[224:227], v[120:123], v[200:203]\n"
        "ds_read_b64_tr_b16 a[224:225], v234 offset:20736\n"
        "v_mfma_f32_16x16x32_bf16 a[36:39], a[228:231], v[108:111], a[36:39]\n"
        "buffer_load_dwordx4 v[108:111], v236, s[56:59], s55 offen offset:1024 sc1\n"
        "v_mfma_f32_16x16x32_bf16 v[40:43], a[228:231], v[112:115], v[40:43]\n"
        "ds_read_b64_tr_b16 a[226:227], v234 offset:25856\n"
        "v_mfma_f32_16x16x32_bf16 v[80:83], a[228:231], v[116:119], v[80:83]\n"
        "s_waitcnt lgkmcnt(3)\n"
        "v_mfma_f32_16x16x32_bf16 v[204:207], a[228:231], v[120:123], v[204:207]\n"
        "buffer_load_dwordx4 v[112:115], v237, s[56:59], s55 offen offset:1024 sc1\n"
        "v_mfma_f32_16x16x32_bf16 a[0:3], a[192:195], v[128:131], a[0:3]\n"
        "ds_read_b64_tr_b16 a[228:229], v235 offset:20768\n"
        "v_mfma_f32_16x16x32_bf16 v[4:7], a[192:195], v[132:135], v[4:7]\n"
        "ds_read_b64_tr_b16 a[230:231], v235 offset:25888\n"
        "v_mfma_f32_16x16x32_bf16 v[44:47], a[192:195], v[136:139], v[44:47]\n"
        "buffer_load_dwordx4 v[116:119], v238, s[56:59], s55 offen offset:1024 sc1\n"
        "v_mfma_f32_16x16x32_bf16 v[168:171], a[192:195], v[140:143], v[168:171]\n"
        "v_mfma_f32_16x16x32_bf16 v[208:211], a[192:195], v[144:147], v[208:211]\n"
        "ds_read_b64_tr_b16 a[192:193], v232 offset:30720\n"
        "v_mfma_f32_16x16x32_bf16 a[4:7], a[196:199], v[128:131], a[4:7]\n"
        "buffer_load_dwordx4 v[120:123], v239, s[56:59], s55 offen offset:1024 sc1\n"
        "v_mfma_f32_16x16x32_bf16 v[8:11], a[196:199], v[132:135], v[8:11]\n"
        "ds_read_b64_tr_b16 a[194:195], v232 offset:35840\n"
        "v_mfma_f32_16x16x32_bf16 v[48:51], a[196:199], v[136:139], v[48:51]\n"
        "v_mfma_f32_16x16x32_bf16 v[172:175], a[196:199], v[140:143], v[172:175]\n"
        "v_mfma_f32_16x16x32_bf16 v[212:215], a[196:199], v[144:147], v[212:215]\n"
        "ds_read_b64_tr_b16 a[196:197], v233 offset:30752\n"
        "v_mfma_f32_16x16x32_bf16 a[8:11], a[200:203], v[128:131], a[8:11]\n"
        "ds_read_b64_tr_b16 a[198:199], v233 offset:35872\n"
        "v_mfma_f32_16x16x32_bf16 v[12:15], a[200:203], v[132:135], v[12:15]\n"
        "v_mfma_f32_16x16x32_bf16 v[52:55], a[200:203], v[136:139], v[52:55]\n"
        "v_mfma_f32_16x16x32_bf16 v[176:179], a[200:203], v[140:143], v[176:179]\n"
        "v_mfma_f32_16x16x32_bf16 v[216:219], a[200:203], v[144:147], v[216:219]\n"
        "ds_read_b64_tr_b16 a[200:201], v232 offset:30784\n"
        "v_mfma_f32_16x16x32_bf16 a[12:15], a[204:207], v[128:131], a[12:15]\n"
        "ds_read_b64_tr_b16 a[202:203], v232 offset:35904\n"
        "v_mfma_f32_16x16x32_bf16 v[16:19], a[204:207], v[132:135], v[16:19]\n"
        "v_mfma_f32_16x16x32_bf16 v[56:59], a[204:207], v[136:139], v[56:59]\n"
        "v_mfma_f32_16x16x32_bf16 v[180:183], a[204:207], v[140:143], v[180:183]\n"
        "v_mfma_f32_16x16x32_bf16 v[220:223], a[204:207], v[144:147], v[220:223]\n"
        "ds_read_b64_tr_b16 a[204:205], v233 offset:30816\n"
        "v_mfma_f32_16x16x32_bf16 a[16:19], a[208:211], v[128:131], a[16:19]\n"
        "ds_read_b64_tr_b16 a[206:207], v233 offset:35936\n"
        "v_mfma_f32_16x16x32_bf16 v[20:23], a[208:211], v[132:135], v[20:23]\n"
        "v_add_u32 v233, s92, v233\n"
        "v_mfma_f32_16x16x32_bf16 v[60:63], a[208:211], v[136:139], v[60:63]\n"
        "v_mfma_f32_16x16x32_bf16 v[184:187], a[208:211], v[140:143], v[184:187]\n"
        "v_mfma_f32_16x16x32_bf16 v[224:227], a[208:211], v[144:147], v[224:227]\n"
        "buffer_load_dwordx4 v[144:147], v240, s[56:59], s55 offen offset:2048 sc1\n"
        "v_mfma_f32_16x16x32_bf16 a[20:23], a[212:215], v[128:131], a[20:23]\n"
        "ds_read_b64_tr_b16 a[208:209], v232 offset:30848\n"
        "v_mfma_f32_16x16x32_bf16 v[24:27], a[212:215], v[132:135], v[24:27]\n"
        "ds_read_b64_tr_b16 a[210:211], v232 offset:35968\n"
        "v_mfma_f32_16x16x32_bf16 v[64:67], a[212:215], v[136:139], v[64:67]\n"
        "v_add_u32 v232, s92, v232\n"
        "v_mfma_f32_16x16x32_bf16 v[188:191], a[212:215], v[140:143], v[188:191]\n"
        "ds_read_b64_tr_b16 a[212:213], v235 offset:30880\n"
        "v_mfma_f32_16x16x32_bf16 a[24:27], a[216:219], v[128:131], a[24:27]\n"
        "ds_read_b64_tr_b16 a[214:215], v235 offset:36000\n"
        "v_mfma_f32_16x16x32_bf16 v[28:31], a[216:219], v[132:135], v[28:31]\n"
        "v_mfma_f32_16x16x32_bf16 v[68:71], a[216:219], v[136:139], v[68:71]\n"
        "v_mfma_f32_16x16x32_bf16 v[192:195], a[216:219], v[140:143], v[192:195]\n"
        "s_waitcnt lgkmcnt(3)\n"
        "ds_read_b64_tr_b16 a[216:217], v234 offset:30912\n"
        "v_mfma_f32_16x16x32_bf16 a[28:31], a[220:223], v[128:131], a[28:31]\n"
        "ds_read_b64_tr_b16 a[218:219], v234 offset:36032\n"
        "v_mfma_f32_16x16x32_bf16 v[32:35], a[220:223], v[132:135], v[32:35]\n"
        "s_mov_b64 s[40:41], %14\n"
        "s_mov_b32 s42, 786432\n"
        "s_mov_b32 s43, 0x20000\n"
        "v_mfma_f32_16x16x32_bf16 v[72:75], a[220:223], v[136:139], v[72:75]\n"
        "s_lshl_b32 s81, s64, 14\n"
        "s_add_u32 s80, s85, 1\n"
        "s_and_b32 s80, s80, 3\n"
        "v_mfma_f32_16x16x32_bf16 v[196:199], a[220:223], v[140:143], v[196:199]\n"
        "s_mul_i32 s80, s80, 196608\n"
        "ds_read_b64_tr_b16 a[220:221], v235 offset:30944\n"
        "v_mfma_f32_16x16x32_bf16 a[32:35], a[224:227], v[128:131], a[32:35]\n"
        "s_add_u32 s80, s80, s81\n"
        "ds_read_b64_tr_b16 a[222:223], v235 offset:36064\n"
        "v_mfma_f32_16x16x32_bf16 v[36:39], a[224:227], v[132:135], v[36:39]\n"
        "s_add_u32 s60, s80, 0\n"
        "s_add_u32 s80, s85, 2\n"
        "s_and_b32 s80, s80, 3\n"
        "v_mfma_f32_16x16x32_bf16 v[76:79], a[224:227], v[136:139], v[76:79]\n"
        "s_mul_i32 s80, s80, 196608\n"
        "s_add_u32 s80, s80, s81\n"
        "s_add_u32 s61, s80, 65536\n"
        "v_mfma_f32_16x16x32_bf16 v[200:203], a[224:227], v[140:143], v[200:203]\n"
        "s_add_u32 s80, s85, 3\n"
        "ds_read_b64_tr_b16 a[224:225], v234 offset:30976\n"
        "v_mfma_f32_16x16x32_bf16 a[36:39], a[228:231], v[128:131], a[36:39]\n"
        "buffer_load_dwordx4 v[128:131], v236, s[56:59], s55 offen offset:2048 sc1\n"
        "v_mfma_f32_16x16x32_bf16 v[40:43], a[228:231], v[132:135], v[40:43]\n"
        "s_and_b32 s80, s80, 3\n"
        "ds_read_b64_tr_b16 a[226:227], v234 offset:36096\n"
        "v_mfma_f32_16x16x32_bf16 v[80:83], a[228:231], v[136:139], v[80:83]\n"
        "s_mul_i32 s80, s80, 196608\n"
        "v_add_u32 v234, s92, v234\n"
        "v_mfma_f32_16x16x32_bf16 v[204:207], a[228:231], v[140:143], v[204:207]\n"
        "buffer_load_dwordx4 v[132:135], v237, s[56:59], s55 offen offset:2048 sc1\n"
        "v_mfma_f32_16x16x32_bf16 a[0:3], a[192:195], v[148:151], a[0:3]\n"
        "s_add_u32 s80, s80, s81\n"
        "ds_read_b64_tr_b16 a[228:229], v235 offset:31008\n"
        "v_mfma_f32_16x16x32_bf16 v[4:7], a[192:195], v[152:155], v[4:7]\n"
        "s_barrier\n"
        "ds_read_b64_tr_b16 a[230:231], v235 offset:36128\n"
        "v_mfma_f32_16x16x32_bf16 v[44:47], a[192:195], v[156:159], v[44:47]\n"
        "buffer_load_dwordx4 v[136:139], v238, s[56:59], s55 offen offset:2048 sc1\n"
        "v_mfma_f32_16x16x32_bf16 v[168:171], a[192:195], v[160:163], v[168:171]\n"
        "s_add_u32 m0, s91, 0\n"
        "v_add_u32 v235, s92, v235\n"
        "v_mfma_f32_16x16x32_bf16 v[208:211], a[192:195], v[164:167], v[208:211]\n"
        "s_add_u32 s62, s80, 131072\n"
        "ds_read_b64_tr_b16 a[192:193], v232 offset:0\n"
        "v_mfma_f32_16x16x32_bf16 a[4:7], a[196:199], v[148:151], a[4:7]\n"
        "buffer_load_dwordx4 v244, s[44:47], s54 offen offset:0 lds\n"
        "v_mfma_f32_16x16x32_bf16 v[8:11], a[196:199], v[152:155], v[8:11]\n"
        "s_sub_u32 s80, s64, s85\n"
        "ds_read_b64_tr_b16 a[194:195], v232 offset:5120\n"
        "v_mfma_f32_16x16x32_bf16 v[48:51], a[196:199], v[156:159], v[48:51]\n"
        "s_and_b32 s80, s80, 3\n"
        "s_sub_u32 s80, s80, 1\n"
        "s_lshl_b32 s80, s80, 16\n"
        "v_mfma_f32_16x16x32_bf16 v[172:175], a[196:199], v[160:163], v[172:175]\n"
        "buffer_load_dwordx4 v245, s[44:47], s54 offen offset:1024 lds\n"
        "v_mfma_f32_16x16x32_bf16 v[212:215], a[196:199], v[164:167], v[212:215]\n"
        "s_mul_i32 s63, s64, 212992\n"
        "ds_read_b64_tr_b16 a[196:197], v233 offset:32\n"
        "v_mfma_f32_16x16x32_bf16 a[8:11], a[200:203], v[148:151], a[8:11]\n"
        "s_add_u32 s63, s63, s80\n"
        "ds_read_b64_tr_b16 a[198:199], v233 offset:5152\n"
        "v_mfma_f32_16x16x32_bf16 v[12:15], a[200:203], v[152:155], v[12:15]\n"
        "buffer_load_dwordx4 v246, s[44:47], s54 offen offset:2048 lds\n"
        "v_mfma_f32_16x16x32_bf16 v[52:55], a[200:203], v[156:159], v[52:55]\n"
        "s_cmp_eq_u32 s64, s85\n"
        "s_cselect_b32 s50, 0, s42\n"
        "s_cselect_b32 s63, 0, s63\n"
        "v_mfma_f32_16x16x32_bf16 v[176:179], a[200:203], v[160:163], v[176:179]\n"
        "s_mov_b64 s[48:49], %14\n"
        "s_mov_b32 s51, 0x20000\n"
        "s_mul_i32 s68, s85, 196608\n"
        "v_mfma_f32_16x16x32_bf16 v[216:219], a[200:203], v[164:167], v[216:219]\n"
        "buffer_load_dwordx4 v[140:143], v239, s[56:59], s55 offen offset:2048 sc1\n"
        "v_mfma_f32_16x16x32_bf16 a[12:15], a[204:207], v[148:151], a[12:15]\n"
        "s_add_u32 s68, s68, s81\n"
        "ds_read_b64_tr_b16 a[200:201], v232 offset:64\n"
        "v_mov_b32 v228, v250\n"
        "v_mfma_f32_16x16x32_bf16 v[16:19], a[204:207], v[152:155], v[16:19]\n"
        "s_mov_b32 s82, 0x100000\n"
        "ds_read_b64_tr_b16 a[202:203], v232 offset:5184\n"
        "v_add_u32 v229, 4096, v250\n"
        "v_mfma_f32_16x16x32_bf16 v[56:59], a[204:207], v[156:159], v[56:59]\n"
        "buffer_load_dwordx4 v247, s[44:47], s54 offen offset:3072 lds\n"
        "v_mfma_f32_16x16x32_bf16 v[180:183], a[204:207], v[160:163], v[180:183]\n"
        "s_add_u32 m0, s91, 4096\n"
        "v_mfma_f32_16x16x32_bf16 v[220:223], a[204:207], v[164:167], v[220:223]\n"
        "s_waitcnt lgkmcnt(2)\n"
        "ds_read_b64_tr_b16 a[204:205], v233 offset:96\n"
        "v_mfma_f32_16x16x32_bf16 a[16:19], a[208:211], v[148:151], a[16:19]\n"
        "buffer_load_dwordx4 v248, s[44:47], s54 offen offset:0 lds\n"
        "v_mfma_f32_16x16x32_bf16 v[20:23], a[208:211], v[152:155], v[20:23]\n"
        "s_add_u32 s91, s91, s92\n"
        "ds_read_b64_tr_b16 a[206:207], v233 offset:5216\n"
        "v_mfma_f32_16x16x32_bf16 v[60:63], a[208:211], v[156:159], v[60:63]\n"
        "s_sub_u32 s92, 0, s92\n"
        "v_mfma_f32_16x16x32_bf16 v[184:187], a[208:211], v[160:163], v[184:187]\n"
        "v_mfma_f32_16x16x32_bf16 v[224:227], a[208:211], v[164:167], v[224:227]\n"
        "buffer_load_dwordx4 v[164:167], v240, s[56:59], s55 offen offset:3072 sc1\n"
        "v_mfma_f32_16x16x32_bf16 a[20:23], a[212:215], v[148:151], a[20:23]\n"
        "ds_read_b64_tr_b16 a[208:209], v232 offset:128\n"
        "v_mfma_f32_16x16x32_bf16 v[24:27], a[212:215], v[152:155], v[24:27]\n"
        "ds_read_b64_tr_b16 a[210:211], v232 offset:5248\n"
        "v_mfma_f32_16x16x32_bf16 v[64:67], a[212:215], v[156:159], v[64:67]\n"
        "v_mfma_f32_16x16x32_bf16 v[188:191], a[212:215], v[160:163], v[188:191]\n"
        "ds_read_b64_tr_b16 a[212:213], v235 offset:160\n"
        "v_mfma_f32_16x16x32_bf16 a[24:27], a[216:219], v[148:151], a[24:27]\n"
        "ds_read_b64_tr_b16 a[214:215], v235 offset:5280\n"
        "v_mfma_f32_16x16x32_bf16 v[28:31], a[216:219], v[152:155], v[28:31]\n"
        "v_mfma_f32_16x16x32_bf16 v[68:71], a[216:219], v[156:159], v[68:71]\n"
        "v_mfma_f32_16x16x32_bf16 v[192:195], a[216:219], v[160:163], v[192:195]\n"
        "ds_read_b64_tr_b16 a[216:217], v234 offset:192\n"
        "v_mfma_f32_16x16x32_bf16 a[28:31], a[220:223], v[148:151], a[28:31]\n"
        "ds_read_b64_tr_b16 a[218:219], v234 offset:5312\n"
        "v_mfma_f32_16x16x32_bf16 v[32:35], a[220:223], v[152:155], v[32:35]\n"
        "v_mfma_f32_16x16x32_bf16 v[72:75], a[220:223], v[156:159], v[72:75]\n"
        "v_mfma_f32_16x16x32_bf16 v[196:199], a[220:223], v[160:163], v[196:199]\n"
        "ds_read_b64_tr_b16 a[220:221], v235 offset:224\n"
        "v_mfma_f32_16x16x32_bf16 a[32:35], a[224:227], v[148:151], a[32:35]\n"
        "ds_read_b64_tr_b16 a[222:223], v235 offset:5344\n"
        "v_mfma_f32_16x16x32_bf16 v[36:39], a[224:227], v[152:155], v[36:39]\n"
        "v_mfma_f32_16x16x32_bf16 v[76:79], a[224:227], v[156:159], v[76:79]\n"
        "v_mfma_f32_16x16x32_bf16 v[200:203], a[224:227], v[160:163], v[200:203]\n"
        "ds_read_b64_tr_b16 a[224:225], v234 offset:256\n"
        "v_mfma_f32_16x16x32_bf16 a[36:39], a[228:231], v[148:151], a[36:39]\n"
        "buffer_load_dwordx4 v[148:151], v236, s[56:59], s55 offen offset:3072 sc1\n"
        "v_mfma_f32_16x16x32_bf16 v[40:43], a[228:231], v[152:155], v[40:43]\n"
        "ds_read_b64_tr_b16 a[226:227], v234 offset:5376\n"
        "v_mfma_f32_16x16x32_bf16 v[80:83], a[228:231], v[156:159], v[80:83]\n"
        "s_waitcnt vmcnt(26)\n"
        "v_mfma_f32_16x16x32_bf16 v[204:207], a[228:231], v[160:163], v[204:207]\n"
        "buffer_load_dwordx4 v[152:155], v237, s[56:59], s55 offen offset:3072 sc1\n"
        "s_branch .Lr_BODY\n"
        ".p2align 5\n"
        "s_nop 0\n"
        "s_nop 0\n"
        "s_nop 0\n"
        ".Lr_BODY:\n"
        "v_mfma_f32_16x16x32_bf16 a[0:3], a[192:195], v[88:91], a[0:3]\n"
        "s_waitcnt vmcnt(26)\n"
        "ds_read_b64_tr_b16 a[228:229], v235 offset:288\n"
        "v_mfma_f32_16x16x32_bf16 v[4:7], a[192:195], v[92:95], v[4:7]\n"
        "s_waitcnt vmcnt(21)\n"
        "ds_read_b64_tr_b16 a[230:231], v235 offset:5408\n"
        "v_mfma_f32_16x16x32_bf16 v[44:47], a[192:195], v[96:99], v[44:47]\n"
        "buffer_load_dwordx4 v[156:159], v238, s[56:59], s55 offen offset:3072 sc1\n"
        "v_mfma_f32_16x16x32_bf16 v[168:171], a[192:195], v[100:103], v[168:171]\n"
        "v_mfma_f32_16x16x32_bf16 v[208:211], a[192:195], v[104:107], v[208:211]\n"
        "s_waitcnt lgkmcnt(2)\n"
        "ds_read_b64_tr_b16 a[192:193], v232 offset:10240\n"
        "v_mfma_f32_16x16x32_bf16 a[4:7], a[196:199], v[88:91], a[4:7]\n"
        "buffer_load_dwordx4 v[160:163], v239, s[56:59], s55 offen offset:3072 sc1\n"
        "v_mfma_f32_16x16x32_bf16 v[8:11], a[196:199], v[92:95], v[8:11]\n"
        "ds_read_b64_tr_b16 a[194:195], v232 offset:15360\n"
        "v_mfma_f32_16x16x32_bf16 v[48:51], a[196:199], v[96:99], v[48:51]\n"
        "s_waitcnt vmcnt(20)\n"
        "v_mfma_f32_16x16x32_bf16 v[172:175], a[196:199], v[100:103], v[172:175]\n"
        "v_cmp_ne_u32 vcc, s95, v242\n"
        "v_mfma_f32_16x16x32_bf16 v[212:215], a[196:199], v[104:107], v[212:215]\n"
        "s_cbranch_vccnz .Lr_SPIN\n"
        ".Lr_SPINRET:\n"
        "ds_read_b64_tr_b16 a[196:197], v233 offset:10272\n"
        "v_mfma_f32_16x16x32_bf16 a[8:11], a[200:203], v[88:91], a[8:11]\n"
        "s_lshl_b32 s55, s89, 5\n"
        "ds_read_b64_tr_b16 a[198:199], v233 offset:15392\n"
        "v_mfma_f32_16x16x32_bf16 v[12:15], a[200:203], v[92:95], v[12:15]\n"
        "s_add_u32 s53, s54, 81920\n"
        "v_mfma_f32_16x16x32_bf16 v[52:55], a[200:203], v[96:99], v[52:55]\n"
        "s_ashr_i32 s52, s52, 4\n"
        "v_mfma_f32_16x16x32_bf16 v[176:179], a[200:203], v[100:103], v[176:179]\n"
        "s_and_b32 s54, s52, 15\n"
        "v_mfma_f32_16x16x32_bf16 v[216:219], a[200:203], v[104:107], v[216:219]\n"
        "s_and_b32 s89, s52, 7\n"
        "ds_read_b64_tr_b16 a[200:201], v232 offset:10304\n"
        "v_mfma_f32_16x16x32_bf16 a[12:15], a[204:207], v[88:91], a[12:15]\n"
        "s_mul_i32 s54, s54, 163840\n"
        "ds_read_b64_tr_b16 a[202:203], v232 offset:15424\n"
        "v_mfma_f32_16x16x32_bf16 v[16:19], a[204:207], v[92:95], v[16:19]\n"
        "s_lshl_b32 s89, s89, 7\n"
        "v_mfma_f32_16x16x32_bf16 v[56:59], a[204:207], v[96:99], v[56:59]\n"
        "v_mfma_f32_16x16x32_bf16 v[180:183], a[204:207], v[100:103], v[180:183]\n"
        "v_mfma_f32_16x16x32_bf16 v[220:223], a[204:207], v[104:107], v[220:223]\n"
        "ds_read_b64_tr_b16 a[204:205], v233 offset:10336\n"
        "v_mfma_f32_16x16x32_bf16 a[16:19], a[208:211], v[88:91], a[16:19]\n"
        "ds_read_b64_tr_b16 a[206:207], v233 offset:15456\n"
        "v_mfma_f32_16x16x32_bf16 v[20:23], a[208:211], v[92:95], v[20:23]\n"
        "v_mfma_f32_16x16x32_bf16 v[60:63], a[208:211], v[96:99], v[60:63]\n"
        "v_mfma_f32_16x16x32_bf16 v[184:187], a[208:211], v[100:103], v[184:187]\n"
        "v_mfma_f32_16x16x32_bf16 v[224:227], a[208:211], v[104:107], v[224:227]\n"
        "buffer_load_dwordx4 v[104:107], v240, s[56:59], s55 offen offset:0 sc1\n"
        "v_mfma_f32_16x16x32_bf16 a[20:23], a[212:215], v[88:91], a[20:23]\n"
        "ds_read_b64_tr_b16 a[208:209], v232 offset:10368\n"
        "v_mfma_f32_16x16x32_bf16 v[24:27], a[212:215], v[92:95], v[24:27]\n"
        "ds_read_b64_tr_b16 a[210:211], v232 offset:15488\n"
        "v_mfma_f32_16x16x32_bf16 v[64:67], a[212:215], v[96:99], v[64:67]\n"
        "v_mfma_f32_16x16x32_bf16 v[188:191], a[212:215], v[100:103], v[188:191]\n"
        "ds_read_b64_tr_b16 a[212:213], v235 offset:10400\n"
        "v_mfma_f32_16x16x32_bf16 a[24:27], a[216:219], v[88:91], a[24:27]\n"
        "ds_read_b64_tr_b16 a[214:215], v235 offset:15520\n"
        "v_mfma_f32_16x16x32_bf16 v[28:31], a[216:219], v[92:95], v[28:31]\n"
        "s_cmp_eq_u32 s93, 5\n"
        "v_mfma_f32_16x16x32_bf16 v[68:71], a[216:219], v[96:99], v[68:71]\n"
        "s_cbranch_scc1 .Lr_PUB2\n"
        ".Lr_PUB2RET:\n"
        "v_mfma_f32_16x16x32_bf16 v[192:195], a[216:219], v[100:103], v[192:195]\n"
        "ds_read_b64_tr_b16 a[216:217], v234 offset:10432\n"
        "v_mfma_f32_16x16x32_bf16 a[28:31], a[220:223], v[88:91], a[28:31]\n"
        "ds_read_b64_tr_b16 a[218:219], v234 offset:15552\n"
        "v_mfma_f32_16x16x32_bf16 v[32:35], a[220:223], v[92:95], v[32:35]\n"
        "v_mfma_f32_16x16x32_bf16 v[72:75], a[220:223], v[96:99], v[72:75]\n"
        "v_mfma_f32_16x16x32_bf16 v[196:199], a[220:223], v[100:103], v[196:199]\n"
        "ds_read_b64_tr_b16 a[220:221], v235 offset:10464\n"
        "v_mfma_f32_16x16x32_bf16 a[32:35], a[224:227], v[88:91], a[32:35]\n"
        "ds_read_b64_tr_b16 a[222:223], v235 offset:15584\n"
        "v_mfma_f32_16x16x32_bf16 v[36:39], a[224:227], v[92:95], v[36:39]\n"
        "v_mfma_f32_16x16x32_bf16 v[76:79], a[224:227], v[96:99], v[76:79]\n"
        "v_mfma_f32_16x16x32_bf16 v[200:203], a[224:227], v[100:103], v[200:203]\n"
        "s_waitcnt lgkmcnt(2)\n"
        "ds_read_b64_tr_b16 a[224:225], v234 offset:10496\n"
        "v_mfma_f32_16x16x32_bf16 a[36:39], a[228:231], v[88:91], a[36:39]\n"
        "buffer_load_dwordx4 v[88:91], v236, s[56:59], s55 offen offset:0 sc1\n"
        "v_mfma_f32_16x16x32_bf16 v[40:43], a[228:231], v[92:95], v[40:43]\n"
        "ds_read_b64_tr_b16 a[226:227], v234 offset:15616\n"
        "v_mfma_f32_16x16x32_bf16 v[80:83], a[228:231], v[96:99], v[80:83]\n"
        "s_waitcnt vmcnt(20)\n"
        "v_mfma_f32_16x16x32_bf16 v[204:207], a[228:231], v[100:103], v[204:207]\n"
        "buffer_load_dwordx4 v[92:95], v237, s[56:59], s55 offen offset:0 sc1\n"
        "v_mfma_f32_16x16x32_bf16 a[0:3], a[192:195], v[108:111], a[0:3]\n"
        "s_waitcnt vmcnt(18)\n"
        "ds_read_b64_tr_b16 a[228:229], v235 offset:10528\n"
        "v_mfma_f32_16x16x32_bf16 v[4:7], a[192:195], v[112:115], v[4:7]\n"
        "s_barrier\n"
        "ds_read_b64_tr_b16 a[230:231], v235 offset:15648\n"
        "v_mfma_f32_16x16x32_bf16 v[44:47], a[192:195], v[116:119], v[44:47]\n"
        "buffer_load_dwordx4 v[96:99], v238, s[56:59], s55 offen offset:0 sc1\n"
        "v_mfma_f32_16x16x32_bf16 v[168:171], a[192:195], v[120:123], v[168:171]\n"
        "s_add_u32 m0, s90, 0\n"
        "v_mfma_f32_16x16x32_bf16 v[208:211], a[192:195], v[124:127], v[208:211]\n"
        "ds_read_b64_tr_b16 a[192:193], v232 offset:20480\n"
        "v_mfma_f32_16x16x32_bf16 a[4:7], a[196:199], v[108:111], a[4:7]\n"
        "buffer_load_dwordx4 v244, s[44:47], s53 offen offset:0 lds\n"
        "v_mfma_f32_16x16x32_bf16 v[8:11], a[196:199], v[112:115], v[8:11]\n"
        "ds_read_b64_tr_b16 a[194:195], v232 offset:25600\n"
        "v_mfma_f32_16x16x32_bf16 v[48:51], a[196:199], v[116:119], v[48:51]\n"
        "v_mfma_f32_16x16x32_bf16 v[172:175], a[196:199], v[120:123], v[172:175]\n"
        "buffer_load_dwordx4 v245, s[44:47], s53 offen offset:1024 lds\n"
        "v_mfma_f32_16x16x32_bf16 v[212:215], a[196:199], v[124:127], v[212:215]\n"
        "ds_read_b64_tr_b16 a[196:197], v233 offset:20512\n"
        "v_mfma_f32_16x16x32_bf16 a[8:11], a[200:203], v[108:111], a[8:11]\n"
        "ds_read_b64_tr_b16 a[198:199], v233 offset:25632\n"
        "v_mfma_f32_16x16x32_bf16 v[12:15], a[200:203], v[112:115], v[12:15]\n"
        "buffer_load_dwordx4 v246, s[44:47], s53 offen offset:2048 lds\n"
        "v_mfma_f32_16x16x32_bf16 v[52:55], a[200:203], v[116:119], v[52:55]\n"
        "v_mfma_f32_16x16x32_bf16 v[176:179], a[200:203], v[120:123], v[176:179]\n"
        "v_mfma_f32_16x16x32_bf16 v[216:219], a[200:203], v[124:127], v[216:219]\n"
        "buffer_load_dwordx4 v[100:103], v239, s[56:59], s55 offen offset:0 sc1\n"
        "v_mfma_f32_16x16x32_bf16 a[12:15], a[204:207], v[108:111], a[12:15]\n"
        "ds_read_b64_tr_b16 a[200:201], v232 offset:20544\n"
        "v_mfma_f32_16x16x32_bf16 v[16:19], a[204:207], v[112:115], v[16:19]\n"
        "ds_read_b64_tr_b16 a[202:203], v232 offset:25664\n"
        "v_mfma_f32_16x16x32_bf16 v[56:59], a[204:207], v[116:119], v[56:59]\n"
        "buffer_load_dwordx4 v247, s[44:47], s53 offen offset:3072 lds\n"
        "v_mfma_f32_16x16x32_bf16 v[180:183], a[204:207], v[120:123], v[180:183]\n"
        "s_add_u32 m0, s90, 4096\n"
        "v_mfma_f32_16x16x32_bf16 v[220:223], a[204:207], v[124:127], v[220:223]\n"
        "ds_read_b64_tr_b16 a[204:205], v233 offset:20576\n"
        "v_mfma_f32_16x16x32_bf16 a[16:19], a[208:211], v[108:111], a[16:19]\n"
        "buffer_load_dwordx4 v248, s[44:47], s53 offen offset:0 lds\n"
        "v_mfma_f32_16x16x32_bf16 v[20:23], a[208:211], v[112:115], v[20:23]\n"
        "s_sub_u32 s90, s90, s92\n"
        "ds_read_b64_tr_b16 a[206:207], v233 offset:25696\n"
        "v_mfma_f32_16x16x32_bf16 v[60:63], a[208:211], v[116:119], v[60:63]\n"
        "v_mfma_f32_16x16x32_bf16 v[184:187], a[208:211], v[120:123], v[184:187]\n"
        "buffer_load_dword v242, v243, s[96:99], s89 offen offset:128 sc1\n"
        "v_mfma_f32_16x16x32_bf16 v[224:227], a[208:211], v[124:127], v[224:227]\n"
        "ds_read_b64_tr_b16 a[208:209], v232 offset:20608\n"
        "v_mfma_f32_16x16x32_bf16 a[20:23], a[212:215], v[108:111], a[20:23]\n"
        "ds_read_b64_tr_b16 a[210:211], v232 offset:25728\n"
        "v_mfma_f32_16x16x32_bf16 v[24:27], a[212:215], v[112:115], v[24:27]\n"
        "buffer_load_dwordx4 v[124:127], v240, s[56:59], s55 offen offset:1024 sc1\n"
        "v_mfma_f32_16x16x32_bf16 v[64:67], a[212:215], v[116:119], v[64:67]\n"
        "v_mfma_f32_16x16x32_bf16 v[188:191], a[212:215], v[120:123], v[188:191]\n"
        "ds_read_b64_tr_b16 a[212:213], v235 offset:20640\n"
        "v_mfma_f32_16x16x32_bf16 a[24:27], a[216:219], v[108:111], a[24:27]\n"
        "ds_read_b64_tr_b16 a[214:215], v235 offset:25760\n"
        "v_mfma_f32_16x16x32_bf16 v[28:31], a[216:219], v[112:115], v[28:31]\n"
        "v_mfma_f32_16x16x32_bf16 v[68:71], a[216:219], v[116:119], v[68:71]\n"
        "v_mfma_f32_16x16x32_bf16 v[192:195], a[216:219], v[120:123], v[192:195]\n"
        "s_waitcnt lgkmcnt(2)\n"
        "ds_read_b64_tr_b16 a[216:217], v234 offset:20672\n"
        "v_mfma_f32_16x16x32_bf16 a[28:31], a[220:223], v[108:111], a[28:31]\n"
        "ds_read_b64_tr_b16 a[218:219], v234 offset:25792\n"
        "v_mfma_f32_16x16x32_bf16 v[32:35], a[220:223], v[112:115], v[32:35]\n"
        "v_mfma_f32_16x16x32_bf16 v[72:75], a[220:223], v[116:119], v[72:75]\n"
        "v_mfma_f32_16x16x32_bf16 v[196:199], a[220:223], v[120:123], v[196:199]\n"
        "ds_read_b64_tr_b16 a[220:221], v235 offset:20704\n"
        "v_mfma_f32_16x16x32_bf16 a[32:35], a[224:227], v[108:111], a[32:35]\n"
        "ds_read_b64_tr_b16 a[222:223], v235 offset:25824\n"
        "v_mfma_f32_16x16x32_bf16 v[36:39], a[224:227], v[112:115], v[36:39]\n"
        "v_mfma_f32_16x16x32_bf16 v[76:79], a[224:227], v[116:119], v[76:79]\n"
        "v_mfma_f32_16x16x32_bf16 v[200:203], a[224:227], v[120:123], v[200:203]\n"
        "ds_read_b64_tr_b16 a[224:225], v234 offset:20736\n"
        "v_mfma_f32_16x16x32_bf16 a[36:39], a[228:231], v[108:111], a[36:39]\n"
        "buffer_load_dwordx4 v[108:111], v236, s[56:59], s55 offen offset:1024 sc1\n"
        "v_mfma_f32_16x16x32_bf16 v[40:43], a[228:231], v[112:115], v[40:43]\n"
        "ds_read_b64_tr_b16 a[226:227], v234 offset:25856\n"
        "v_mfma_f32_16x16x32_bf16 v[80:83], a[228:231], v[116:119], v[80:83]\n"
        "s_waitcnt vmcnt(26)\n"
        "v_mfma_f32_16x16x32_bf16 v[204:207], a[228:231], v[120:123], v[204:207]\n"
        "buffer_load_dwordx4 v[112:115], v237, s[56:59], s55 offen offset:1024 sc1\n"
        "v_mfma_f32_16x16x32_bf16 a[0:3], a[192:195], v[128:131], a[0:3]\n"
        "s_waitcnt vmcnt(26)\n"
        "ds_read_b64_tr_b16 a[228:229], v235 offset:20768\n"
        "v_mfma_f32_16x16x32_bf16 v[4:7], a[192:195], v[132:135], v[4:7]\n"
        "s_waitcnt vmcnt(21)\n"
        "ds_read_b64_tr_b16 a[230:231], v235 offset:25888\n"
        "v_mfma_f32_16x16x32_bf16 v[44:47], a[192:195], v[136:139], v[44:47]\n"
        "buffer_load_dwordx4 v[116:119], v238, s[56:59], s55 offen offset:1024 sc1\n"
        "v_mfma_f32_16x16x32_bf16 v[168:171], a[192:195], v[140:143], v[168:171]\n"
        "v_mfma_f32_16x16x32_bf16 v[208:211], a[192:195], v[144:147], v[208:211]\n"
        "ds_read_b64_tr_b16 a[192:193], v232 offset:30720\n"
        "v_mfma_f32_16x16x32_bf16 a[4:7], a[196:199], v[128:131], a[4:7]\n"
        "buffer_load_dwordx4 v[120:123], v239, s[56:59], s55 offen offset:1024 sc1\n"
        "v_mfma_f32_16x16x32_bf16 v[8:11], a[196:199], v[132:135], v[8:11]\n"
        "ds_read_b64_tr_b16 a[194:195], v232 offset:35840\n"
        "v_mfma_f32_16x16x32_bf16 v[48:51], a[196:199], v[136:139], v[48:51]\n"
        "v_mfma_f32_16x16x32_bf16 v[172:175], a[196:199], v[140:143], v[172:175]\n"
        "v_mfma_f32_16x16x32_bf16 v[212:215], a[196:199], v[144:147], v[212:215]\n"
        "ds_read_b64_tr_b16 a[196:197], v233 offset:30752\n"
        "v_mfma_f32_16x16x32_bf16 a[8:11], a[200:203], v[128:131], a[8:11]\n"
        "ds_read_b64_tr_b16 a[198:199], v233 offset:35872\n"
        "v_mfma_f32_16x16x32_bf16 v[12:15], a[200:203], v[132:135], v[12:15]\n"
        "v_mfma_f32_16x16x32_bf16 v[52:55], a[200:203], v[136:139], v[52:55]\n"
        "v_mfma_f32_16x16x32_bf16 v[176:179], a[200:203], v[140:143], v[176:179]\n"
        "v_mfma_f32_16x16x32_bf16 v[216:219], a[200:203], v[144:147], v[216:219]\n"
        "ds_read_b64_tr_b16 a[200:201], v232 offset:30784\n"
        "v_mfma_f32_16x16x32_bf16 a[12:15], a[204:207], v[128:131], a[12:15]\n"
        "ds_read_b64_tr_b16 a[202:203], v232 offset:35904\n"
        "v_mfma_f32_16x16x32_bf16 v[16:19], a[204:207], v[132:135], v[16:19]\n"
        "v_mfma_f32_16x16x32_bf16 v[56:59], a[204:207], v[136:139], v[56:59]\n"
        "v_mfma_f32_16x16x32_bf16 v[180:183], a[204:207], v[140:143], v[180:183]\n"
        "v_mfma_f32_16x16x32_bf16 v[220:223], a[204:207], v[144:147], v[220:223]\n"
        "ds_read_b64_tr_b16 a[204:205], v233 offset:30816\n"
        "v_mfma_f32_16x16x32_bf16 a[16:19], a[208:211], v[128:131], a[16:19]\n"
        "ds_read_b64_tr_b16 a[206:207], v233 offset:35936\n"
        "v_mfma_f32_16x16x32_bf16 v[20:23], a[208:211], v[132:135], v[20:23]\n"
        "v_add_u32 v233, s92, v233\n"
        "v_mfma_f32_16x16x32_bf16 v[60:63], a[208:211], v[136:139], v[60:63]\n"
        "v_mfma_f32_16x16x32_bf16 v[184:187], a[208:211], v[140:143], v[184:187]\n"
        "s_waitcnt lgkmcnt(2)\n"
        "v_mfma_f32_16x16x32_bf16 v[224:227], a[208:211], v[144:147], v[224:227]\n"
        "buffer_load_dwordx4 v[144:147], v240, s[56:59], s55 offen offset:2048 sc1\n"
        "v_mfma_f32_16x16x32_bf16 a[20:23], a[212:215], v[128:131], a[20:23]\n"
        "ds_read_b64_tr_b16 a[208:209], v232 offset:30848\n"
        "v_mfma_f32_16x16x32_bf16 v[24:27], a[212:215], v[132:135], v[24:27]\n"
        "ds_read_b64_tr_b16 a[210:211], v232 offset:35968\n"
        "v_mfma_f32_16x16x32_bf16 v[64:67], a[212:215], v[136:139], v[64:67]\n"
        "v_add_u32 v232, s92, v232\n"
        "v_mfma_f32_16x16x32_bf16 v[188:191], a[212:215], v[140:143], v[188:191]\n"
        "ds_read_b64_tr_b16 a[212:213], v235 offset:30880\n"
        "v_mfma_f32_16x16x32_bf16 a[24:27], a[216:219], v[128:131], a[24:27]\n"
        "ds_read_b64_tr_b16 a[214:215], v235 offset:36000\n"
        "v_mfma_f32_16x16x32_bf16 v[28:31], a[216:219], v[132:135], v[28:31]\n"
        "v_mfma_f32_16x16x32_bf16 v[68:71], a[216:219], v[136:139], v[68:71]\n"
        "v_mfma_f32_16x16x32_bf16 v[192:195], a[216:219], v[140:143], v[192:195]\n"
        "ds_read_b64_tr_b16 a[216:217], v234 offset:30912\n"
        "v_mfma_f32_16x16x32_bf16 a[28:31], a[220:223], v[128:131], a[28:31]\n"
        "ds_read_b64_tr_b16 a[218:219], v234 offset:36032\n"
        "v_mfma_f32_16x16x32_bf16 v[32:35], a[220:223], v[132:135], v[32:35]\n"
        "v_mfma_f32_16x16x32_bf16 v[72:75], a[220:223], v[136:139], v[72:75]\n"
        "v_mfma_f32_16x16x32_bf16 v[196:199], a[220:223], v[140:143], v[196:199]\n"
        "ds_read_b64_tr_b16 a[220:221], v235 offset:30944\n"
        "v_mfma_f32_16x16x32_bf16 a[32:35], a[224:227], v[128:131], a[32:35]\n"
        "ds_read_b64_tr_b16 a[222:223], v235 offset:36064\n"
        "v_mfma_f32_16x16x32_bf16 v[36:39], a[224:227], v[132:135], v[36:39]\n"
        "v_mfma_f32_16x16x32_bf16 v[76:79], a[224:227], v[136:139], v[76:79]\n"
        "v_mfma_f32_16x16x32_bf16 v[200:203], a[224:227], v[140:143], v[200:203]\n"
        "ds_read_b64_tr_b16 a[224:225], v234 offset:30976\n"
        "v_mfma_f32_16x16x32_bf16 a[36:39], a[228:231], v[128:131], a[36:39]\n"
        "buffer_load_dwordx4 v[128:131], v236, s[56:59], s55 offen offset:2048 sc1\n"
        "v_mfma_f32_16x16x32_bf16 v[40:43], a[228:231], v[132:135], v[40:43]\n"
        "ds_read_b64_tr_b16 a[226:227], v234 offset:36096\n"
        "v_mfma_f32_16x16x32_bf16 v[80:83], a[228:231], v[136:139], v[80:83]\n"
        "s_waitcnt vmcnt(21)\n"
        "v_add_u32 v234, s92, v234\n"
        "v_mfma_f32_16x16x32_bf16 v[204:207], a[228:231], v[140:143], v[204:207]\n"
        "buffer_load_dwordx4 v[132:135], v237, s[56:59], s55 offen offset:2048 sc1\n"
        "v_mfma_f32_16x16x32_bf16 a[0:3], a[192:195], v[148:151], a[0:3]\n"
        "s_waitcnt vmcnt(19)\n"
        "ds_read_b64_tr_b16 a[228:229], v235 offset:31008\n"
        "v_mfma_f32_16x16x32_bf16 v[4:7], a[192:195], v[152:155], v[4:7]\n"
        "s_barrier\n"
        "ds_read_b64_tr_b16 a[230:231], v235 offset:36128\n"
        "v_mfma_f32_16x16x32_bf16 v[44:47], a[192:195], v[156:159], v[44:47]\n"
        "buffer_load_dwordx4 v[136:139], v238, s[56:59], s55 offen offset:2048 sc1\n"
        "v_mfma_f32_16x16x32_bf16 v[168:171], a[192:195], v[160:163], v[168:171]\n"
        "s_add_u32 m0, s91, 0\n"
        "v_add_u32 v235, s92, v235\n"
        "v_mfma_f32_16x16x32_bf16 v[208:211], a[192:195], v[164:167], v[208:211]\n"
        "ds_read_b64_tr_b16 a[192:193], v232 offset:0\n"
        "v_mfma_f32_16x16x32_bf16 a[4:7], a[196:199], v[148:151], a[4:7]\n"
        "buffer_load_dwordx4 v244, s[44:47], s54 offen offset:0 lds\n"
        "v_mfma_f32_16x16x32_bf16 v[8:11], a[196:199], v[152:155], v[8:11]\n"
        "ds_read_b64_tr_b16 a[194:195], v232 offset:5120\n"
        "v_mfma_f32_16x16x32_bf16 v[48:51], a[196:199], v[156:159], v[48:51]\n"
        "v_mfma_f32_16x16x32_bf16 v[172:175], a[196:199], v[160:163], v[172:175]\n"
        "buffer_load_dwordx4 v245, s[44:47], s54 offen offset:1024 lds\n"
        "v_mfma_f32_16x16x32_bf16 v[212:215], a[196:199], v[164:167], v[212:215]\n"
        "ds_read_b64_tr_b16 a[196:197], v233 offset:32\n"
        "v_mfma_f32_16x16x32_bf16 a[8:11], a[200:203], v[148:151], a[8:11]\n"
        "ds_read_b64_tr_b16 a[198:199], v233 offset:5152\n"
        "v_mfma_f32_16x16x32_bf16 v[12:15], a[200:203], v[152:155], v[12:15]\n"
        "buffer_load_dwordx4 v246, s[44:47], s54 offen offset:2048 lds\n"
        "v_mfma_f32_16x16x32_bf16 v[52:55], a[200:203], v[156:159], v[52:55]\n"
        "v_mfma_f32_16x16x32_bf16 v[176:179], a[200:203], v[160:163], v[176:179]\n"
        "s_waitcnt lgkmcnt(2)\n"
        "v_mfma_f32_16x16x32_bf16 v[216:219], a[200:203], v[164:167], v[216:219]\n"
        "buffer_load_dwordx4 v[140:143], v239, s[56:59], s55 offen offset:2048 sc1\n"
        "v_mfma_f32_16x16x32_bf16 a[12:15], a[204:207], v[148:151], a[12:15]\n"
        "ds_read_b64_tr_b16 a[200:201], v232 offset:64\n"
        "v_mfma_f32_16x16x32_bf16 v[16:19], a[204:207], v[152:155], v[16:19]\n"
        "ds_read_b64_tr_b16 a[202:203], v232 offset:5184\n"
        "v_mfma_f32_16x16x32_bf16 v[56:59], a[204:207], v[156:159], v[56:59]\n"
        "buffer_load_dwordx4 v247, s[44:47], s54 offen offset:3072 lds\n"
        "v_mfma_f32_16x16x32_bf16 v[180:183], a[204:207], v[160:163], v[180:183]\n"
        "s_add_u32 m0, s91, 4096\n"
        "v_mfma_f32_16x16x32_bf16 v[220:223], a[204:207], v[164:167], v[220:223]\n"
        "ds_read_b64_tr_b16 a[204:205], v233 offset:96\n"
        "v_mfma_f32_16x16x32_bf16 a[16:19], a[208:211], v[148:151], a[16:19]\n"
        "buffer_load_dwordx4 v248, s[44:47], s54 offen offset:0 lds\n"
        "v_mfma_f32_16x16x32_bf16 v[20:23], a[208:211], v[152:155], v[20:23]\n"
        "s_add_u32 s91, s91, s92\n"
        "ds_read_b64_tr_b16 a[206:207], v233 offset:5216\n"
        "v_mfma_f32_16x16x32_bf16 v[60:63], a[208:211], v[156:159], v[60:63]\n"
        "s_sub_u32 s92, 0, s92\n"
        "v_mfma_f32_16x16x32_bf16 v[184:187], a[208:211], v[160:163], v[184:187]\n"
        "v_mfma_f32_16x16x32_bf16 v[224:227], a[208:211], v[164:167], v[224:227]\n"
        "buffer_load_dwordx4 v[164:167], v240, s[56:59], s55 offen offset:3072 sc1\n"
        "v_mfma_f32_16x16x32_bf16 a[20:23], a[212:215], v[148:151], a[20:23]\n"
        "ds_read_b64_tr_b16 a[208:209], v232 offset:128\n"
        "v_mfma_f32_16x16x32_bf16 v[24:27], a[212:215], v[152:155], v[24:27]\n"
        "ds_read_b64_tr_b16 a[210:211], v232 offset:5248\n"
        "v_mfma_f32_16x16x32_bf16 v[64:67], a[212:215], v[156:159], v[64:67]\n"
        "v_mfma_f32_16x16x32_bf16 v[188:191], a[212:215], v[160:163], v[188:191]\n"
        "ds_read_b64_tr_b16 a[212:213], v235 offset:160\n"
        "v_mfma_f32_16x16x32_bf16 a[24:27], a[216:219], v[148:151], a[24:27]\n"
        "ds_read_b64_tr_b16 a[214:215], v235 offset:5280\n"
        "v_mfma_f32_16x16x32_bf16 v[28:31], a[216:219], v[152:155], v[28:31]\n"
        "v_mfma_f32_16x16x32_bf16 v[68:71], a[216:219], v[156:159], v[68:71]\n"
        "v_mfma_f32_16x16x32_bf16 v[192:195], a[216:219], v[160:163], v[192:195]\n"
        "ds_read_b64_tr_b16 a[216:217], v234 offset:192\n"
        "v_mfma_f32_16x16x32_bf16 a[28:31], a[220:223], v[148:151], a[28:31]\n"
        "ds_read_b64_tr_b16 a[218:219], v234 offset:5312\n"
        "v_mfma_f32_16x16x32_bf16 v[32:35], a[220:223], v[152:155], v[32:35]\n"
        "v_mfma_f32_16x16x32_bf16 v[72:75], a[220:223], v[156:159], v[72:75]\n"
        "v_mfma_f32_16x16x32_bf16 v[196:199], a[220:223], v[160:163], v[196:199]\n"
        "ds_read_b64_tr_b16 a[220:221], v235 offset:224\n"
        "v_mfma_f32_16x16x32_bf16 a[32:35], a[224:227], v[148:151], a[32:35]\n"
        "ds_read_b64_tr_b16 a[222:223], v235 offset:5344\n"
        "v_mfma_f32_16x16x32_bf16 v[36:39], a[224:227], v[152:155], v[36:39]\n"
        "v_mfma_f32_16x16x32_bf16 v[76:79], a[224:227], v[156:159], v[76:79]\n"
        "v_mfma_f32_16x16x32_bf16 v[200:203], a[224:227], v[160:163], v[200:203]\n"
        "ds_read_b64_tr_b16 a[224:225], v234 offset:256\n"
        "v_mfma_f32_16x16x32_bf16 a[36:39], a[228:231], v[148:151], a[36:39]\n"
        "buffer_load_dwordx4 v[148:151], v236, s[56:59], s55 offen offset:3072 sc1\n"
        "v_mfma_f32_16x16x32_bf16 v[40:43], a[228:231], v[152:155], v[40:43]\n"
        "ds_read_b64_tr_b16 a[226:227], v234 offset:5376\n"
        "v_mfma_f32_16x16x32_bf16 v[80:83], a[228:231], v[156:159], v[80:83]\n"
        "s_waitcnt vmcnt(26)\n"
        "v_mfma_f32_16x16x32_bf16 v[204:207], a[228:231], v[160:163], v[204:207]\n"
        "buffer_load_dwordx4 v[152:155], v237, s[56:59], s55 offen offset:3072 sc1\n"
        "s_sub_u32 s93, s93, 1\n"
        "s_cbranch_scc0 .Lr_BODY\n"
        "v_mfma_f32_16x16x32_bf16 a[0:3], a[192:195], v[88:91], a[0:3]\n"
        "s_waitcnt vmcnt(26)\n"
        "ds_read_b64_tr_b16 a[228:229], v235 offset:288\n"
        "v_mfma_f32_16x16x32_bf16 v[4:7], a[192:195], v[92:95], v[4:7]\n"
        "s_waitcnt vmcnt(21)\n"
        "ds_read_b64_tr_b16 a[230:231], v235 offset:5408\n"
        "v_mfma_f32_16x16x32_bf16 v[44:47], a[192:195], v[96:99], v[44:47]\n"
        "buffer_load_dwordx4 v[156:159], v238, s[56:59], s55 offen offset:3072 sc1\n"
        "v_mfma_f32_16x16x32_bf16 v[168:171], a[192:195], v[100:103], v[168:171]\n"
        "v_mfma_f32_16x16x32_bf16 v[208:211], a[192:195], v[104:107], v[208:211]\n"
        "s_waitcnt lgkmcnt(15)\n"
        "ds_read_b64_tr_b16 a[192:193], v232 offset:10240\n"
        "v_mfma_f32_16x16x32_bf16 a[4:7], a[196:199], v[88:91], a[4:7]\n"
        "buffer_load_dwordx4 v[160:163], v239, s[56:59], s55 offen offset:3072 sc1\n"
        "v_mfma_f32_16x16x32_bf16 v[8:11], a[196:199], v[92:95], v[8:11]\n"
        "ds_read_b64_tr_b16 a[194:195], v232 offset:15360\n"
        "v_mfma_f32_16x16x32_bf16 v[48:51], a[196:199], v[96:99], v[48:51]\n"
        "v_mfma_f32_16x16x32_bf16 v[172:175], a[196:199], v[100:103], v[172:175]\n"
        "v_mfma_f32_16x16x32_bf16 v[212:215], a[196:199], v[104:107], v[212:215]\n"
        "s_waitcnt lgkmcnt(2)\n"
        "ds_read_b64_tr_b16 a[196:197], v233 offset:10272\n"
        "v_mfma_f32_16x16x32_bf16 a[8:11], a[200:203], v[88:91], a[8:11]\n"
        "ds_read_b64_tr_b16 a[198:199], v233 offset:15392\n"
        "v_mfma_f32_16x16x32_bf16 v[12:15], a[200:203], v[92:95], v[12:15]\n"
        "v_mfma_f32_16x16x32_bf16 v[52:55], a[200:203], v[96:99], v[52:55]\n"
        "v_mfma_f32_16x16x32_bf16 v[176:179], a[200:203], v[100:103], v[176:179]\n"
        "v_mfma_f32_16x16x32_bf16 v[216:219], a[200:203], v[104:107], v[216:219]\n"
        "ds_read_b64_tr_b16 a[200:201], v232 offset:10304\n"
        "v_mfma_f32_16x16x32_bf16 a[12:15], a[204:207], v[88:91], a[12:15]\n"
        "ds_read_b64_tr_b16 a[202:203], v232 offset:15424\n"
        "v_mfma_f32_16x16x32_bf16 v[16:19], a[204:207], v[92:95], v[16:19]\n"
        "v_mfma_f32_16x16x32_bf16 v[56:59], a[204:207], v[96:99], v[56:59]\n"
        "v_mfma_f32_16x16x32_bf16 v[180:183], a[204:207], v[100:103], v[180:183]\n"
        "v_mfma_f32_16x16x32_bf16 v[220:223], a[204:207], v[104:107], v[220:223]\n"
        "ds_read_b64_tr_b16 a[204:205], v233 offset:10336\n"
        "v_mfma_f32_16x16x32_bf16 a[16:19], a[208:211], v[88:91], a[16:19]\n"
        "ds_read_b64_tr_b16 a[206:207], v233 offset:15456\n"
        "v_mfma_f32_16x16x32_bf16 v[20:23], a[208:211], v[92:95], v[20:23]\n"
        "v_mfma_f32_16x16x32_bf16 v[60:63], a[208:211], v[96:99], v[60:63]\n"
        "v_mfma_f32_16x16x32_bf16 v[184:187], a[208:211], v[100:103], v[184:187]\n"
        "v_mfma_f32_16x16x32_bf16 v[224:227], a[208:211], v[104:107], v[224:227]\n"
        "ds_read_b64_tr_b16 a[208:209], v232 offset:10368\n"
        "v_mfma_f32_16x16x32_bf16 a[20:23], a[212:215], v[88:91], a[20:23]\n"
        "ds_read_b64_tr_b16 a[210:211], v232 offset:15488\n"
        "v_mfma_f32_16x16x32_bf16 v[24:27], a[212:215], v[92:95], v[24:27]\n"
        "v_mfma_f32_16x16x32_bf16 v[64:67], a[212:215], v[96:99], v[64:67]\n"
        "v_mfma_f32_16x16x32_bf16 v[188:191], a[212:215], v[100:103], v[188:191]\n"
        "ds_read_b64_tr_b16 a[212:213], v235 offset:10400\n"
        "v_mfma_f32_16x16x32_bf16 a[24:27], a[216:219], v[88:91], a[24:27]\n"
        "ds_read_b64_tr_b16 a[214:215], v235 offset:15520\n"
        "v_mfma_f32_16x16x32_bf16 v[28:31], a[216:219], v[92:95], v[28:31]\n"
        "v_mfma_f32_16x16x32_bf16 v[68:71], a[216:219], v[96:99], v[68:71]\n"
        "v_mfma_f32_16x16x32_bf16 v[192:195], a[216:219], v[100:103], v[192:195]\n"
        "ds_read_b64_tr_b16 a[216:217], v234 offset:10432\n"
        "v_mfma_f32_16x16x32_bf16 a[28:31], a[220:223], v[88:91], a[28:31]\n"
        "ds_read_b64_tr_b16 a[218:219], v234 offset:15552\n"
        "v_mfma_f32_16x16x32_bf16 v[32:35], a[220:223], v[92:95], v[32:35]\n"
        "v_mfma_f32_16x16x32_bf16 v[72:75], a[220:223], v[96:99], v[72:75]\n"
        "v_mfma_f32_16x16x32_bf16 v[196:199], a[220:223], v[100:103], v[196:199]\n"
        "ds_read_b64_tr_b16 a[220:221], v235 offset:10464\n"
        "v_mfma_f32_16x16x32_bf16 a[32:35], a[224:227], v[88:91], a[32:35]\n"
        "ds_read_b64_tr_b16 a[222:223], v235 offset:15584\n"
        "v_mfma_f32_16x16x32_bf16 v[36:39], a[224:227], v[92:95], v[36:39]\n"
        "v_mfma_f32_16x16x32_bf16 v[76:79], a[224:227], v[96:99], v[76:79]\n"
        "v_mfma_f32_16x16x32_bf16 v[200:203], a[224:227], v[100:103], v[200:203]\n"
        "ds_read_b64_tr_b16 a[224:225], v234 offset:10496\n"
        "v_mfma_f32_16x16x32_bf16 a[36:39], a[228:231], v[88:91], a[36:39]\n"
        "ds_read_b64_tr_b16 a[226:227], v234 offset:15616\n"
        "v_mfma_f32_16x16x32_bf16 v[40:43], a[228:231], v[92:95], v[40:43]\n"
        "v_mfma_f32_16x16x32_bf16 v[80:83], a[228:231], v[96:99], v[80:83]\n"
        "v_mfma_f32_16x16x32_bf16 v[204:207], a[228:231], v[100:103], v[204:207]\n"
        "s_waitcnt vmcnt(18) lgkmcnt(2)\n"
        "ds_read_b64_tr_b16 a[228:229], v235 offset:10528\n"
        "v_mfma_f32_16x16x32_bf16 a[0:3], a[192:195], v[108:111], a[0:3]\n"
        "s_waitcnt vmcnt(17)\n"
        "ds_read_b64_tr_b16 a[230:231], v235 offset:15648\n"
        "v_mfma_f32_16x16x32_bf16 v[4:7], a[192:195], v[112:115], v[4:7]\n"
        "s_waitcnt vmcnt(16)\n"
        "s_barrier\n"
        "v_mfma_f32_16x16x32_bf16 v[44:47], a[192:195], v[116:119], v[44:47]\n"
        "s_waitcnt vmcnt(15)\n"
        "ds_read_b64_tr_b16 a[120:121], v232 offset:30720\n"
        "v_mfma_f32_16x16x32_bf16 v[168:171], a[192:195], v[120:123], v[168:171]\n"
        "ds_read_b64_tr_b16 a[122:123], v232 offset:35840\n"
        "v_mfma_f32_16x16x32_bf16 v[208:211], a[192:195], v[124:127], v[208:211]\n"
        "ds_read_b64_tr_b16 a[192:193], v232 offset:20480\n"
        "v_mfma_f32_16x16x32_bf16 a[4:7], a[196:199], v[108:111], a[4:7]\n"
        "ds_read_b64_tr_b16 a[194:195], v232 offset:25600\n"
        "v_mfma_f32_16x16x32_bf16 v[8:11], a[196:199], v[112:115], v[8:11]\n"
        "ds_read_b64_tr_b16 a[124:125], v233 offset:30752\n"
        "v_mfma_f32_16x16x32_bf16 v[48:51], a[196:199], v[116:119], v[48:51]\n"
        "ds_read_b64_tr_b16 a[126:127], v233 offset:35872\n"
        "v_mfma_f32_16x16x32_bf16 v[172:175], a[196:199], v[120:123], v[172:175]\n"
        "ds_read_b64_tr_b16 a[128:129], v232 offset:30784\n"
        "v_mfma_f32_16x16x32_bf16 v[212:215], a[196:199], v[124:127], v[212:215]\n"
        "ds_read_b64_tr_b16 a[196:197], v233 offset:20512\n"
        "v_mfma_f32_16x16x32_bf16 a[8:11], a[200:203], v[108:111], a[8:11]\n"
        "ds_read_b64_tr_b16 a[198:199], v233 offset:25632\n"
        "v_mfma_f32_16x16x32_bf16 v[12:15], a[200:203], v[112:115], v[12:15]\n"
        "ds_read_b64_tr_b16 a[130:131], v232 offset:35904\n"
        "v_mfma_f32_16x16x32_bf16 v[52:55], a[200:203], v[116:119], v[52:55]\n"
        "ds_read_b64_tr_b16 a[132:133], v233 offset:30816\n"
        "v_mfma_f32_16x16x32_bf16 v[176:179], a[200:203], v[120:123], v[176:179]\n"
        "ds_read_b64_tr_b16 a[134:135], v233 offset:35936\n"
        "v_mfma_f32_16x16x32_bf16 v[216:219], a[200:203], v[124:127], v[216:219]\n"
        "ds_read_b64_tr_b16 a[200:201], v232 offset:20544\n"
        "v_mfma_f32_16x16x32_bf16 a[12:15], a[204:207], v[108:111], a[12:15]\n"
        "ds_read_b64_tr_b16 a[202:203], v232 offset:25664\n"
        "v_mfma_f32_16x16x32_bf16 v[16:19], a[204:207], v[112:115], v[16:19]\n"
        "ds_read_b64_tr_b16 a[136:137], v232 offset:30848\n"
        "v_mfma_f32_16x16x32_bf16 v[56:59], a[204:207], v[116:119], v[56:59]\n"
        "ds_read_b64_tr_b16 a[138:139], v232 offset:35968\n"
        "v_mfma_f32_16x16x32_bf16 v[180:183], a[204:207], v[120:123], v[180:183]\n"
        "ds_read_b64_tr_b16 a[140:141], v235 offset:30880\n"
        "v_mfma_f32_16x16x32_bf16 v[220:223], a[204:207], v[124:127], v[220:223]\n"
        "ds_read_b64_tr_b16 a[204:205], v233 offset:20576\n"
        "v_mfma_f32_16x16x32_bf16 a[16:19], a[208:211], v[108:111], a[16:19]\n"
        "ds_read_b64_tr_b16 a[206:207], v233 offset:25696\n"
        "v_mfma_f32_16x16x32_bf16 v[20:23], a[208:211], v[112:115], v[20:23]\n"
        "ds_read_b64_tr_b16 a[142:143], v235 offset:36000\n"
        "v_mfma_f32_16x16x32_bf16 v[60:63], a[208:211], v[116:119], v[60:63]\n"
        "ds_read_b64_tr_b16 a[144:145], v234 offset:30912\n"
        "v_mfma_f32_16x16x32_bf16 v[184:187], a[208:211], v[120:123], v[184:187]\n"
        "ds_read_b64_tr_b16 a[146:147], v234 offset:36032\n"
        "v_mfma_f32_16x16x32_bf16 v[224:227], a[208:211], v[124:127], v[224:227]\n"
        "ds_read_b64_tr_b16 a[208:209], v232 offset:20608\n"
        "v_mfma_f32_16x16x32_bf16 a[20:23], a[212:215], v[108:111], a[20:23]\n"
        "ds_read_b64_tr_b16 a[210:211], v232 offset:25728\n"
        "v_mfma_f32_16x16x32_bf16 v[24:27], a[212:215], v[112:115], v[24:27]\n"
        "ds_read_b64_tr_b16 a[148:149], v235 offset:30944\n"
        "v_mfma_f32_16x16x32_bf16 v[64:67], a[212:215], v[116:119], v[64:67]\n"
        "ds_read_b64_tr_b16 a[150:151], v235 offset:36064\n"
        "v_mfma_f32_16x16x32_bf16 v[188:191], a[212:215], v[120:123], v[188:191]\n"
        "ds_read_b64_tr_b16 a[212:213], v235 offset:20640\n"
        "v_mfma_f32_16x16x32_bf16 a[24:27], a[216:219], v[108:111], a[24:27]\n"
        "ds_read_b64_tr_b16 a[214:215], v235 offset:25760\n"
        "v_mfma_f32_16x16x32_bf16 v[28:31], a[216:219], v[112:115], v[28:31]\n"
        "ds_read_b64_tr_b16 a[152:153], v234 offset:30976\n"
        "v_mfma_f32_16x16x32_bf16 v[68:71], a[216:219], v[116:119], v[68:71]\n"
        "ds_read_b64_tr_b16 a[154:155], v234 offset:36096\n"
        "v_mfma_f32_16x16x32_bf16 v[192:195], a[216:219], v[120:123], v[192:195]\n"
        "ds_read_b64_tr_b16 a[216:217], v234 offset:20672\n"
        "v_mfma_f32_16x16x32_bf16 a[28:31], a[220:223], v[108:111], a[28:31]\n"
        "ds_read_b64_tr_b16 a[218:219], v234 offset:25792\n"
        "v_mfma_f32_16x16x32_bf16 v[32:35], a[220:223], v[112:115], v[32:35]\n"
        "ds_read_b64_tr_b16 a[156:157], v235 offset:31008\n"
        "v_mfma_f32_16x16x32_bf16 v[72:75], a[220:223], v[116:119], v[72:75]\n"
        "ds_read_b64_tr_b16 a[158:159], v235 offset:36128\n"
        "v_mfma_f32_16x16x32_bf16 v[196:199], a[220:223], v[120:123], v[196:199]\n"
        "s_waitcnt lgkmcnt(6)\n"
        "ds_read_b64_tr_b16 a[220:221], v235 offset:20704\n"
        "v_mfma_f32_16x16x32_bf16 a[32:35], a[224:227], v[108:111], a[32:35]\n"
        "ds_read_b64_tr_b16 a[222:223], v235 offset:25824\n"
        "v_mfma_f32_16x16x32_bf16 v[36:39], a[224:227], v[112:115], v[36:39]\n"
        "v_mfma_f32_16x16x32_bf16 v[76:79], a[224:227], v[116:119], v[76:79]\n"
        "v_mfma_f32_16x16x32_bf16 v[200:203], a[224:227], v[120:123], v[200:203]\n"
        "ds_read_b64_tr_b16 a[224:225], v234 offset:20736\n"
        "v_mfma_f32_16x16x32_bf16 a[36:39], a[228:231], v[108:111], a[36:39]\n"
        "ds_read_b64_tr_b16 a[226:227], v234 offset:25856\n"
        "v_mfma_f32_16x16x32_bf16 v[40:43], a[228:231], v[112:115], v[40:43]\n"
        "v_mfma_f32_16x16x32_bf16 v[80:83], a[228:231], v[116:119], v[80:83]\n"
        "v_mfma_f32_16x16x32_bf16 v[204:207], a[228:231], v[120:123], v[204:207]\n"
        "s_waitcnt vmcnt(12)\n"
        "ds_read_b64_tr_b16 a[228:229], v235 offset:20768\n"
        "v_mfma_f32_16x16x32_bf16 v[4:7], a[192:195], v[132:135], v[4:7]\n"
        "s_waitcnt vmcnt(2)\n"
        "ds_read_b64_tr_b16 a[230:231], v235 offset:25888\n"
        "v_mfma_f32_16x16x32_bf16 v[4:7], a[120:123], v[152:155], v[4:7]\n"
        "v_mfma_f32_16x16x32_bf16 v[44:47], a[192:195], v[136:139], v[44:47]\n"
        "s_waitcnt vmcnt(1)\n"
        "v_mfma_f32_16x16x32_bf16 v[44:47], a[120:123], v[156:159], v[44:47]\n"
        "v_mfma_f32_16x16x32_bf16 v[168:171], a[192:195], v[140:143], v[168:171]\n"
        "s_waitcnt vmcnt(0)\n"
        "v_mfma_f32_16x16x32_bf16 v[168:171], a[120:123], v[160:163], v[168:171]\n"
        "v_mfma_f32_16x16x32_bf16 v[208:211], a[192:195], v[144:147], v[208:211]\n"
        "v_mfma_f32_16x16x32_bf16 v[208:211], a[120:123], v[164:167], v[208:211]\n"
        "v_mfma_f32_16x16x32_bf16 a[0:3], a[192:195], v[128:131], a[0:3]\n"
        "v_mfma_f32_16x16x32_bf16 a[0:3], a[120:123], v[148:151], a[0:3]\n"
        "v_cvt_pk_bf16_f32 v4, v4, v5\n"
        "v_cvt_pk_bf16_f32 v5, v6, v7\n"
        "v_mfma_f32_16x16x32_bf16 v[8:11], a[196:199], v[132:135], v[8:11]\n"
        "v_cvt_pk_bf16_f32 v44, v44, v45\n"
        "v_cvt_pk_bf16_f32 v45, v46, v47\n"
        "v_mfma_f32_16x16x32_bf16 v[8:11], a[124:127], v[152:155], v[8:11]\n"
        "v_cvt_pk_bf16_f32 v168, v168, v169\n"
        "v_cvt_pk_bf16_f32 v169, v170, v171\n"
        "v_mfma_f32_16x16x32_bf16 v[48:51], a[196:199], v[136:139], v[48:51]\n"
        "v_cvt_pk_bf16_f32 v84, v208, v209\n"
        "v_cvt_pk_bf16_f32 v85, v210, v211\n"
        "v_mfma_f32_16x16x32_bf16 v[48:51], a[124:127], v[156:159], v[48:51]\n"
        "v_mfma_f32_16x16x32_bf16 v[172:175], a[196:199], v[140:143], v[172:175]\n"
        "v_mfma_f32_16x16x32_bf16 v[172:175], a[124:127], v[160:163], v[172:175]\n"
        "v_cvt_pk_bf16_f32 v6, v8, v9\n"
        "v_cvt_pk_bf16_f32 v7, v10, v11\n"
        "v_mfma_f32_16x16x32_bf16 v[212:215], a[196:199], v[144:147], v[212:215]\n"
        "buffer_store_dwordx4 v[4:7], v228, s[40:43], s60 offen offset:0 sc1\n"
        "v_mfma_f32_16x16x32_bf16 v[212:215], a[124:127], v[164:167], v[212:215]\n"
        "v_mfma_f32_16x16x32_bf16 a[4:7], a[196:199], v[128:131], a[4:7]\n"
        "v_cvt_pk_bf16_f32 v46, v48, v49\n"
        "v_cvt_pk_bf16_f32 v47, v50, v51\n"
        "v_mfma_f32_16x16x32_bf16 a[4:7], a[124:127], v[148:151], a[4:7]\n"
        "v_cvt_pk_bf16_f32 v170, v172, v173\n"
        "v_cvt_pk_bf16_f32 v171, v174, v175\n"
        "v_mfma_f32_16x16x32_bf16 v[12:15], a[200:203], v[132:135], v[12:15]\n"
        "buffer_store_dwordx4 v[44:47], v228, s[40:43], s61 offen offset:0 sc1\n"
        "v_mfma_f32_16x16x32_bf16 v[12:15], a[128:131], v[152:155], v[12:15]\n"
        "v_cvt_pk_bf16_f32 v86, v212, v213\n"
        "v_cvt_pk_bf16_f32 v87, v214, v215\n"
        "v_mfma_f32_16x16x32_bf16 v[52:55], a[200:203], v[136:139], v[52:55]\n"
        "v_mfma_f32_16x16x32_bf16 v[52:55], a[128:131], v[156:159], v[52:55]\n"
        "buffer_store_dwordx4 v[168:171], v228, s[40:43], s62 offen offset:0 sc1\n"
        "v_mfma_f32_16x16x32_bf16 v[176:179], a[200:203], v[140:143], v[176:179]\n"
        "v_mfma_f32_16x16x32_bf16 v[176:179], a[128:131], v[160:163], v[176:179]\n"
        "v_mfma_f32_16x16x32_bf16 v[216:219], a[200:203], v[144:147], v[216:219]\n"
        "v_cvt_pk_bf16_f32 v12, v12, v13\n"
        "v_cvt_pk_bf16_f32 v13, v14, v15\n"
        "v_mfma_f32_16x16x32_bf16 v[216:219], a[128:131], v[164:167], v[216:219]\n"
        "buffer_store_dwordx4 v[84:87], v229, s[48:51], s63 offen offset:1024 sc1\n"
        "v_mfma_f32_16x16x32_bf16 a[8:11], a[200:203], v[128:131], a[8:11]\n"
        "v_cvt_pk_bf16_f32 v52, v52, v53\n"
        "v_cvt_pk_bf16_f32 v53, v54, v55\n"
        "v_mfma_f32_16x16x32_bf16 a[8:11], a[128:131], v[148:151], a[8:11]\n"
        "v_cvt_pk_bf16_f32 v176, v176, v177\n"
        "v_cvt_pk_bf16_f32 v177, v178, v179\n"
        "v_mfma_f32_16x16x32_bf16 v[16:19], a[204:207], v[132:135], v[16:19]\n"
        "v_cvt_pk_bf16_f32 v84, v216, v217\n"
        "v_cvt_pk_bf16_f32 v85, v218, v219\n"
        "v_mfma_f32_16x16x32_bf16 v[16:19], a[132:135], v[152:155], v[16:19]\n"
        "v_mfma_f32_16x16x32_bf16 v[56:59], a[204:207], v[136:139], v[56:59]\n"
        "v_mfma_f32_16x16x32_bf16 v[56:59], a[132:135], v[156:159], v[56:59]\n"
        "v_mfma_f32_16x16x32_bf16 v[180:183], a[204:207], v[140:143], v[180:183]\n"
        "v_mfma_f32_16x16x32_bf16 v[180:183], a[132:135], v[160:163], v[180:183]\n"
        "v_mfma_f32_16x16x32_bf16 v[220:223], a[204:207], v[144:147], v[220:223]\n"
        "v_mfma_f32_16x16x32_bf16 v[220:223], a[132:135], v[164:167], v[220:223]\n"
        "v_mfma_f32_16x16x32_bf16 a[12:15], a[204:207], v[128:131], a[12:15]\n"
        "v_mfma_f32_16x16x32_bf16 a[12:15], a[132:135], v[148:151], a[12:15]\n"
        "v_cvt_pk_bf16_f32 v14, v16, v17\n"
        "v_cvt_pk_bf16_f32 v15, v18, v19\n"
        "v_mfma_f32_16x16x32_bf16 v[20:23], a[208:211], v[132:135], v[20:23]\n"
        "v_cvt_pk_bf16_f32 v54, v56, v57\n"
        "v_cvt_pk_bf16_f32 v55, v58, v59\n"
        "v_mfma_f32_16x16x32_bf16 v[20:23], a[136:139], v[152:155], v[20:23]\n"
        "v_cvt_pk_bf16_f32 v178, v180, v181\n"
        "v_cvt_pk_bf16_f32 v179, v182, v183\n"
        "v_mfma_f32_16x16x32_bf16 v[60:63], a[208:211], v[136:139], v[60:63]\n"
        "buffer_store_dwordx4 v[12:15], v228, s[40:43], s60 offen offset:1024 sc1\n"
        "v_mfma_f32_16x16x32_bf16 v[60:63], a[136:139], v[156:159], v[60:63]\n"
        "v_cvt_pk_bf16_f32 v86, v220, v221\n"
        "v_cvt_pk_bf16_f32 v87, v222, v223\n"
        "v_mfma_f32_16x16x32_bf16 v[184:187], a[208:211], v[140:143], v[184:187]\n"
        "v_cvt_pk_bf16_f32 v20, v20, v21\n"
        "v_cvt_pk_bf16_f32 v21, v22, v23\n"
        "v_mfma_f32_16x16x32_bf16 v[184:187], a[136:139], v[160:163], v[184:187]\n"
        "buffer_store_dwordx4 v[52:55], v228, s[40:43], s61 offen offset:1024 sc1\n"
        "v_mfma_f32_16x16x32_bf16 v[224:227], a[208:211], v[144:147], v[224:227]\n"
        "v_cvt_pk_bf16_f32 v60, v60, v61\n"
        "v_cvt_pk_bf16_f32 v61, v62, v63\n"
        "v_mfma_f32_16x16x32_bf16 v[224:227], a[136:139], v[164:167], v[224:227]\n"
        "v_mfma_f32_16x16x32_bf16 a[16:19], a[208:211], v[128:131], a[16:19]\n"
        "buffer_store_dwordx4 v[176:179], v228, s[40:43], s62 offen offset:1024 sc1\n"
        "v_mfma_f32_16x16x32_bf16 a[16:19], a[136:139], v[148:151], a[16:19]\n"
        "v_cvt_pk_bf16_f32 v184, v184, v185\n"
        "v_cvt_pk_bf16_f32 v185, v186, v187\n"
        "v_mfma_f32_16x16x32_bf16 v[24:27], a[212:215], v[132:135], v[24:27]\n"
        "v_mfma_f32_16x16x32_bf16 v[24:27], a[140:143], v[152:155], v[24:27]\n"
        "buffer_store_dwordx4 v[84:87], v229, s[48:51], s63 offen offset:2048 sc1\n"
        "v_mfma_f32_16x16x32_bf16 v[64:67], a[212:215], v[136:139], v[64:67]\n"
        "v_mfma_f32_16x16x32_bf16 v[64:67], a[140:143], v[156:159], v[64:67]\n"
        "v_cvt_pk_bf16_f32 v84, v224, v225\n"
        "v_cvt_pk_bf16_f32 v85, v226, v227\n"
        "v_mfma_f32_16x16x32_bf16 v[188:191], a[212:215], v[140:143], v[188:191]\n"
        "buffer_store_dwordx4 v[84:87], v229, s[48:51], s63 offen offset:3072 sc1\n"
        "v_mfma_f32_16x16x32_bf16 v[188:191], a[140:143], v[160:163], v[188:191]\n"
        "v_cvt_pk_bf16_f32 v22, v24, v25\n"
        "v_cvt_pk_bf16_f32 v23, v26, v27\n"
        "v_mfma_f32_16x16x32_bf16 a[20:23], a[212:215], v[128:131], a[20:23]\n"
        "s_waitcnt lgkmcnt(0)\n"
        "v_cvt_pk_bf16_f32 v62, v64, v65\n"
        "v_cvt_pk_bf16_f32 v63, v66, v67\n"
        "v_mfma_f32_16x16x32_bf16 a[20:23], a[140:143], v[148:151], a[20:23]\n"
        "buffer_store_dwordx4 v[20:23], v228, s[40:43], s60 offen offset:2048 sc1\n"
        "v_mfma_f32_16x16x32_bf16 v[28:31], a[216:219], v[132:135], v[28:31]\n"
        "v_cvt_pk_bf16_f32 v186, v188, v189\n"
        "v_cvt_pk_bf16_f32 v187, v190, v191\n"
        "v_mfma_f32_16x16x32_bf16 v[28:31], a[144:147], v[152:155], v[28:31]\n"
        "v_mfma_f32_16x16x32_bf16 v[68:71], a[216:219], v[136:139], v[68:71]\n"
        "buffer_store_dwordx4 v[60:63], v228, s[40:43], s61 offen offset:2048 sc1\n"
        "v_mfma_f32_16x16x32_bf16 v[68:71], a[144:147], v[156:159], v[68:71]\n"
        "v_mfma_f32_16x16x32_bf16 v[192:195], a[216:219], v[140:143], v[192:195]\n"
        "v_mfma_f32_16x16x32_bf16 v[192:195], a[144:147], v[160:163], v[192:195]\n"
        "buffer_store_dwordx4 v[184:187], v228, s[40:43], s62 offen offset:2048 sc1\n"
        "v_mfma_f32_16x16x32_bf16 a[24:27], a[216:219], v[128:131], a[24:27]\n"
        "v_mfma_f32_16x16x32_bf16 a[24:27], a[144:147], v[148:151], a[24:27]\n"
        "v_cvt_pk_bf16_f32 v28, v28, v29\n"
        "v_cvt_pk_bf16_f32 v29, v30, v31\n"
        "v_mfma_f32_16x16x32_bf16 v[32:35], a[220:223], v[132:135], v[32:35]\n"
        "v_cvt_pk_bf16_f32 v68, v68, v69\n"
        "v_cvt_pk_bf16_f32 v69, v70, v71\n"
        "v_mfma_f32_16x16x32_bf16 v[32:35], a[148:151], v[152:155], v[32:35]\n"
        "v_cvt_pk_bf16_f32 v192, v192, v193\n"
        "v_cvt_pk_bf16_f32 v193, v194, v195\n"
        "v_mfma_f32_16x16x32_bf16 v[72:75], a[220:223], v[136:139], v[72:75]\n"
        "v_mfma_f32_16x16x32_bf16 v[72:75], a[148:151], v[156:159], v[72:75]\n"
        "v_mfma_f32_16x16x32_bf16 v[196:199], a[220:223], v[140:143], v[196:199]\n"
        "v_mfma_f32_16x16x32_bf16 v[196:199], a[148:151], v[160:163], v[196:199]\n"
        "v_mfma_f32_16x16x32_bf16 a[28:31], a[220:223], v[128:131], a[28:31]\n"
        "v_mfma_f32_16x16x32_bf16 a[28:31], a[148:151], v[148:151], a[28:31]\n"
        "v_cvt_pk_bf16_f32 v30, v32, v33\n"
        "v_cvt_pk_bf16_f32 v31, v34, v35\n"
        "v_mfma_f32_16x16x32_bf16 v[36:39], a[224:227], v[132:135], v[36:39]\n"
        "buffer_store_dwordx4 v[28:31], v228, s[40:43], s60 offen offset:3072 sc1\n"
        "v_mfma_f32_16x16x32_bf16 v[36:39], a[152:155], v[152:155], v[36:39]\n"
        "v_cvt_pk_bf16_f32 v70, v72, v73\n"
        "v_cvt_pk_bf16_f32 v71, v74, v75\n"
        "v_mfma_f32_16x16x32_bf16 v[76:79], a[224:227], v[136:139], v[76:79]\n"
        "v_cvt_pk_bf16_f32 v194, v196, v197\n"
        "v_cvt_pk_bf16_f32 v195, v198, v199\n"
        "v_mfma_f32_16x16x32_bf16 v[76:79], a[152:155], v[156:159], v[76:79]\n"
        "buffer_store_dwordx4 v[68:71], v228, s[40:43], s61 offen offset:3072 sc1\n"
        "v_mfma_f32_16x16x32_bf16 v[200:203], a[224:227], v[140:143], v[200:203]\n"
        "v_cvt_pk_bf16_f32 v36, v36, v37\n"
        "v_cvt_pk_bf16_f32 v37, v38, v39\n"
        "v_mfma_f32_16x16x32_bf16 v[200:203], a[152:155], v[160:163], v[200:203]\n"
        "v_mfma_f32_16x16x32_bf16 v[40:43], a[228:231], v[132:135], v[40:43]\n"
        "buffer_store_dwordx4 v[192:195], v228, s[40:43], s62 offen offset:3072 sc1\n"
        "v_mfma_f32_16x16x32_bf16 v[40:43], a[156:159], v[152:155], v[40:43]\n"
        "v_cvt_pk_bf16_f32 v76, v76, v77\n"
        "v_cvt_pk_bf16_f32 v77, v78, v79\n"
        "v_mfma_f32_16x16x32_bf16 v[80:83], a[228:231], v[136:139], v[80:83]\n"
        "v_mfma_f32_16x16x32_bf16 v[80:83], a[156:159], v[156:159], v[80:83]\n"
        "v_mfma_f32_16x16x32_bf16 v[204:207], a[228:231], v[140:143], v[204:207]\n"
        "v_cvt_pk_bf16_f32 v200, v200, v201\n"
        "v_cvt_pk_bf16_f32 v201, v202, v203\n"
        "v_mfma_f32_16x16x32_bf16 v[204:207], a[156:159], v[160:163], v[204:207]\n"
        "v_cvt_pk_bf16_f32 v38, v40, v41\n"
        "v_cvt_pk_bf16_f32 v39, v42, v43\n"
        "v_mfma_f32_16x16x32_bf16 a[32:35], a[224:227], v[128:131], a[32:35]\n"
        "buffer_store_dwordx4 v[36:39], v229, s[40:43], s60 offen offset:0 sc1\n"
        "v_mfma_f32_16x16x32_bf16 a[32:35], a[152:155], v[148:151], a[32:35]\n"
        "v_cvt_pk_bf16_f32 v78, v80, v81\n"
        "v_cvt_pk_bf16_f32 v79, v82, v83\n"
        "v_mfma_f32_16x16x32_bf16 a[36:39], a[228:231], v[128:131], a[36:39]\n"
        "v_cvt_pk_bf16_f32 v202, v204, v205\n"
        "v_cvt_pk_bf16_f32 v203, v206, v207\n"
        "v_mfma_f32_16x16x32_bf16 a[36:39], a[156:159], v[148:151], a[36:39]\n"
        "buffer_store_dwordx4 v[76:79], v229, s[40:43], s61 offen offset:0 sc1\n"
        "s_nop 0\n"
        "s_nop 0\n"
        "s_nop 0\n"
        "buffer_store_dwordx4 v[200:203], v229, s[40:43], s62 offen offset:0 sc1\n"
        "s_lshl_b32 s80, %6, 2\n"
        "s_add_u32 s80, s80, 4352\n"
        "s_lshl_b32 s81, %7, 7\n"
        "s_add_u32 s67, s80, s81\n"
        "s_and_b32 s81, s81, 384\n"
        "s_add_u32 s80, s80, s81\n"
        "v_min_u32 v1, 2, v0\n"
        "v_add3_u32 v1, v1, s85, 1\n"
        "v_and_b32 v1, 3, v1\n"
        "v_lshl_add_u32 v243, v1, 9, s80\n"
        "buffer_load_dword v242, v243, s[96:99], 0 offen sc1\n"
        "s_mov_b64 s[48:49], %15\n"
        "s_mov_b32 s50, 735040\n"
        "s_mov_b32 s51, 0x20000\n"
        "v_and_b32 v1, 15, v0\n"
        "v_mul_u32_u24 v1, 2560, v1\n"
        "v_lshrrev_b32 v2, 4, v0\n"
        "v_lshl_add_u32 v232, v2, 3, v1\n"
        "s_mul_i32 s81, s78, 160\n"
        "s_lshl_b32 s80, %6, 2\n"
        "s_add_u32 s80, s80, s85\n"
        "s_mul_i32 s80, s80, 40960\n"
        "s_add_u32 s64, s80, s81\n"
        "s_sub_u32 s65, s80, s81\n"
        "s_lshr_b32 s80, %6, 1\n"
        "s_mul_i32 s80, s80, 40960\n"
        "s_add_u32 s80, s80, 655360\n"
        "s_add_u32 s66, s80, s81\n"
        "s_add_u32 s60, s68, 65536\n"
        "s_add_u32 s61, s68, 131072\n"
        "s_cmp_eq_u32 %6, s85\n"
        "s_cselect_b32 s46, s42, 0\n"
        "s_cselect_b32 s58, s50, 0\n"
        "s_mov_b64 s[44:45], %14\n"
        "s_mov_b32 s47, 0x20000\n"
        "s_mov_b64 s[56:57], %15\n"
        "v_mov_b32 v236, 0\n"
        "v_mov_b32 v237, 0\n"
        "v_mov_b32 v240, 0\n"
        "v_mov_b32 v241, 0\n"
        "v_bfe_u32 v3, v0, 2, 2\n"
        "v_cmp_eq_u32 vcc, v2, v3\n"
        "v_and_b32 v3, 1, v0\n"
        "v_lshlrev_b32 v3, 4, v3\n"
        "v_mov_b32 v2, 0x3f80\n"
        "v_lshlrev_b32 v2, v3, v2\n"
        "v_cndmask_b32 v2, 0, v2, vcc\n"
        "v_and_b32 v3, 2, v0\n"
        "v_cmp_eq_u32 vcc, 0, v3\n"
        "v_accvgpr_read_b32 v88, a0\n"
        "v_accvgpr_read_b32 v89, a1\n"
        "v_accvgpr_read_b32 v90, a2\n"
        "v_accvgpr_read_b32 v91, a3\n"
        "v_accvgpr_read_b32 v92, a4\n"
        "v_accvgpr_read_b32 v93, a5\n"
        "v_accvgpr_read_b32 v94, a6\n"
        "v_accvgpr_read_b32 v95, a7\n"
        "v_accvgpr_read_b32 v96, a8\n"
        "v_accvgpr_read_b32 v97, a9\n"
        "v_accvgpr_read_b32 v98, a10\n"
        "v_accvgpr_read_b32 v99, a11\n"
        "v_accvgpr_read_b32 v100, a12\n"
        "v_accvgpr_read_b32 v101, a13\n"
        "v_accvgpr_read_b32 v102, a14\n"
        "v_accvgpr_read_b32 v103, a15\n"
        "v_accvgpr_read_b32 v104, a16\n"
        "v_accvgpr_read_b32 v105, a17\n"
        "v_accvgpr_read_b32 v106, a18\n"
        "v_accvgpr_read_b32 v107, a19\n"
        "v_accvgpr_read_b32 v108, a20\n"
        "v_accvgpr_read_b32 v109, a21\n"
        "v_accvgpr_read_b32 v110, a22\n"
        "v_accvgpr_read_b32 v111, a23\n"
        "v_accvgpr_read_b32 v112, a24\n"
        "v_accvgpr_read_b32 v113, a25\n"
        "v_accvgpr_read_b32 v114, a26\n"
        "v_accvgpr_read_b32 v115, a27\n"
        "v_accvgpr_read_b32 v116, a28\n"
        "v_accvgpr_read_b32 v117, a29\n"
        "v_accvgpr_read_b32 v118, a30\n"
        "v_accvgpr_read_b32 v119, a31\n"
        "v_accvgpr_read_b32 v120, a32\n"
        "v_accvgpr_read_b32 v121, a33\n"
        "v_accvgpr_read_b32 v122, a34\n"
        "v_accvgpr_read_b32 v123, a35\n"
        "v_accvgpr_read_b32 v124, a36\n"
        "v_accvgpr_read_b32 v125, a37\n"
        "v_accvgpr_read_b32 v126, a38\n"
        "v_accvgpr_read_b32 v127, a39\n"
        "v_cndmask_b32 v238, 0, v2, vcc\n"
        "v_cndmask_b32 v239, v2, v236, vcc\n"
        "v_mov_b32 v1, s95\n"
        "s_mov_b32 s53, 0\n"
        "s_waitcnt vmcnt(1) lgkmcnt(0)\n"
        "buffer_store_dword v1, off, s[96:99], s67 sc1\n"
        "s_waitcnt vmcnt(1)\n"
        ".Lr_POLL:\n"
        "v_cmp_eq_u32 vcc, s95, v242\n"
        "s_andn2_b32 s54, vcc_lo, s53\n"
        "s_or_b32 s53, s53, vcc_lo\n"
        "s_bitcmp1_b32 s54, 0\n"
        "s_cbranch_scc0 .Lr_NOTYET0\n"
        "buffer_load_dwordx4 v[12:15], v228, s[40:43], s61 offen offset:0 sc1\n"
        "buffer_load_dwordx4 v[24:27], v228, s[40:43], s61 offen offset:1024 sc1\n"
        "buffer_load_dwordx4 v[36:39], v228, s[40:43], s61 offen offset:2048 sc1\n"
        "buffer_load_dwordx4 v[48:51], v228, s[40:43], s61 offen offset:3072 sc1\n"
        "buffer_load_dwordx4 v[60:63], v229, s[40:43], s61 offen offset:0 sc1\n"
        "buffer_load_dwordx4 v[72:75], v229, s[44:47], s61 offen offset:1024 sc1\n"
        "buffer_load_dwordx4 v[128:131], v229, s[44:47], s61 offen offset:2048 sc1\n"
        "buffer_load_dwordx4 v[140:143], v229, s[44:47], s61 offen offset:3072 sc1\n"
        ".Lr_NOTYET0:\n"
        "s_bitcmp1_b32 s54, 1\n"
        "s_cbranch_scc0 .Lr_NOTYET1\n"
        "buffer_load_dwordx4 v[8:11], v228, s[40:43], s60 offen offset:0 sc1\n"
        "buffer_load_dwordx4 v[20:23], v228, s[40:43], s60 offen offset:1024 sc1\n"
        "buffer_load_dwordx4 v[32:35], v228, s[40:43], s60 offen offset:2048 sc1\n"
        "buffer_load_dwordx4 v[44:47], v228, s[40:43], s60 offen offset:3072 sc1\n"
        "buffer_load_dwordx4 v[56:59], v229, s[40:43], s60 offen offset:0 sc1\n"
        "buffer_load_dwordx4 v[68:71], v229, s[44:47], s60 offen offset:1024 sc1\n"
        "buffer_load_dwordx4 v[80:83], v229, s[44:47], s60 offen offset:2048 sc1\n"
        "buffer_load_dwordx4 v[136:139], v229, s[44:47], s60 offen offset:3072 sc1\n"
        ".Lr_NOTYET1:\n"
        "s_bitcmp1_b32 s54, 2\n"
        "s_cbranch_scc0 .Lr_NOTYET2\n"
        "buffer_load_dwordx4 v[4:7], v228, s[40:43], s68 offen offset:0 sc1\n"
        "buffer_load_dwordx4 v[16:19], v228, s[40:43], s68 offen offset:1024 sc1\n"
        "buffer_load_dwordx4 v[28:31], v228, s[40:43], s68 offen offset:2048 sc1\n"
        "buffer_load_dwordx4 v[40:43], v228, s[40:43], s68 offen offset:3072 sc1\n"
        "buffer_load_dwordx4 v[52:55], v229, s[40:43], s68 offen offset:0 sc1\n"
        "buffer_load_dwordx4 v[64:67], v229, s[44:47], s68 offen offset:1024 sc1\n"
        "buffer_load_dwordx4 v[76:79], v229, s[44:47], s68 offen offset:2048 sc1\n"
        "buffer_load_dwordx4 v[132:135], v229, s[44:47], s68 offen offset:3072 sc1\n"
        ".Lr_NOTYET2:\n"
        "s_cmp_eq_u32 s53, -1\n"
        "s_cbranch_scc1 .Lr_POLLED\n"
        "buffer_load_dword v242, v243, s[96:99], 0 offen sc1\n"
        "s_sub_u32 s82, s82, 1\n"
        "s_waitcnt vmcnt(0)\n"
        "s_cbranch_scc0 .Lr_POLL\n"
        ".Lr_POLLED:\n"
        "s_waitcnt vmcnt(7)\n"
        "v_mfma_f32_16x16x32_bf16 v[88:91], v[238:241], v[4:7], v[88:91]\n"
        "v_mfma_f32_16x16x32_bf16 v[92:95], v[236:239], v[4:7], v[92:95]\n"
        "v_mfma_f32_16x16x32_bf16 v[88:91], v[238:241], v[8:11], v[88:91]\n"
        "v_mfma_f32_16x16x32_bf16 v[92:95], v[236:239], v[8:11], v[92:95]\n"
        "v_mfma_f32_16x16x32_bf16 v[88:91], v[238:241], v[12:15], v[88:91]\n"
        "v_mfma_f32_16x16x32_bf16 v[92:95], v[236:239], v[12:15], v[92:95]\n"
        "s_waitcnt vmcnt(6)\n"
        "v_mfma_f32_16x16x32_bf16 v[96:99], v[238:241], v[16:19], v[96:99]\n"
        "v_mfma_f32_16x16x32_bf16 v[100:103], v[236:239], v[16:19], v[100:103]\n"
        "v_mfma_f32_16x16x32_bf16 v[96:99], v[238:241], v[20:23], v[96:99]\n"
        "v_mfma_f32_16x16x32_bf16 v[100:103], v[236:239], v[20:23], v[100:103]\n"
        "v_mfma_f32_16x16x32_bf16 v[96:99], v[238:241], v[24:27], v[96:99]\n"
        "v_mfma_f32_16x16x32_bf16 v[100:103], v[236:239], v[24:27], v[100:103]\n"
        "v_cvt_pk_bf16_f32 v88, v88, v89\n"
        "v_cvt_pk_bf16_f32 v89, v90, v91\n"
        "buffer_store_dwordx2 v[88:89], v232, s[48:51], s64 offen offset:0\n"
        "v_cvt_pk_bf16_f32 v92, v92, v93\n"
        "v_cvt_pk_bf16_f32 v93, v94, v95\n"
        "buffer_store_dwordx2 v[92:93], v232, s[48:51], s64 offen offset:32\n"
        "s_waitcnt vmcnt(7)\n"
        "v_mfma_f32_16x16x32_bf16 v[104:107], v[238:241], v[28:31], v[104:107]\n"
        "v_mfma_f32_16x16x32_bf16 v[108:111], v[236:239], v[28:31], v[108:111]\n"
        "v_mfma_f32_16x16x32_bf16 v[104:107], v[238:241], v[32:35], v[104:107]\n"
        "v_mfma_f32_16x16x32_bf16 v[108:111], v[236:239], v[32:35], v[108:111]\n"
        "v_mfma_f32_16x16x32_bf16 v[104:107], v[238:241], v[36:39], v[104:107]\n"
        "v_mfma_f32_16x16x32_bf16 v[108:111], v[236:239], v[36:39], v[108:111]\n"
        "v_cvt_pk_bf16_f32 v96, v96, v97\n"
        "v_cvt_pk_bf16_f32 v97, v98, v99\n"
        "buffer_store_dwordx2 v[96:97], v232, s[48:51], s64 offen offset:64\n"
        "v_cvt_pk_bf16_f32 v100, v100, v101\n"
        "v_cvt_pk_bf16_f32 v101, v102, v103\n"
        "buffer_store_dwordx2 v[100:101], v232, s[48:51], s64 offen offset:96\n"
        "s_waitcnt vmcnt(8)\n"
        "v_mfma_f32_16x16x32_bf16 v[112:115], v[238:241], v[40:43], v[112:115]\n"
        "v_mfma_f32_16x16x32_bf16 v[116:119], v[236:239], v[40:43], v[116:119]\n"
        "v_mfma_f32_16x16x32_bf16 v[112:115], v[238:241], v[44:47], v[112:115]\n"
        "v_mfma_f32_16x16x32_bf16 v[116:119], v[236:239], v[44:47], v[116:119]\n"
        "v_mfma_f32_16x16x32_bf16 v[112:115], v[238:241], v[48:51], v[112:115]\n"
        "v_mfma_f32_16x16x32_bf16 v[116:119], v[236:239], v[48:51], v[116:119]\n"
        "v_cvt_pk_bf16_f32 v104, v104, v105\n"
        "v_cvt_pk_bf16_f32 v105, v106, v107\n"
        "buffer_store_dwordx2 v[104:105], v232, s[48:51], s64 offen offset:128\n"
        "v_cvt_pk_bf16_f32 v108, v108, v109\n"
        "v_cvt_pk_bf16_f32 v109, v110, v111\n"
        "buffer_store_dwordx2 v[108:109], v232, s[48:51], s65 offen offset:160\n"
        "s_waitcnt vmcnt(9)\n"
        "v_mfma_f32_16x16x32_bf16 v[120:123], v[238:241], v[52:55], v[120:123]\n"
        "v_mfma_f32_16x16x32_bf16 v[124:127], v[236:239], v[52:55], v[124:127]\n"
        "v_mfma_f32_16x16x32_bf16 v[120:123], v[238:241], v[56:59], v[120:123]\n"
        "v_mfma_f32_16x16x32_bf16 v[124:127], v[236:239], v[56:59], v[124:127]\n"
        "v_mfma_f32_16x16x32_bf16 v[120:123], v[238:241], v[60:63], v[120:123]\n"
        "v_mfma_f32_16x16x32_bf16 v[124:127], v[236:239], v[60:63], v[124:127]\n"
        "v_cvt_pk_bf16_f32 v112, v112, v113\n"
        "v_cvt_pk_bf16_f32 v113, v114, v115\n"
        "buffer_store_dwordx2 v[112:113], v232, s[48:51], s65 offen offset:192\n"
        "v_cvt_pk_bf16_f32 v116, v116, v117\n"
        "v_cvt_pk_bf16_f32 v117, v118, v119\n"
        "buffer_store_dwordx2 v[116:117], v232, s[48:51], s65 offen offset:224\n"
        "s_waitcnt vmcnt(10)\n"
        "v_mfma_f32_16x16x32_bf16 v[208:211], v[238:241], v[64:67], v[208:211]\n"
        "v_mfma_f32_16x16x32_bf16 v[212:215], v[236:239], v[64:67], v[212:215]\n"
        "v_mfma_f32_16x16x32_bf16 v[208:211], v[238:241], v[68:71], v[208:211]\n"
        "v_mfma_f32_16x16x32_bf16 v[212:215], v[236:239], v[68:71], v[212:215]\n"
        "v_mfma_f32_16x16x32_bf16 v[208:211], v[238:241], v[72:75], v[208:211]\n"
        "v_mfma_f32_16x16x32_bf16 v[212:215], v[236:239], v[72:75], v[212:215]\n"
        "v_cvt_pk_bf16_f32 v120, v120, v121\n"
        "v_cvt_pk_bf16_f32 v121, v122, v123\n"
        "buffer_store_dwordx2 v[120:121], v232, s[48:51], s65 offen offset:256\n"
        "v_cvt_pk_bf16_f32 v124, v124, v125\n"
        "v_cvt_pk_bf16_f32 v125, v126, v127\n"
        "buffer_store_dwordx2 v[124:125], v232, s[48:51], s65 offen offset:288\n"
        "s_waitcnt vmcnt(11)\n"
        "v_mfma_f32_16x16x32_bf16 v[216:219], v[238:241], v[76:79], v[216:219]\n"
        "v_mfma_f32_16x16x32_bf16 v[220:223], v[236:239], v[76:79], v[220:223]\n"
        "v_mfma_f32_16x16x32_bf16 v[216:219], v[238:241], v[80:83], v[216:219]\n"
        "v_mfma_f32_16x16x32_bf16 v[220:223], v[236:239], v[80:83], v[220:223]\n"
        "v_mfma_f32_16x16x32_bf16 v[216:219], v[238:241], v[128:131], v[216:219]\n"
        "v_mfma_f32_16x16x32_bf16 v[220:223], v[236:239], v[128:131], v[220:223]\n"
        "v_cvt_pk_bf16_f32 v208, v208, v209\n"
        "v_cvt_pk_bf16_f32 v209, v210, v211\n"
        "buffer_store_dwordx2 v[208:209], v232, s[56:59], s66 offen offset:0\n"
        "v_cvt_pk_bf16_f32 v212, v212, v213\n"
        "v_cvt_pk_bf16_f32 v213, v214, v215\n"
        "buffer_store_dwordx2 v[212:213], v232, s[56:59], s66 offen offset:32\n"
        "s_waitcnt vmcnt(12)\n"
        "v_mfma_f32_16x16x32_bf16 v[224:227], v[238:241], v[132:135], v[224:227]\n"
        "v_mfma_f32_16x16x32_bf16 v[224:227], v[238:241], v[136:139], v[224:227]\n"
        "v_mfma_f32_16x16x32_bf16 v[224:227], v[238:241], v[140:143], v[224:227]\n"
        "v_cvt_pk_bf16_f32 v216, v216, v217\n"
        "v_cvt_pk_bf16_f32 v217, v218, v219\n"
        "buffer_store_dwordx2 v[216:217], v232, s[56:59], s66 offen offset:64\n"
        "v_cvt_pk_bf16_f32 v220, v220, v221\n"
        "v_cvt_pk_bf16_f32 v221, v222, v223\n"
        "buffer_store_dwordx2 v[220:221], v232, s[56:59], s66 offen offset:96\n"
        "s_nop 4\n"
        "v_cvt_pk_bf16_f32 v224, v224, v225\n"
        "v_cvt_pk_bf16_f32 v225, v226, v227\n"
        "buffer_store_dwordx2 v[224:225], v232, s[56:59], s66 offen offset:128\n"
        "s_or_b32 s80, %6, %7\n"
        "s_cbranch_scc1 .Lr_NOBUMP\n"
        "s_add_u32 s80, s94, 1\n"
        "v_mov_b32 v1, s80\n"
        "buffer_store_dword v1, off, s[96:99], 0 sc1\n"
        ".Lr_NOBUMP:\n"
        "s_waitcnt vmcnt(0) lgkmcnt(0)\n"
        "s_branch .Lr_FIN\n"
        ".Lr_SPIN:\n"
        "s_sub_u32 s82, s82, 1\n"
        "s_cbranch_scc1 .Lr_SPINRET\n"
        "s_sleep 2\n"
        "buffer_load_dword v242, v243, s[96:99], s89 offen offset:128 sc1\n"
        "s_waitcnt vmcnt(0)\n"
        "v_cmp_ne_u32 vcc, s95, v242\n"
        "s_cbranch_vccnz .Lr_SPIN\n"
        "s_branch .Lr_SPINRET\n"
        ".Lr_PUB2:\n"
        "s_waitcnt vmcnt(32)\n"
        "s_lshl_b32 s80, %7, 8\n"
        "s_lshl_b32 s81, %6, 2\n"
        "s_add_u32 s80, s80, s81\n"
        "v_mov_b32 v1, s95\n"
        "buffer_store_dword v1, off, s[96:99], s80 offset:256 sc1\n"
        "s_branch .Lr_PUB2RET\n"
        ".Lr_EXIT:\n"
        "s_waitcnt vmcnt(0) lgkmcnt(0)\n"
        ".Lr_FIN:\n"
        "s_mov_b32 %0, s77\n"
        ".Lr_CODE_END:\n"
            OPERANDS);
    }
}

extern "C" void kernel_launch(void* const* d_in, const int* in_sizes, int n_in, void* d_out, int out_size, void* d_ws,
                              size_t ws_size, hipStream_t stream) {
    const cptr* in = (const cptr*)d_in;
    fused_kernel<<<256, 256, 0, stream>>>(in[0], in[1], in[2], in[3], in[5], in[6], in[7], in[8], in[4], in[9], d_ws, d_out);
}
```
